```python
import torch

def _softmax(x, axis=-1):
    return torch.softmax(x, dim=axis)
def _gelu(x):
    return torch.nn.functional.gelu(x, approximate="tanh")
def _silu(x):
    return torch.nn.functional.silu(x)
def _sigmoid(x):
    return torch.sigmoid(x)
def _layernorm(x, axis=-1, eps=1e-5):
    m = x.mean(dim=axis, keepdim=True)
    v = x.var(dim=axis, keepdim=True, unbiased=False)
    return (x - m) / torch.sqrt(v + eps)
def _scatter_add(tgt, idx, src, axis=0):
    return tgt.clone().index_add_(axis, idx, src)
def _segment_sum(src, seg, n):
    out = torch.zeros((n,) + src.shape[1:], dtype=src.dtype)
    return out.index_add_(0, seg, src)
def _pad(x, pw):
    flat = tuple(v for pair in reversed(pw) for v in pair)
    return torch.nn.functional.pad(x, flat)

kernel_name = 'recurrent_scan__grouped_kv_mqa__glu_gated_reglu__prompt'

def setup_inputs():

    _w = (lambda a: (lambda t: t.to(torch.bfloat16) if t.is_floating_point() else t)(torch.as_tensor(a)))
    def _A_setup():
        B, S, D, H = 1, 2048, 512, 32
        Dh = D // H
        return {
            "x": _w((lambda *s: torch.randn(*s, dtype=torch.bfloat16) * 0.05)(B, S, D)),
            "wq": _w((lambda *s: torch.randn(*s, dtype=torch.bfloat16) * 0.2)(D, H * Dh)),
            "wk": _w((lambda *s: torch.randn(*s, dtype=torch.bfloat16) * 0.2)(D, Dh)),
            "wv": _w((lambda *s: torch.randn(*s, dtype=torch.bfloat16) * 0.2)(D, Dh)),
            "wo": _w((lambda *s: torch.randn(*s, dtype=torch.bfloat16) * 0.2)(H * Dh, D)),
        }
    _A_in = _A_setup()

    def _B_setup():
        B, S, D, F = 1, 2048, 512, 1792
        return {
            "x": _w((lambda *s: torch.randn(*s, dtype=torch.bfloat16) * 0.05)(B, S, D)),
            "W_gate": _w((lambda *s: torch.randn(*s, dtype=torch.bfloat16) * 0.1)(D, F) * (2.0 / D) ** 0.5),
            "W_up": _w((lambda *s: torch.randn(*s, dtype=torch.bfloat16) * 0.1)(D, F) * (2.0 / D) ** 0.5),
            "W_down": _w((lambda *s: torch.randn(*s, dtype=torch.bfloat16) * 0.1)(F, D) * (1.0 / F) ** 0.5),
        }
    _B_in = _B_setup()

    return {
        "A_x": _A_in["x"],
        "A_wq": _A_in["wq"],
        "A_wk": _A_in["wk"],
        "A_wv": _A_in["wv"],
        "A_wo": _A_in["wo"],
        "B_x": _B_in["x"],
        "B_W_gate": _B_in["W_gate"],
        "B_W_up": _B_in["W_up"],
        "B_W_down": _B_in["W_down"],
    }

def reference(A_x, A_wq, A_wk, A_wv, A_wo, B_x, B_W_gate, B_W_up, B_W_down):
    def _A_ref(x, wq, wk, wv, wo):
        B, S, D = x.shape
        H = 32
        Dh = D // H
        q = (lambda x, *d: x.permute(*d))((x @ wq).reshape(B, S, H, Dh), 0, 2, 1, 3)
        k = x @ wk
        v = x @ wv
        scores = torch.einsum("bhqd,bkd->bhqk", q, k) * (Dh ** -0.5)
        p = _softmax(scores, axis=-1)
        o = torch.einsum("bhqk,bkd->bhqd", p, v)
        return (lambda x, *d: x.permute(*d))(o, 0, 2, 1, 3).reshape(B, S, H * Dh) @ wo

    def _B_ref(x, W_gate, W_up, W_down):
        gate = torch.einsum("bsd,df->bsf", x, W_gate)
        up = torch.einsum("bsd,df->bsf", x, W_up)
        gate = gate * (lambda x, dt: x.to(dt))(gate > 0, gate.dtype)
        h = gate * up
        return torch.einsum("bsf,fd->bsd", h, W_down)

    x = A_x
    xn = _layernorm(x, axis=-1)
    s = _A_ref(xn, A_wq, A_wk, A_wv, A_wo)
    g = _silu(_B_ref(xn, B_W_gate, B_W_up, B_W_down))
    return x + s * g
```

```cpp
#include <hip/hip_runtime.h>
#include <hip/hip_bf16.h>
#include <stdint.h>

typedef short bf16x8 __attribute__((ext_vector_type(8)));
typedef short s16x4 __attribute__((ext_vector_type(4)));
typedef float f32x16 __attribute__((ext_vector_type(16)));
typedef float f32x4 __attribute__((ext_vector_type(4)));
typedef float f32x2 __attribute__((ext_vector_type(2)));
typedef __bf16 b16x2 __attribute__((ext_vector_type(2)));
typedef unsigned u32x4 __attribute__((ext_vector_type(4)));
typedef unsigned u32x2 __attribute__((ext_vector_type(2)));
#define LAS __attribute__((address_space(3)))
#define GAS __attribute__((address_space(1)))
#define DI __device__ __forceinline__
#define MFMA(a, b, c) __builtin_amdgcn_mfma_f32_32x32x16_bf16((a), (b), (c), 0, 0, 0)
#define RLX_AGENT __ATOMIC_RELAXED, __HIP_MEMORY_SCOPE_AGENT
typedef LAS char* lptr;

constexpr unsigned WS_XN = 0, WS_KV = 2u << 20, WS_H = 4u << 20, WS_O = 12u << 20, WS_FL = 16u << 20;
constexpr int FL_H = 256;
constexpr int LDS_K2 = 81920, LDS_K1 = 65792;
constexpr unsigned SPIN_CAP = 1u << 20;

DI unsigned pk(float a, float b) { f32x2 v = {a, b}; return __builtin_bit_cast(unsigned, __builtin_convertvector(v, b16x2)); }
DI bf16x8 pack8(const f32x16& x, int o) { u32x4 r = {pk(x[o], x[o + 1]), pk(x[o + 2], x[o + 3]), pk(x[o + 4], x[o + 5]), pk(x[o + 6], x[o + 7])}; return __builtin_bit_cast(bf16x8, r); }
DI float blo(unsigned u) { return __uint_as_float(u << 16); }
DI float bhi(unsigned u) { return __uint_as_float(u & 0xffff0000u); }
DI bf16x8 lr128(lptr p) { return *(const LAS bf16x8*)p; }
DI f32x4 lr128f(lptr p) { return *(const LAS f32x4*)p; }
DI void lw128f(lptr p, float a, float b, float c, float d) { f32x4 v = {a, b, c, d}; *(LAS f32x4*)p = v; }
DI bf16x8 ltr(lptr p, int o0, int o1) {
    s16x4 a = __builtin_amdgcn_ds_read_tr16_b64_v4i16((LAS s16x4*)(p + o0)), b = __builtin_amdgcn_ds_read_tr16_b64_v4i16((LAS s16x4*)(p + o1));
    return __builtin_shufflevector(a, b, 0, 1, 2, 3, 4, 5, 6, 7); }
DI void glds16(unsigned voff, const void* sbase, unsigned lds) {
    unsigned sv; asm volatile("s_mov_b32 %0, m0\n\ts_mov_b32 m0, %3\n\ts_nop 0\n\tglobal_load_lds_dwordx4 %1, %2\n\ts_mov_b32 m0, %0" : "=&s"(sv) : "v"(voff), "s"(sbase), "s"(lds) : "memory"); }
#define BAR(N) asm volatile("s_waitcnt vmcnt(" #N ") lgkmcnt(0)\n\ts_barrier" ::: "memory")
#define BARL() asm volatile("s_waitcnt lgkmcnt(0)\n\ts_barrier" ::: "memory")
DI void st_sc1(unsigned voff, void* sbase, bf16x8 v) { asm volatile("global_store_dwordx4 %0, %1, %2 sc1" ::"v"(voff), "v"(v), "s"(sbase) : "memory"); }
#define LD_SC1(dst, voff, sbase) asm volatile("global_load_dwordx4 %0, %1, %2 sc1" : "=v"(dst) : "v"(voff), "s"(sbase) : "memory")
template <int C> DI float dpp(float x) { return __builtin_bit_cast(float, __builtin_amdgcn_mov_dpp(__builtin_bit_cast(int, x), C, 0xf, 0xf, true)); }
DI float sum32(float x) {
    x += dpp<0xB1>(x); x += dpp<0x4E>(x); x += dpp<0x141>(x); x += dpp<0x140>(x);
    auto s = __builtin_amdgcn_permlane16_swap(__float_as_uint(x), __float_as_uint(x), false, false);
    return __uint_as_float(s[0]) + __uint_as_float(s[1]); }
DI float xhalf_max(float m) { auto r = __builtin_amdgcn_permlane32_swap(__float_as_uint(m), __float_as_uint(m), false, false); return fmaxf(__uint_as_float(r[0]), __uint_as_float(r[1])); }
DI float max16(const f32x16& s, float m) {
#pragma unroll
    for (int i = 0; i < 16; i += 2) m = fmaxf(fmaxf(m, s[i]), s[i + 1]);
    return m; }
DI f32x16 splat16(float v) { f32x16 r;
#pragma unroll
    for (int i = 0; i < 16; ++i) r[i] = v;
    return r; }

__global__ __launch_bounds__(512) void k1_kernel(const char* x, const char* wk, const char* wv, char* ws) {
    extern __shared__ char smem[];
    const lptr L = (lptr)smem; const unsigned L0 = (unsigned)(size_t)L;
    const int b = blockIdx.x, tid = threadIdx.x, l = tid & 63, w = __builtin_amdgcn_readfirstlane(tid >> 6), r = l & 31, h = l >> 5;
    constexpr int WK = 0, WV = 16384 + 128, XL = 33024, RED = 49408;
    const int tok0 = 256 * (b & 7) + 16 * (b >> 3), tl = 2 * w + h;
    const char* xr = x + (size_t)(tok0 + tl) * 1024 + r * 16;
    const u32x4 xa = *(const u32x4*)xr, xb = *(const u32x4*)(xr + 512);
    glds16(l * 16, wk + w * 2048, L0 + WK + w * 2048); glds16(l * 16, wk + w * 2048 + 1024, L0 + WK + w * 2048 + 1024);
    glds16(l * 16, wv + w * 2048, L0 + WV + w * 2048); glds16(l * 16, wv + w * 2048 + 1024, L0 + WV + w * 2048 + 1024);
    if (tid < 3) { const int f = b * 3 + tid; if (f < FL_H + 16) *(unsigned*)(ws + WS_FL + f * 128) = 0u; }
    float v[16];
#pragma unroll
    for (int i = 0; i < 4; ++i) { v[2 * i] = blo(xa[i]); v[2 * i + 1] = bhi(xa[i]); v[8 + 2 * i] = blo(xb[i]); v[9 + 2 * i] = bhi(xb[i]); }
    float s = 0.f;
#pragma unroll
    for (int i = 0; i < 16; ++i) s += v[i];
    const float mean = sum32(s) * (1.f / 512.f); float q2 = 0.f;
#pragma unroll
    for (int i = 0; i < 16; ++i) { v[i] -= mean; q2 += v[i] * v[i]; }
    const float rstd = __builtin_amdgcn_rsqf(sum32(q2) * (1.f / 512.f) + 1e-5f);
    u32x4 ya, yb;
#pragma unroll
    for (int i = 0; i < 4; ++i) { ya[i] = pk(v[2 * i] * rstd, v[2 * i + 1] * rstd); yb[i] = pk(v[8 + 2 * i] * rstd, v[9 + 2 * i] * rstd); }
    { const lptr p = L + XL + r * 256 + ((tl ^ (r & 15)) << 4); *(LAS u32x4*)p = ya; *(LAS u32x4*)(p + 8192) = yb; }
    BAR(0);
    { const int c = tid >> 4, tk = tid & 15; const lptr p = L + XL + c * 256 + ((tk ^ (c & 15)) << 4);
      char* g = ws + WS_XN + (tok0 >> 5) * 32768 + c * 512 + ((tok0 & 16) + tk) * 16;
      *(u32x4*)g = *(LAS u32x4*)p; *(u32x4*)(g + 16384) = *(LAS u32x4*)(p + 8192); }
    f32x16 aK = splat16(0.f), aV = splat16(0.f);
    const lptr wp = L + ((l & 16) ? WV : WK) + (64 * w + 8 * h + ((l >> 2) & 3)) * 32 + (l & 3) * 8;
#pragma unroll
    for (int s4 = 0; s4 < 4; ++s4) { const int c = 8 * w + 2 * s4 + h;
        const bf16x8 xf = lr128(L + XL + c * 256 + (((l ^ c) & 15) << 4)), wf = ltr(wp, s4 * 512, s4 * 512 + 128);
        aK = MFMA(wf, xf, aK); aV = MFMA(xf, wf, aV); }
    { const bool vv = l & 16; const lptr p = L + RED + w * 2048 + l * 16;
      lw128f(p, vv ? aV[0] : aK[0], vv ? aV[1] : aK[1], vv ? aV[2] : aK[2], vv ? aV[3] : aK[3]);
      lw128f(p + 1024, vv ? aV[4] : aK[4], vv ? aV[5] : aK[5], vv ? aV[6] : aK[6], vv ? aV[7] : aK[7]); }
    BARL();
    if (w == 0) { f32x4 a = lr128f(L + RED + l * 16), c = lr128f(L + RED + 1024 + l * 16);
#pragma unroll
        for (int i = 1; i < 8; ++i) { a += lr128f(L + RED + i * 2048 + l * 16); c += lr128f(L + RED + i * 2048 + 1024 + l * 16); }
        const u32x4 o = {pk(a[0], a[1]), pk(a[2], a[3]), pk(c[0], c[1]), pk(c[2], c[3])}; const int sh = (tok0 >> 4) & 1;
        const int off = (l & 16) ? 1024 + sh * 512 + h * 256 + (l & 15) * 16 : (16 * sh + (l & 15) + 32 * h) * 16;
        *(u32x4*)(ws + WS_KV + (tok0 >> 5) * 2048 + off) = o; }
}

DI void attn_role(int b, const lptr L, unsigned L0, const char* wq, char* ws) {
    const int tid = threadIdx.x, l = tid & 63, w = __builtin_amdgcn_readfirstlane(tid >> 6), h = l >> 5, t = w & 1, kq = w >> 1;
    const int i5 = b >> 3, tg = (b & 7) * 4 + (i5 & 3), hg = i5 >> 2, TT = 2 * tg + t;
    constexpr int SL = 8256, TS = 2064;
    const unsigned l16 = l * 16;
    bf16x8 xf[8];
    { const char* xp = ws + WS_XN + (TT * 32 + 8 * kq) * 1024 + l16;
#pragma unroll
      for (int s = 0; s < 8; ++s) xf[s] = *(const bf16x8*)(xp + s * 1024); }
    { const unsigned vo = (l >> 3) * 1024 + (((l & 7) ^ (((l >> 4) & 1) << 2)) << 4);
#pragma unroll
      for (int n = 0; n < 8; ++n) glds16(vo, wq + hg * 128 + (w * 8 + n) * 8192, L0 + (w * 8 + n) * 1024); }
    const char* kvs = ws + WS_KV + w * 1024; const unsigned kvd = L0 + (w >> 1) * TS + (w & 1) * 1024;
    glds16(l16, kvs, kvd + 8 * SL);
    BAR(1);
    bf16x8 qf;
    { const int q = (l >> 2) & 3, p = l & 3, ch = ((l >> 4) & 1) * 2 + (p >> 1);
      const int a0 = kq * 16384 + (8 * h + q) * 128 + ((ch ^ ((q >> 1) << 2)) << 4) + 8 * (p & 1);
      f32x16 c0 = splat16(0.f), c1 = splat16(0.f);
#pragma unroll
      for (int s = 0; s < 8; ++s) { c0 = MFMA(ltr(L + a0, s * 2048, s * 2048 + 512), xf[s], c0); c1 = MFMA(ltr(L + (a0 ^ 64), s * 2048, s * 2048 + 512), xf[s], c1); }
      BARL();
      const lptr pw = L + (t * 16 + kq) * 2048 + l16;
#pragma unroll
      for (int u = 0; u < 4; ++u) { lw128f(pw + (u >> 1) * 8192 + (u & 1) * 1024, c0[4 * u], c0[4 * u + 1], c0[4 * u + 2], c0[4 * u + 3]);
                                    lw128f(pw + 16384 + (u >> 1) * 8192 + (u & 1) * 1024, c1[4 * u], c1[4 * u + 1], c1[4 * u + 2], c1[4 * u + 3]); }
      BARL();
      const lptr pr = L + (t * 16 + kq * 4) * 2048 + l16; f32x4 a = lr128f(pr), c = lr128f(pr + 1024);
#pragma unroll
      for (int u = 1; u < 4; ++u) { a += lr128f(pr + u * 2048); c += lr128f(pr + u * 2048 + 1024); }
      const float sc = 0.25f * 1.4426950408889634f; a *= sc; c *= sc;
      const u32x4 qq = {pk(a[0], a[1]), pk(a[2], a[3]), pk(c[0], c[1]), pk(c[2], c[3])}; qf = __builtin_bit_cast(bf16x8, qq); }
    BARL();
    if (tid < 36) { const u32x4 one = {0x3F803F80u, 0x3F803F80u, 0x3F803F80u, 0x3F803F80u}; *(LAS u32x4*)(L + tid * TS + 2048) = one; }
#pragma unroll
    for (int c = 1; c < 8; ++c) glds16(l16, kvs + c * 8192, kvd + (c - 1) * SL);
    BAR(7);
    glds16(l16, kvs + 8 * 8192, kvd + 7 * SL);
    f32x16 ns;
    { float m = -3.0e38f; const f32x16 z = splat16(0.f);
#pragma unroll
      for (int u = 0; u < 4; ++u) m = max16(MFMA(lr128(L + 8 * SL + u * TS + l16), qf, z), m);
      ns = splat16(-(xhalf_max(m) + 90.f)); }
    f32x16 o = splat16(0.f);
    const int va0 = (l & 16) ? 1024 + h * 256 + (l & 15) * 16 : 2048, va1 = (l & 16) ? va0 + 512 : 2048;
    int so = 8 * SL;
#pragma unroll 1
    for (int c = 0; c < 16; ++c) {
        const lptr pkk = L + so + l16, p0 = L + so + va0, p1 = L + so + va1;
        f32x16 s = MFMA(lr128(pkk), qf, ns);
#pragma unroll
        for (int u = 0; u < 4; ++u) {
            f32x16 sn; if (u < 3) sn = MFMA(lr128(pkk + (u + 1) * TS), qf, ns);
            const bf16x8 v0 = lr128(p0 + u * TS), v1 = lr128(p1 + u * TS);
#pragma unroll
            for (int i = 0; i < 16; ++i) s[i] = __builtin_amdgcn_exp2f(s[i]);
            o = MFMA(v0, pack8(s, 0), o); o = MFMA(v1, pack8(s, 8), o);
            if (u < 3) s = sn; }
        if (c < 7) { BAR(7); glds16(l16, kvs + (c + 9) * 8192, kvd + so); } else if (c == 7) { BAR(0); }
        so = (so == 8 * SL) ? 0 : so + SL; }
    float bad = o[0];
#pragma unroll
    for (int i = 8; i < 16; ++i) bad = fmaxf(bad, fabsf(o[i]));
    if (__builtin_amdgcn_ballot_w64(!(bad < 1.0e33f)) != 0ull || __builtin_amdgcn_ballot_w64(!(o[0] > 0.f)) != 0ull) {
        const char* kv = ws + WS_KV; float m = -3.0e38f; const f32x16 z = splat16(0.f);
#pragma unroll 1
        for (int u = 0; u < 64; ++u) m = max16(MFMA(*(const bf16x8*)(kv + u * 2048 + l16), qf, z), m);
        ns = splat16(-xhalf_max(m)); o = z;
        const u32x4 one = {0x3F803F80u, 0x3F803F80u, 0x3F803F80u, 0x3F803F80u}; const int vo = 1024 + h * 256 + (l & 15) * 16;
#pragma unroll 1
        for (int u = 0; u < 64; ++u) { f32x16 s = MFMA(*(const bf16x8*)(kv + u * 2048 + l16), qf, ns);
            u32x4 v0 = *(const u32x4*)(kv + u * 2048 + vo), v1 = *(const u32x4*)(kv + u * 2048 + vo + 512);
            if (!(l & 16)) { v0 = one; v1 = one; }
#pragma unroll
            for (int i = 0; i < 16; ++i) s[i] = __builtin_amdgcn_exp2f(s[i]);
            o = MFMA(__builtin_bit_cast(bf16x8, v0), pack8(s, 0), o); o = MFMA(__builtin_bit_cast(bf16x8, v1), pack8(s, 8), o); } }
    const float inv = 1.f / o[0];
#pragma unroll
    for (int i = 8; i < 16; ++i) o[i] *= inv;
    st_sc1(l16, ws + WS_O + (TT * 32 + 4 * hg + kq) * 1024, pack8(o, 8));
    BAR(0);
    if (tid == 0) __hip_atomic_store((GAS unsigned*)(ws + WS_FL + (tg * 8 + hg) * 128), 1u, RLX_AGENT);
}

DI void ffn_role(int b, const lptr L, unsigned L0, const char* x, const char* wo, const char* wg, const char* wu, const char* wd, char* out, char* ws) {
    const int tid = threadIdx.x, l = tid & 63, w = __builtin_amdgcn_readfirstlane(tid >> 6), h = l >> 5;
    const int xcd = b & 7, i5 = b >> 3, q = (l >> 2) & 3, p = l & 3; const unsigned l16 = l * 16;
    __builtin_amdgcn_s_setprio(2);
    if (i5 < 28) {
        const int th = i5 & 1, fg = i5 >> 1, ft = w & 3, tk = w >> 2, TT0 = xcd * 8 + th * 4;
        const unsigned vw = (l >> 4) * 3584 + (((l & 15) ^ ((l >> 4) << 2)) << 4);
        const char* sg = wg + w * 4 * 3584 + fg * 256; const char* su = wu + w * 4 * 3584 + fg * 256;
        const char* sx = ws + WS_XN + ((TT0 + (w >> 1)) * 32 + (w & 1)) * 1024; const unsigned dw = L0 + w * 1024;
#define F1_ISSUE(st, buf) do { glds16(vw, sg + (st) * 114688, dw + (buf)); glds16(vw, su + (st) * 114688, dw + (buf) + 8192); glds16(l16, sx + (st) * 2048, dw + (buf) + 16384); } while (0)
        F1_ISSUE(0, 0); F1_ISSUE(1, 24576);
        const int aa = (8 * h + q) * 256 + (((ft * 4 + ((l >> 4) & 1) * 2 + (p >> 1)) ^ (q << 2)) << 4) + 8 * (p & 1), ab = 16384 + tk * 4096 + l16;
        f32x16 g0 = splat16(0.f), g1 = g0, u0 = g0, u1 = g0; int cur = 0, nxt = 49152;
#pragma unroll 1
        for (int st = 0; st < 16; ++st) {
            if (st < 15) BAR(3); else BAR(0);
            if (st < 14) F1_ISSUE(st + 2, nxt);
            const lptr pa = L + cur + aa, pb = L + cur + ab;
#pragma unroll
            for (int s = 0; s < 2; ++s) { const bf16x8 fa = ltr(pa, s * 4096, s * 4096 + 1024), fu = ltr(pa, 8192 + s * 4096, 8192 + s * 4096 + 1024);
                const bf16x8 b0 = lr128(pb + s * 1024), b1 = lr128(pb + 2048 + s * 1024);
                g0 = MFMA(fa, b0, g0); g1 = MFMA(fa, b1, g1); u0 = MFMA(fu, b0, u0); u1 = MFMA(fu, b1, u1); }
            nxt = cur; cur = (cur == 49152) ? 0 : cur + 24576; }
#pragma unroll
        for (int i = 0; i < 16; ++i) { g0[i] = fmaxf(g0[i], 0.f) * u0[i]; g1[i] = fmaxf(g1[i], 0.f) * u1[i]; }
        char* hp = ws + WS_H + ((TT0 + 2 * tk) * 112 + fg * 8 + ft * 2) * 1024;
        st_sc1(l16, hp, pack8(g0, 0)); st_sc1(l16, hp + 1024, pack8(g0, 8)); st_sc1(l16, hp + 114688, pack8(g1, 0)); st_sc1(l16, hp + 115712, pack8(g1, 8));
        BAR(0);
        if (tid == 0) __hip_atomic_fetch_add((GAS unsigned*)(ws + WS_FL + (FL_H + xcd * 2 + th) * 128), 1u, RLX_AGENT);
    } else {
        const char* s = wd + (i5 - 28) * 458752 + w * 1024;
#pragma unroll 1
        for (int n = 0; n < 56; ++n) { glds16(l16, s + n * 8192, L0 + w * 1024); asm volatile("s_waitcnt vmcnt(24)" ::: "memory"); }
        BAR(0);
    }
    const int tg = xcd * 4 + (i5 & 3), cg = i5 >> 2, t = w & 1, kq = w >> 1;
    const unsigned vo = (l >> 3) * 1024 + (((l & 7) ^ (((l >> 4) & 1) << 2)) << 4);
    const int at = (4 * h + q) * 128 + (((((l >> 4) & 1) * 2 + (p >> 1)) ^ ((q >> 1) << 2)) << 4) + 8 * (p & 1);
    float gg[8];
    {   const char* sw = wd + cg * 128 + w * 14 * 16384; const unsigned dw = L0 + w * 8192;
#define F2_W(n) do { glds16(vo, sw + (n) * 16384, dw + ((n) & 3) * 2048); glds16(vo, sw + (n) * 16384 + 8192, dw + ((n) & 3) * 2048 + 1024); } while (0)
        F2_W(0); F2_W(1); F2_W(2); F2_W(3);
        { GAS unsigned* f = (GAS unsigned*)(ws + WS_FL + (FL_H + xcd * 2 + ((i5 >> 1) & 1)) * 128);
          for (unsigned sp = 0; sp < SPIN_CAP; ++sp) { if (__builtin_amdgcn_readfirstlane(__hip_atomic_load(f, RLX_AGENT)) >= 14u) break; __builtin_amdgcn_s_sleep(4); } }
        const char* sh = ws + WS_H + (2 * tg * 112 + w * 14) * 1024;
        bf16x8 hb[4][2];
#define F2_H(n) do { LD_SC1(hb[(n) & 3][0], l16, sh + (n) * 1024); LD_SC1(hb[(n) & 3][1], l16, sh + 114688 + (n) * 1024); } while (0)
        F2_H(0); F2_H(1); F2_H(2); F2_H(3);
        f32x16 d00 = splat16(0.f), d01 = d00, d10 = d00, d11 = d00;
        const lptr a0 = L + w * 8192 + at, a1 = L + w * 8192 + (at ^ 64);
#pragma unroll
        for (int n = 0; n < 14; ++n) {
            asm volatile("s_waitcnt vmcnt(%2)" : "+v"(hb[n & 3][0]), "+v"(hb[n & 3][1]) : "n"(n < 3 ? 6 + 2 * n : n < 11 ? 12 : 52 - 4 * n) : "memory");
            bf16x8 f0 = ltr(a0, (n & 3) * 2048, (n & 3) * 2048 + 1024), f1 = ltr(a1, (n & 3) * 2048, (n & 3) * 2048 + 1024);
            asm volatile("s_waitcnt lgkmcnt(0)" : "+v"(f0), "+v"(f1)::"memory");
            if (n < 10) F2_W(n + 4);
            d00 = MFMA(f0, hb[n & 3][0], d00); d01 = MFMA(f1, hb[n & 3][0], d01); d10 = MFMA(f0, hb[n & 3][1], d10); d11 = MFMA(f1, hb[n & 3][1], d11);
            if (n < 10) { asm volatile("" : "+v"(d00), "+v"(d01), "+v"(d10), "+v"(d11)); F2_H(n + 4); } }
        const lptr pw = L + w * 2048 + l16, pr = L + kq * 16384 + l16; f32x4 a, c;
#define F2_RED(dA, dB, tt) do { BARL(); _Pragma("unroll") for (int u = 0; u < 4; ++u) { lw128f(pw + (u >> 1) * 16384 + (u & 1) * 1024, dA[4 * u], dA[4 * u + 1], dA[4 * u + 2], dA[4 * u + 3]); \
            lw128f(pw + 32768 + (u >> 1) * 16384 + (u & 1) * 1024, dB[4 * u], dB[4 * u + 1], dB[4 * u + 2], dB[4 * u + 3]); } BARL(); \
            if (t == tt) { a = lr128f(pr); c = lr128f(pr + 1024); _Pragma("unroll") for (int u = 1; u < 8; ++u) { a += lr128f(pr + u * 2048); c += lr128f(pr + u * 2048 + 1024); } } } while (0)
        F2_RED(d00, d01, 0); F2_RED(d10, d11, 1);
#pragma unroll
        for (int i = 0; i < 4; ++i) { gg[i] = a[i] * __builtin_amdgcn_rcpf(1.f + __builtin_amdgcn_exp2f(a[i] * -1.4426950408889634f));
                                      gg[4 + i] = c[i] * __builtin_amdgcn_rcpf(1.f + __builtin_amdgcn_exp2f(c[i] * -1.4426950408889634f)); } }
    BARL();
#pragma unroll
    for (int n = 0; n < 8; ++n) glds16(vo, wo + cg * 128 + (w * 8 + n) * 8192, L0 + (w * 8 + n) * 1024);
    const size_t ob = (size_t)(tg * 64 + t * 32 + (l & 31)) * 1024 + cg * 128 + kq * 32;
    const u32x2 x0 = *(const u32x2*)(x + ob + 8 * h), x1 = *(const u32x2*)(x + ob + 16 + 8 * h);
    BAR(0);
    { GAS unsigned* f = (GAS unsigned*)(ws + WS_FL + (tg * 8 + 2 * kq + (l & 1)) * 128);
      for (unsigned sp = 0; sp < SPIN_CAP; ++sp) { if (__builtin_amdgcn_ballot_w64(__hip_atomic_load(f, RLX_AGENT) == 0u) == 0ull) break; __builtin_amdgcn_s_sleep(2); } }
    bf16x8 of[8];
    { const char* so = ws + WS_O + ((2 * tg + t) * 32 + 8 * kq) * 1024;
#pragma unroll
      for (int s = 0; s < 8; ++s) LD_SC1(of[s], l16, so + s * 1024); }
    f32x16 c0 = splat16(0.f), c1 = c0;
    const lptr a0 = L + kq * 16384 + at, a1 = L + kq * 16384 + (at ^ 64);
#pragma unroll
    for (int s = 0; s < 8; ++s) { const bf16x8 f0 = ltr(a0, s * 2048, s * 2048 + 1024), f1 = ltr(a1, s * 2048, s * 2048 + 1024);
        asm volatile("s_waitcnt vmcnt(%1)" : "+v"(of[s]) : "n"(7 - s) : "memory");
        c0 = MFMA(f0, of[s], c0); c1 = MFMA(f1, of[s], c1); }
    BARL();
    const lptr pw = L + (t * 16 + kq) * 2048 + l16;
#pragma unroll
    for (int u = 0; u < 4; ++u) { lw128f(pw + (u >> 1) * 8192 + (u & 1) * 1024, c0[4 * u], c0[4 * u + 1], c0[4 * u + 2], c0[4 * u + 3]);
                                  lw128f(pw + 16384 + (u >> 1) * 8192 + (u & 1) * 1024, c1[4 * u], c1[4 * u + 1], c1[4 * u + 2], c1[4 * u + 3]); }
    BARL();
    const lptr pr = L + (t * 16 + kq * 4) * 2048 + l16; f32x4 a = lr128f(pr), c = lr128f(pr + 1024);
#pragma unroll
    for (int u = 1; u < 4; ++u) { a += lr128f(pr + u * 2048); c += lr128f(pr + u * 2048 + 1024); }
    const unsigned A0 = pk(blo(x0[0]) + a[0] * gg[0], bhi(x0[0]) + a[1] * gg[1]), A1 = pk(blo(x0[1]) + a[2] * gg[2], bhi(x0[1]) + a[3] * gg[3]);
    const unsigned B0 = pk(blo(x1[0]) + c[0] * gg[4], bhi(x1[0]) + c[1] * gg[5]), B1 = pk(blo(x1[1]) + c[2] * gg[6], bhi(x1[1]) + c[3] * gg[7]);
    auto s0 = __builtin_amdgcn_permlane32_swap(A0, B0, false, false), s1 = __builtin_amdgcn_permlane32_swap(A1, B1, false, false);
    const u32x4 ov = {s0[0], s1[0], s0[1], s1[1]};
    *(u32x4*)(out + ob + 16 * h) = ov;
}

__global__ __launch_bounds__(512, 4) void k2_kernel(const char* x, const char* wq, const char* wo, const char* wg, const char* wu, const char* wd, char* out, char* ws) {
    extern __shared__ char smem[];
    const lptr L = (lptr)smem; const unsigned L0 = (unsigned)(size_t)L;
    const int b = blockIdx.x;
    if (b < 256) attn_role(b, L, L0, wq, ws); else ffn_role(b - 256, L, L0, x, wo, wg, wu, wd, out, ws);
}

extern "C" void kernel_launch(void* const* d_in, const int* in_sizes, int n_in, void* d_out, int out_size, void* d_ws, size_t ws_size, hipStream_t stream) {
    (void)hipFuncSetAttribute((const void*)k1_kernel, hipFuncAttributeMaxDynamicSharedMemorySize, LDS_K1);
    (void)hipFuncSetAttribute((const void*)k2_kernel, hipFuncAttributeMaxDynamicSharedMemorySize, LDS_K2);
    k1_kernel<<<128, 512, LDS_K1, stream>>>((const char*)d_in[0], (const char*)d_in[2], (const char*)d_in[3], (char*)d_ws);
    k2_kernel<<<512, 512, LDS_K2, stream>>>((const char*)d_in[0], (const char*)d_in[1], (const char*)d_in[4], (const char*)d_in[6], (const char*)d_in[7], (const char*)d_in[8], (char*)d_out, (char*)d_ws);
}
```

```cpp
#include <hip/hip_runtime.h>
#include <hip/hip_bf16.h>
#include <stdint.h>

typedef short bf16x8 __attribute__((ext_vector_type(8)));
typedef short s16x4 __attribute__((ext_vector_type(4)));
typedef float f32x16 __attribute__((ext_vector_type(16)));
typedef float f32x4 __attribute__((ext_vector_type(4)));
typedef float f32x2 __attribute__((ext_vector_type(2)));
typedef __bf16 b16x2 __attribute__((ext_vector_type(2)));
typedef unsigned u32x4 __attribute__((ext_vector_type(4)));
typedef unsigned u32x2 __attribute__((ext_vector_type(2)));
#define LAS __attribute__((address_space(3)))
#define GAS __attribute__((address_space(1)))
#define DI __device__ __forceinline__
#define MFMA(a, b, c) __builtin_amdgcn_mfma_f32_32x32x16_bf16((a), (b), (c), 0, 0, 0)
#define RLX_AGENT __ATOMIC_RELAXED, __HIP_MEMORY_SCOPE_AGENT
typedef LAS char* lptr;

constexpr unsigned WS_XN = 0, WS_KV = 2u << 20, WS_H = 4u << 20, WS_O = 12u << 20, WS_FL = 16u << 20;
constexpr int FL_H = 256;
constexpr int LDS_K2 = 81920, LDS_K1 = 65792;
constexpr unsigned SPIN_CAP = 1u << 20;
#define NOFFN 0
#define P_LOOP 0
#define P_LOOP2 0
#define P_ATT 1
#define PRIO(n) __builtin_amdgcn_s_setprio(n)
#define TS(blk, i)

DI unsigned pk(float a, float b) { f32x2 v = {a, b}; return __builtin_bit_cast(unsigned, __builtin_convertvector(v, b16x2)); }
DI bf16x8 pack8(const f32x16& x, int o) { u32x4 r = {pk(x[o], x[o + 1]), pk(x[o + 2], x[o + 3]), pk(x[o + 4], x[o + 5]), pk(x[o + 6], x[o + 7])}; return __builtin_bit_cast(bf16x8, r); }
DI float blo(unsigned u) { return __uint_as_float(u << 16); }
DI float bhi(unsigned u) { return __uint_as_float(u & 0xffff0000u); }
DI bf16x8 lr128(lptr p) { return *(const LAS bf16x8*)p; }
DI f32x4 lr128f(lptr p) { return *(const LAS f32x4*)p; }
DI void lw128f(lptr p, float a, float b, float c, float d) { f32x4 v = {a, b, c, d}; *(LAS f32x4*)p = v; }
DI bf16x8 ltr(lptr p, int o0, int o1) {
    s16x4 a = __builtin_amdgcn_ds_read_tr16_b64_v4i16((LAS s16x4*)(p + o0)), b = __builtin_amdgcn_ds_read_tr16_b64_v4i16((LAS s16x4*)(p + o1));
    return __builtin_shufflevector(a, b, 0, 1, 2, 3, 4, 5, 6, 7); }
DI void glds16(unsigned voff, const void* sbase, unsigned lds) {
    unsigned sv; asm volatile("s_mov_b32 %0, m0\n\ts_mov_b32 m0, %3\n\ts_nop 0\n\tglobal_load_lds_dwordx4 %1, %2\n\ts_mov_b32 m0, %0" : "=&s"(sv) : "v"(voff), "s"(sbase), "s"(lds) : "memory"); }
#define BAR(N) asm volatile("s_waitcnt vmcnt(" #N ") lgkmcnt(0)\n\ts_barrier" ::: "memory")
#define BARL() asm volatile("s_waitcnt lgkmcnt(0)\n\ts_barrier" ::: "memory")
DI void st_sc1(unsigned voff, void* sbase, bf16x8 v) { asm volatile("global_store_dwordx4 %0, %1, %2 sc1" ::"v"(voff), "v"(v), "s"(sbase) : "memory"); }
#define LD_SC1(dst, voff, sbase) asm volatile("global_load_dwordx4 %0, %1, %2 sc1" : "=v"(dst) : "v"(voff), "s"(sbase) : "memory")
#define touch(voff, sbase) asm volatile("global_load_dword %0, %1, %2" : "+v"(jk) : "v"(voff), "s"(sbase) : "memory")
#define TOUCH_END() asm volatile("s_waitcnt vmcnt(0)" : "+v"(jk)::"memory")
template <int C> DI float dpp(float x) { return __builtin_bit_cast(float, __builtin_amdgcn_mov_dpp(__builtin_bit_cast(int, x), C, 0xf, 0xf, true)); }
DI float sum32(float x) {
    x += dpp<0xB1>(x); x += dpp<0x4E>(x); x += dpp<0x141>(x); x += dpp<0x140>(x);
    auto s = __builtin_amdgcn_permlane16_swap(__float_as_uint(x), __float_as_uint(x), false, false);
    return __uint_as_float(s[0]) + __uint_as_float(s[1]); }
DI float xhalf_max(float m) { auto r = __builtin_amdgcn_permlane32_swap(__float_as_uint(m), __float_as_uint(m), false, false); return fmaxf(__uint_as_float(r[0]), __uint_as_float(r[1])); }
DI float max16(const f32x16& s, float m) {
#pragma unroll
    for (int i = 0; i < 16; i += 2) m = fmaxf(fmaxf(m, s[i]), s[i + 1]);
    return m; }
DI f32x16 splat16(float v) { f32x16 r;
#pragma unroll
    for (int i = 0; i < 16; ++i) r[i] = v;
    return r; }

__global__ __launch_bounds__(512) void k1_kernel(const char* x, const char* wk, const char* wv, const char* wq, const char* wg, const char* wu, char* ws) {
    extern __shared__ char smem[];
    const lptr L = (lptr)smem; const unsigned L0 = (unsigned)(size_t)L;
    const int b = blockIdx.x, tid = threadIdx.x, l = tid & 63, w = __builtin_amdgcn_readfirstlane(tid >> 6), r = l & 31, h = l >> 5;
    TS(512 + b, 0);
    if (b >= 128) {
        const int j = (b - 128) >> 3, fq = (b & 7) >> 1;
        const int n = tid & 255, row = n / 7; unsigned jk = 0;
        if (w < 4) touch((j * 256 + n) * 128, wq);
        if (n < 224) touch((j * 32 + row) * 3584 + fq * 896 + (n - row * 7) * 128, w < 4 ? wg : wu);
        TOUCH_END(); TS(512 + b, 2); return; }
    constexpr int WK = 0, WV = 16384 + 128, XL = 33024, RED = 49408;
    const int tok0 = 256 * (b & 7) + 16 * (b >> 3), tl = 2 * w + h;
    const char* xr = x + (size_t)(tok0 + tl) * 1024 + r * 16;
    const u32x4 xa = *(const u32x4*)xr, xb = *(const u32x4*)(xr + 512);
    glds16(l * 16, wk + w * 2048, L0 + WK + w * 2048); glds16(l * 16, wk + w * 2048 + 1024, L0 + WK + w * 2048 + 1024);
    glds16(l * 16, wv + w * 2048, L0 + WV + w * 2048); glds16(l * 16, wv + w * 2048 + 1024, L0 + WV + w * 2048 + 1024);
    if (tid < 3) { const int f = b * 3 + tid; if (f < FL_H + 8) *(unsigned*)(ws + WS_FL + f * 128) = 0u; }
    float v[16];
#pragma unroll
    for (int i = 0; i < 4; ++i) { v[2 * i] = blo(xa[i]); v[2 * i + 1] = bhi(xa[i]); v[8 + 2 * i] = blo(xb[i]); v[9 + 2 * i] = bhi(xb[i]); }
    float s = 0.f;
#pragma unroll
    for (int i = 0; i < 16; ++i) s += v[i];
    const float mean = sum32(s) * (1.f / 512.f); float q2 = 0.f;
#pragma unroll
    for (int i = 0; i < 16; ++i) { v[i] -= mean; q2 += v[i] * v[i]; }
    const float rstd = __builtin_amdgcn_rsqf(sum32(q2) * (1.f / 512.f) + 1e-5f);
    u32x4 ya, yb;
#pragma unroll
    for (int i = 0; i < 4; ++i) { ya[i] = pk(v[2 * i] * rstd, v[2 * i + 1] * rstd); yb[i] = pk(v[8 + 2 * i] * rstd, v[9 + 2 * i] * rstd); }
    { const lptr p = L + XL + r * 256 + ((tl ^ (r & 15)) << 4); *(LAS u32x4*)p = ya; *(LAS u32x4*)(p + 8192) = yb; }
    BAR(0); TS(512 + b, 1);
    { const int c = tid >> 4, tk = tid & 15; const lptr p = L + XL + c * 256 + ((tk ^ (c & 15)) << 4);
      char* g = ws + WS_XN + (tok0 >> 5) * 32768 + c * 512 + ((tok0 & 16) + tk) * 16;
      *(u32x4*)g = *(LAS u32x4*)p; *(u32x4*)(g + 16384) = *(LAS u32x4*)(p + 8192); }
    f32x16 aK = splat16(0.f), aV = splat16(0.f);
    const lptr wp = L + ((l & 16) ? WV : WK) + (64 * w + 8 * h + ((l >> 2) & 3)) * 32 + (l & 3) * 8;
#pragma unroll
    for (int s4 = 0; s4 < 4; ++s4) { const int c = 8 * w + 2 * s4 + h;
        const bf16x8 xf = lr128(L + XL + c * 256 + (((l ^ c) & 15) << 4)), wf = ltr(wp, s4 * 512, s4 * 512 + 128);
        aK = MFMA(wf, xf, aK); aV = MFMA(xf, wf, aV); }
    { const bool vv = l & 16; const lptr p = L + RED + w * 2048 + l * 16;
      lw128f(p, vv ? aV[0] : aK[0], vv ? aV[1] : aK[1], vv ? aV[2] : aK[2], vv ? aV[3] : aK[3]);
      lw128f(p + 1024, vv ? aV[4] : aK[4], vv ? aV[5] : aK[5], vv ? aV[6] : aK[6], vv ? aV[7] : aK[7]); }
    BARL();
    if (w == 0) { f32x4 a = lr128f(L + RED + l * 16), c = lr128f(L + RED + 1024 + l * 16);
#pragma unroll
        for (int i = 1; i < 8; ++i) { a += lr128f(L + RED + i * 2048 + l * 16); c += lr128f(L + RED + i * 2048 + 1024 + l * 16); }
        const u32x4 o = {pk(a[0], a[1]), pk(a[2], a[3]), pk(c[0], c[1]), pk(c[2], c[3])}; const int sh = (tok0 >> 4) & 1;
        const int off = (l & 16) ? 1024 + sh * 512 + h * 256 + (l & 15) * 16 : (16 * sh + (l & 15) + 32 * h) * 16;
        *(u32x4*)(ws + WS_KV + (tok0 >> 5) * 2048 + off) = o; }
    TS(512 + b, 2);
}

DI void attn_role(int b, const lptr L, unsigned L0, const char* wq, char* ws) {
    const int tid = threadIdx.x, l = tid & 63, w = __builtin_amdgcn_readfirstlane(tid >> 6), h = l >> 5, t = w & 1, kq = w >> 1;
    const int i5 = b >> 3, tg = (b & 7) * 4 + (i5 & 3), hg = i5 >> 2, TT = 2 * tg + t;
    constexpr int SL = 8256, TS_ = 2064;
    const unsigned l16 = l * 16;
    PRIO(3); TS(b, 0);
    bf16x8 xf[8];
    { const char* xp = ws + WS_XN + (TT * 32 + 8 * kq) * 1024 + l16;
#pragma unroll
      for (int s = 0; s < 8; ++s) xf[s] = *(const bf16x8*)(xp + s * 1024); }
    { const unsigned vo = (l >> 3) * 1024 + (((l & 7) ^ (((l >> 4) & 1) << 2)) << 4);
#pragma unroll
      for (int n = 0; n < 8; ++n) glds16(vo, wq + hg * 128 + (w * 8 + n) * 8192, L0 + (w * 8 + n) * 1024); }
    const char* kvs = ws + WS_KV + w * 1024; const unsigned kvd = L0 + (w >> 1) * TS_ + (w & 1) * 1024;
    glds16(l16, kvs, kvd + 8 * SL);
    BAR(1); TS(b, 1);
    bf16x8 qf;
    { const int q = (l >> 2) & 3, p = l & 3, ch = ((l >> 4) & 1) * 2 + (p >> 1);
      const int a0 = kq * 16384 + (8 * h + q) * 128 + ((ch ^ ((q >> 1) << 2)) << 4) + 8 * (p & 1);
      f32x16 c0 = splat16(0.f), c1 = splat16(0.f);
#pragma unroll
      for (int s = 0; s < 8; ++s) { c0 = MFMA(ltr(L + a0, s * 2048, s * 2048 + 512), xf[s], c0); c1 = MFMA(ltr(L + (a0 ^ 64), s * 2048, s * 2048 + 512), xf[s], c1); }
      BARL();
      const lptr pw = L + (t * 16 + kq) * 2048 + l16;
#pragma unroll
      for (int u = 0; u < 4; ++u) { lw128f(pw + (u >> 1) * 8192 + (u & 1) * 1024, c0[4 * u], c0[4 * u + 1], c0[4 * u + 2], c0[4 * u + 3]);
                                    lw128f(pw + 16384 + (u >> 1) * 8192 + (u & 1) * 1024, c1[4 * u], c1[4 * u + 1], c1[4 * u + 2], c1[4 * u + 3]); }
      BARL();
      const lptr pr = L + (t * 16 + kq * 4) * 2048 + l16; f32x4 a = lr128f(pr), c = lr128f(pr + 1024);
#pragma unroll
      for (int u = 1; u < 4; ++u) { a += lr128f(pr + u * 2048); c += lr128f(pr + u * 2048 + 1024); }
      const float sc = 0.25f * 1.4426950408889634f; a *= sc; c *= sc;
      const u32x4 qq = {pk(a[0], a[1]), pk(a[2], a[3]), pk(c[0], c[1]), pk(c[2], c[3])}; qf = __builtin_bit_cast(bf16x8, qq); }
    BARL();
    if (tid < 36) { const u32x4 one = {0x3F803F80u, 0x3F803F80u, 0x3F803F80u, 0x3F803F80u}; *(LAS u32x4*)(L + tid * TS_ + 2048) = one; }
#pragma unroll
    for (int c = 1; c < 8; ++c) glds16(l16, kvs + c * 8192, kvd + (c - 1) * SL);
    BAR(7);
    f32x16 ns; bf16x8 k0 = lr128(L + 8 * SL + TS_ + l16), k1 = lr128(L + 8 * SL + 2 * TS_ + l16);
    { const f32x16 z = splat16(0.f); const bf16x8 kf = lr128(L + 8 * SL + l16);
      float m = max16(MFMA(kf, qf, z), -3.0e38f); m = max16(MFMA(k0, qf, z), m); m = max16(MFMA(k1, qf, z), m); m = max16(MFMA(lr128(L + 8 * SL + 3 * TS_ + l16), qf, z), m);
      ns = splat16(-(xhalf_max(m) + 90.f)); }
    f32x16 o = splat16(0.f);
    { const unsigned bk = L0 + l16, b0 = L0 + ((l & 16) ? 1024 + h * 256 + (l & 15) * 16 : 2048), b1 = (l & 16) ? b0 + 512 : b0;
      f32x16 sA = MFMA(lr128(L + 8 * SL + l16), qf, ns);
      bf16x8 v0 = lr128(L + 8 * SL + (b0 - L0)), v1 = {0, 0, 0, 0, 0, 0, 0, 0}, pb = v1;
      unsigned aK = bk + 8 * SL, a0 = b0 + 8 * SL, a1 = b1 + 8 * SL, vo = l16 + 9 * 8192, so = 8 * SL, sn, c = 0, sv;
      BAR(6); TS(b, 2);
      glds16(l16, kvs + 8 * 8192, kvd + 7 * SL);
      PRIO(P_ATT);
      asm volatile(
        "s_mov_b32 %[sv], m0\ns_nop 7\ns_nop 7\n9:\ns_add_u32 %[sn], %[so], 8256\ns_cmp_eq_u32 %[so], 66048\ns_cselect_b32 %[sn], 0, %[sn]\n"
        "v_mfma_f32_32x32x16_bf16 v[48:63], v[100:103], v[96:99], v[80:95]\nds_read_b128 v[100:103], v124 offset:6192\nv_exp_f32 v32, v32\nv_exp_f32 v33, v33\nv_exp_f32 v34, v34\n"
        "v_exp_f32 v35, v35\nv_exp_f32 v36, v36\nv_exp_f32 v37, v37\ns_waitcnt lgkmcnt(1)\nv_mfma_f32_32x32x16_bf16 v[64:79], v[112:115], v[120:123], v[64:79]\nv_exp_f32 v38, v38\n"
        "v_exp_f32 v39, v39\nv_cvt_pk_bf16_f32 v116, v32, v33\nv_cvt_pk_bf16_f32 v117, v34, v35\nv_cvt_pk_bf16_f32 v118, v36, v37\nv_cvt_pk_bf16_f32 v119, v38, v39\n"
        "v_exp_f32 v40, v40\nv_exp_f32 v41, v41\nv_exp_f32 v42, v42\nv_mfma_f32_32x32x16_bf16 v[64:79], v[108:111], v[116:119], v[64:79]\nds_read_b128 v[112:115], v126 offset:0\n"
        "ds_read_b128 v[108:111], v125 offset:2064\nv_add_u32 v127, %[sn], %[bk]\nv_add_u32 v31, %[sn], %[b0]\nv_add_u32 v30, %[sn], %[b1]\nv_exp_f32 v43, v43\nv_exp_f32 v44, v44\n"
        "v_exp_f32 v45, v45\nv_exp_f32 v46, v46\nv_exp_f32 v47, v47\nv_cvt_pk_bf16_f32 v120, v40, v41\nv_cvt_pk_bf16_f32 v121, v42, v43\nv_cvt_pk_bf16_f32 v122, v44, v45\n"
        "v_cvt_pk_bf16_f32 v123, v46, v47\nv_mfma_f32_32x32x16_bf16 v[32:47], v[104:107], v[96:99], v[80:95]\nds_read_b128 v[104:107], v127 offset:0\nv_exp_f32 v48, v48\n"
        "v_exp_f32 v49, v49\nv_exp_f32 v50, v50\nv_exp_f32 v51, v51\nv_exp_f32 v52, v52\nv_exp_f32 v53, v53\ns_waitcnt lgkmcnt(1)\n"
        "v_mfma_f32_32x32x16_bf16 v[64:79], v[112:115], v[120:123], v[64:79]\nv_exp_f32 v54, v54\nv_exp_f32 v55, v55\nv_cvt_pk_bf16_f32 v116, v48, v49\n"
        "v_cvt_pk_bf16_f32 v117, v50, v51\nv_cvt_pk_bf16_f32 v118, v52, v53\nv_cvt_pk_bf16_f32 v119, v54, v55\nv_exp_f32 v56, v56\nv_exp_f32 v57, v57\nv_exp_f32 v58, v58\n"
        "v_mfma_f32_32x32x16_bf16 v[64:79], v[108:111], v[116:119], v[64:79]\nds_read_b128 v[112:115], v126 offset:2064\nds_read_b128 v[108:111], v125 offset:4128\n"
        "v_exp_f32 v59, v59\nv_exp_f32 v60, v60\nv_exp_f32 v61, v61\nv_exp_f32 v62, v62\nv_exp_f32 v63, v63\nv_cvt_pk_bf16_f32 v120, v56, v57\nv_cvt_pk_bf16_f32 v121, v58, v59\n"
        "v_cvt_pk_bf16_f32 v122, v60, v61\nv_cvt_pk_bf16_f32 v123, v62, v63\nv_mfma_f32_32x32x16_bf16 v[48:63], v[100:103], v[96:99], v[80:95]\n"
        "ds_read_b128 v[100:103], v127 offset:2064\nv_exp_f32 v32, v32\nv_exp_f32 v33, v33\nv_exp_f32 v34, v34\nv_exp_f32 v35, v35\nv_exp_f32 v36, v36\nv_exp_f32 v37, v37\n"
        "s_waitcnt lgkmcnt(1)\nv_mfma_f32_32x32x16_bf16 v[64:79], v[112:115], v[120:123], v[64:79]\nv_exp_f32 v38, v38\nv_exp_f32 v39, v39\nv_cvt_pk_bf16_f32 v116, v32, v33\n"
        "v_cvt_pk_bf16_f32 v117, v34, v35\nv_cvt_pk_bf16_f32 v118, v36, v37\nv_cvt_pk_bf16_f32 v119, v38, v39\nv_exp_f32 v40, v40\nv_exp_f32 v41, v41\nv_exp_f32 v42, v42\n"
        "v_mfma_f32_32x32x16_bf16 v[64:79], v[108:111], v[116:119], v[64:79]\nds_read_b128 v[112:115], v126 offset:4128\nds_read_b128 v[108:111], v125 offset:6192\n"
        "v_exp_f32 v43, v43\nv_exp_f32 v44, v44\nv_exp_f32 v45, v45\nv_exp_f32 v46, v46\nv_exp_f32 v47, v47\nv_cvt_pk_bf16_f32 v120, v40, v41\nv_cvt_pk_bf16_f32 v121, v42, v43\n"
        "v_cvt_pk_bf16_f32 v122, v44, v45\nv_cvt_pk_bf16_f32 v123, v46, v47\nv_mfma_f32_32x32x16_bf16 v[32:47], v[104:107], v[96:99], v[80:95]\n"
        "ds_read_b128 v[104:107], v127 offset:4128\nv_exp_f32 v48, v48\nv_exp_f32 v49, v49\nv_exp_f32 v50, v50\nv_exp_f32 v51, v51\nv_exp_f32 v52, v52\nv_exp_f32 v53, v53\n"
        "s_waitcnt lgkmcnt(1)\nv_mfma_f32_32x32x16_bf16 v[64:79], v[112:115], v[120:123], v[64:79]\nv_exp_f32 v54, v54\nv_exp_f32 v55, v55\nv_cvt_pk_bf16_f32 v116, v48, v49\n"
        "v_cvt_pk_bf16_f32 v117, v50, v51\nv_cvt_pk_bf16_f32 v118, v52, v53\nv_cvt_pk_bf16_f32 v119, v54, v55\nv_exp_f32 v56, v56\nv_exp_f32 v57, v57\nv_exp_f32 v58, v58\n"
        "v_mfma_f32_32x32x16_bf16 v[64:79], v[108:111], v[116:119], v[64:79]\nds_read_b128 v[112:115], v126 offset:6192\nds_read_b128 v[108:111], v31 offset:0\nv_exp_f32 v59, v59\n"
        "v_exp_f32 v60, v60\nv_exp_f32 v61, v61\nv_exp_f32 v62, v62\nv_exp_f32 v63, v63\nv_cvt_pk_bf16_f32 v120, v56, v57\nv_cvt_pk_bf16_f32 v121, v58, v59\n"
        "v_cvt_pk_bf16_f32 v122, v60, v61\nv_cvt_pk_bf16_f32 v123, v62, v63\ns_cmp_gt_u32 %[c], 7\ns_cbranch_scc1 12f\ns_cmp_eq_u32 %[c], 7\ns_cbranch_scc1 11f\n"
        "s_add_u32 m0, %[so], %[kd]\ns_waitcnt vmcnt(6) lgkmcnt(0)\ns_barrier\nglobal_load_lds_dwordx4 %[vo], %[src]\nv_add_u32 %[vo], 0x2000, %[vo]\ns_branch 12f\n11:\n"
        "s_waitcnt vmcnt(0) lgkmcnt(0)\ns_barrier\n12:\ns_add_u32 %[c], %[c], 1\ns_mov_b32 %[so], %[sn]\ns_add_u32 %[sn], %[so], 8256\ns_cmp_eq_u32 %[so], 66048\n"
        "s_cselect_b32 %[sn], 0, %[sn]\nv_mfma_f32_32x32x16_bf16 v[48:63], v[100:103], v[96:99], v[80:95]\nds_read_b128 v[100:103], v127 offset:6192\nv_exp_f32 v32, v32\n"
        "v_exp_f32 v33, v33\nv_exp_f32 v34, v34\nv_exp_f32 v35, v35\nv_exp_f32 v36, v36\nv_exp_f32 v37, v37\ns_waitcnt lgkmcnt(1)\n"
        "v_mfma_f32_32x32x16_bf16 v[64:79], v[112:115], v[120:123], v[64:79]\nv_exp_f32 v38, v38\nv_exp_f32 v39, v39\nv_cvt_pk_bf16_f32 v116, v32, v33\n"
        "v_cvt_pk_bf16_f32 v117, v34, v35\nv_cvt_pk_bf16_f32 v118, v36, v37\nv_cvt_pk_bf16_f32 v119, v38, v39\nv_exp_f32 v40, v40\nv_exp_f32 v41, v41\nv_exp_f32 v42, v42\n"
        "v_mfma_f32_32x32x16_bf16 v[64:79], v[108:111], v[116:119], v[64:79]\nds_read_b128 v[112:115], v30 offset:0\nds_read_b128 v[108:111], v31 offset:2064\n"
        "v_add_u32 v124, %[sn], %[bk]\nv_add_u32 v125, %[sn], %[b0]\nv_add_u32 v126, %[sn], %[b1]\nv_exp_f32 v43, v43\nv_exp_f32 v44, v44\nv_exp_f32 v45, v45\nv_exp_f32 v46, v46\n"
        "v_exp_f32 v47, v47\nv_cvt_pk_bf16_f32 v120, v40, v41\nv_cvt_pk_bf16_f32 v121, v42, v43\nv_cvt_pk_bf16_f32 v122, v44, v45\nv_cvt_pk_bf16_f32 v123, v46, v47\n"
        "v_mfma_f32_32x32x16_bf16 v[32:47], v[104:107], v[96:99], v[80:95]\nds_read_b128 v[104:107], v124 offset:0\nv_exp_f32 v48, v48\nv_exp_f32 v49, v49\nv_exp_f32 v50, v50\n"
        "v_exp_f32 v51, v51\nv_exp_f32 v52, v52\nv_exp_f32 v53, v53\ns_waitcnt lgkmcnt(1)\nv_mfma_f32_32x32x16_bf16 v[64:79], v[112:115], v[120:123], v[64:79]\nv_exp_f32 v54, v54\n"
        "v_exp_f32 v55, v55\nv_cvt_pk_bf16_f32 v116, v48, v49\nv_cvt_pk_bf16_f32 v117, v50, v51\nv_cvt_pk_bf16_f32 v118, v52, v53\nv_cvt_pk_bf16_f32 v119, v54, v55\n"
        "v_exp_f32 v56, v56\nv_exp_f32 v57, v57\nv_exp_f32 v58, v58\nv_mfma_f32_32x32x16_bf16 v[64:79], v[108:111], v[116:119], v[64:79]\nds_read_b128 v[112:115], v30 offset:2064\n"
        "ds_read_b128 v[108:111], v31 offset:4128\nv_exp_f32 v59, v59\nv_exp_f32 v60, v60\nv_exp_f32 v61, v61\nv_exp_f32 v62, v62\nv_exp_f32 v63, v63\n"
        "v_cvt_pk_bf16_f32 v120, v56, v57\nv_cvt_pk_bf16_f32 v121, v58, v59\nv_cvt_pk_bf16_f32 v122, v60, v61\nv_cvt_pk_bf16_f32 v123, v62, v63\n"
        "v_mfma_f32_32x32x16_bf16 v[48:63], v[100:103], v[96:99], v[80:95]\nds_read_b128 v[100:103], v124 offset:2064\nv_exp_f32 v32, v32\nv_exp_f32 v33, v33\nv_exp_f32 v34, v34\n"
        "v_exp_f32 v35, v35\nv_exp_f32 v36, v36\nv_exp_f32 v37, v37\ns_waitcnt lgkmcnt(1)\nv_mfma_f32_32x32x16_bf16 v[64:79], v[112:115], v[120:123], v[64:79]\nv_exp_f32 v38, v38\n"
        "v_exp_f32 v39, v39\nv_cvt_pk_bf16_f32 v116, v32, v33\nv_cvt_pk_bf16_f32 v117, v34, v35\nv_cvt_pk_bf16_f32 v118, v36, v37\nv_cvt_pk_bf16_f32 v119, v38, v39\n"
        "v_exp_f32 v40, v40\nv_exp_f32 v41, v41\nv_exp_f32 v42, v42\nv_mfma_f32_32x32x16_bf16 v[64:79], v[108:111], v[116:119], v[64:79]\nds_read_b128 v[112:115], v30 offset:4128\n"
        "ds_read_b128 v[108:111], v31 offset:6192\nv_exp_f32 v43, v43\nv_exp_f32 v44, v44\nv_exp_f32 v45, v45\nv_exp_f32 v46, v46\nv_exp_f32 v47, v47\n"
        "v_cvt_pk_bf16_f32 v120, v40, v41\nv_cvt_pk_bf16_f32 v121, v42, v43\nv_cvt_pk_bf16_f32 v122, v44, v45\nv_cvt_pk_bf16_f32 v123, v46, v47\n"
        "v_mfma_f32_32x32x16_bf16 v[32:47], v[104:107], v[96:99], v[80:95]\nds_read_b128 v[104:107], v124 offset:4128\nv_exp_f32 v48, v48\nv_exp_f32 v49, v49\nv_exp_f32 v50, v50\n"
        "v_exp_f32 v51, v51\nv_exp_f32 v52, v52\nv_exp_f32 v53, v53\ns_waitcnt lgkmcnt(1)\nv_mfma_f32_32x32x16_bf16 v[64:79], v[112:115], v[120:123], v[64:79]\nv_exp_f32 v54, v54\n"
        "v_exp_f32 v55, v55\nv_cvt_pk_bf16_f32 v116, v48, v49\nv_cvt_pk_bf16_f32 v117, v50, v51\nv_cvt_pk_bf16_f32 v118, v52, v53\nv_cvt_pk_bf16_f32 v119, v54, v55\n"
        "v_exp_f32 v56, v56\nv_exp_f32 v57, v57\nv_exp_f32 v58, v58\nv_mfma_f32_32x32x16_bf16 v[64:79], v[108:111], v[116:119], v[64:79]\nds_read_b128 v[112:115], v30 offset:6192\n"
        "ds_read_b128 v[108:111], v125 offset:0\nv_exp_f32 v59, v59\nv_exp_f32 v60, v60\nv_exp_f32 v61, v61\nv_exp_f32 v62, v62\nv_exp_f32 v63, v63\n"
        "v_cvt_pk_bf16_f32 v120, v56, v57\nv_cvt_pk_bf16_f32 v121, v58, v59\nv_cvt_pk_bf16_f32 v122, v60, v61\nv_cvt_pk_bf16_f32 v123, v62, v63\ns_cmp_gt_u32 %[c], 7\n"
        "s_cbranch_scc1 22f\ns_cmp_eq_u32 %[c], 7\ns_cbranch_scc1 21f\ns_add_u32 m0, %[so], %[kd]\ns_waitcnt vmcnt(6) lgkmcnt(0)\ns_barrier\nglobal_load_lds_dwordx4 %[vo], %[src]\n"
        "v_add_u32 %[vo], 0x2000, %[vo]\ns_branch 22f\n21:\ns_waitcnt vmcnt(0) lgkmcnt(0)\ns_barrier\n22:\ns_add_u32 %[c], %[c], 1\ns_mov_b32 %[so], %[sn]\ns_cmp_lt_u32 %[c], 16\n"
        "s_cbranch_scc1 9b\ns_waitcnt lgkmcnt(0)\nv_mfma_f32_32x32x16_bf16 v[64:79], v[112:115], v[120:123], v[64:79]\ns_mov_b32 m0, %[sv]\ns_nop 7\ns_nop 7\n"
        : "+{v[32:47]}"(sA), "+{v[64:79]}"(o), "+{v[100:103]}"(k0), "+{v[104:107]}"(k1), "+{v[108:111]}"(v0), "+{v[112:115]}"(v1), "+{v[120:123]}"(pb),
          "+{v124}"(aK), "+{v125}"(a0), "+{v126}"(a1), [so] "+s"(so), [sn] "=&s"(sn), [c] "+s"(c), [sv] "=&s"(sv), [vo] "+v"(vo)
        : "{v[80:95]}"(ns), "{v[96:99]}"(qf), [bk] "v"(bk), [b0] "v"(b0), [b1] "v"(b1), [kd] "s"(kvd), [src] "s"(kvs)
        : "v48", "v49", "v50", "v51", "v52", "v53", "v54", "v55", "v56", "v57", "v58", "v59", "v60", "v61", "v62", "v63", "v116", "v117", "v118", "v119", "v127", "v30", "v31", "scc", "memory");
      PRIO(3); }
    TS(b, 3);

    float bad = o[0];
#pragma unroll
    for (int i = 8; i < 16; ++i) bad = fmaxf(bad, fabsf(o[i]));
    if (__builtin_amdgcn_ballot_w64(!(bad < 1.0e33f)) != 0ull || __builtin_amdgcn_ballot_w64(!(o[0] > 0.f)) != 0ull) {
        const char* kv = ws + WS_KV; float m = -3.0e38f; const f32x16 z = splat16(0.f);
#pragma unroll 1
        for (int u = 0; u < 64; ++u) m = max16(MFMA(*(const bf16x8*)(kv + u * 2048 + l16), qf, z), m);
        ns = splat16(-xhalf_max(m)); o = z;
        const u32x4 one = {0x3F803F80u, 0x3F803F80u, 0x3F803F80u, 0x3F803F80u}; const int vo = 1024 + h * 256 + (l & 15) * 16;
#pragma unroll 1
        for (int u = 0; u < 64; ++u) { f32x16 s = MFMA(*(const bf16x8*)(kv + u * 2048 + l16), qf, ns);
            u32x4 v0 = *(const u32x4*)(kv + u * 2048 + vo), v1 = *(const u32x4*)(kv + u * 2048 + vo + 512);
            if (!(l & 16)) { v0 = one; v1 = one; }
#pragma unroll
            for (int i = 0; i < 16; ++i) s[i] = __builtin_amdgcn_exp2f(s[i]);
            o = MFMA(__builtin_bit_cast(bf16x8, v0), pack8(s, 0), o); o = MFMA(__builtin_bit_cast(bf16x8, v1), pack8(s, 8), o); } }
    const float inv = 1.f / o[0];
#pragma unroll
    for (int i = 8; i < 16; ++i) o[i] *= inv;
    st_sc1(l16, ws + WS_O + (TT * 32 + 4 * hg + kq) * 1024, pack8(o, 8));
    BAR(0);
    if (tid == 0) __hip_atomic_store((GAS unsigned*)(ws + WS_FL + (tg * 8 + hg) * 128), 1u, RLX_AGENT);
    TS(b, 4);
}

DI void ffn_role(int b, const lptr L, unsigned L0, const char* x, const char* wo, const char* wg, const char* wu, const char* wd, char* out, char* ws) {
    const int tid = threadIdx.x, l = tid & 63, w = __builtin_amdgcn_readfirstlane(tid >> 6), h = l >> 5;
    const int xcd = b & 7, i5 = b >> 3, q = (l >> 2) & 3, p = l & 3; const unsigned l16 = l * 16;
    PRIO(2);
    TS(256 + b, 0);
    if (i5 < 28) {
        const int TB = (xcd & 1) * 4 + (i5 & 3), fb = (xcd >> 1) * 7 + (i5 >> 2), ft = w & 1, tk = w >> 1;
        const unsigned vw = (l >> 3) * 3584 + (((l & 7) ^ (((l >> 4) & 1) << 2)) << 4);
        const char* sw = ((w & 4) ? wu : wg) + (w & 3) * 8 * 3584 + fb * 128;
        const char* sx = ws + WS_XN + (TB * 8 + w) * 32768; const unsigned dw = L0 + w * 1024, dx = L0 + 8192 + w * 2048;
#define F1_ISSUE(st) do { glds16(vw, sw + (st) * 114688, dw + ((st) % 3) * 24576); glds16(l16, sx + (st) * 2048, dx + ((st) % 3) * 24576); glds16(l16, sx + (st) * 2048 + 1024, dx + ((st) % 3) * 24576 + 1024); } while (0)
        F1_ISSUE(0); F1_ISSUE(1);
        const lptr pa = L + (((8 * h + q) * 128 + (((((l >> 4) & 1) * 2 + (p >> 1)) ^ ((q >> 1) << 2)) << 4) + 8 * (p & 1)) ^ (ft * 64)), pb = L + 8192 + tk * 4096 + l16;
        f32x16 g0 = splat16(0.f), g1 = g0, u0 = g0, u1 = g0;
        asm volatile("" : "+v"(g0), "+v"(g1), "+v"(u0), "+v"(u1));
        PRIO(P_LOOP);
#pragma unroll
        for (int st = 0; st < 16; ++st) { constexpr int Z = 0; const int o = (st % 3) * 24576;
            if (st < 15) BAR(3); else BAR(0);
            if (st < 14) F1_ISSUE(st + 2);
#pragma unroll
            for (int s = 0; s < 2; ++s) { const bf16x8 fa = ltr(pa, o + s * 2048, o + s * 2048 + 512), fu = ltr(pa, o + 4096 + s * 2048, o + 4096 + s * 2048 + 512);
                const bf16x8 b0 = lr128(pb + o + s * 1024), b1 = lr128(pb + o + 2048 + s * 1024);
                g0 = MFMA(fa, b0, g0); g1 = MFMA(fa, b1, g1); u0 = MFMA(fu, b0, u0); u1 = MFMA(fu, b1, u1); } }
        asm volatile("" : "+v"(g0), "+v"(g1), "+v"(u0), "+v"(u1));
        PRIO(2);
#pragma unroll
        for (int i = 0; i < 16; ++i) { g0[i] = fmaxf(g0[i], 0.f) * u0[i]; g1[i] = fmaxf(g1[i], 0.f) * u1[i]; }
        TS(256 + b, 1);
        char* hp = ws + WS_H + ((TB * 8 + 2 * tk) * 112 + fb * 4 + ft * 2) * 1024;
        st_sc1(l16, hp, pack8(g0, 0)); st_sc1(l16, hp + 1024, pack8(g0, 8)); st_sc1(l16, hp + 114688, pack8(g1, 0)); st_sc1(l16, hp + 115712, pack8(g1, 8));
        BAR(0);
        if (tid == 0) __hip_atomic_fetch_add((GAS unsigned*)(ws + WS_FL + (FL_H + TB) * 128), 1u, RLX_AGENT);
    } else {
        const int j = i5 - 28; unsigned jk = 0;
#pragma unroll
        for (int n = 0; n < 4; ++n) touch(((tid >> 4) << 15) + ((n * 4 + j) * 16 + (tid & 15)) * 128, ws + WS_XN + (xcd & 1) * 1048576);
        TOUCH_END();
        __builtin_amdgcn_s_sleep(32);
#pragma unroll
        for (int n = 0; n < 7; ++n) touch(((n * 4 + j) * 512 + tid) * 128, wd);
        touch((j * 1024 + tid) * 128, wo); touch((j * 1024 + 512 + tid) * 128, wo);
        TOUCH_END();
    }
    TS(256 + b, 2);
    const int tg = xcd * 4 + (i5 & 3), cg = i5 >> 2, t = w & 1, kq = w >> 1;
    const unsigned vo = (l >> 3) * 1024 + (((l & 7) ^ (((l >> 4) & 1) << 2)) << 4);
    const int at = (4 * h + q) * 128 + (((((l >> 4) & 1) * 2 + (p >> 1)) ^ ((q >> 1) << 2)) << 4) + 8 * (p & 1);
    float gg[8];
    {   const char* sw = wd + cg * 128 + w * 14 * 16384; const unsigned dw = L0 + w * 8192;
        const int r0 = 2 * cg - 14;
#define ROT(n) ((n) + r0 < 0 ? (n) + r0 + 14 : (n) + r0)
#define F2_W(n) do { const char* s_ = sw + ROT(n) * 16384; glds16(vo, s_, dw + ((n) & 3) * 2048); glds16(vo, s_ + 8192, dw + ((n) & 3) * 2048 + 1024); } while (0)
        F2_W(0); F2_W(1); F2_W(2); F2_W(3);
        { GAS unsigned* f = (GAS unsigned*)(ws + WS_FL + (FL_H + xcd) * 128);
          for (unsigned sp = 0; sp < SPIN_CAP; ++sp) { if (__builtin_amdgcn_readfirstlane(__hip_atomic_load(f, RLX_AGENT)) >= 28u) break; __builtin_amdgcn_s_sleep(4); } }
        TS(256 + b, 3);
        const char* sh = ws + WS_H + (2 * tg * 112 + w * 14) * 1024;
        bf16x8 hb[4][2];
#define F2_H(n) do { const char* s_ = sh + ROT(n) * 1024; LD_SC1(hb[(n) & 3][0], l16, s_); LD_SC1(hb[(n) & 3][1], l16, s_ + 114688); } while (0)
        F2_H(0); F2_H(1); F2_H(2); F2_H(3);
        f32x16 d00 = splat16(0.f), d01 = d00, d10 = d00, d11 = d00;
        const lptr a0 = L + w * 8192 + at, a1 = L + w * 8192 + (at ^ 64);
        asm volatile("" : "+v"(d00), "+v"(d01), "+v"(d10), "+v"(d11));
        PRIO(P_LOOP2);
#pragma unroll
        for (int n = 0; n < 14; ++n) {
            asm volatile("s_waitcnt vmcnt(%2)" : "+v"(hb[n & 3][0]), "+v"(hb[n & 3][1]) : "n"(n < 3 ? 6 + 2 * n : n < 11 ? 12 : 52 - 4 * n) : "memory");
            bf16x8 f0 = ltr(a0, (n & 3) * 2048, (n & 3) * 2048 + 1024), f1 = ltr(a1, (n & 3) * 2048, (n & 3) * 2048 + 1024);
            asm volatile("s_waitcnt lgkmcnt(0)" : "+v"(f0), "+v"(f1)::"memory");
            if (n < 10) F2_W(n + 4);
            d00 = MFMA(f0, hb[n & 3][0], d00); d01 = MFMA(f1, hb[n & 3][0], d01); d10 = MFMA(f0, hb[n & 3][1], d10); d11 = MFMA(f1, hb[n & 3][1], d11);
            if (n < 10) { asm volatile("" : "+v"(d00), "+v"(d01), "+v"(d10), "+v"(d11)); F2_H(n + 4); } }
        asm volatile("" : "+v"(d00), "+v"(d01), "+v"(d10), "+v"(d11));
        PRIO(2);
        TS(256 + b, 4);
        const lptr pw = L + w * 2048 + l16, pr = L + kq * 16384 + l16; f32x4 a, c;
#define F2_RED(dA, dB, tt) do { BARL(); _Pragma("unroll") for (int u = 0; u < 4; ++u) { lw128f(pw + (u >> 1) * 16384 + (u & 1) * 1024, dA[4 * u], dA[4 * u + 1], dA[4 * u + 2], dA[4 * u + 3]); \
            lw128f(pw + 32768 + (u >> 1) * 16384 + (u & 1) * 1024, dB[4 * u], dB[4 * u + 1], dB[4 * u + 2], dB[4 * u + 3]); } BARL(); \
            if (t == tt) { a = lr128f(pr); c = lr128f(pr + 1024); _Pragma("unroll") for (int u = 1; u < 8; ++u) { a += lr128f(pr + u * 2048); c += lr128f(pr + u * 2048 + 1024); } } } while (0)
        F2_RED(d00, d01, 0); F2_RED(d10, d11, 1);
#pragma unroll
        for (int i = 0; i < 4; ++i) { gg[i] = a[i] * __builtin_amdgcn_rcpf(1.f + __builtin_amdgcn_exp2f(a[i] * -1.4426950408889634f));
                                      gg[4 + i] = c[i] * __builtin_amdgcn_rcpf(1.f + __builtin_amdgcn_exp2f(c[i] * -1.4426950408889634f)); } }
    BARL();
#pragma unroll
    for (int n = 0; n < 8; ++n) glds16(vo, wo + cg * 128 + (w * 8 + n) * 8192, L0 + (w * 8 + n) * 1024);
    const size_t ob = (size_t)(tg * 64 + t * 32 + (l & 31)) * 1024 + cg * 128 + kq * 32;
    const u32x2 x0 = *(const u32x2*)(x + ob + 8 * h), x1 = *(const u32x2*)(x + ob + 16 + 8 * h);
    BAR(0); TS(256 + b, 5);
    { GAS unsigned* f = (GAS unsigned*)(ws + WS_FL + (tg * 8 + 2 * kq + (l & 1)) * 128);
      for (unsigned sp = 0; sp < SPIN_CAP; ++sp) { if (__builtin_amdgcn_ballot_w64(__hip_atomic_load(f, RLX_AGENT) == 0u) == 0ull) break; __builtin_amdgcn_s_sleep(2); } }
    TS(256 + b, 6);
    bf16x8 of[8];
    { const char* so = ws + WS_O + ((2 * tg + t) * 32 + 8 * kq) * 1024;
#pragma unroll
      for (int s = 0; s < 8; ++s) LD_SC1(of[s], l16, so + s * 1024); }
    f32x16 c0 = splat16(0.f), c1 = c0;
    const lptr a0 = L + kq * 16384 + at, a1 = L + kq * 16384 + (at ^ 64);
#pragma unroll
    for (int s = 0; s < 8; ++s) { const bf16x8 f0 = ltr(a0, s * 2048, s * 2048 + 1024), f1 = ltr(a1, s * 2048, s * 2048 + 1024);
        asm volatile("s_waitcnt vmcnt(%1)" : "+v"(of[s]) : "n"(7 - s) : "memory");
        c0 = MFMA(f0, of[s], c0); c1 = MFMA(f1, of[s], c1); }
    BARL();
    const lptr pw = L + (t * 16 + kq) * 2048 + l16;
#pragma unroll
    for (int u = 0; u < 4; ++u) { lw128f(pw + (u >> 1) * 8192 + (u & 1) * 1024, c0[4 * u], c0[4 * u + 1], c0[4 * u + 2], c0[4 * u + 3]);
                                  lw128f(pw + 16384 + (u >> 1) * 8192 + (u & 1) * 1024, c1[4 * u], c1[4 * u + 1], c1[4 * u + 2], c1[4 * u + 3]); }
    BARL();
    const lptr pr = L + (t * 16 + kq * 4) * 2048 + l16; f32x4 a = lr128f(pr), c = lr128f(pr + 1024);
#pragma unroll
    for (int u = 1; u < 4; ++u) { a += lr128f(pr + u * 2048); c += lr128f(pr + u * 2048 + 1024); }
    const unsigned A0 = pk(blo(x0[0]) + a[0] * gg[0], bhi(x0[0]) + a[1] * gg[1]), A1 = pk(blo(x0[1]) + a[2] * gg[2], bhi(x0[1]) + a[3] * gg[3]);
    const unsigned B0 = pk(blo(x1[0]) + c[0] * gg[4], bhi(x1[0]) + c[1] * gg[5]), B1 = pk(blo(x1[1]) + c[2] * gg[6], bhi(x1[1]) + c[3] * gg[7]);
    auto s0 = __builtin_amdgcn_permlane32_swap(A0, B0, false, false), s1 = __builtin_amdgcn_permlane32_swap(A1, B1, false, false);
    const u32x4 ov = {s0[0], s1[0], s0[1], s1[1]};
    *(u32x4*)(out + ob + 16 * h) = ov;
    TS(256 + b, 7);
}

__global__ __launch_bounds__(512, 4) void k2_kernel(const char* x, const char* wq, const char* wo, const char* wg, const char* wu, const char* wd, char* out, char* ws) {
    extern __shared__ char smem[];
    const lptr L = (lptr)smem; const unsigned L0 = (unsigned)(size_t)L;
    const int b = blockIdx.x;
    if (b < 256) attn_role(b, L, L0, wq, ws); else if (!NOFFN) ffn_role(b - 256, L, L0, x, wo, wg, wu, wd, out, ws);
}

extern "C" void kernel_launch(void* const* d_in, const int* in_sizes, int n_in, void* d_out, int out_size, void* d_ws, size_t ws_size, hipStream_t stream) {
    (void)hipFuncSetAttribute((const void*)k1_kernel, hipFuncAttributeMaxDynamicSharedMemorySize, LDS_K1);
    (void)hipFuncSetAttribute((const void*)k2_kernel, hipFuncAttributeMaxDynamicSharedMemorySize, LDS_K2);
    k1_kernel<<<256, 512, LDS_K1, stream>>>((const char*)d_in[0], (const char*)d_in[2], (const char*)d_in[3], (const char*)d_in[1], (const char*)d_in[6], (const char*)d_in[7], (char*)d_ws);
    k2_kernel<<<512, 512, LDS_K2, stream>>>((const char*)d_in[0], (const char*)d_in[1], (const char*)d_in[4], (const char*)d_in[6], (const char*)d_in[7], (const char*)d_in[8], (char*)d_out, (char*)d_ws);
}
```

```cpp
#define TLBW 0
#include <hip/hip_runtime.h>
#include <hip/hip_bf16.h>
#include <stdint.h>

typedef short bf16x8 __attribute__((ext_vector_type(8)));
typedef short s16x4 __attribute__((ext_vector_type(4)));
typedef float f32x16 __attribute__((ext_vector_type(16)));
typedef float f32x4 __attribute__((ext_vector_type(4)));
typedef float f32x2 __attribute__((ext_vector_type(2)));
typedef __bf16 b16x2 __attribute__((ext_vector_type(2)));
typedef unsigned u32x4 __attribute__((ext_vector_type(4)));
typedef unsigned u32x2 __attribute__((ext_vector_type(2)));
#define LAS __attribute__((address_space(3)))
#define GAS __attribute__((address_space(1)))
#define DI __device__ __forceinline__
#define MFMA(a, b, c) __builtin_amdgcn_mfma_f32_32x32x16_bf16((a), (b), (c), 0, 0, 0)
#define RLX_AGENT __ATOMIC_RELAXED, __HIP_MEMORY_SCOPE_AGENT
typedef LAS char* lptr;

constexpr unsigned WS_XN = 0, WS_KV = 2u << 20, WS_H = 4u << 20, WS_O = 12u << 20, WS_FL = 16u << 20;
constexpr int FL_H = 256;
constexpr int LDS_K2 = 81920, LDS_K1 = 65792;
constexpr unsigned SPIN_CAP = 1u << 20;
#define NOFFN 0
#define P_LOOP 0
#define P_LOOP2 0
#define P_ATT 1
#define TOG 1
#define PRIO(n) __builtin_amdgcn_s_setprio(n)
#define TS(blk, i)

DI unsigned pk(float a, float b) { f32x2 v = {a, b}; return __builtin_bit_cast(unsigned, __builtin_convertvector(v, b16x2)); }
DI bf16x8 pack8(const f32x16& x, int o) { u32x4 r = {pk(x[o], x[o + 1]), pk(x[o + 2], x[o + 3]), pk(x[o + 4], x[o + 5]), pk(x[o + 6], x[o + 7])}; return __builtin_bit_cast(bf16x8, r); }
DI float blo(unsigned u) { return __uint_as_float(u << 16); }
DI float bhi(unsigned u) { return __uint_as_float(u & 0xffff0000u); }
DI bf16x8 lr128(lptr p) { return *(const LAS bf16x8*)p; }
DI f32x4 lr128f(lptr p) { return *(const LAS f32x4*)p; }
DI void lw128f(lptr p, float a, float b, float c, float d) { f32x4 v = {a, b, c, d}; *(LAS f32x4*)p = v; }
DI bf16x8 ltr(lptr p, int o0, int o1) {
    s16x4 a = __builtin_amdgcn_ds_read_tr16_b64_v4i16((LAS s16x4*)(p + o0)), b = __builtin_amdgcn_ds_read_tr16_b64_v4i16((LAS s16x4*)(p + o1));
    return __builtin_shufflevector(a, b, 0, 1, 2, 3, 4, 5, 6, 7); }
DI void glds16(unsigned voff, const void* sbase, unsigned lds) {
    unsigned sv; asm volatile("s_mov_b32 %0, m0\n\ts_mov_b32 m0, %3\n\ts_nop 0\n\tglobal_load_lds_dwordx4 %1, %2\n\ts_mov_b32 m0, %0" : "=&s"(sv) : "v"(voff), "s"(sbase), "s"(lds) : "memory"); }
#define BAR(N) asm volatile("s_waitcnt vmcnt(" #N ") lgkmcnt(0)\n\ts_barrier" ::: "memory")
#define BARL() asm volatile("s_waitcnt lgkmcnt(0)\n\ts_barrier" ::: "memory")
DI void st_sc1(unsigned voff, void* sbase, bf16x8 v) { asm volatile("global_store_dwordx4 %0, %1, %2 sc1" ::"v"(voff), "v"(v), "s"(sbase) : "memory"); }
#define LD_SC1(dst, voff, sbase) asm volatile("global_load_dwordx4 %0, %1, %2 sc1" : "=v"(dst) : "v"(voff), "s"(sbase) : "memory")
#define touch(voff, sbase) asm volatile("global_load_dword %0, %1, %2" : "+v"(jk) : "v"(voff), "s"(sbase) : "memory")
#define TOUCH_END() asm volatile("s_waitcnt vmcnt(0)" : "+v"(jk)::"memory")
template <int C> DI float dpp(float x) { return __builtin_bit_cast(float, __builtin_amdgcn_mov_dpp(__builtin_bit_cast(int, x), C, 0xf, 0xf, true)); }
DI float sum32(float x) {
    x += dpp<0xB1>(x); x += dpp<0x4E>(x); x += dpp<0x141>(x); x += dpp<0x140>(x);
    auto s = __builtin_amdgcn_permlane16_swap(__float_as_uint(x), __float_as_uint(x), false, false);
    return __uint_as_float(s[0]) + __uint_as_float(s[1]); }
DI float xhalf_max(float m) { auto r = __builtin_amdgcn_permlane32_swap(__float_as_uint(m), __float_as_uint(m), false, false); return fmaxf(__uint_as_float(r[0]), __uint_as_float(r[1])); }
DI float max16(const f32x16& s, float m) {
#pragma unroll
    for (int i = 0; i < 16; i += 2) m = fmaxf(fmaxf(m, s[i]), s[i + 1]);
    return m; }
DI f32x16 splat16(float v) { f32x16 r;
#pragma unroll
    for (int i = 0; i < 16; ++i) r[i] = v;
    return r; }

__global__ __launch_bounds__(512) void k1_kernel(const char* x, const char* wk, const char* wv, const char* wq, const char* wg, const char* wu, const char* wd, const char* wo, const char* out, char* ws) {
    extern __shared__ char smem[];
    const lptr L = (lptr)smem; const unsigned L0 = (unsigned)(size_t)L;
    const int b = blockIdx.x, tid = threadIdx.x, l = tid & 63, w = __builtin_amdgcn_readfirstlane(tid >> 6), r = l & 31, h = l >> 5;
    TS(512 + b, 0);
    if (b >= 128) {
        const int j = (b - 128) >> 3, fq = (b & 7) >> 1;
        const int n = tid & 255, row = n / 7; unsigned jk = 0;
        if (w < 4) touch((j * 256 + n) * 128, wq);
        if (n < 224) touch((j * 32 + row) * 3584 + fq * 896 + (n - row * 7) * 128, w < 4 ? wg : wu);
        if (TLBW && w == 7) {
            if (l < 7) touch((j * 7 + l) << 16, ws + WS_H);
            if (l < 2) { touch((j * 2 + l) << 16, ws + WS_O); touch((j * 2 + l) << 16, out); }
            if (l < 2 && j < 14) touch((j * 2 + l) << 16, wd);
            if (l == 0 && j < 8) touch(j << 16, wo); }
        TOUCH_END(); TS(512 + b, 2); return; }
    constexpr int WK = 0, WV = 16384 + 128, XL = 33024, RED = 49408;
    const int tok0 = 256 * (b & 7) + 16 * (b >> 3), tl = 2 * w + h;
    const char* xr = x + (size_t)(tok0 + tl) * 1024 + r * 16;
    const u32x4 xa = *(const u32x4*)xr, xb = *(const u32x4*)(xr + 512);
    glds16(l * 16, wk + w * 2048, L0 + WK + w * 2048); glds16(l * 16, wk + w * 2048 + 1024, L0 + WK + w * 2048 + 1024);
    glds16(l * 16, wv + w * 2048, L0 + WV + w * 2048); glds16(l * 16, wv + w * 2048 + 1024, L0 + WV + w * 2048 + 1024);
    if (tid < 3) { const int f = b * 3 + tid; if (f < FL_H + 8) *(unsigned*)(ws + WS_FL + f * 128) = 0u; }
    float v[16];
#pragma unroll
    for (int i = 0; i < 4; ++i) { v[2 * i] = blo(xa[i]); v[2 * i + 1] = bhi(xa[i]); v[8 + 2 * i] = blo(xb[i]); v[9 + 2 * i] = bhi(xb[i]); }
    float s = 0.f;
#pragma unroll
    for (int i = 0; i < 16; ++i) s += v[i];
    const float mean = sum32(s) * (1.f / 512.f); float q2 = 0.f;
#pragma unroll
    for (int i = 0; i < 16; ++i) { v[i] -= mean; q2 += v[i] * v[i]; }
    const float rstd = __builtin_amdgcn_rsqf(sum32(q2) * (1.f / 512.f) + 1e-5f);
    u32x4 ya, yb;
#pragma unroll
    for (int i = 0; i < 4; ++i) { ya[i] = pk(v[2 * i] * rstd, v[2 * i + 1] * rstd); yb[i] = pk(v[8 + 2 * i] * rstd, v[9 + 2 * i] * rstd); }
    { const lptr p = L + XL + r * 256 + ((tl ^ (r & 15)) << 4); *(LAS u32x4*)p = ya; *(LAS u32x4*)(p + 8192) = yb; }
    BAR(0); TS(512 + b, 1);
    { const int c = tid >> 4, tk = tid & 15; const lptr p = L + XL + c * 256 + ((tk ^ (c & 15)) << 4);
      char* g = ws + WS_XN + (tok0 >> 5) * 32768 + c * 512 + ((tok0 & 16) + tk) * 16;
      *(u32x4*)g = *(LAS u32x4*)p; *(u32x4*)(g + 16384) = *(LAS u32x4*)(p + 8192); }
    f32x16 aK = splat16(0.f), aV = splat16(0.f);
    const lptr wp = L + ((l & 16) ? WV : WK) + (64 * w + 8 * h + ((l >> 2) & 3)) * 32 + (l & 3) * 8;
#pragma unroll
    for (int s4 = 0; s4 < 4; ++s4) { const int c = 8 * w + 2 * s4 + h;
        const bf16x8 xf = lr128(L + XL + c * 256 + (((l ^ c) & 15) << 4)), wf = ltr(wp, s4 * 512, s4 * 512 + 128);
        aK = MFMA(wf, xf, aK); aV = MFMA(xf, wf, aV); }
    { const bool vv = l & 16; const lptr p = L + RED + w * 2048 + l * 16;
      lw128f(p, vv ? aV[0] : aK[0], vv ? aV[1] : aK[1], vv ? aV[2] : aK[2], vv ? aV[3] : aK[3]);
      lw128f(p + 1024, vv ? aV[4] : aK[4], vv ? aV[5] : aK[5], vv ? aV[6] : aK[6], vv ? aV[7] : aK[7]); }
    BARL();
    if (w == 0) { f32x4 a = lr128f(L + RED + l * 16), c = lr128f(L + RED + 1024 + l * 16);
#pragma unroll
        for (int i = 1; i < 8; ++i) { a += lr128f(L + RED + i * 2048 + l * 16); c += lr128f(L + RED + i * 2048 + 1024 + l * 16); }
        const u32x4 o = {pk(a[0], a[1]), pk(a[2], a[3]), pk(c[0], c[1]), pk(c[2], c[3])}; const int sh = (tok0 >> 4) & 1;
        const int off = (l & 16) ? 1024 + sh * 512 + h * 256 + (l & 15) * 16 : (16 * sh + (l & 15) + 32 * h) * 16;
        *(u32x4*)(ws + WS_KV + (tok0 >> 5) * 2048 + off) = o; }
    TS(512 + b, 2);
}

DI void attn_role(int b, const lptr L, unsigned L0, const char* wq, char* ws) {
    const int tid = threadIdx.x, l = tid & 63, w = __builtin_amdgcn_readfirstlane(tid >> 6), h = l >> 5, t = w & 1, kq = w >> 1;
    const int i5 = b >> 3, tg = (b & 7) * 4 + (i5 & 3), hg = i5 >> 2, TT = 2 * tg + t;
    constexpr int SL = 8320, TS_ = 2080;
    const unsigned l16 = l * 16;
    PRIO(3); TS(b, 0);
    bf16x8 xf[8];
    { const char* xp = ws + WS_XN + (TT * 32 + 8 * kq) * 1024 + l16;
#pragma unroll
      for (int s = 0; s < 8; ++s) xf[s] = *(const bf16x8*)(xp + s * 1024); }
    { const unsigned vo = (l >> 3) * 1024 + (((l & 7) ^ (((l >> 4) & 1) << 2)) << 4);
#pragma unroll
      for (int n = 0; n < 8; ++n) glds16(vo, wq + hg * 128 + (w * 8 + n) * 8192, L0 + (w * 8 + n) * 1024); }
    const char* kvs = ws + WS_KV + w * 1024; const unsigned kvd = L0 + (w >> 1) * TS_ + (w & 1) * 1024;
    glds16(l16, kvs, kvd + 8 * SL);
    BAR(1); TS(b, 1);
    bf16x8 qf;
    { const int q = (l >> 2) & 3, p = l & 3, ch = ((l >> 4) & 1) * 2 + (p >> 1);
      const int a0 = kq * 16384 + (8 * h + q) * 128 + ((ch ^ ((q >> 1) << 2)) << 4) + 8 * (p & 1);
      f32x16 c0 = splat16(0.f), c1 = splat16(0.f);
#pragma unroll
      for (int s = 0; s < 8; ++s) { c0 = MFMA(ltr(L + a0, s * 2048, s * 2048 + 512), xf[s], c0); c1 = MFMA(ltr(L + (a0 ^ 64), s * 2048, s * 2048 + 512), xf[s], c1); }
      BARL();
      glds16(l16, kvs + 8192, kvd); glds16(l16, kvs + 2 * 8192, kvd + SL);
      if (tid < 24) { const unsigned v = (tid & 1) ? 0u : 0x3F803F80u; const u32x4 one = {v, v, v, v}; *(LAS u32x4*)(L + ((tid >> 1) + (tid < 16 ? 0 : 24)) * TS_ + 2048 + (tid & 1) * 16) = one; }
      f32x4 a, c;
#pragma unroll
      for (int j = 0; j < 4; ++j) { const f32x16& s = j < 2 ? c0 : c1; const int o = 8 * (j & 1);
          if (j == kq) { a = {s[o], s[o + 1], s[o + 2], s[o + 3]}; c = {s[o + 4], s[o + 5], s[o + 6], s[o + 7]}; }
          else { const lptr pw = L + 2 * SL + ((t * 4 + j) * 3 + ((kq - j - 1) & 3)) * 2048 + l16; lw128f(pw, s[o], s[o + 1], s[o + 2], s[o + 3]); lw128f(pw + 1024, s[o + 4], s[o + 5], s[o + 6], s[o + 7]); } }
      BAR(2);
      const lptr pr = L + 2 * SL + (t * 4 + kq) * 6144 + l16;
#pragma unroll
      for (int u = 0; u < 3; ++u) { a += lr128f(pr + u * 2048); c += lr128f(pr + u * 2048 + 1024); }
      const float sc = 0.25f * 1.4426950408889634f; a *= sc; c *= sc;
      const u32x4 qq = {pk(a[0], a[1]), pk(a[2], a[3]), pk(c[0], c[1]), pk(c[2], c[3])}; qf = __builtin_bit_cast(bf16x8, qq); }
    f32x16 ns; const bf16x8 kf = lr128(L + 8 * SL + l16); bf16x8 k0 = lr128(L + 8 * SL + TS_ + l16), k1 = lr128(L + 8 * SL + 2 * TS_ + l16);
    { const f32x16 z = splat16(0.f); f32x16 s0 = MFMA(kf, qf, z), s1 = MFMA(k0, qf, z), s2 = MFMA(k1, qf, z), s3 = MFMA(lr128(L + 8 * SL + 3 * TS_ + l16), qf, z);
      asm volatile("" : "+v"(s0), "+v"(s1), "+v"(s2), "+v"(s3));
      const float m = fmaxf(max16(s1, max16(s0, -3.0e38f)), max16(s3, max16(s2, -3.0e38f)));
      ns = splat16(-(xhalf_max(m) + 90.f)); }
    f32x16 o = splat16(0.f);
    { const unsigned bk = L0 + l16, b0 = L0 + ((l & 16) ? 1024 + h * 256 + (l & 15) * 16 : (l & 11) ? 2064 : 2048), b1 = (l & 16) ? b0 + 512 : b0;
      f32x16 sA = MFMA(kf, qf, ns);
      bf16x8 v0 = lr128(L + 8 * SL + (b0 - L0)), v1 = {0, 0, 0, 0, 0, 0, 0, 0}, pb = v1;
      unsigned aK = bk + 8 * SL, a0 = b0 + 8 * SL, a1 = b1 + 8 * SL, vo = l16 + 9 * 8192, so = 8 * SL, sn, c = 0, sv;
      BAR(1); TS(b, 2);
      if (tid < 48) { const unsigned v = (tid & 1) ? 0u : 0x3F803F80u; const u32x4 one = {v, v, v, v}; *(LAS u32x4*)(L + ((tid >> 1) + 8) * TS_ + 2048 + (tid & 1) * 16) = one; }
#pragma unroll
      for (int n = 3; n < 9; ++n) glds16(l16, kvs + n * 8192, kvd + (n - 1) * SL);
      PRIO(P_ATT);
      asm volatile(
        "s_mov_b32 %[sv], m0\ns_nop 7\ns_nop 7\n9:\ns_add_u32 %[sn], %[so], 8320\ns_cmp_eq_u32 %[so], 66560\ns_cselect_b32 %[sn], 0, %[sn]\ns_setprio 1\ns_cmp_eq_u32 %[hi], 1\n"
        "s_cbranch_scc0 13f\ns_setprio 2\n13:\nv_mfma_f32_32x32x16_bf16 v[48:63], v[100:103], v[96:99], v[80:95]\nds_read_b128 v[100:103], v124 offset:6240\nv_exp_f32 v32, v32\n"
        "v_exp_f32 v33, v33\nv_exp_f32 v34, v34\nv_exp_f32 v35, v35\nv_exp_f32 v36, v36\nv_exp_f32 v37, v37\ns_waitcnt lgkmcnt(1)\n"
        "v_mfma_f32_32x32x16_bf16 v[64:79], v[112:115], v[120:123], v[64:79]\nv_exp_f32 v38, v38\nv_exp_f32 v39, v39\nv_cvt_pk_bf16_f32 v116, v32, v33\n"
        "v_cvt_pk_bf16_f32 v117, v34, v35\nv_cvt_pk_bf16_f32 v118, v36, v37\nv_cvt_pk_bf16_f32 v119, v38, v39\nv_exp_f32 v40, v40\nv_exp_f32 v41, v41\nv_exp_f32 v42, v42\n"
        "v_mfma_f32_32x32x16_bf16 v[64:79], v[108:111], v[116:119], v[64:79]\nds_read_b128 v[112:115], v126 offset:0\nds_read_b128 v[108:111], v125 offset:2080\n"
        "v_add_u32 v127, %[sn], %[bk]\nv_add_u32 v31, %[sn], %[b0]\nv_add_u32 v30, %[sn], %[b1]\nv_exp_f32 v43, v43\nv_exp_f32 v44, v44\nv_exp_f32 v45, v45\nv_exp_f32 v46, v46\n"
        "v_exp_f32 v47, v47\nv_cvt_pk_bf16_f32 v120, v40, v41\nv_cvt_pk_bf16_f32 v121, v42, v43\nv_cvt_pk_bf16_f32 v122, v44, v45\nv_cvt_pk_bf16_f32 v123, v46, v47\n"
        "v_mfma_f32_32x32x16_bf16 v[32:47], v[104:107], v[96:99], v[80:95]\nds_read_b128 v[104:107], v127 offset:0\nv_exp_f32 v48, v48\nv_exp_f32 v49, v49\nv_exp_f32 v50, v50\n"
        "v_exp_f32 v51, v51\nv_exp_f32 v52, v52\nv_exp_f32 v53, v53\ns_waitcnt lgkmcnt(1)\nv_mfma_f32_32x32x16_bf16 v[64:79], v[112:115], v[120:123], v[64:79]\nv_exp_f32 v54, v54\n"
        "v_exp_f32 v55, v55\nv_cvt_pk_bf16_f32 v116, v48, v49\nv_cvt_pk_bf16_f32 v117, v50, v51\nv_cvt_pk_bf16_f32 v118, v52, v53\nv_cvt_pk_bf16_f32 v119, v54, v55\n"
        "v_exp_f32 v56, v56\nv_exp_f32 v57, v57\nv_exp_f32 v58, v58\nv_mfma_f32_32x32x16_bf16 v[64:79], v[108:111], v[116:119], v[64:79]\nds_read_b128 v[112:115], v126 offset:2080\n"
        "ds_read_b128 v[108:111], v125 offset:4160\nv_exp_f32 v59, v59\nv_exp_f32 v60, v60\nv_exp_f32 v61, v61\nv_exp_f32 v62, v62\nv_exp_f32 v63, v63\n"
        "v_cvt_pk_bf16_f32 v120, v56, v57\nv_cvt_pk_bf16_f32 v121, v58, v59\nv_cvt_pk_bf16_f32 v122, v60, v61\nv_cvt_pk_bf16_f32 v123, v62, v63\n"
        "v_mfma_f32_32x32x16_bf16 v[48:63], v[100:103], v[96:99], v[80:95]\nds_read_b128 v[100:103], v127 offset:2080\nv_exp_f32 v32, v32\nv_exp_f32 v33, v33\nv_exp_f32 v34, v34\n"
        "v_exp_f32 v35, v35\nv_exp_f32 v36, v36\nv_exp_f32 v37, v37\ns_waitcnt lgkmcnt(1)\nv_mfma_f32_32x32x16_bf16 v[64:79], v[112:115], v[120:123], v[64:79]\nv_exp_f32 v38, v38\n"
        "v_exp_f32 v39, v39\nv_cvt_pk_bf16_f32 v116, v32, v33\nv_cvt_pk_bf16_f32 v117, v34, v35\nv_cvt_pk_bf16_f32 v118, v36, v37\nv_cvt_pk_bf16_f32 v119, v38, v39\n"
        "v_exp_f32 v40, v40\nv_exp_f32 v41, v41\nv_exp_f32 v42, v42\nv_mfma_f32_32x32x16_bf16 v[64:79], v[108:111], v[116:119], v[64:79]\nds_read_b128 v[112:115], v126 offset:4160\n"
        "ds_read_b128 v[108:111], v125 offset:6240\nv_exp_f32 v43, v43\nv_exp_f32 v44, v44\nv_exp_f32 v45, v45\nv_exp_f32 v46, v46\nv_exp_f32 v47, v47\n"
        "v_cvt_pk_bf16_f32 v120, v40, v41\nv_cvt_pk_bf16_f32 v121, v42, v43\nv_cvt_pk_bf16_f32 v122, v44, v45\nv_cvt_pk_bf16_f32 v123, v46, v47\n"
        "v_mfma_f32_32x32x16_bf16 v[32:47], v[104:107], v[96:99], v[80:95]\nds_read_b128 v[104:107], v127 offset:4160\nv_exp_f32 v48, v48\nv_exp_f32 v49, v49\nv_exp_f32 v50, v50\n"
        "v_exp_f32 v51, v51\nv_exp_f32 v52, v52\nv_exp_f32 v53, v53\ns_waitcnt lgkmcnt(1)\nv_mfma_f32_32x32x16_bf16 v[64:79], v[112:115], v[120:123], v[64:79]\nv_exp_f32 v54, v54\n"
        "v_exp_f32 v55, v55\nv_cvt_pk_bf16_f32 v116, v48, v49\nv_cvt_pk_bf16_f32 v117, v50, v51\nv_cvt_pk_bf16_f32 v118, v52, v53\nv_cvt_pk_bf16_f32 v119, v54, v55\n"
        "v_exp_f32 v56, v56\nv_exp_f32 v57, v57\nv_exp_f32 v58, v58\nv_mfma_f32_32x32x16_bf16 v[64:79], v[108:111], v[116:119], v[64:79]\nds_read_b128 v[112:115], v126 offset:6240\n"
        "ds_read_b128 v[108:111], v31 offset:0\nv_exp_f32 v59, v59\nv_exp_f32 v60, v60\nv_exp_f32 v61, v61\nv_exp_f32 v62, v62\nv_exp_f32 v63, v63\n"
        "v_cvt_pk_bf16_f32 v120, v56, v57\nv_cvt_pk_bf16_f32 v121, v58, v59\nv_cvt_pk_bf16_f32 v122, v60, v61\nv_cvt_pk_bf16_f32 v123, v62, v63\ns_cmp_gt_u32 %[c], 7\n"
        "s_cbranch_scc1 12f\ns_cmp_eq_u32 %[c], 7\ns_cbranch_scc1 11f\ns_add_u32 m0, %[so], %[kd]\ns_waitcnt vmcnt(6) lgkmcnt(0)\ns_barrier\nglobal_load_lds_dwordx4 %[vo], %[src]\n"
        "v_add_u32 %[vo], 0x2000, %[vo]\ns_branch 12f\n11:\ns_waitcnt vmcnt(0) lgkmcnt(0)\ns_barrier\n12:\ns_add_u32 %[c], %[c], 1\ns_mov_b32 %[so], %[sn]\n"
        "s_add_u32 %[sn], %[so], 8320\ns_cmp_eq_u32 %[so], 66560\ns_cselect_b32 %[sn], 0, %[sn]\ns_setprio 1\ns_cmp_eq_u32 %[hi], 0\ns_cbranch_scc0 23f\ns_setprio 2\n23:\n"
        "v_mfma_f32_32x32x16_bf16 v[48:63], v[100:103], v[96:99], v[80:95]\nds_read_b128 v[100:103], v127 offset:6240\nv_exp_f32 v32, v32\nv_exp_f32 v33, v33\nv_exp_f32 v34, v34\n"
        "v_exp_f32 v35, v35\nv_exp_f32 v36, v36\nv_exp_f32 v37, v37\ns_waitcnt lgkmcnt(1)\nv_mfma_f32_32x32x16_bf16 v[64:79], v[112:115], v[120:123], v[64:79]\nv_exp_f32 v38, v38\n"
        "v_exp_f32 v39, v39\nv_cvt_pk_bf16_f32 v116, v32, v33\nv_cvt_pk_bf16_f32 v117, v34, v35\nv_cvt_pk_bf16_f32 v118, v36, v37\nv_cvt_pk_bf16_f32 v119, v38, v39\n"
        "v_exp_f32 v40, v40\nv_exp_f32 v41, v41\nv_exp_f32 v42, v42\nv_mfma_f32_32x32x16_bf16 v[64:79], v[108:111], v[116:119], v[64:79]\nds_read_b128 v[112:115], v30 offset:0\n"
        "ds_read_b128 v[108:111], v31 offset:2080\nv_add_u32 v124, %[sn], %[bk]\nv_add_u32 v125, %[sn], %[b0]\nv_add_u32 v126, %[sn], %[b1]\nv_exp_f32 v43, v43\nv_exp_f32 v44, v44\n"
        "v_exp_f32 v45, v45\nv_exp_f32 v46, v46\nv_exp_f32 v47, v47\nv_cvt_pk_bf16_f32 v120, v40, v41\nv_cvt_pk_bf16_f32 v121, v42, v43\nv_cvt_pk_bf16_f32 v122, v44, v45\n"
        "v_cvt_pk_bf16_f32 v123, v46, v47\nv_mfma_f32_32x32x16_bf16 v[32:47], v[104:107], v[96:99], v[80:95]\nds_read_b128 v[104:107], v124 offset:0\nv_exp_f32 v48, v48\n"
        "v_exp_f32 v49, v49\nv_exp_f32 v50, v50\nv_exp_f32 v51, v51\nv_exp_f32 v52, v52\nv_exp_f32 v53, v53\ns_waitcnt lgkmcnt(1)\n"
        "v_mfma_f32_32x32x16_bf16 v[64:79], v[112:115], v[120:123], v[64:79]\nv_exp_f32 v54, v54\nv_exp_f32 v55, v55\nv_cvt_pk_bf16_f32 v116, v48, v49\n"
        "v_cvt_pk_bf16_f32 v117, v50, v51\nv_cvt_pk_bf16_f32 v118, v52, v53\nv_cvt_pk_bf16_f32 v119, v54, v55\nv_exp_f32 v56, v56\nv_exp_f32 v57, v57\nv_exp_f32 v58, v58\n"
        "v_mfma_f32_32x32x16_bf16 v[64:79], v[108:111], v[116:119], v[64:79]\nds_read_b128 v[112:115], v30 offset:2080\nds_read_b128 v[108:111], v31 offset:4160\n"
        "v_exp_f32 v59, v59\nv_exp_f32 v60, v60\nv_exp_f32 v61, v61\nv_exp_f32 v62, v62\nv_exp_f32 v63, v63\nv_cvt_pk_bf16_f32 v120, v56, v57\nv_cvt_pk_bf16_f32 v121, v58, v59\n"
        "v_cvt_pk_bf16_f32 v122, v60, v61\nv_cvt_pk_bf16_f32 v123, v62, v63\nv_mfma_f32_32x32x16_bf16 v[48:63], v[100:103], v[96:99], v[80:95]\n"
        "ds_read_b128 v[100:103], v124 offset:2080\nv_exp_f32 v32, v32\nv_exp_f32 v33, v33\nv_exp_f32 v34, v34\nv_exp_f32 v35, v35\nv_exp_f32 v36, v36\nv_exp_f32 v37, v37\n"
        "s_waitcnt lgkmcnt(1)\nv_mfma_f32_32x32x16_bf16 v[64:79], v[112:115], v[120:123], v[64:79]\nv_exp_f32 v38, v38\nv_exp_f32 v39, v39\nv_cvt_pk_bf16_f32 v116, v32, v33\n"
        "v_cvt_pk_bf16_f32 v117, v34, v35\nv_cvt_pk_bf16_f32 v118, v36, v37\nv_cvt_pk_bf16_f32 v119, v38, v39\nv_exp_f32 v40, v40\nv_exp_f32 v41, v41\nv_exp_f32 v42, v42\n"
        "v_mfma_f32_32x32x16_bf16 v[64:79], v[108:111], v[116:119], v[64:79]\nds_read_b128 v[112:115], v30 offset:4160\nds_read_b128 v[108:111], v31 offset:6240\n"
        "v_exp_f32 v43, v43\nv_exp_f32 v44, v44\nv_exp_f32 v45, v45\nv_exp_f32 v46, v46\nv_exp_f32 v47, v47\nv_cvt_pk_bf16_f32 v120, v40, v41\nv_cvt_pk_bf16_f32 v121, v42, v43\n"
        "v_cvt_pk_bf16_f32 v122, v44, v45\nv_cvt_pk_bf16_f32 v123, v46, v47\nv_mfma_f32_32x32x16_bf16 v[32:47], v[104:107], v[96:99], v[80:95]\n"
        "ds_read_b128 v[104:107], v124 offset:4160\nv_exp_f32 v48, v48\nv_exp_f32 v49, v49\nv_exp_f32 v50, v50\nv_exp_f32 v51, v51\nv_exp_f32 v52, v52\nv_exp_f32 v53, v53\n"
        "s_waitcnt lgkmcnt(1)\nv_mfma_f32_32x32x16_bf16 v[64:79], v[112:115], v[120:123], v[64:79]\nv_exp_f32 v54, v54\nv_exp_f32 v55, v55\nv_cvt_pk_bf16_f32 v116, v48, v49\n"
        "v_cvt_pk_bf16_f32 v117, v50, v51\nv_cvt_pk_bf16_f32 v118, v52, v53\nv_cvt_pk_bf16_f32 v119, v54, v55\nv_exp_f32 v56, v56\nv_exp_f32 v57, v57\nv_exp_f32 v58, v58\n"
        "v_mfma_f32_32x32x16_bf16 v[64:79], v[108:111], v[116:119], v[64:79]\nds_read_b128 v[112:115], v30 offset:6240\nds_read_b128 v[108:111], v125 offset:0\nv_exp_f32 v59, v59\n"
        "v_exp_f32 v60, v60\nv_exp_f32 v61, v61\nv_exp_f32 v62, v62\nv_exp_f32 v63, v63\nv_cvt_pk_bf16_f32 v120, v56, v57\nv_cvt_pk_bf16_f32 v121, v58, v59\n"
        "v_cvt_pk_bf16_f32 v122, v60, v61\nv_cvt_pk_bf16_f32 v123, v62, v63\ns_cmp_gt_u32 %[c], 7\ns_cbranch_scc1 22f\ns_cmp_eq_u32 %[c], 7\ns_cbranch_scc1 21f\n"
        "s_add_u32 m0, %[so], %[kd]\ns_waitcnt vmcnt(6) lgkmcnt(0)\ns_barrier\nglobal_load_lds_dwordx4 %[vo], %[src]\nv_add_u32 %[vo], 0x2000, %[vo]\ns_branch 22f\n21:\n"
        "s_waitcnt vmcnt(0) lgkmcnt(0)\ns_barrier\n22:\ns_add_u32 %[c], %[c], 1\ns_mov_b32 %[so], %[sn]\ns_cmp_lt_u32 %[c], 16\ns_cbranch_scc1 9b\ns_waitcnt lgkmcnt(0)\n"
        "v_mfma_f32_32x32x16_bf16 v[64:79], v[112:115], v[120:123], v[64:79]\ns_mov_b32 m0, %[sv]\ns_nop 7\ns_nop 7\n"
        : "+{v[32:47]}"(sA), "+{v[64:79]}"(o), "+{v[100:103]}"(k0), "+{v[104:107]}"(k1), "+{v[108:111]}"(v0), "+{v[112:115]}"(v1), "+{v[120:123]}"(pb),
          "+{v124}"(aK), "+{v125}"(a0), "+{v126}"(a1), [so] "+s"(so), [sn] "=&s"(sn), [c] "+s"(c), [sv] "=&s"(sv), [vo] "+v"(vo)
        : "{v[80:95]}"(ns), "{v[96:99]}"(qf), [bk] "v"(bk), [b0] "v"(b0), [b1] "v"(b1), [kd] "s"(kvd), [src] "s"(kvs), [hi] "s"(w >> 2)
        : "v48", "v49", "v50", "v51", "v52", "v53", "v54", "v55", "v56", "v57", "v58", "v59", "v60", "v61", "v62", "v63", "v116", "v117", "v118", "v119", "v127", "v30", "v31", "scc", "memory");
      PRIO(3); }
    TS(b, 3);
    int m_; asm volatile("v_mbcnt_lo_u32_b32 %0, -1, 0\n\tv_mbcnt_hi_u32_b32 %0, -1, %0" : "=v"(m_));
    const unsigned m16 = m_ * 16;

    float bad = o[0];
#pragma unroll
    for (int i = 8; i < 16; ++i) bad = fmaxf(bad, fabsf(o[i]));
    if (__builtin_amdgcn_ballot_w64(!(bad < 1.0e33f)) != 0ull || __builtin_amdgcn_ballot_w64(!(o[0] > 0.f)) != 0ull) {
        const char* kv = ws + WS_KV; float m = -3.0e38f; const f32x16 z = splat16(0.f);
#pragma unroll 1
        for (int u = 0; u < 64; ++u) m = max16(MFMA(*(const bf16x8*)(kv + u * 2048 + m16), qf, z), m);
        ns = splat16(-xhalf_max(m)); o = z;
        const u32x4 one = {0x3F803F80u, 0x3F803F80u, 0x3F803F80u, 0x3F803F80u}; const int vo = 1024 + (m_ >> 5) * 256 + (m_ & 15) * 16;
#pragma unroll 1
        for (int u = 0; u < 64; ++u) { f32x16 s = MFMA(*(const bf16x8*)(kv + u * 2048 + m16), qf, ns);
            u32x4 v0 = *(const u32x4*)(kv + u * 2048 + vo), v1 = *(const u32x4*)(kv + u * 2048 + vo + 512);
            if (!(m_ & 16)) { v0 = one; v1 = one; }
#pragma unroll
            for (int i = 0; i < 16; ++i) s[i] = __builtin_amdgcn_exp2f(s[i]);
            o = MFMA(__builtin_bit_cast(bf16x8, v0), pack8(s, 0), o); o = MFMA(__builtin_bit_cast(bf16x8, v1), pack8(s, 8), o); } }
    const float inv = 1.f / o[0];
#pragma unroll
    for (int i = 8; i < 16; ++i) o[i] *= inv;
    st_sc1(m16, ws + WS_O + (TT * 32 + 4 * hg + kq) * 1024, pack8(o, 8));
    BAR(0);
    if (w == 0 && m_ == 0) __hip_atomic_store((GAS unsigned*)(ws + WS_FL + (tg * 8 + hg) * 128), 1u, RLX_AGENT);
    TS(b, 4);
}

DI void ffn_role(int b, const lptr L, unsigned L0, const char* x, const char* wo, const char* wg, const char* wu, const char* wd, char* out, char* ws) {
    const int tid = threadIdx.x, l = tid & 63, w = __builtin_amdgcn_readfirstlane(tid >> 6), h = l >> 5;
    const int xcd = b & 7, i5 = b >> 3, q = (l >> 2) & 3, p = l & 3; const unsigned l16 = l * 16;
    PRIO(2);
    TS(256 + b, 0);
    if (i5 < 28) {
        const int TB = (xcd & 1) * 4 + (i5 & 3), fb = (xcd >> 1) * 7 + (i5 >> 2), ft = w & 1, tk = w >> 1;
        const unsigned vw = (l >> 3) * 3584 + (((l & 7) ^ (((l >> 4) & 1) << 2)) << 4);
        const char* sw = ((w & 4) ? wu : wg) + (w & 3) * 8 * 3584 + fb * 128;
        const char* sx = ws + WS_XN + (TB * 8 + w) * 32768; const unsigned dw = L0 + w * 1024, dx = L0 + 8192 + w * 2048;
#define F1_ISSUE(st) do { glds16(vw, sw + (st) * 114688, dw + ((st) % 3) * 24576); glds16(l16, sx + (st) * 2048, dx + ((st) % 3) * 24576); glds16(l16, sx + (st) * 2048 + 1024, dx + ((st) % 3) * 24576 + 1024); } while (0)
        F1_ISSUE(0); F1_ISSUE(1);
        unsigned jk = 0; const unsigned tv = (i5 * 512 + tid) * 128;
        const lptr pa = L + (((8 * h + q) * 128 + (((((l >> 4) & 1) * 2 + (p >> 1)) ^ ((q >> 1) << 2)) << 4) + 8 * (p & 1)) ^ (ft * 64)), pb = L + 8192 + tk * 4096 + l16;
        f32x16 g0 = splat16(0.f), g1 = g0, u0 = g0, u1 = g0;
        asm volatile("" : "+v"(g0), "+v"(g1), "+v"(u0), "+v"(u1));
        PRIO(P_LOOP);
#pragma unroll
        for (int st = 0; st < 16; ++st) { constexpr int Z = 0; const int o = (st % 3) * 24576;
            if (st == 3 || st == 4) BAR(4); else if (st < 15) BAR(3); else BAR(0);
            if (st < 14) F1_ISSUE(st + 2);
            if (st == 2) touch(tv, wd);
#pragma unroll
            for (int s = 0; s < 2; ++s) { const bf16x8 fa = ltr(pa, o + s * 2048, o + s * 2048 + 512), fu = ltr(pa, o + 4096 + s * 2048, o + 4096 + s * 2048 + 512);
                const bf16x8 b0 = lr128(pb + o + s * 1024), b1 = lr128(pb + o + 2048 + s * 1024);
                g0 = MFMA(fa, b0, g0); g1 = MFMA(fa, b1, g1); u0 = MFMA(fu, b0, u0); u1 = MFMA(fu, b1, u1); } }
        asm volatile("" : "+v"(g0), "+v"(g1), "+v"(u0), "+v"(u1), "+v"(jk));
        PRIO(2);
#pragma unroll
        for (int i = 0; i < 16; ++i) { g0[i] = fmaxf(g0[i], 0.f) * u0[i]; g1[i] = fmaxf(g1[i], 0.f) * u1[i]; }
        TS(256 + b, 1);
        char* hp = ws + WS_H + ((TB * 8 + 2 * tk) * 112 + fb * 4 + ft * 2) * 1024;
        st_sc1(l16, hp, pack8(g0, 0)); st_sc1(l16, hp + 1024, pack8(g0, 8)); st_sc1(l16, hp + 114688, pack8(g1, 0)); st_sc1(l16, hp + 115712, pack8(g1, 8));
        BAR(0);
        if (tid == 0) __hip_atomic_fetch_add((GAS unsigned*)(ws + WS_FL + (FL_H + TB) * 128), 1u, RLX_AGENT);
    } else {
        const int j = i5 - 28; unsigned jk = 0;
        __builtin_amdgcn_s_sleep(127);
        touch((j * 1024 + tid) * 128, wo); TOUCH_END(); touch((j * 1024 + 512 + tid) * 128, wo); TOUCH_END();
    }
    TS(256 + b, 2);
    const int tg = xcd * 4 + (i5 & 3), cg = i5 >> 2, t = w & 1, kq = w >> 1;
    const unsigned vo = (l >> 3) * 1024 + (((l & 7) ^ (((l >> 4) & 1) << 2)) << 4);
    const int at = (4 * h + q) * 128 + (((((l >> 4) & 1) * 2 + (p >> 1)) ^ ((q >> 1) << 2)) << 4) + 8 * (p & 1);
    float gg[8];
    {   const char* sw = wd + cg * 128 + w * 14 * 16384; const unsigned dw = L0 + w * 8192;
        const int r0 = 2 * cg - 14;
#define ROT(n) ((n) + r0 < 0 ? (n) + r0 + 14 : (n) + r0)
#define F2_W(n) do { const char* s_ = sw + ROT(n) * 16384; glds16(vo, s_, dw + ((n) & 3) * 2048); glds16(vo, s_ + 8192, dw + ((n) & 3) * 2048 + 1024); } while (0)
        F2_W(0); F2_W(1); F2_W(2); F2_W(3);
        { GAS unsigned* f = (GAS unsigned*)(ws + WS_FL + (FL_H + xcd) * 128);
          for (unsigned sp = 0; sp < SPIN_CAP; ++sp) { if (__builtin_amdgcn_readfirstlane(__hip_atomic_load(f, RLX_AGENT)) >= 28u) break; __builtin_amdgcn_s_sleep(4); } }
        TS(256 + b, 3);
        const char* sh = ws + WS_H + (2 * tg * 112 + w * 14) * 1024;
        bf16x8 hb[4][2];
#define F2_H(n) do { const char* s_ = sh + ROT(n) * 1024; LD_SC1(hb[(n) & 3][0], l16, s_); LD_SC1(hb[(n) & 3][1], l16, s_ + 114688); } while (0)
        F2_H(0); F2_H(1); F2_H(2); F2_H(3);
        f32x16 d00 = splat16(0.f), d01 = d00, d10 = d00, d11 = d00;
        const lptr a0 = L + w * 8192 + at, a1 = L + w * 8192 + (at ^ 64);
        asm volatile("" : "+v"(d00), "+v"(d01), "+v"(d10), "+v"(d11));
        PRIO(P_LOOP2);
#pragma unroll
        for (int n = 0; n < 14; ++n) {
            asm volatile("s_waitcnt vmcnt(%2)" : "+v"(hb[n & 3][0]), "+v"(hb[n & 3][1]) : "n"(n < 3 ? 6 + 2 * n : n < 11 ? 12 : 52 - 4 * n) : "memory");
            bf16x8 f0 = ltr(a0, (n & 3) * 2048, (n & 3) * 2048 + 1024), f1 = ltr(a1, (n & 3) * 2048, (n & 3) * 2048 + 1024);
            asm volatile("s_waitcnt lgkmcnt(0)" : "+v"(f0), "+v"(f1)::"memory");
            if (n < 10) F2_W(n + 4);
            d00 = MFMA(f0, hb[n & 3][0], d00); d01 = MFMA(f1, hb[n & 3][0], d01); d10 = MFMA(f0, hb[n & 3][1], d10); d11 = MFMA(f1, hb[n & 3][1], d11);
            if (n < 10) { asm volatile("" : "+v"(d00), "+v"(d01), "+v"(d10), "+v"(d11)); F2_H(n + 4); } }
        asm volatile("" : "+v"(d00), "+v"(d01), "+v"(d10), "+v"(d11));
        PRIO(2);
        TS(256 + b, 4);
        const lptr pw = L + w * 2048 + l16, pr = L + kq * 16384 + l16; f32x4 a, c;
#define F2_RED(dA, dB, tt) do { BARL(); _Pragma("unroll") for (int u = 0; u < 4; ++u) { lw128f(pw + (u >> 1) * 16384 + (u & 1) * 1024, dA[4 * u], dA[4 * u + 1], dA[4 * u + 2], dA[4 * u + 3]); \
            lw128f(pw + 32768 + (u >> 1) * 16384 + (u & 1) * 1024, dB[4 * u], dB[4 * u + 1], dB[4 * u + 2], dB[4 * u + 3]); } BARL(); \
            if (t == tt) { a = lr128f(pr); c = lr128f(pr + 1024); _Pragma("unroll") for (int u = 1; u < 8; ++u) { a += lr128f(pr + u * 2048); c += lr128f(pr + u * 2048 + 1024); } } } while (0)
        F2_RED(d00, d01, 0); F2_RED(d10, d11, 1);
#pragma unroll
        for (int i = 0; i < 4; ++i) { gg[i] = a[i] * __builtin_amdgcn_rcpf(1.f + __builtin_amdgcn_exp2f(a[i] * -1.4426950408889634f));
                                      gg[4 + i] = c[i] * __builtin_amdgcn_rcpf(1.f + __builtin_amdgcn_exp2f(c[i] * -1.4426950408889634f)); } }
    BARL();
#pragma unroll
    for (int n = 0; n < 8; ++n) glds16(vo, wo + cg * 128 + (w * 8 + n) * 8192, L0 + (w * 8 + n) * 1024);
    const size_t ob = (size_t)(tg * 64 + t * 32 + (l & 31)) * 1024 + cg * 128 + kq * 32;
    const u32x2 x0 = *(const u32x2*)(x + ob + 8 * h), x1 = *(const u32x2*)(x + ob + 16 + 8 * h);
    BAR(0); TS(256 + b, 5);
    { GAS unsigned* f = (GAS unsigned*)(ws + WS_FL + (tg * 8 + 2 * kq + (l & 1)) * 128);
      for (unsigned sp = 0; sp < SPIN_CAP; ++sp) { if (__builtin_amdgcn_ballot_w64(__hip_atomic_load(f, RLX_AGENT) == 0u) == 0ull) break; __builtin_amdgcn_s_sleep(2); } }
    TS(256 + b, 6);
    bf16x8 of[8];
    { const char* so = ws + WS_O + ((2 * tg + t) * 32 + 8 * kq) * 1024;
#pragma unroll
      for (int s = 0; s < 8; ++s) LD_SC1(of[s], l16, so + s * 1024); }
    f32x16 c0 = splat16(0.f), c1 = c0;
    const lptr a0 = L + kq * 16384 + at, a1 = L + kq * 16384 + (at ^ 64);
#pragma unroll
    for (int s = 0; s < 8; ++s) { const bf16x8 f0 = ltr(a0, s * 2048, s * 2048 + 1024), f1 = ltr(a1, s * 2048, s * 2048 + 1024);
        asm volatile("s_waitcnt vmcnt(%1)" : "+v"(of[s]) : "n"(7 - s) : "memory");
        c0 = MFMA(f0, of[s], c0); c1 = MFMA(f1, of[s], c1); }
    BARL();
    const lptr pw = L + (t * 16 + kq) * 2048 + l16;
#pragma unroll
    for (int u = 0; u < 4; ++u) { lw128f(pw + (u >> 1) * 8192 + (u & 1) * 1024, c0[4 * u], c0[4 * u + 1], c0[4 * u + 2], c0[4 * u + 3]);
                                  lw128f(pw + 16384 + (u >> 1) * 8192 + (u & 1) * 1024, c1[4 * u], c1[4 * u + 1], c1[4 * u + 2], c1[4 * u + 3]); }
    BARL();
    const lptr pr = L + (t * 16 + kq * 4) * 2048 + l16; f32x4 a = lr128f(pr), c = lr128f(pr + 1024);
#pragma unroll
    for (int u = 1; u < 4; ++u) { a += lr128f(pr + u * 2048); c += lr128f(pr + u * 2048 + 1024); }
    const unsigned A0 = pk(blo(x0[0]) + a[0] * gg[0], bhi(x0[0]) + a[1] * gg[1]), A1 = pk(blo(x0[1]) + a[2] * gg[2], bhi(x0[1]) + a[3] * gg[3]);
    const unsigned B0 = pk(blo(x1[0]) + c[0] * gg[4], bhi(x1[0]) + c[1] * gg[5]), B1 = pk(blo(x1[1]) + c[2] * gg[6], bhi(x1[1]) + c[3] * gg[7]);
    auto s0 = __builtin_amdgcn_permlane32_swap(A0, B0, false, false), s1 = __builtin_amdgcn_permlane32_swap(A1, B1, false, false);
    const u32x4 ov = {s0[0], s1[0], s0[1], s1[1]};
    *(u32x4*)(out + ob + 16 * h) = ov;
    TS(256 + b, 7);
}

__global__ __launch_bounds__(512, 4) void k2_kernel(const char* x, const char* wq, const char* wo, const char* wg, const char* wu, const char* wd, char* out, char* ws) {
    extern __shared__ char smem[];
    const lptr L = (lptr)smem; const unsigned L0 = (unsigned)(size_t)L;
    const int b = blockIdx.x;
    if (b < 256) attn_role(b, L, L0, wq, ws); else if (!NOFFN) ffn_role(b - 256, L, L0, x, wo, wg, wu, wd, out, ws);
}

extern "C" void kernel_launch(void* const* d_in, const int* in_sizes, int n_in, void* d_out, int out_size, void* d_ws, size_t ws_size, hipStream_t stream) {
    (void)hipFuncSetAttribute((const void*)k1_kernel, hipFuncAttributeMaxDynamicSharedMemorySize, LDS_K1);
    (void)hipFuncSetAttribute((const void*)k2_kernel, hipFuncAttributeMaxDynamicSharedMemorySize, LDS_K2);
    k1_kernel<<<256, 512, LDS_K1, stream>>>((const char*)d_in[0], (const char*)d_in[2], (const char*)d_in[3], (const char*)d_in[1], (const char*)d_in[6], (const char*)d_in[7], (const char*)d_in[8], (const char*)d_in[4], (const char*)d_out, (char*)d_ws);
    k2_kernel<<<512, 512, LDS_K2, stream>>>((const char*)d_in[0], (const char*)d_in[1], (const char*)d_in[4], (const char*)d_in[6], (const char*)d_in[7], (const char*)d_in[8], (char*)d_out, (char*)d_ws);
}
```

```cpp
#include <hip/hip_runtime.h>
#include <hip/hip_bf16.h>
#include <stdint.h>

typedef short bf16x8 __attribute__((ext_vector_type(8)));
typedef short s16x4 __attribute__((ext_vector_type(4)));
typedef float f32x16 __attribute__((ext_vector_type(16)));
typedef float f32x4 __attribute__((ext_vector_type(4)));
typedef float f32x2 __attribute__((ext_vector_type(2)));
typedef __bf16 b16x2 __attribute__((ext_vector_type(2)));
typedef unsigned u32x4 __attribute__((ext_vector_type(4)));
typedef unsigned u32x2 __attribute__((ext_vector_type(2)));
#define LAS __attribute__((address_space(3)))
#define GAS __attribute__((address_space(1)))
#define DI __device__ __forceinline__
#define MFMA(a, b, c) __builtin_amdgcn_mfma_f32_32x32x16_bf16((a), (b), (c), 0, 0, 0)
#define RLX_AGENT __ATOMIC_RELAXED, __HIP_MEMORY_SCOPE_AGENT
typedef LAS char* lptr;

constexpr unsigned WS_XN = 0, WS_KV = 2u << 20, WS_H = 4u << 20, WS_O = 12u << 20, WS_FL = 16u << 20;
constexpr int FL_H = 256;
constexpr int LDS_K2 = 81920, LDS_K1 = 65792;
constexpr unsigned SPIN_CAP = 1u << 20;
#define NOFFN 0
#define P_LOOP 0
#define P_LOOP2 0
#define P_ATT 1
#define TOG 0
#define TLBW 0
#define PRIO(n) __builtin_amdgcn_s_setprio(n)
#define TS(blk, i)

DI unsigned pk(float a, float b) { f32x2 v = {a, b}; return __builtin_bit_cast(unsigned, __builtin_convertvector(v, b16x2)); }
DI bf16x8 pack8(const f32x16& x, int o) { u32x4 r = {pk(x[o], x[o + 1]), pk(x[o + 2], x[o + 3]), pk(x[o + 4], x[o + 5]), pk(x[o + 6], x[o + 7])}; return __builtin_bit_cast(bf16x8, r); }
DI float blo(unsigned u) { return __uint_as_float(u << 16); }
DI float bhi(unsigned u) { return __uint_as_float(u & 0xffff0000u); }
DI bf16x8 lr128(lptr p) { return *(const LAS bf16x8*)p; }
DI f32x4 lr128f(lptr p) { return *(const LAS f32x4*)p; }
DI void lw128f(lptr p, float a, float b, float c, float d) { f32x4 v = {a, b, c, d}; *(LAS f32x4*)p = v; }
DI bf16x8 ltr(lptr p, int o0, int o1) {
    s16x4 a = __builtin_amdgcn_ds_read_tr16_b64_v4i16((LAS s16x4*)(p + o0)), b = __builtin_amdgcn_ds_read_tr16_b64_v4i16((LAS s16x4*)(p + o1));
    return __builtin_shufflevector(a, b, 0, 1, 2, 3, 4, 5, 6, 7); }
DI void glds16(unsigned voff, const void* sbase, unsigned lds) {
    unsigned sv; asm volatile("s_mov_b32 %0, m0\n\ts_mov_b32 m0, %3\n\ts_nop 0\n\tglobal_load_lds_dwordx4 %1, %2\n\ts_mov_b32 m0, %0" : "=&s"(sv) : "v"(voff), "s"(sbase), "s"(lds) : "memory"); }
DI const char* uni(const char* p) { const unsigned long long a = (unsigned long long)p; return (const char*)(((unsigned long long)(unsigned)__builtin_amdgcn_readfirstlane((unsigned)(a >> 32)) << 32) | (unsigned)__builtin_amdgcn_readfirstlane((unsigned)a)); }
#define BAR(N) asm volatile("s_waitcnt vmcnt(" #N ") lgkmcnt(0)\n\ts_barrier" ::: "memory")
#define BARL() asm volatile("s_waitcnt lgkmcnt(0)\n\ts_barrier" ::: "memory")
DI void st_sc1(unsigned voff, void* sbase, bf16x8 v) { asm volatile("global_store_dwordx4 %0, %1, %2 sc1" ::"v"(voff), "v"(v), "s"(sbase) : "memory"); }
#define LD_SC1(dst, voff, sbase) asm volatile("global_load_dwordx4 %0, %1, %2 sc1" : "=v"(dst) : "v"(voff), "s"(sbase) : "memory")
#define touch(voff, sbase) asm volatile("global_load_dword %0, %1, %2" : "+v"(jk) : "v"(voff), "s"(sbase) : "memory")
#define TOUCH_END() asm volatile("s_waitcnt vmcnt(0)" : "+v"(jk)::"memory")
template <int C> DI float dpp(float x) { return __builtin_bit_cast(float, __builtin_amdgcn_mov_dpp(__builtin_bit_cast(int, x), C, 0xf, 0xf, true)); }
DI float sum32(float x) {
    x += dpp<0xB1>(x); x += dpp<0x4E>(x); x += dpp<0x141>(x); x += dpp<0x140>(x);
    auto s = __builtin_amdgcn_permlane16_swap(__float_as_uint(x), __float_as_uint(x), false, false);
    return __uint_as_float(s[0]) + __uint_as_float(s[1]); }
DI float xhalf_max(float m) { auto r = __builtin_amdgcn_permlane32_swap(__float_as_uint(m), __float_as_uint(m), false, false); return fmaxf(__uint_as_float(r[0]), __uint_as_float(r[1])); }
DI float max16(const f32x16& s, float m) {
#pragma unroll
    for (int i = 0; i < 16; i += 2) m = fmaxf(fmaxf(m, s[i]), s[i + 1]);
    return m; }
DI f32x16 splat16(float v) { f32x16 r;
#pragma unroll
    for (int i = 0; i < 16; ++i) r[i] = v;
    return r; }

__global__ __launch_bounds__(512) void k1_kernel(const char* x, const char* wk, const char* wv, const char* wq, const char* wg, const char* wu, const char* wd, const char* wo, const char* out, char* ws) {
    extern __shared__ char smem[];
    const lptr L = (lptr)smem; const unsigned L0 = (unsigned)(size_t)L;
    const int b = blockIdx.x, tid = threadIdx.x, l = tid & 63, w = __builtin_amdgcn_readfirstlane(tid >> 6), r = l & 31, h = l >> 5;
    TS(512 + b, 0);
    if (b >= 128) {
        const int j = (b - 128) >> 3, fq = (b & 7) >> 1;
        const int n = tid & 255, row = n / 7; unsigned jk = 0;
        if (w < 4) touch((j * 256 + n) * 128, wq);
        if (n < 224) touch((j * 32 + row) * 3584 + fq * 896 + (n - row * 7) * 128, w < 4 ? wg : wu);
        if (TLBW && w == 7) {
            if (l < 7) touch((j * 7 + l) << 16, ws + WS_H);
            if (l < 2) { touch((j * 2 + l) << 16, ws + WS_O); touch((j * 2 + l) << 16, out); }
            if (l < 2 && j < 14) touch((j * 2 + l) << 16, wd);
            if (l == 0 && j < 8) touch(j << 16, wo); }
        TOUCH_END(); TS(512 + b, 2); return; }
    constexpr int WK = 0, WV = 16384 + 128, XL = 33024, RED = 49408;
    const int tok0 = 256 * (b & 7) + 16 * (b >> 3), tl = 2 * w + h;
    const char* xr = x + (size_t)(tok0 + tl) * 1024 + r * 16;
    const u32x4 xa = *(const u32x4*)xr, xb = *(const u32x4*)(xr + 512);
    glds16(l * 16, wk + w * 2048, L0 + WK + w * 2048); glds16(l * 16, wk + w * 2048 + 1024, L0 + WK + w * 2048 + 1024);
    glds16(l * 16, wv + w * 2048, L0 + WV + w * 2048); glds16(l * 16, wv + w * 2048 + 1024, L0 + WV + w * 2048 + 1024);
    if (tid < 3) { const int f = b * 3 + tid; if (f < FL_H + 8) *(unsigned*)(ws + WS_FL + f * 128) = 0u; }
    float v[16];
#pragma unroll
    for (int i = 0; i < 4; ++i) { v[2 * i] = blo(xa[i]); v[2 * i + 1] = bhi(xa[i]); v[8 + 2 * i] = blo(xb[i]); v[9 + 2 * i] = bhi(xb[i]); }
    float s = 0.f;
#pragma unroll
    for (int i = 0; i < 16; ++i) s += v[i];
    const float mean = sum32(s) * (1.f / 512.f); float q2 = 0.f;
#pragma unroll
    for (int i = 0; i < 16; ++i) { v[i] -= mean; q2 += v[i] * v[i]; }
    const float rstd = __builtin_amdgcn_rsqf(sum32(q2) * (1.f / 512.f) + 1e-5f);
    u32x4 ya, yb;
#pragma unroll
    for (int i = 0; i < 4; ++i) { ya[i] = pk(v[2 * i] * rstd, v[2 * i + 1] * rstd); yb[i] = pk(v[8 + 2 * i] * rstd, v[9 + 2 * i] * rstd); }
    { const lptr p = L + XL + r * 256 + ((tl ^ (r & 15)) << 4); *(LAS u32x4*)p = ya; *(LAS u32x4*)(p + 8192) = yb; }
    BAR(0); TS(512 + b, 1);
    { const int c = tid >> 4, tk = tid & 15; const lptr p = L + XL + c * 256 + ((tk ^ (c & 15)) << 4);
      char* g = ws + WS_XN + (tok0 >> 5) * 32768 + c * 512 + ((tok0 & 16) + tk) * 16;
      *(u32x4*)g = *(LAS u32x4*)p; *(u32x4*)(g + 16384) = *(LAS u32x4*)(p + 8192); }
    f32x16 aK = splat16(0.f), aV = splat16(0.f);
    const lptr wp = L + ((l & 16) ? WV : WK) + (64 * w + 8 * h + ((l >> 2) & 3)) * 32 + (l & 3) * 8;
#pragma unroll
    for (int s4 = 0; s4 < 4; ++s4) { const int c = 8 * w + 2 * s4 + h;
        const bf16x8 xf = lr128(L + XL + c * 256 + (((l ^ c) & 15) << 4)), wf = ltr(wp, s4 * 512, s4 * 512 + 128);
        aK = MFMA(wf, xf, aK); aV = MFMA(xf, wf, aV); }
    { const bool vv = l & 16; const lptr p = L + RED + w * 2048 + l * 16;
      lw128f(p, vv ? aV[0] : aK[0], vv ? aV[1] : aK[1], vv ? aV[2] : aK[2], vv ? aV[3] : aK[3]);
      lw128f(p + 1024, vv ? aV[4] : aK[4], vv ? aV[5] : aK[5], vv ? aV[6] : aK[6], vv ? aV[7] : aK[7]); }
    BARL();
    if (w == 0) { f32x4 a = lr128f(L + RED + l * 16), c = lr128f(L + RED + 1024 + l * 16);
#pragma unroll
        for (int i = 1; i < 8; ++i) { a += lr128f(L + RED + i * 2048 + l * 16); c += lr128f(L + RED + i * 2048 + 1024 + l * 16); }
        const u32x4 o = {pk(a[0], a[1]), pk(a[2], a[3]), pk(c[0], c[1]), pk(c[2], c[3])}; const int sh = (tok0 >> 4) & 1;
        const int off = (l & 16) ? 1024 + sh * 512 + h * 256 + (l & 15) * 16 : (16 * sh + (l & 15) + 32 * h) * 16;
        *(u32x4*)(ws + WS_KV + (tok0 >> 5) * 2048 + off) = o; }
    TS(512 + b, 2);
}

DI void attn_role(int b, const lptr L, unsigned L0, const char* wq, char* ws) {
    const int tid = threadIdx.x, l = tid & 63, w = __builtin_amdgcn_readfirstlane(tid >> 6), h = l >> 5, t = w & 1, kq = w >> 1;
    const int i5 = b >> 3, tg = (b & 7) * 4 + (i5 & 3), hg = i5 >> 2, TT = 2 * tg + t;
    constexpr int SL = 8320, TS_ = 2080;
    const unsigned l16 = l * 16;
    PRIO(3); TS(b, 0);
    bf16x8 xf[8];
    { const char* xp = ws + WS_XN + (TT * 32 + 8 * kq) * 1024 + l16;
#pragma unroll
      for (int s = 0; s < 8; ++s) xf[s] = *(const bf16x8*)(xp + s * 1024); }
    { const unsigned vo = (l >> 3) * 1024 + (((l & 7) ^ (((l >> 4) & 1) << 2)) << 4);
#pragma unroll
      for (int n = 0; n < 8; ++n) glds16(vo, wq + hg * 128 + (w * 8 + n) * 8192, L0 + (w * 8 + n) * 1024); }
    const char* kvs = ws + WS_KV + w * 1024; const unsigned kvd = L0 + (w >> 1) * TS_ + (w & 1) * 1024;
    glds16(l16, kvs, kvd + 8 * SL);
    BAR(1); TS(b, 1);
    bf16x8 qf;
    { const int q = (l >> 2) & 3, p = l & 3, ch = ((l >> 4) & 1) * 2 + (p >> 1);
      const int a0 = kq * 16384 + (8 * h + q) * 128 + ((ch ^ ((q >> 1) << 2)) << 4) + 8 * (p & 1);
      f32x16 c0 = splat16(0.f), c1 = splat16(0.f);
#pragma unroll
      for (int s = 0; s < 8; ++s) { c0 = MFMA(ltr(L + a0, s * 2048, s * 2048 + 512), xf[s], c0); c1 = MFMA(ltr(L + (a0 ^ 64), s * 2048, s * 2048 + 512), xf[s], c1); }
      BARL();
      glds16(l16, kvs + 8192, kvd); glds16(l16, kvs + 2 * 8192, kvd + SL);
      if (tid < 24) { const unsigned v = (tid & 1) ? 0u : 0x3F803F80u; const u32x4 one = {v, v, v, v}; *(LAS u32x4*)(L + ((tid >> 1) + (tid < 16 ? 0 : 24)) * TS_ + 2048 + (tid & 1) * 16) = one; }
      f32x4 a, c;
#pragma unroll
      for (int j = 0; j < 4; ++j) { const f32x16& s = j < 2 ? c0 : c1; const int o = 8 * (j & 1);
          if (j == kq) { a = {s[o], s[o + 1], s[o + 2], s[o + 3]}; c = {s[o + 4], s[o + 5], s[o + 6], s[o + 7]}; }
          else { const lptr pw = L + 2 * SL + ((t * 4 + j) * 3 + ((kq - j - 1) & 3)) * 2048 + l16; lw128f(pw, s[o], s[o + 1], s[o + 2], s[o + 3]); lw128f(pw + 1024, s[o + 4], s[o + 5], s[o + 6], s[o + 7]); } }
      BAR(2);
      const lptr pr = L + 2 * SL + (t * 4 + kq) * 6144 + l16;
#pragma unroll
      for (int u = 0; u < 3; ++u) { a += lr128f(pr + u * 2048); c += lr128f(pr + u * 2048 + 1024); }
      const float sc = 0.25f * 1.4426950408889634f; a *= sc; c *= sc;
      const u32x4 qq = {pk(a[0], a[1]), pk(a[2], a[3]), pk(c[0], c[1]), pk(c[2], c[3])}; qf = __builtin_bit_cast(bf16x8, qq); }
    f32x16 ns; const bf16x8 kf = lr128(L + 8 * SL + l16); bf16x8 k0 = lr128(L + 8 * SL + TS_ + l16), k1 = lr128(L + 8 * SL + 2 * TS_ + l16);
    { const f32x16 z = splat16(0.f); f32x16 s0 = MFMA(kf, qf, z), s1 = MFMA(k0, qf, z), s2 = MFMA(k1, qf, z), s3 = MFMA(lr128(L + 8 * SL + 3 * TS_ + l16), qf, z);
      asm volatile("" : "+v"(s0), "+v"(s1), "+v"(s2), "+v"(s3));
      const float m = fmaxf(max16(s1, max16(s0, -3.0e38f)), max16(s3, max16(s2, -3.0e38f)));
      ns = splat16(-(xhalf_max(m) + 90.f)); }
    f32x16 o = splat16(0.f);
    { const unsigned bk = L0 + l16, b0 = L0 + ((l & 16) ? 1024 + h * 256 + (l & 15) * 16 : (l & 11) ? 2064 : 2048), b1 = (l & 16) ? b0 + 512 : b0;
      f32x16 sA = MFMA(kf, qf, ns);
      bf16x8 v0 = lr128(L + 8 * SL + (b0 - L0)), v1 = lr128(L + 8 * SL + (b1 - L0)), vb = {0, 0, 0, 0, 0, 0, 0, 0}, pb = vb;
      unsigned aK = bk + 8 * SL, a0 = b0 + 8 * SL, a1 = b1 + 8 * SL, vo = l16 + 3 * 8192, so = 8 * SL, sd = 2 * SL, sn, c = 0, sv;
      BAR(1); TS(b, 2);
      if (tid < 48) { const unsigned v = (tid & 1) ? 0u : 0x3F803F80u; const u32x4 one = {v, v, v, v}; *(LAS u32x4*)(L + ((tid >> 1) + 8) * TS_ + 2048 + (tid & 1) * 16) = one; }
      PRIO(P_ATT);
      asm volatile(
        "s_mov_b32 %[sv], m0\ns_nop 7\ns_nop 7\n9:\ns_add_u32 %[sn], %[so], 8320\ns_cmp_eq_u32 %[so], 66560\ns_cselect_b32 %[sn], 0, %[sn]\n"
        "v_mfma_f32_32x32x16_bf16 v[48:63], v[100:103], v[96:99], v[80:95]\nds_read_b128 v[100:103], v124 offset:6240\nv_exp_f32 v32, v32\nv_exp_f32 v33, v33\nv_exp_f32 v34, v34\n"
        "v_exp_f32 v35, v35\nv_exp_f32 v36, v36\nv_exp_f32 v37, v37\nv_mfma_f32_32x32x16_bf16 v[64:79], v[24:27], v[120:123], v[64:79]\nds_read_b128 v[24:27], v126 offset:2080\n"
        "v_exp_f32 v38, v38\nv_exp_f32 v39, v39\nv_cvt_pk_bf16_f32 v116, v32, v33\nv_cvt_pk_bf16_f32 v117, v34, v35\nv_cvt_pk_bf16_f32 v118, v36, v37\n"
        "v_cvt_pk_bf16_f32 v119, v38, v39\nv_exp_f32 v40, v40\nv_exp_f32 v41, v41\nv_exp_f32 v42, v42\ns_waitcnt lgkmcnt(2)\n"
        "v_mfma_f32_32x32x16_bf16 v[64:79], v[108:111], v[116:119], v[64:79]\nds_read_b128 v[108:111], v125 offset:2080\nv_add_u32 v127, %[sn], %[bk]\nv_add_u32 v31, %[sn], %[b0]\n"
        "v_add_u32 v30, %[sn], %[b1]\nv_exp_f32 v43, v43\nv_exp_f32 v44, v44\nv_exp_f32 v45, v45\nv_exp_f32 v46, v46\nv_exp_f32 v47, v47\nv_cvt_pk_bf16_f32 v120, v40, v41\n"
        "v_cvt_pk_bf16_f32 v121, v42, v43\nv_cvt_pk_bf16_f32 v122, v44, v45\nv_cvt_pk_bf16_f32 v123, v46, v47\nv_mfma_f32_32x32x16_bf16 v[32:47], v[104:107], v[96:99], v[80:95]\n"
        "ds_read_b128 v[104:107], v127 offset:0\nv_exp_f32 v48, v48\nv_exp_f32 v49, v49\nv_exp_f32 v50, v50\nv_exp_f32 v51, v51\nv_exp_f32 v52, v52\nv_exp_f32 v53, v53\n"
        "v_mfma_f32_32x32x16_bf16 v[64:79], v[112:115], v[120:123], v[64:79]\nds_read_b128 v[112:115], v126 offset:4160\nv_exp_f32 v54, v54\nv_exp_f32 v55, v55\n"
        "v_cvt_pk_bf16_f32 v116, v48, v49\nv_cvt_pk_bf16_f32 v117, v50, v51\nv_cvt_pk_bf16_f32 v118, v52, v53\nv_cvt_pk_bf16_f32 v119, v54, v55\nv_exp_f32 v56, v56\n"
        "v_exp_f32 v57, v57\nv_exp_f32 v58, v58\ns_waitcnt lgkmcnt(2)\nv_mfma_f32_32x32x16_bf16 v[64:79], v[108:111], v[116:119], v[64:79]\n"
        "ds_read_b128 v[108:111], v125 offset:4160\nv_exp_f32 v59, v59\nv_exp_f32 v60, v60\nv_exp_f32 v61, v61\nv_exp_f32 v62, v62\nv_exp_f32 v63, v63\n"
        "v_cvt_pk_bf16_f32 v120, v56, v57\nv_cvt_pk_bf16_f32 v121, v58, v59\nv_cvt_pk_bf16_f32 v122, v60, v61\nv_cvt_pk_bf16_f32 v123, v62, v63\n"
        "v_mfma_f32_32x32x16_bf16 v[48:63], v[100:103], v[96:99], v[80:95]\nds_read_b128 v[100:103], v127 offset:2080\nv_exp_f32 v32, v32\nv_exp_f32 v33, v33\nv_exp_f32 v34, v34\n"
        "v_exp_f32 v35, v35\nv_exp_f32 v36, v36\nv_exp_f32 v37, v37\nv_mfma_f32_32x32x16_bf16 v[64:79], v[24:27], v[120:123], v[64:79]\nds_read_b128 v[24:27], v126 offset:6240\n"
        "v_exp_f32 v38, v38\nv_exp_f32 v39, v39\nv_cvt_pk_bf16_f32 v116, v32, v33\nv_cvt_pk_bf16_f32 v117, v34, v35\nv_cvt_pk_bf16_f32 v118, v36, v37\n"
        "v_cvt_pk_bf16_f32 v119, v38, v39\nv_exp_f32 v40, v40\nv_exp_f32 v41, v41\nv_exp_f32 v42, v42\ns_waitcnt lgkmcnt(2)\n"
        "v_mfma_f32_32x32x16_bf16 v[64:79], v[108:111], v[116:119], v[64:79]\nds_read_b128 v[108:111], v125 offset:6240\nv_exp_f32 v43, v43\nv_exp_f32 v44, v44\nv_exp_f32 v45, v45\n"
        "v_exp_f32 v46, v46\nv_exp_f32 v47, v47\nv_cvt_pk_bf16_f32 v120, v40, v41\nv_cvt_pk_bf16_f32 v121, v42, v43\nv_cvt_pk_bf16_f32 v122, v44, v45\n"
        "v_cvt_pk_bf16_f32 v123, v46, v47\nv_mfma_f32_32x32x16_bf16 v[32:47], v[104:107], v[96:99], v[80:95]\nds_read_b128 v[104:107], v127 offset:4160\nv_exp_f32 v48, v48\n"
        "v_exp_f32 v49, v49\nv_exp_f32 v50, v50\nv_exp_f32 v51, v51\nv_exp_f32 v52, v52\nv_exp_f32 v53, v53\nv_mfma_f32_32x32x16_bf16 v[64:79], v[112:115], v[120:123], v[64:79]\n"
        "ds_read_b128 v[112:115], v30 offset:0\nv_exp_f32 v54, v54\nv_exp_f32 v55, v55\nv_cvt_pk_bf16_f32 v116, v48, v49\nv_cvt_pk_bf16_f32 v117, v50, v51\n"
        "v_cvt_pk_bf16_f32 v118, v52, v53\nv_cvt_pk_bf16_f32 v119, v54, v55\nv_exp_f32 v56, v56\nv_exp_f32 v57, v57\nv_exp_f32 v58, v58\ns_waitcnt lgkmcnt(2)\n"
        "v_mfma_f32_32x32x16_bf16 v[64:79], v[108:111], v[116:119], v[64:79]\nds_read_b128 v[108:111], v31 offset:0\nv_exp_f32 v59, v59\nv_exp_f32 v60, v60\nv_exp_f32 v61, v61\n"
        "v_exp_f32 v62, v62\nv_exp_f32 v63, v63\nv_cvt_pk_bf16_f32 v120, v56, v57\nv_cvt_pk_bf16_f32 v121, v58, v59\nv_cvt_pk_bf16_f32 v122, v60, v61\n"
        "v_cvt_pk_bf16_f32 v123, v62, v63\ns_cmp_gt_u32 %[c], 7\ns_cbranch_scc1 12f\ns_waitcnt vmcnt(0) lgkmcnt(0)\ns_barrier\ns_cmp_eq_u32 %[c], 7\ns_cbranch_scc1 12f\n"
        "s_add_u32 m0, %[sd], %[kd]\ns_add_u32 %[sd], %[sd], 8320\nglobal_load_lds_dwordx4 %[vo], %[src]\ns_cmp_eq_u32 %[sd], 74880\ns_cselect_b32 %[sd], 0, %[sd]\n"
        "v_add_u32 %[vo], 0x2000, %[vo]\ns_cmp_eq_u32 %[c], 6\ns_cbranch_scc1 12f\ns_add_u32 m0, %[sd], %[kd]\ns_add_u32 %[sd], %[sd], 8320\nglobal_load_lds_dwordx4 %[vo], %[src]\n"
        "s_cmp_eq_u32 %[sd], 74880\ns_cselect_b32 %[sd], 0, %[sd]\nv_add_u32 %[vo], 0x2000, %[vo]\n12:\ns_add_u32 %[c], %[c], 1\ns_mov_b32 %[so], %[sn]\n"
        "s_add_u32 %[sn], %[so], 8320\ns_cmp_eq_u32 %[so], 66560\ns_cselect_b32 %[sn], 0, %[sn]\nv_mfma_f32_32x32x16_bf16 v[48:63], v[100:103], v[96:99], v[80:95]\n"
        "ds_read_b128 v[100:103], v127 offset:6240\nv_exp_f32 v32, v32\nv_exp_f32 v33, v33\nv_exp_f32 v34, v34\nv_exp_f32 v35, v35\nv_exp_f32 v36, v36\nv_exp_f32 v37, v37\n"
        "v_mfma_f32_32x32x16_bf16 v[64:79], v[24:27], v[120:123], v[64:79]\nds_read_b128 v[24:27], v30 offset:2080\nv_exp_f32 v38, v38\nv_exp_f32 v39, v39\n"
        "v_cvt_pk_bf16_f32 v116, v32, v33\nv_cvt_pk_bf16_f32 v117, v34, v35\nv_cvt_pk_bf16_f32 v118, v36, v37\nv_cvt_pk_bf16_f32 v119, v38, v39\nv_exp_f32 v40, v40\n"
        "v_exp_f32 v41, v41\nv_exp_f32 v42, v42\ns_waitcnt lgkmcnt(2)\nv_mfma_f32_32x32x16_bf16 v[64:79], v[108:111], v[116:119], v[64:79]\n"
        "ds_read_b128 v[108:111], v31 offset:2080\nv_add_u32 v124, %[sn], %[bk]\nv_add_u32 v125, %[sn], %[b0]\nv_add_u32 v126, %[sn], %[b1]\nv_exp_f32 v43, v43\nv_exp_f32 v44, v44\n"
        "v_exp_f32 v45, v45\nv_exp_f32 v46, v46\nv_exp_f32 v47, v47\nv_cvt_pk_bf16_f32 v120, v40, v41\nv_cvt_pk_bf16_f32 v121, v42, v43\nv_cvt_pk_bf16_f32 v122, v44, v45\n"
        "v_cvt_pk_bf16_f32 v123, v46, v47\nv_mfma_f32_32x32x16_bf16 v[32:47], v[104:107], v[96:99], v[80:95]\nds_read_b128 v[104:107], v124 offset:0\nv_exp_f32 v48, v48\n"
        "v_exp_f32 v49, v49\nv_exp_f32 v50, v50\nv_exp_f32 v51, v51\nv_exp_f32 v52, v52\nv_exp_f32 v53, v53\nv_mfma_f32_32x32x16_bf16 v[64:79], v[112:115], v[120:123], v[64:79]\n"
        "ds_read_b128 v[112:115], v30 offset:4160\nv_exp_f32 v54, v54\nv_exp_f32 v55, v55\nv_cvt_pk_bf16_f32 v116, v48, v49\nv_cvt_pk_bf16_f32 v117, v50, v51\n"
        "v_cvt_pk_bf16_f32 v118, v52, v53\nv_cvt_pk_bf16_f32 v119, v54, v55\nv_exp_f32 v56, v56\nv_exp_f32 v57, v57\nv_exp_f32 v58, v58\ns_waitcnt lgkmcnt(2)\n"
        "v_mfma_f32_32x32x16_bf16 v[64:79], v[108:111], v[116:119], v[64:79]\nds_read_b128 v[108:111], v31 offset:4160\nv_exp_f32 v59, v59\nv_exp_f32 v60, v60\nv_exp_f32 v61, v61\n"
        "v_exp_f32 v62, v62\nv_exp_f32 v63, v63\nv_cvt_pk_bf16_f32 v120, v56, v57\nv_cvt_pk_bf16_f32 v121, v58, v59\nv_cvt_pk_bf16_f32 v122, v60, v61\n"
        "v_cvt_pk_bf16_f32 v123, v62, v63\nv_mfma_f32_32x32x16_bf16 v[48:63], v[100:103], v[96:99], v[80:95]\nds_read_b128 v[100:103], v124 offset:2080\nv_exp_f32 v32, v32\n"
        "v_exp_f32 v33, v33\nv_exp_f32 v34, v34\nv_exp_f32 v35, v35\nv_exp_f32 v36, v36\nv_exp_f32 v37, v37\nv_mfma_f32_32x32x16_bf16 v[64:79], v[24:27], v[120:123], v[64:79]\n"
        "ds_read_b128 v[24:27], v30 offset:6240\nv_exp_f32 v38, v38\nv_exp_f32 v39, v39\nv_cvt_pk_bf16_f32 v116, v32, v33\nv_cvt_pk_bf16_f32 v117, v34, v35\n"
        "v_cvt_pk_bf16_f32 v118, v36, v37\nv_cvt_pk_bf16_f32 v119, v38, v39\nv_exp_f32 v40, v40\nv_exp_f32 v41, v41\nv_exp_f32 v42, v42\ns_waitcnt lgkmcnt(2)\n"
        "v_mfma_f32_32x32x16_bf16 v[64:79], v[108:111], v[116:119], v[64:79]\nds_read_b128 v[108:111], v31 offset:6240\nv_exp_f32 v43, v43\nv_exp_f32 v44, v44\nv_exp_f32 v45, v45\n"
        "v_exp_f32 v46, v46\nv_exp_f32 v47, v47\nv_cvt_pk_bf16_f32 v120, v40, v41\nv_cvt_pk_bf16_f32 v121, v42, v43\nv_cvt_pk_bf16_f32 v122, v44, v45\n"
        "v_cvt_pk_bf16_f32 v123, v46, v47\nv_mfma_f32_32x32x16_bf16 v[32:47], v[104:107], v[96:99], v[80:95]\nds_read_b128 v[104:107], v124 offset:4160\nv_exp_f32 v48, v48\n"
        "v_exp_f32 v49, v49\nv_exp_f32 v50, v50\nv_exp_f32 v51, v51\nv_exp_f32 v52, v52\nv_exp_f32 v53, v53\nv_mfma_f32_32x32x16_bf16 v[64:79], v[112:115], v[120:123], v[64:79]\n"
        "ds_read_b128 v[112:115], v126 offset:0\nv_exp_f32 v54, v54\nv_exp_f32 v55, v55\nv_cvt_pk_bf16_f32 v116, v48, v49\nv_cvt_pk_bf16_f32 v117, v50, v51\n"
        "v_cvt_pk_bf16_f32 v118, v52, v53\nv_cvt_pk_bf16_f32 v119, v54, v55\nv_exp_f32 v56, v56\nv_exp_f32 v57, v57\nv_exp_f32 v58, v58\ns_waitcnt lgkmcnt(2)\n"
        "v_mfma_f32_32x32x16_bf16 v[64:79], v[108:111], v[116:119], v[64:79]\nds_read_b128 v[108:111], v125 offset:0\nv_exp_f32 v59, v59\nv_exp_f32 v60, v60\nv_exp_f32 v61, v61\n"
        "v_exp_f32 v62, v62\nv_exp_f32 v63, v63\nv_cvt_pk_bf16_f32 v120, v56, v57\nv_cvt_pk_bf16_f32 v121, v58, v59\nv_cvt_pk_bf16_f32 v122, v60, v61\n"
        "v_cvt_pk_bf16_f32 v123, v62, v63\ns_cmp_gt_u32 %[c], 7\ns_cbranch_scc1 22f\ns_waitcnt vmcnt(0) lgkmcnt(0)\ns_barrier\ns_cmp_eq_u32 %[c], 7\ns_cbranch_scc1 22f\n"
        "s_add_u32 m0, %[sd], %[kd]\ns_add_u32 %[sd], %[sd], 8320\nglobal_load_lds_dwordx4 %[vo], %[src]\ns_cmp_eq_u32 %[sd], 74880\ns_cselect_b32 %[sd], 0, %[sd]\n"
        "v_add_u32 %[vo], 0x2000, %[vo]\ns_cmp_eq_u32 %[c], 6\ns_cbranch_scc1 22f\ns_add_u32 m0, %[sd], %[kd]\ns_add_u32 %[sd], %[sd], 8320\nglobal_load_lds_dwordx4 %[vo], %[src]\n"
        "s_cmp_eq_u32 %[sd], 74880\ns_cselect_b32 %[sd], 0, %[sd]\nv_add_u32 %[vo], 0x2000, %[vo]\n22:\ns_add_u32 %[c], %[c], 1\ns_mov_b32 %[so], %[sn]\ns_cmp_lt_u32 %[c], 16\n"
        "s_cbranch_scc1 9b\ns_waitcnt lgkmcnt(0)\nv_mfma_f32_32x32x16_bf16 v[64:79], v[24:27], v[120:123], v[64:79]\ns_mov_b32 m0, %[sv]\ns_nop 7\ns_nop 7\n"
        : "+{v[32:47]}"(sA), "+{v[64:79]}"(o), "+{v[100:103]}"(k0), "+{v[104:107]}"(k1), "+{v[108:111]}"(v0), "+{v[112:115]}"(v1), "+{v[24:27]}"(vb), "+{v[120:123]}"(pb),
          "+{v124}"(aK), "+{v125}"(a0), "+{v126}"(a1), [so] "+s"(so), [sd] "+s"(sd), [sn] "=&s"(sn), [c] "+s"(c), [sv] "=&s"(sv), [vo] "+v"(vo)
        : "{v[80:95]}"(ns), "{v[96:99]}"(qf), [bk] "v"(bk), [b0] "v"(b0), [b1] "v"(b1), [kd] "s"(kvd), [src] "s"(kvs)
        : "v48", "v49", "v50", "v51", "v52", "v53", "v54", "v55", "v56", "v57", "v58", "v59", "v60", "v61", "v62", "v63", "v116", "v117", "v118", "v119", "v127", "v30", "v31", "scc", "memory"
 );

      PRIO(3); }
    TS(b, 3);
    int m_; asm volatile("v_mbcnt_lo_u32_b32 %0, -1, 0\n\tv_mbcnt_hi_u32_b32 %0, -1, %0" : "=v"(m_));
    const unsigned m16 = m_ * 16;

    float bad = o[0];
#pragma unroll
    for (int i = 8; i < 16; ++i) bad = fmaxf(bad, fabsf(o[i]));
    if (__builtin_amdgcn_ballot_w64(!(bad < 1.0e33f)) != 0ull || __builtin_amdgcn_ballot_w64(!(o[0] > 0.f)) != 0ull) {
        const char* kv = ws + WS_KV; float m = -3.0e38f; const f32x16 z = splat16(0.f);
#pragma unroll 1
        for (int u = 0; u < 64; ++u) m = max16(MFMA(*(const bf16x8*)(kv + u * 2048 + m16), qf, z), m);
        ns = splat16(-xhalf_max(m)); o = z;
        const u32x4 one = {0x3F803F80u, 0x3F803F80u, 0x3F803F80u, 0x3F803F80u}; const int vo = 1024 + (m_ >> 5) * 256 + (m_ & 15) * 16;
#pragma unroll 1
        for (int u = 0; u < 64; ++u) { f32x16 s = MFMA(*(const bf16x8*)(kv + u * 2048 + m16), qf, ns);
            u32x4 v0 = *(const u32x4*)(kv + u * 2048 + vo), v1 = *(const u32x4*)(kv + u * 2048 + vo + 512);
            if (!(m_ & 16)) { v0 = one; v1 = one; }
#pragma unroll
            for (int i = 0; i < 16; ++i) s[i] = __builtin_amdgcn_exp2f(s[i]);
            o = MFMA(__builtin_bit_cast(bf16x8, v0), pack8(s, 0), o); o = MFMA(__builtin_bit_cast(bf16x8, v1), pack8(s, 8), o); } }
    const float inv = 1.f / o[0];
#pragma unroll
    for (int i = 8; i < 16; ++i) o[i] *= inv;
    st_sc1(m16, ws + WS_O + (TT * 32 + 4 * hg + kq) * 1024, pack8(o, 8));
    BAR(0);
    if (w == 0 && m_ == 0) __hip_atomic_store((GAS unsigned*)(ws + WS_FL + (tg * 8 + hg) * 128), 1u, RLX_AGENT);
    TS(b, 4);
}

DI void ffn_role(int b, const lptr L, unsigned L0, const char* x, const char* wo, const char* wg, const char* wu, const char* wd, char* out, char* ws) {
    const int tid = threadIdx.x, l = tid & 63, w = __builtin_amdgcn_readfirstlane(tid >> 6), h = l >> 5;
    const int xcd = b & 7, i5 = b >> 3, q = (l >> 2) & 3, p = l & 3; const unsigned l16 = l * 16;
    PRIO(2);
    TS(256 + b, 0);
    if (i5 < 28) {
        const int TB = (xcd & 1) * 4 + (i5 & 3), fb = (xcd >> 1) * 7 + (i5 >> 2), ft = w & 1, tk = w >> 1;
        const unsigned vw = (l >> 3) * 3584 + (((l & 7) ^ (((l >> 4) & 1) << 2)) << 4);
        const char* sw = ((w & 4) ? wu : wg) + (w & 3) * 8 * 3584 + fb * 128;
        const char* sx = ws + WS_XN + (TB * 8 + w) * 32768; const unsigned dw = L0 + w * 1024, dx = L0 + 8192 + w * 2048;
#define F1_ISSUE(st) do { glds16(vw, sw + (st) * 114688, dw + ((st) % 3) * 24576); glds16(l16, sx + (st) * 2048, dx + ((st) % 3) * 24576); glds16(l16, sx + (st) * 2048 + 1024, dx + ((st) % 3) * 24576 + 1024); } while (0)
        F1_ISSUE(0); F1_ISSUE(1); F1_ISSUE(2);
        unsigned jk = 0, sv; const unsigned tv = (i5 * 512 + tid) * 128;
        const unsigned pa = L0 + (((8 * h + q) * 128 + (((((l >> 4) & 1) * 2 + (p >> 1)) ^ ((q >> 1) << 2)) << 4) + 8 * (p & 1)) ^ (ft * 64)), pb = L0 + 8192 + tk * 4096 + l16;
        f32x16 g0, g1, u0, u1;
        PRIO(P_LOOP);
        asm volatile(
        "s_mov_b32 %[sv], m0\ns_mov_b64 s[40:41], %[sw]\ns_mov_b64 s[42:43], %[sx]\ns_waitcnt vmcnt(6)\ns_barrier\nds_read_b64_tr_b16 v[96:97], %[pa] offset:0\n"
        "ds_read_b64_tr_b16 v[98:99], %[pa] offset:512\nds_read_b128 v[104:107], %[pb] offset:0\nds_read_b64_tr_b16 v[100:101], %[pa] offset:4096\n"
        "ds_read_b64_tr_b16 v[102:103], %[pa] offset:4608\nds_read_b128 v[108:111], %[pb] offset:2048\ns_waitcnt lgkmcnt(0)\n"
        "v_mfma_f32_32x32x16_bf16 v[32:47], v[96:99], v[104:107], 0\nds_read_b64_tr_b16 v[112:113], %[pa] offset:2048\nds_read_b64_tr_b16 v[114:115], %[pa] offset:2560\n"
        "v_mfma_f32_32x32x16_bf16 v[48:63], v[96:99], v[108:111], 0\nds_read_b128 v[120:123], %[pb] offset:1024\nds_read_b64_tr_b16 v[116:117], %[pa] offset:6144\n"
        "v_mfma_f32_32x32x16_bf16 v[64:79], v[100:103], v[104:107], 0\nds_read_b64_tr_b16 v[118:119], %[pa] offset:6656\nds_read_b128 v[124:127], %[pb] offset:3072\n"
        "v_mfma_f32_32x32x16_bf16 v[80:95], v[100:103], v[108:111], 0\ns_waitcnt vmcnt(3) lgkmcnt(0)\ns_barrier\n"
        "v_mfma_f32_32x32x16_bf16 v[32:47], v[112:115], v[120:123], v[32:47]\ns_add_u32 m0, %[dw], 0\ns_add_u32 s40, s40, 0x1c000\ns_addc_u32 s41, s41, 0\n"
        "global_load_lds_dwordx4 %[vw], s[40:41]\nds_read_b64_tr_b16 v[96:97], %[pa] offset:24576\nds_read_b64_tr_b16 v[98:99], %[pa] offset:25088\n"
        "v_mfma_f32_32x32x16_bf16 v[48:63], v[112:115], v[124:127], v[48:63]\ns_add_u32 m0, %[dx], 0\ns_add_u32 s42, s42, 0x800\ns_addc_u32 s43, s43, 0\n"
        "global_load_lds_dwordx4 %[vx], s[42:43]\nds_read_b128 v[104:107], %[pb] offset:24576\nds_read_b64_tr_b16 v[100:101], %[pa] offset:28672\n"
        "v_mfma_f32_32x32x16_bf16 v[64:79], v[116:119], v[120:123], v[64:79]\nglobal_load_lds_dwordx4 %[vx], s[42:43] offset:1024\n"
        "ds_read_b64_tr_b16 v[102:103], %[pa] offset:29184\nds_read_b128 v[108:111], %[pb] offset:26624\nv_mfma_f32_32x32x16_bf16 v[80:95], v[116:119], v[124:127], v[80:95]\n"
        "s_waitcnt lgkmcnt(0)\nv_mfma_f32_32x32x16_bf16 v[32:47], v[96:99], v[104:107], v[32:47]\nds_read_b64_tr_b16 v[112:113], %[pa] offset:26624\n"
        "ds_read_b64_tr_b16 v[114:115], %[pa] offset:27136\nv_mfma_f32_32x32x16_bf16 v[48:63], v[96:99], v[108:111], v[48:63]\nds_read_b128 v[120:123], %[pb] offset:25600\n"
        "ds_read_b64_tr_b16 v[116:117], %[pa] offset:30720\nv_mfma_f32_32x32x16_bf16 v[64:79], v[100:103], v[104:107], v[64:79]\nds_read_b64_tr_b16 v[118:119], %[pa] offset:31232\n"
        "ds_read_b128 v[124:127], %[pb] offset:27648\nv_mfma_f32_32x32x16_bf16 v[80:95], v[100:103], v[108:111], v[80:95]\ns_waitcnt vmcnt(3) lgkmcnt(0)\ns_barrier\n"
        "v_mfma_f32_32x32x16_bf16 v[32:47], v[112:115], v[120:123], v[32:47]\ns_add_u32 m0, %[dw], 24576\ns_add_u32 s40, s40, 0x1c000\ns_addc_u32 s41, s41, 0\n"
        "global_load_lds_dwordx4 %[vw], s[40:41]\nds_read_b64_tr_b16 v[96:97], %[pa] offset:49152\nds_read_b64_tr_b16 v[98:99], %[pa] offset:49664\n"
        "v_mfma_f32_32x32x16_bf16 v[48:63], v[112:115], v[124:127], v[48:63]\ns_add_u32 m0, %[dx], 24576\ns_add_u32 s42, s42, 0x800\ns_addc_u32 s43, s43, 0\n"
        "global_load_lds_dwordx4 %[vx], s[42:43]\nds_read_b128 v[104:107], %[pb] offset:49152\nds_read_b64_tr_b16 v[100:101], %[pa] offset:53248\n"
        "v_mfma_f32_32x32x16_bf16 v[64:79], v[116:119], v[120:123], v[64:79]\nglobal_load_lds_dwordx4 %[vx], s[42:43] offset:1024\n"
        "ds_read_b64_tr_b16 v[102:103], %[pa] offset:53760\nds_read_b128 v[108:111], %[pb] offset:51200\nv_mfma_f32_32x32x16_bf16 v[80:95], v[116:119], v[124:127], v[80:95]\n"
        "s_waitcnt lgkmcnt(0)\nv_mfma_f32_32x32x16_bf16 v[32:47], v[96:99], v[104:107], v[32:47]\nds_read_b64_tr_b16 v[112:113], %[pa] offset:51200\n"
        "ds_read_b64_tr_b16 v[114:115], %[pa] offset:51712\nv_mfma_f32_32x32x16_bf16 v[48:63], v[96:99], v[108:111], v[48:63]\nds_read_b128 v[120:123], %[pb] offset:50176\n"
        "ds_read_b64_tr_b16 v[116:117], %[pa] offset:55296\nv_mfma_f32_32x32x16_bf16 v[64:79], v[100:103], v[104:107], v[64:79]\nds_read_b64_tr_b16 v[118:119], %[pa] offset:55808\n"
        "ds_read_b128 v[124:127], %[pb] offset:52224\nv_mfma_f32_32x32x16_bf16 v[80:95], v[100:103], v[108:111], v[80:95]\ns_waitcnt vmcnt(3) lgkmcnt(0)\ns_barrier\n"
        "v_mfma_f32_32x32x16_bf16 v[32:47], v[112:115], v[120:123], v[32:47]\ns_add_u32 m0, %[dw], 49152\ns_add_u32 s40, s40, 0x1c000\ns_addc_u32 s41, s41, 0\n"
        "global_load_lds_dwordx4 %[vw], s[40:41]\nds_read_b64_tr_b16 v[96:97], %[pa] offset:0\nds_read_b64_tr_b16 v[98:99], %[pa] offset:512\n"
        "v_mfma_f32_32x32x16_bf16 v[48:63], v[112:115], v[124:127], v[48:63]\ns_add_u32 m0, %[dx], 49152\ns_add_u32 s42, s42, 0x800\ns_addc_u32 s43, s43, 0\n"
        "global_load_lds_dwordx4 %[vx], s[42:43]\nds_read_b128 v[104:107], %[pb] offset:0\nds_read_b64_tr_b16 v[100:101], %[pa] offset:4096\n"
        "v_mfma_f32_32x32x16_bf16 v[64:79], v[116:119], v[120:123], v[64:79]\nglobal_load_lds_dwordx4 %[vx], s[42:43] offset:1024\nglobal_load_dword %[jk], %[tv], %[wd]\n"
        "ds_read_b64_tr_b16 v[102:103], %[pa] offset:4608\nds_read_b128 v[108:111], %[pb] offset:2048\nv_mfma_f32_32x32x16_bf16 v[80:95], v[116:119], v[124:127], v[80:95]\n"
        "s_waitcnt lgkmcnt(0)\nv_mfma_f32_32x32x16_bf16 v[32:47], v[96:99], v[104:107], v[32:47]\nds_read_b64_tr_b16 v[112:113], %[pa] offset:2048\n"
        "ds_read_b64_tr_b16 v[114:115], %[pa] offset:2560\nv_mfma_f32_32x32x16_bf16 v[48:63], v[96:99], v[108:111], v[48:63]\nds_read_b128 v[120:123], %[pb] offset:1024\n"
        "ds_read_b64_tr_b16 v[116:117], %[pa] offset:6144\nv_mfma_f32_32x32x16_bf16 v[64:79], v[100:103], v[104:107], v[64:79]\nds_read_b64_tr_b16 v[118:119], %[pa] offset:6656\n"
        "ds_read_b128 v[124:127], %[pb] offset:3072\nv_mfma_f32_32x32x16_bf16 v[80:95], v[100:103], v[108:111], v[80:95]\ns_waitcnt vmcnt(4) lgkmcnt(0)\ns_barrier\n"
        "v_mfma_f32_32x32x16_bf16 v[32:47], v[112:115], v[120:123], v[32:47]\ns_add_u32 m0, %[dw], 0\ns_add_u32 s40, s40, 0x1c000\ns_addc_u32 s41, s41, 0\n"
        "global_load_lds_dwordx4 %[vw], s[40:41]\nds_read_b64_tr_b16 v[96:97], %[pa] offset:24576\nds_read_b64_tr_b16 v[98:99], %[pa] offset:25088\n"
        "v_mfma_f32_32x32x16_bf16 v[48:63], v[112:115], v[124:127], v[48:63]\ns_add_u32 m0, %[dx], 0\ns_add_u32 s42, s42, 0x800\ns_addc_u32 s43, s43, 0\n"
        "global_load_lds_dwordx4 %[vx], s[42:43]\nds_read_b128 v[104:107], %[pb] offset:24576\nds_read_b64_tr_b16 v[100:101], %[pa] offset:28672\n"
        "v_mfma_f32_32x32x16_bf16 v[64:79], v[116:119], v[120:123], v[64:79]\nglobal_load_lds_dwordx4 %[vx], s[42:43] offset:1024\n"
        "ds_read_b64_tr_b16 v[102:103], %[pa] offset:29184\nds_read_b128 v[108:111], %[pb] offset:26624\nv_mfma_f32_32x32x16_bf16 v[80:95], v[116:119], v[124:127], v[80:95]\n"
        "s_waitcnt lgkmcnt(0)\nv_mfma_f32_32x32x16_bf16 v[32:47], v[96:99], v[104:107], v[32:47]\nds_read_b64_tr_b16 v[112:113], %[pa] offset:26624\n"
        "ds_read_b64_tr_b16 v[114:115], %[pa] offset:27136\nv_mfma_f32_32x32x16_bf16 v[48:63], v[96:99], v[108:111], v[48:63]\nds_read_b128 v[120:123], %[pb] offset:25600\n"
        "ds_read_b64_tr_b16 v[116:117], %[pa] offset:30720\nv_mfma_f32_32x32x16_bf16 v[64:79], v[100:103], v[104:107], v[64:79]\nds_read_b64_tr_b16 v[118:119], %[pa] offset:31232\n"
        "ds_read_b128 v[124:127], %[pb] offset:27648\nv_mfma_f32_32x32x16_bf16 v[80:95], v[100:103], v[108:111], v[80:95]\ns_waitcnt vmcnt(4) lgkmcnt(0)\ns_barrier\n"
        "v_mfma_f32_32x32x16_bf16 v[32:47], v[112:115], v[120:123], v[32:47]\ns_add_u32 m0, %[dw], 24576\ns_add_u32 s40, s40, 0x1c000\ns_addc_u32 s41, s41, 0\n"
        "global_load_lds_dwordx4 %[vw], s[40:41]\nds_read_b64_tr_b16 v[96:97], %[pa] offset:49152\nds_read_b64_tr_b16 v[98:99], %[pa] offset:49664\n"
        "v_mfma_f32_32x32x16_bf16 v[48:63], v[112:115], v[124:127], v[48:63]\ns_add_u32 m0, %[dx], 24576\ns_add_u32 s42, s42, 0x800\ns_addc_u32 s43, s43, 0\n"
        "global_load_lds_dwordx4 %[vx], s[42:43]\nds_read_b128 v[104:107], %[pb] offset:49152\nds_read_b64_tr_b16 v[100:101], %[pa] offset:53248\n"
        "v_mfma_f32_32x32x16_bf16 v[64:79], v[116:119], v[120:123], v[64:79]\nglobal_load_lds_dwordx4 %[vx], s[42:43] offset:1024\n"
        "ds_read_b64_tr_b16 v[102:103], %[pa] offset:53760\nds_read_b128 v[108:111], %[pb] offset:51200\nv_mfma_f32_32x32x16_bf16 v[80:95], v[116:119], v[124:127], v[80:95]\n"
        "s_waitcnt lgkmcnt(0)\nv_mfma_f32_32x32x16_bf16 v[32:47], v[96:99], v[104:107], v[32:47]\nds_read_b64_tr_b16 v[112:113], %[pa] offset:51200\n"
        "ds_read_b64_tr_b16 v[114:115], %[pa] offset:51712\nv_mfma_f32_32x32x16_bf16 v[48:63], v[96:99], v[108:111], v[48:63]\nds_read_b128 v[120:123], %[pb] offset:50176\n"
        "ds_read_b64_tr_b16 v[116:117], %[pa] offset:55296\nv_mfma_f32_32x32x16_bf16 v[64:79], v[100:103], v[104:107], v[64:79]\nds_read_b64_tr_b16 v[118:119], %[pa] offset:55808\n"
        "ds_read_b128 v[124:127], %[pb] offset:52224\nv_mfma_f32_32x32x16_bf16 v[80:95], v[100:103], v[108:111], v[80:95]\ns_waitcnt vmcnt(3) lgkmcnt(0)\ns_barrier\n"
        "v_mfma_f32_32x32x16_bf16 v[32:47], v[112:115], v[120:123], v[32:47]\ns_add_u32 m0, %[dw], 49152\ns_add_u32 s40, s40, 0x1c000\ns_addc_u32 s41, s41, 0\n"
        "global_load_lds_dwordx4 %[vw], s[40:41]\nds_read_b64_tr_b16 v[96:97], %[pa] offset:0\nds_read_b64_tr_b16 v[98:99], %[pa] offset:512\n"
        "v_mfma_f32_32x32x16_bf16 v[48:63], v[112:115], v[124:127], v[48:63]\ns_add_u32 m0, %[dx], 49152\ns_add_u32 s42, s42, 0x800\ns_addc_u32 s43, s43, 0\n"
        "global_load_lds_dwordx4 %[vx], s[42:43]\nds_read_b128 v[104:107], %[pb] offset:0\nds_read_b64_tr_b16 v[100:101], %[pa] offset:4096\n"
        "v_mfma_f32_32x32x16_bf16 v[64:79], v[116:119], v[120:123], v[64:79]\nglobal_load_lds_dwordx4 %[vx], s[42:43] offset:1024\nds_read_b64_tr_b16 v[102:103], %[pa] offset:4608\n"
        "ds_read_b128 v[108:111], %[pb] offset:2048\nv_mfma_f32_32x32x16_bf16 v[80:95], v[116:119], v[124:127], v[80:95]\ns_waitcnt lgkmcnt(0)\n"
        "v_mfma_f32_32x32x16_bf16 v[32:47], v[96:99], v[104:107], v[32:47]\nds_read_b64_tr_b16 v[112:113], %[pa] offset:2048\nds_read_b64_tr_b16 v[114:115], %[pa] offset:2560\n"
        "v_mfma_f32_32x32x16_bf16 v[48:63], v[96:99], v[108:111], v[48:63]\nds_read_b128 v[120:123], %[pb] offset:1024\nds_read_b64_tr_b16 v[116:117], %[pa] offset:6144\n"
        "v_mfma_f32_32x32x16_bf16 v[64:79], v[100:103], v[104:107], v[64:79]\nds_read_b64_tr_b16 v[118:119], %[pa] offset:6656\nds_read_b128 v[124:127], %[pb] offset:3072\n"
        "v_mfma_f32_32x32x16_bf16 v[80:95], v[100:103], v[108:111], v[80:95]\ns_waitcnt vmcnt(3) lgkmcnt(0)\ns_barrier\n"
        "v_mfma_f32_32x32x16_bf16 v[32:47], v[112:115], v[120:123], v[32:47]\ns_add_u32 m0, %[dw], 0\ns_add_u32 s40, s40, 0x1c000\ns_addc_u32 s41, s41, 0\n"
        "global_load_lds_dwordx4 %[vw], s[40:41]\nds_read_b64_tr_b16 v[96:97], %[pa] offset:24576\nds_read_b64_tr_b16 v[98:99], %[pa] offset:25088\n"
        "v_mfma_f32_32x32x16_bf16 v[48:63], v[112:115], v[124:127], v[48:63]\ns_add_u32 m0, %[dx], 0\ns_add_u32 s42, s42, 0x800\ns_addc_u32 s43, s43, 0\n"
        "global_load_lds_dwordx4 %[vx], s[42:43]\nds_read_b128 v[104:107], %[pb] offset:24576\nds_read_b64_tr_b16 v[100:101], %[pa] offset:28672\n"
        "v_mfma_f32_32x32x16_bf16 v[64:79], v[116:119], v[120:123], v[64:79]\nglobal_load_lds_dwordx4 %[vx], s[42:43] offset:1024\n"
        "ds_read_b64_tr_b16 v[102:103], %[pa] offset:29184\nds_read_b128 v[108:111], %[pb] offset:26624\nv_mfma_f32_32x32x16_bf16 v[80:95], v[116:119], v[124:127], v[80:95]\n"
        "s_waitcnt lgkmcnt(0)\nv_mfma_f32_32x32x16_bf16 v[32:47], v[96:99], v[104:107], v[32:47]\nds_read_b64_tr_b16 v[112:113], %[pa] offset:26624\n"
        "ds_read_b64_tr_b16 v[114:115], %[pa] offset:27136\nv_mfma_f32_32x32x16_bf16 v[48:63], v[96:99], v[108:111], v[48:63]\nds_read_b128 v[120:123], %[pb] offset:25600\n"
        "ds_read_b64_tr_b16 v[116:117], %[pa] offset:30720\nv_mfma_f32_32x32x16_bf16 v[64:79], v[100:103], v[104:107], v[64:79]\nds_read_b64_tr_b16 v[118:119], %[pa] offset:31232\n"
        "ds_read_b128 v[124:127], %[pb] offset:27648\nv_mfma_f32_32x32x16_bf16 v[80:95], v[100:103], v[108:111], v[80:95]\ns_waitcnt vmcnt(3) lgkmcnt(0)\ns_barrier\n"
        "v_mfma_f32_32x32x16_bf16 v[32:47], v[112:115], v[120:123], v[32:47]\ns_add_u32 m0, %[dw], 24576\ns_add_u32 s40, s40, 0x1c000\ns_addc_u32 s41, s41, 0\n"
        "global_load_lds_dwordx4 %[vw], s[40:41]\nds_read_b64_tr_b16 v[96:97], %[pa] offset:49152\nds_read_b64_tr_b16 v[98:99], %[pa] offset:49664\n"
        "v_mfma_f32_32x32x16_bf16 v[48:63], v[112:115], v[124:127], v[48:63]\ns_add_u32 m0, %[dx], 24576\ns_add_u32 s42, s42, 0x800\ns_addc_u32 s43, s43, 0\n"
        "global_load_lds_dwordx4 %[vx], s[42:43]\nds_read_b128 v[104:107], %[pb] offset:49152\nds_read_b64_tr_b16 v[100:101], %[pa] offset:53248\n"
        "v_mfma_f32_32x32x16_bf16 v[64:79], v[116:119], v[120:123], v[64:79]\nglobal_load_lds_dwordx4 %[vx], s[42:43] offset:1024\n"
        "ds_read_b64_tr_b16 v[102:103], %[pa] offset:53760\nds_read_b128 v[108:111], %[pb] offset:51200\nv_mfma_f32_32x32x16_bf16 v[80:95], v[116:119], v[124:127], v[80:95]\n"
        "s_waitcnt lgkmcnt(0)\nv_mfma_f32_32x32x16_bf16 v[32:47], v[96:99], v[104:107], v[32:47]\nds_read_b64_tr_b16 v[112:113], %[pa] offset:51200\n"
        "ds_read_b64_tr_b16 v[114:115], %[pa] offset:51712\nv_mfma_f32_32x32x16_bf16 v[48:63], v[96:99], v[108:111], v[48:63]\nds_read_b128 v[120:123], %[pb] offset:50176\n"
        "ds_read_b64_tr_b16 v[116:117], %[pa] offset:55296\nv_mfma_f32_32x32x16_bf16 v[64:79], v[100:103], v[104:107], v[64:79]\nds_read_b64_tr_b16 v[118:119], %[pa] offset:55808\n"
        "ds_read_b128 v[124:127], %[pb] offset:52224\nv_mfma_f32_32x32x16_bf16 v[80:95], v[100:103], v[108:111], v[80:95]\ns_waitcnt vmcnt(3) lgkmcnt(0)\ns_barrier\n"
        "v_mfma_f32_32x32x16_bf16 v[32:47], v[112:115], v[120:123], v[32:47]\ns_add_u32 m0, %[dw], 49152\ns_add_u32 s40, s40, 0x1c000\ns_addc_u32 s41, s41, 0\n"
        "global_load_lds_dwordx4 %[vw], s[40:41]\nds_read_b64_tr_b16 v[96:97], %[pa] offset:0\nds_read_b64_tr_b16 v[98:99], %[pa] offset:512\n"
        "v_mfma_f32_32x32x16_bf16 v[48:63], v[112:115], v[124:127], v[48:63]\ns_add_u32 m0, %[dx], 49152\ns_add_u32 s42, s42, 0x800\ns_addc_u32 s43, s43, 0\n"
        "global_load_lds_dwordx4 %[vx], s[42:43]\nds_read_b128 v[104:107], %[pb] offset:0\nds_read_b64_tr_b16 v[100:101], %[pa] offset:4096\n"
        "v_mfma_f32_32x32x16_bf16 v[64:79], v[116:119], v[120:123], v[64:79]\nglobal_load_lds_dwordx4 %[vx], s[42:43] offset:1024\nds_read_b64_tr_b16 v[102:103], %[pa] offset:4608\n"
        "ds_read_b128 v[108:111], %[pb] offset:2048\nv_mfma_f32_32x32x16_bf16 v[80:95], v[116:119], v[124:127], v[80:95]\ns_waitcnt lgkmcnt(0)\n"
        "v_mfma_f32_32x32x16_bf16 v[32:47], v[96:99], v[104:107], v[32:47]\nds_read_b64_tr_b16 v[112:113], %[pa] offset:2048\nds_read_b64_tr_b16 v[114:115], %[pa] offset:2560\n"
        "v_mfma_f32_32x32x16_bf16 v[48:63], v[96:99], v[108:111], v[48:63]\nds_read_b128 v[120:123], %[pb] offset:1024\nds_read_b64_tr_b16 v[116:117], %[pa] offset:6144\n"
        "v_mfma_f32_32x32x16_bf16 v[64:79], v[100:103], v[104:107], v[64:79]\nds_read_b64_tr_b16 v[118:119], %[pa] offset:6656\nds_read_b128 v[124:127], %[pb] offset:3072\n"
        "v_mfma_f32_32x32x16_bf16 v[80:95], v[100:103], v[108:111], v[80:95]\ns_waitcnt vmcnt(3) lgkmcnt(0)\ns_barrier\n"
        "v_mfma_f32_32x32x16_bf16 v[32:47], v[112:115], v[120:123], v[32:47]\ns_add_u32 m0, %[dw], 0\ns_add_u32 s40, s40, 0x1c000\ns_addc_u32 s41, s41, 0\n"
        "global_load_lds_dwordx4 %[vw], s[40:41]\nds_read_b64_tr_b16 v[96:97], %[pa] offset:24576\nds_read_b64_tr_b16 v[98:99], %[pa] offset:25088\n"
        "v_mfma_f32_32x32x16_bf16 v[48:63], v[112:115], v[124:127], v[48:63]\ns_add_u32 m0, %[dx], 0\ns_add_u32 s42, s42, 0x800\ns_addc_u32 s43, s43, 0\n"
        "global_load_lds_dwordx4 %[vx], s[42:43]\nds_read_b128 v[104:107], %[pb] offset:24576\nds_read_b64_tr_b16 v[100:101], %[pa] offset:28672\n"
        "v_mfma_f32_32x32x16_bf16 v[64:79], v[116:119], v[120:123], v[64:79]\nglobal_load_lds_dwordx4 %[vx], s[42:43] offset:1024\n"
        "ds_read_b64_tr_b16 v[102:103], %[pa] offset:29184\nds_read_b128 v[108:111], %[pb] offset:26624\nv_mfma_f32_32x32x16_bf16 v[80:95], v[116:119], v[124:127], v[80:95]\n"
        "s_waitcnt lgkmcnt(0)\nv_mfma_f32_32x32x16_bf16 v[32:47], v[96:99], v[104:107], v[32:47]\nds_read_b64_tr_b16 v[112:113], %[pa] offset:26624\n"
        "ds_read_b64_tr_b16 v[114:115], %[pa] offset:27136\nv_mfma_f32_32x32x16_bf16 v[48:63], v[96:99], v[108:111], v[48:63]\nds_read_b128 v[120:123], %[pb] offset:25600\n"
        "ds_read_b64_tr_b16 v[116:117], %[pa] offset:30720\nv_mfma_f32_32x32x16_bf16 v[64:79], v[100:103], v[104:107], v[64:79]\nds_read_b64_tr_b16 v[118:119], %[pa] offset:31232\n"
        "ds_read_b128 v[124:127], %[pb] offset:27648\nv_mfma_f32_32x32x16_bf16 v[80:95], v[100:103], v[108:111], v[80:95]\ns_waitcnt vmcnt(3) lgkmcnt(0)\ns_barrier\n"
        "v_mfma_f32_32x32x16_bf16 v[32:47], v[112:115], v[120:123], v[32:47]\ns_add_u32 m0, %[dw], 24576\ns_add_u32 s40, s40, 0x1c000\ns_addc_u32 s41, s41, 0\n"
        "global_load_lds_dwordx4 %[vw], s[40:41]\nds_read_b64_tr_b16 v[96:97], %[pa] offset:49152\nds_read_b64_tr_b16 v[98:99], %[pa] offset:49664\n"
        "v_mfma_f32_32x32x16_bf16 v[48:63], v[112:115], v[124:127], v[48:63]\ns_add_u32 m0, %[dx], 24576\ns_add_u32 s42, s42, 0x800\ns_addc_u32 s43, s43, 0\n"
        "global_load_lds_dwordx4 %[vx], s[42:43]\nds_read_b128 v[104:107], %[pb] offset:49152\nds_read_b64_tr_b16 v[100:101], %[pa] offset:53248\n"
        "v_mfma_f32_32x32x16_bf16 v[64:79], v[116:119], v[120:123], v[64:79]\nglobal_load_lds_dwordx4 %[vx], s[42:43] offset:1024\n"
        "ds_read_b64_tr_b16 v[102:103], %[pa] offset:53760\nds_read_b128 v[108:111], %[pb] offset:51200\nv_mfma_f32_32x32x16_bf16 v[80:95], v[116:119], v[124:127], v[80:95]\n"
        "s_waitcnt lgkmcnt(0)\nv_mfma_f32_32x32x16_bf16 v[32:47], v[96:99], v[104:107], v[32:47]\nds_read_b64_tr_b16 v[112:113], %[pa] offset:51200\n"
        "ds_read_b64_tr_b16 v[114:115], %[pa] offset:51712\nv_mfma_f32_32x32x16_bf16 v[48:63], v[96:99], v[108:111], v[48:63]\nds_read_b128 v[120:123], %[pb] offset:50176\n"
        "ds_read_b64_tr_b16 v[116:117], %[pa] offset:55296\nv_mfma_f32_32x32x16_bf16 v[64:79], v[100:103], v[104:107], v[64:79]\nds_read_b64_tr_b16 v[118:119], %[pa] offset:55808\n"
        "ds_read_b128 v[124:127], %[pb] offset:52224\nv_mfma_f32_32x32x16_bf16 v[80:95], v[100:103], v[108:111], v[80:95]\ns_waitcnt vmcnt(3) lgkmcnt(0)\ns_barrier\n"
        "v_mfma_f32_32x32x16_bf16 v[32:47], v[112:115], v[120:123], v[32:47]\ns_add_u32 m0, %[dw], 49152\ns_add_u32 s40, s40, 0x1c000\ns_addc_u32 s41, s41, 0\n"
        "global_load_lds_dwordx4 %[vw], s[40:41]\nds_read_b64_tr_b16 v[96:97], %[pa] offset:0\nds_read_b64_tr_b16 v[98:99], %[pa] offset:512\n"
        "v_mfma_f32_32x32x16_bf16 v[48:63], v[112:115], v[124:127], v[48:63]\ns_add_u32 m0, %[dx], 49152\ns_add_u32 s42, s42, 0x800\ns_addc_u32 s43, s43, 0\n"
        "global_load_lds_dwordx4 %[vx], s[42:43]\nds_read_b128 v[104:107], %[pb] offset:0\nds_read_b64_tr_b16 v[100:101], %[pa] offset:4096\n"
        "v_mfma_f32_32x32x16_bf16 v[64:79], v[116:119], v[120:123], v[64:79]\nglobal_load_lds_dwordx4 %[vx], s[42:43] offset:1024\nds_read_b64_tr_b16 v[102:103], %[pa] offset:4608\n"
        "ds_read_b128 v[108:111], %[pb] offset:2048\nv_mfma_f32_32x32x16_bf16 v[80:95], v[116:119], v[124:127], v[80:95]\ns_waitcnt lgkmcnt(0)\n"
        "v_mfma_f32_32x32x16_bf16 v[32:47], v[96:99], v[104:107], v[32:47]\nds_read_b64_tr_b16 v[112:113], %[pa] offset:2048\nds_read_b64_tr_b16 v[114:115], %[pa] offset:2560\n"
        "v_mfma_f32_32x32x16_bf16 v[48:63], v[96:99], v[108:111], v[48:63]\nds_read_b128 v[120:123], %[pb] offset:1024\nds_read_b64_tr_b16 v[116:117], %[pa] offset:6144\n"
        "v_mfma_f32_32x32x16_bf16 v[64:79], v[100:103], v[104:107], v[64:79]\nds_read_b64_tr_b16 v[118:119], %[pa] offset:6656\nds_read_b128 v[124:127], %[pb] offset:3072\n"
        "v_mfma_f32_32x32x16_bf16 v[80:95], v[100:103], v[108:111], v[80:95]\ns_waitcnt vmcnt(3) lgkmcnt(0)\ns_barrier\n"
        "v_mfma_f32_32x32x16_bf16 v[32:47], v[112:115], v[120:123], v[32:47]\ns_add_u32 m0, %[dw], 0\ns_add_u32 s40, s40, 0x1c000\ns_addc_u32 s41, s41, 0\n"
        "global_load_lds_dwordx4 %[vw], s[40:41]\nds_read_b64_tr_b16 v[96:97], %[pa] offset:24576\nds_read_b64_tr_b16 v[98:99], %[pa] offset:25088\n"
        "v_mfma_f32_32x32x16_bf16 v[48:63], v[112:115], v[124:127], v[48:63]\ns_add_u32 m0, %[dx], 0\ns_add_u32 s42, s42, 0x800\ns_addc_u32 s43, s43, 0\n"
        "global_load_lds_dwordx4 %[vx], s[42:43]\nds_read_b128 v[104:107], %[pb] offset:24576\nds_read_b64_tr_b16 v[100:101], %[pa] offset:28672\n"
        "v_mfma_f32_32x32x16_bf16 v[64:79], v[116:119], v[120:123], v[64:79]\nglobal_load_lds_dwordx4 %[vx], s[42:43] offset:1024\n"
        "ds_read_b64_tr_b16 v[102:103], %[pa] offset:29184\nds_read_b128 v[108:111], %[pb] offset:26624\nv_mfma_f32_32x32x16_bf16 v[80:95], v[116:119], v[124:127], v[80:95]\n"
        "s_waitcnt lgkmcnt(0)\nv_mfma_f32_32x32x16_bf16 v[32:47], v[96:99], v[104:107], v[32:47]\nds_read_b64_tr_b16 v[112:113], %[pa] offset:26624\n"
        "ds_read_b64_tr_b16 v[114:115], %[pa] offset:27136\nv_mfma_f32_32x32x16_bf16 v[48:63], v[96:99], v[108:111], v[48:63]\nds_read_b128 v[120:123], %[pb] offset:25600\n"
        "ds_read_b64_tr_b16 v[116:117], %[pa] offset:30720\nv_mfma_f32_32x32x16_bf16 v[64:79], v[100:103], v[104:107], v[64:79]\nds_read_b64_tr_b16 v[118:119], %[pa] offset:31232\n"
        "ds_read_b128 v[124:127], %[pb] offset:27648\nv_mfma_f32_32x32x16_bf16 v[80:95], v[100:103], v[108:111], v[80:95]\ns_waitcnt vmcnt(3) lgkmcnt(0)\ns_barrier\n"
        "v_mfma_f32_32x32x16_bf16 v[32:47], v[112:115], v[120:123], v[32:47]\nds_read_b64_tr_b16 v[96:97], %[pa] offset:49152\nds_read_b64_tr_b16 v[98:99], %[pa] offset:49664\n"
        "v_mfma_f32_32x32x16_bf16 v[48:63], v[112:115], v[124:127], v[48:63]\nds_read_b128 v[104:107], %[pb] offset:49152\nds_read_b64_tr_b16 v[100:101], %[pa] offset:53248\n"
        "v_mfma_f32_32x32x16_bf16 v[64:79], v[116:119], v[120:123], v[64:79]\nds_read_b64_tr_b16 v[102:103], %[pa] offset:53760\nds_read_b128 v[108:111], %[pb] offset:51200\n"
        "v_mfma_f32_32x32x16_bf16 v[80:95], v[116:119], v[124:127], v[80:95]\ns_waitcnt lgkmcnt(0)\nv_mfma_f32_32x32x16_bf16 v[32:47], v[96:99], v[104:107], v[32:47]\n"
        "ds_read_b64_tr_b16 v[112:113], %[pa] offset:51200\nds_read_b64_tr_b16 v[114:115], %[pa] offset:51712\nv_mfma_f32_32x32x16_bf16 v[48:63], v[96:99], v[108:111], v[48:63]\n"
        "ds_read_b128 v[120:123], %[pb] offset:50176\nds_read_b64_tr_b16 v[116:117], %[pa] offset:55296\nv_mfma_f32_32x32x16_bf16 v[64:79], v[100:103], v[104:107], v[64:79]\n"
        "ds_read_b64_tr_b16 v[118:119], %[pa] offset:55808\nds_read_b128 v[124:127], %[pb] offset:52224\nv_mfma_f32_32x32x16_bf16 v[80:95], v[100:103], v[108:111], v[80:95]\n"
        "s_waitcnt vmcnt(0) lgkmcnt(0)\ns_barrier\nv_mfma_f32_32x32x16_bf16 v[32:47], v[112:115], v[120:123], v[32:47]\nds_read_b64_tr_b16 v[96:97], %[pa] offset:0\n"
        "ds_read_b64_tr_b16 v[98:99], %[pa] offset:512\nv_mfma_f32_32x32x16_bf16 v[48:63], v[112:115], v[124:127], v[48:63]\nds_read_b128 v[104:107], %[pb] offset:0\n"
        "ds_read_b64_tr_b16 v[100:101], %[pa] offset:4096\nv_mfma_f32_32x32x16_bf16 v[64:79], v[116:119], v[120:123], v[64:79]\nds_read_b64_tr_b16 v[102:103], %[pa] offset:4608\n"
        "ds_read_b128 v[108:111], %[pb] offset:2048\nv_mfma_f32_32x32x16_bf16 v[80:95], v[116:119], v[124:127], v[80:95]\ns_waitcnt lgkmcnt(0)\n"
        "v_mfma_f32_32x32x16_bf16 v[32:47], v[96:99], v[104:107], v[32:47]\nds_read_b64_tr_b16 v[112:113], %[pa] offset:2048\nds_read_b64_tr_b16 v[114:115], %[pa] offset:2560\n"
        "v_mfma_f32_32x32x16_bf16 v[48:63], v[96:99], v[108:111], v[48:63]\nds_read_b128 v[120:123], %[pb] offset:1024\nds_read_b64_tr_b16 v[116:117], %[pa] offset:6144\n"
        "v_mfma_f32_32x32x16_bf16 v[64:79], v[100:103], v[104:107], v[64:79]\nds_read_b64_tr_b16 v[118:119], %[pa] offset:6656\nds_read_b128 v[124:127], %[pb] offset:3072\n"
        "v_mfma_f32_32x32x16_bf16 v[80:95], v[100:103], v[108:111], v[80:95]\ns_waitcnt lgkmcnt(0)\nv_mfma_f32_32x32x16_bf16 v[32:47], v[112:115], v[120:123], v[32:47]\n"
        "v_mfma_f32_32x32x16_bf16 v[48:63], v[112:115], v[124:127], v[48:63]\nv_mfma_f32_32x32x16_bf16 v[64:79], v[116:119], v[120:123], v[64:79]\n"
        "v_mfma_f32_32x32x16_bf16 v[80:95], v[116:119], v[124:127], v[80:95]\ns_mov_b32 m0, %[sv]\ns_nop 7\ns_nop 7\n"
          : "=&{v[32:47]}"(g0), "=&{v[48:63]}"(g1), "=&{v[64:79]}"(u0), "=&{v[80:95]}"(u1), [jk] "+v"(jk), [sv] "=&s"(sv)
          : [pa] "v"(pa), [pb] "v"(pb), [vw] "v"(vw), [vx] "v"(l16), [tv] "v"(tv), [dw] "s"(dw), [dx] "s"(dx), [sw] "s"(sw + 2 * 114688), [sx] "s"(sx + 2 * 2048), [wd] "s"(wd)
          : "v96", "v97", "v98", "v99", "v100", "v101", "v102", "v103", "v104", "v105", "v106", "v107", "v108", "v109", "v110", "v111", "v112", "v113", "v114", "v115", "v116", "v117", "v118", "v119", "v120", "v121", "v122", "v123", "v124", "v125", "v126", "v127", "s40", "s41", "s42", "s43", "scc", "memory");
        PRIO(2);
#pragma unroll
        for (int i = 0; i < 16; ++i) { g0[i] = fmaxf(g0[i], 0.f) * u0[i]; g1[i] = fmaxf(g1[i], 0.f) * u1[i]; }
        TS(256 + b, 1);
        char* hp = ws + WS_H + ((TB * 8 + 2 * tk) * 112 + fb * 4 + ft * 2) * 1024;
        st_sc1(l16, hp, pack8(g0, 0)); st_sc1(l16, hp + 1024, pack8(g0, 8)); st_sc1(l16, hp + 114688, pack8(g1, 0)); st_sc1(l16, hp + 115712, pack8(g1, 8));
        BAR(0);
        if (tid == 0) __hip_atomic_fetch_add((GAS unsigned*)(ws + WS_FL + (FL_H + TB) * 128), 1u, RLX_AGENT);
    } else {
        const int j = i5 - 28; unsigned jk = 0;
        __builtin_amdgcn_s_sleep(127);
        touch((j * 1024 + tid) * 128, wo); TOUCH_END(); touch((j * 1024 + 512 + tid) * 128, wo); TOUCH_END();
    }
    TS(256 + b, 2);
    const int tg = xcd * 4 + (i5 & 3), cg = i5 >> 2, t = w & 1, kq = w >> 1;
    const unsigned vo = (l >> 3) * 1024 + (((l & 7) ^ (((l >> 4) & 1) << 2)) << 4);
    const int at = (4 * h + q) * 128 + (((((l >> 4) & 1) * 2 + (p >> 1)) ^ ((q >> 1) << 2)) << 4) + 8 * (p & 1);
    float gg[8];
    {   const char* sw = wd + cg * 128 + w * 14 * 16384; const unsigned dw = L0 + w * 8192;
        const int r0 = 2 * cg - 14;
#define ROT(n) ((n) + r0 < 0 ? (n) + r0 + 14 : (n) + r0)
#define F2_W(n) do { const char* s_ = sw + ROT(n) * 16384; glds16(vo, s_, dw + ((n) & 3) * 2048); glds16(vo, s_ + 8192, dw + ((n) & 3) * 2048 + 1024); } while (0)
        F2_W(0); F2_W(1); F2_W(2); F2_W(3);
        { GAS unsigned* f = (GAS unsigned*)(ws + WS_FL + (FL_H + xcd) * 128);
          for (unsigned sp = 0; sp < SPIN_CAP; ++sp) { if (__builtin_amdgcn_readfirstlane(__hip_atomic_load(f, RLX_AGENT)) >= 28u) break; __builtin_amdgcn_s_sleep(4); } }
        TS(256 + b, 3);
        const char* sh = uni(ws + WS_H + (2 * tg * 112 + w * 14) * 1024);
        f32x16 d00, d01, d10, d11; unsigned ro = ROT(0) * 1024, sv;
        PRIO(P_LOOP2);
        asm volatile(
        "s_mov_b32 %[sv], m0\ns_mov_b64 s[44:45], %[sh]\ns_mov_b64 s[46:47], %[sw]\ns_add_u32 s40, s44, %[ro]\ns_addc_u32 s41, s45, 0\n"
        "global_load_dwordx4 v[112:115], %[h0], s[40:41] sc1\nglobal_load_dwordx4 v[116:119], %[h1], s[40:41] sc1\ns_add_u32 %[ro], %[ro], 1024\ns_cmp_eq_u32 %[ro], 14336\n"
        "s_cselect_b32 %[ro], 0, %[ro]\ns_add_u32 s40, s44, %[ro]\ns_addc_u32 s41, s45, 0\nglobal_load_dwordx4 v[120:123], %[h0], s[40:41] sc1\n"
        "global_load_dwordx4 v[124:127], %[h1], s[40:41] sc1\ns_add_u32 %[ro], %[ro], 1024\ns_cmp_eq_u32 %[ro], 14336\ns_cselect_b32 %[ro], 0, %[ro]\ns_add_u32 s40, s44, %[ro]\n"
        "s_addc_u32 s41, s45, 0\nglobal_load_dwordx4 v[16:19], %[h0], s[40:41] sc1\nglobal_load_dwordx4 v[20:23], %[h1], s[40:41] sc1\ns_add_u32 %[ro], %[ro], 1024\n"
        "s_cmp_eq_u32 %[ro], 14336\ns_cselect_b32 %[ro], 0, %[ro]\ns_add_u32 s40, s44, %[ro]\ns_addc_u32 s41, s45, 0\nglobal_load_dwordx4 v[24:27], %[h0], s[40:41] sc1\n"
        "global_load_dwordx4 v[28:31], %[h1], s[40:41] sc1\ns_add_u32 %[ro], %[ro], 1024\ns_cmp_eq_u32 %[ro], 14336\ns_cselect_b32 %[ro], 0, %[ro]\ns_waitcnt vmcnt(8)\n"
        "ds_read_b64_tr_b16 v[96:97], %[a0] offset:0\nds_read_b64_tr_b16 v[98:99], %[a0] offset:1024\nds_read_b64_tr_b16 v[100:101], %[a1] offset:0\n"
        "ds_read_b64_tr_b16 v[102:103], %[a1] offset:1024\ns_waitcnt vmcnt(6) lgkmcnt(0)\nv_mfma_f32_32x32x16_bf16 v[32:47], v[104:107], v[112:115], 0\n"
        "ds_read_b64_tr_b16 v[104:105], %[a0] offset:2048\nds_read_b64_tr_b16 v[106:107], %[a0] offset:3072\ns_lshl_b32 s42, %[ro], 4\ns_add_u32 s42, s46, s42\n"
        "s_addc_u32 s43, s47, 0\ns_add_u32 m0, %[dw], 0\nv_mfma_f32_32x32x16_bf16 v[48:63], v[108:111], v[112:115], 0\nds_read_b64_tr_b16 v[108:109], %[a1] offset:2048\n"
        "ds_read_b64_tr_b16 v[110:111], %[a1] offset:3072\nglobal_load_lds_dwordx4 %[w0], s[42:43]\nv_mfma_f32_32x32x16_bf16 v[64:79], v[104:107], v[116:119], 0\n"
        "global_load_lds_dwordx4 %[w1], s[42:43] offset:1024\nv_mfma_f32_32x32x16_bf16 v[80:95], v[108:111], v[116:119], 0\ns_add_u32 s40, s44, %[ro]\ns_addc_u32 s41, s45, 0\n"
        "global_load_dwordx4 v[112:115], %[h0], s[40:41] sc1\nglobal_load_dwordx4 v[116:119], %[h1], s[40:41] sc1\ns_add_u32 %[ro], %[ro], 1024\ns_cmp_eq_u32 %[ro], 14336\n"
        "s_cselect_b32 %[ro], 0, %[ro]\ns_waitcnt vmcnt(8) lgkmcnt(0)\nv_mfma_f32_32x32x16_bf16 v[32:47], v[96:99], v[120:123], v[32:47]\n"
        "ds_read_b64_tr_b16 v[96:97], %[a0] offset:4096\nds_read_b64_tr_b16 v[98:99], %[a0] offset:5120\ns_lshl_b32 s42, %[ro], 4\ns_add_u32 s42, s46, s42\ns_addc_u32 s43, s47, 0\n"
        "s_add_u32 m0, %[dw], 2048\nv_mfma_f32_32x32x16_bf16 v[48:63], v[100:103], v[120:123], v[48:63]\nds_read_b64_tr_b16 v[100:101], %[a1] offset:4096\n"
        "ds_read_b64_tr_b16 v[102:103], %[a1] offset:5120\nglobal_load_lds_dwordx4 %[w0], s[42:43]\nv_mfma_f32_32x32x16_bf16 v[64:79], v[96:99], v[124:127], v[64:79]\n"
        "global_load_lds_dwordx4 %[w1], s[42:43] offset:1024\nv_mfma_f32_32x32x16_bf16 v[80:95], v[100:103], v[124:127], v[80:95]\ns_add_u32 s40, s44, %[ro]\n"
        "s_addc_u32 s41, s45, 0\nglobal_load_dwordx4 v[120:123], %[h0], s[40:41] sc1\nglobal_load_dwordx4 v[124:127], %[h1], s[40:41] sc1\ns_add_u32 %[ro], %[ro], 1024\n"
        "s_cmp_eq_u32 %[ro], 14336\ns_cselect_b32 %[ro], 0, %[ro]\ns_waitcnt vmcnt(10) lgkmcnt(0)\nv_mfma_f32_32x32x16_bf16 v[32:47], v[104:107], v[16:19], v[32:47]\n"
        "ds_read_b64_tr_b16 v[104:105], %[a0] offset:6144\nds_read_b64_tr_b16 v[106:107], %[a0] offset:7168\ns_lshl_b32 s42, %[ro], 4\ns_add_u32 s42, s46, s42\n"
        "s_addc_u32 s43, s47, 0\ns_add_u32 m0, %[dw], 4096\nv_mfma_f32_32x32x16_bf16 v[48:63], v[108:111], v[16:19], v[48:63]\nds_read_b64_tr_b16 v[108:109], %[a1] offset:6144\n"
        "ds_read_b64_tr_b16 v[110:111], %[a1] offset:7168\nglobal_load_lds_dwordx4 %[w0], s[42:43]\nv_mfma_f32_32x32x16_bf16 v[64:79], v[104:107], v[20:23], v[64:79]\n"
        "global_load_lds_dwordx4 %[w1], s[42:43] offset:1024\nv_mfma_f32_32x32x16_bf16 v[80:95], v[108:111], v[20:23], v[80:95]\ns_add_u32 s40, s44, %[ro]\ns_addc_u32 s41, s45, 0\n"
        "global_load_dwordx4 v[16:19], %[h0], s[40:41] sc1\nglobal_load_dwordx4 v[20:23], %[h1], s[40:41] sc1\ns_add_u32 %[ro], %[ro], 1024\ns_cmp_eq_u32 %[ro], 14336\n"
        "s_cselect_b32 %[ro], 0, %[ro]\ns_waitcnt vmcnt(10) lgkmcnt(0)\nv_mfma_f32_32x32x16_bf16 v[32:47], v[96:99], v[24:27], v[32:47]\n"
        "ds_read_b64_tr_b16 v[96:97], %[a0] offset:0\nds_read_b64_tr_b16 v[98:99], %[a0] offset:1024\ns_lshl_b32 s42, %[ro], 4\ns_add_u32 s42, s46, s42\ns_addc_u32 s43, s47, 0\n"
        "s_add_u32 m0, %[dw], 6144\nv_mfma_f32_32x32x16_bf16 v[48:63], v[100:103], v[24:27], v[48:63]\nds_read_b64_tr_b16 v[100:101], %[a1] offset:0\n"
        "ds_read_b64_tr_b16 v[102:103], %[a1] offset:1024\nglobal_load_lds_dwordx4 %[w0], s[42:43]\nv_mfma_f32_32x32x16_bf16 v[64:79], v[96:99], v[28:31], v[64:79]\n"
        "global_load_lds_dwordx4 %[w1], s[42:43] offset:1024\nv_mfma_f32_32x32x16_bf16 v[80:95], v[100:103], v[28:31], v[80:95]\ns_add_u32 s40, s44, %[ro]\ns_addc_u32 s41, s45, 0\n"
        "global_load_dwordx4 v[24:27], %[h0], s[40:41] sc1\nglobal_load_dwordx4 v[28:31], %[h1], s[40:41] sc1\ns_add_u32 %[ro], %[ro], 1024\ns_cmp_eq_u32 %[ro], 14336\n"
        "s_cselect_b32 %[ro], 0, %[ro]\ns_waitcnt vmcnt(10) lgkmcnt(0)\nv_mfma_f32_32x32x16_bf16 v[32:47], v[104:107], v[112:115], v[32:47]\n"
        "ds_read_b64_tr_b16 v[104:105], %[a0] offset:2048\nds_read_b64_tr_b16 v[106:107], %[a0] offset:3072\ns_lshl_b32 s42, %[ro], 4\ns_add_u32 s42, s46, s42\n"
        "s_addc_u32 s43, s47, 0\ns_add_u32 m0, %[dw], 0\nv_mfma_f32_32x32x16_bf16 v[48:63], v[108:111], v[112:115], v[48:63]\nds_read_b64_tr_b16 v[108:109], %[a1] offset:2048\n"
        "ds_read_b64_tr_b16 v[110:111], %[a1] offset:3072\nglobal_load_lds_dwordx4 %[w0], s[42:43]\nv_mfma_f32_32x32x16_bf16 v[64:79], v[104:107], v[116:119], v[64:79]\n"
        "global_load_lds_dwordx4 %[w1], s[42:43] offset:1024\nv_mfma_f32_32x32x16_bf16 v[80:95], v[108:111], v[116:119], v[80:95]\ns_add_u32 s40, s44, %[ro]\n"
        "s_addc_u32 s41, s45, 0\nglobal_load_dwordx4 v[112:115], %[h0], s[40:41] sc1\nglobal_load_dwordx4 v[116:119], %[h1], s[40:41] sc1\ns_add_u32 %[ro], %[ro], 1024\n"
        "s_cmp_eq_u32 %[ro], 14336\ns_cselect_b32 %[ro], 0, %[ro]\ns_waitcnt vmcnt(10) lgkmcnt(0)\nv_mfma_f32_32x32x16_bf16 v[32:47], v[96:99], v[120:123], v[32:47]\n"
        "ds_read_b64_tr_b16 v[96:97], %[a0] offset:4096\nds_read_b64_tr_b16 v[98:99], %[a0] offset:5120\ns_lshl_b32 s42, %[ro], 4\ns_add_u32 s42, s46, s42\ns_addc_u32 s43, s47, 0\n"
        "s_add_u32 m0, %[dw], 2048\nv_mfma_f32_32x32x16_bf16 v[48:63], v[100:103], v[120:123], v[48:63]\nds_read_b64_tr_b16 v[100:101], %[a1] offset:4096\n"
        "ds_read_b64_tr_b16 v[102:103], %[a1] offset:5120\nglobal_load_lds_dwordx4 %[w0], s[42:43]\nv_mfma_f32_32x32x16_bf16 v[64:79], v[96:99], v[124:127], v[64:79]\n"
        "global_load_lds_dwordx4 %[w1], s[42:43] offset:1024\nv_mfma_f32_32x32x16_bf16 v[80:95], v[100:103], v[124:127], v[80:95]\ns_add_u32 s40, s44, %[ro]\n"
        "s_addc_u32 s41, s45, 0\nglobal_load_dwordx4 v[120:123], %[h0], s[40:41] sc1\nglobal_load_dwordx4 v[124:127], %[h1], s[40:41] sc1\ns_add_u32 %[ro], %[ro], 1024\n"
        "s_cmp_eq_u32 %[ro], 14336\ns_cselect_b32 %[ro], 0, %[ro]\ns_waitcnt vmcnt(10) lgkmcnt(0)\nv_mfma_f32_32x32x16_bf16 v[32:47], v[104:107], v[16:19], v[32:47]\n"
        "ds_read_b64_tr_b16 v[104:105], %[a0] offset:6144\nds_read_b64_tr_b16 v[106:107], %[a0] offset:7168\ns_lshl_b32 s42, %[ro], 4\ns_add_u32 s42, s46, s42\n"
        "s_addc_u32 s43, s47, 0\ns_add_u32 m0, %[dw], 4096\nv_mfma_f32_32x32x16_bf16 v[48:63], v[108:111], v[16:19], v[48:63]\nds_read_b64_tr_b16 v[108:109], %[a1] offset:6144\n"
        "ds_read_b64_tr_b16 v[110:111], %[a1] offset:7168\nglobal_load_lds_dwordx4 %[w0], s[42:43]\nv_mfma_f32_32x32x16_bf16 v[64:79], v[104:107], v[20:23], v[64:79]\n"
        "global_load_lds_dwordx4 %[w1], s[42:43] offset:1024\nv_mfma_f32_32x32x16_bf16 v[80:95], v[108:111], v[20:23], v[80:95]\ns_add_u32 s40, s44, %[ro]\ns_addc_u32 s41, s45, 0\n"
        "global_load_dwordx4 v[16:19], %[h0], s[40:41] sc1\nglobal_load_dwordx4 v[20:23], %[h1], s[40:41] sc1\ns_add_u32 %[ro], %[ro], 1024\ns_cmp_eq_u32 %[ro], 14336\n"
        "s_cselect_b32 %[ro], 0, %[ro]\ns_waitcnt vmcnt(10) lgkmcnt(0)\nv_mfma_f32_32x32x16_bf16 v[32:47], v[96:99], v[24:27], v[32:47]\n"
        "ds_read_b64_tr_b16 v[96:97], %[a0] offset:0\nds_read_b64_tr_b16 v[98:99], %[a0] offset:1024\ns_lshl_b32 s42, %[ro], 4\ns_add_u32 s42, s46, s42\ns_addc_u32 s43, s47, 0\n"
        "s_add_u32 m0, %[dw], 6144\nv_mfma_f32_32x32x16_bf16 v[48:63], v[100:103], v[24:27], v[48:63]\nds_read_b64_tr_b16 v[100:101], %[a1] offset:0\n"
        "ds_read_b64_tr_b16 v[102:103], %[a1] offset:1024\nglobal_load_lds_dwordx4 %[w0], s[42:43]\nv_mfma_f32_32x32x16_bf16 v[64:79], v[96:99], v[28:31], v[64:79]\n"
        "global_load_lds_dwordx4 %[w1], s[42:43] offset:1024\nv_mfma_f32_32x32x16_bf16 v[80:95], v[100:103], v[28:31], v[80:95]\ns_add_u32 s40, s44, %[ro]\ns_addc_u32 s41, s45, 0\n"
        "global_load_dwordx4 v[24:27], %[h0], s[40:41] sc1\nglobal_load_dwordx4 v[28:31], %[h1], s[40:41] sc1\ns_add_u32 %[ro], %[ro], 1024\ns_cmp_eq_u32 %[ro], 14336\n"
        "s_cselect_b32 %[ro], 0, %[ro]\ns_waitcnt vmcnt(10) lgkmcnt(0)\nv_mfma_f32_32x32x16_bf16 v[32:47], v[104:107], v[112:115], v[32:47]\n"
        "ds_read_b64_tr_b16 v[104:105], %[a0] offset:2048\nds_read_b64_tr_b16 v[106:107], %[a0] offset:3072\ns_lshl_b32 s42, %[ro], 4\ns_add_u32 s42, s46, s42\n"
        "s_addc_u32 s43, s47, 0\ns_add_u32 m0, %[dw], 0\nv_mfma_f32_32x32x16_bf16 v[48:63], v[108:111], v[112:115], v[48:63]\nds_read_b64_tr_b16 v[108:109], %[a1] offset:2048\n"
        "ds_read_b64_tr_b16 v[110:111], %[a1] offset:3072\nglobal_load_lds_dwordx4 %[w0], s[42:43]\nv_mfma_f32_32x32x16_bf16 v[64:79], v[104:107], v[116:119], v[64:79]\n"
        "global_load_lds_dwordx4 %[w1], s[42:43] offset:1024\nv_mfma_f32_32x32x16_bf16 v[80:95], v[108:111], v[116:119], v[80:95]\ns_add_u32 s40, s44, %[ro]\n"
        "s_addc_u32 s41, s45, 0\nglobal_load_dwordx4 v[112:115], %[h0], s[40:41] sc1\nglobal_load_dwordx4 v[116:119], %[h1], s[40:41] sc1\ns_add_u32 %[ro], %[ro], 1024\n"
        "s_cmp_eq_u32 %[ro], 14336\ns_cselect_b32 %[ro], 0, %[ro]\ns_waitcnt vmcnt(10) lgkmcnt(0)\nv_mfma_f32_32x32x16_bf16 v[32:47], v[96:99], v[120:123], v[32:47]\n"
        "ds_read_b64_tr_b16 v[96:97], %[a0] offset:4096\nds_read_b64_tr_b16 v[98:99], %[a0] offset:5120\ns_lshl_b32 s42, %[ro], 4\ns_add_u32 s42, s46, s42\ns_addc_u32 s43, s47, 0\n"
        "s_add_u32 m0, %[dw], 2048\nv_mfma_f32_32x32x16_bf16 v[48:63], v[100:103], v[120:123], v[48:63]\nds_read_b64_tr_b16 v[100:101], %[a1] offset:4096\n"
        "ds_read_b64_tr_b16 v[102:103], %[a1] offset:5120\nglobal_load_lds_dwordx4 %[w0], s[42:43]\nv_mfma_f32_32x32x16_bf16 v[64:79], v[96:99], v[124:127], v[64:79]\n"
        "global_load_lds_dwordx4 %[w1], s[42:43] offset:1024\nv_mfma_f32_32x32x16_bf16 v[80:95], v[100:103], v[124:127], v[80:95]\ns_add_u32 s40, s44, %[ro]\n"
        "s_addc_u32 s41, s45, 0\nglobal_load_dwordx4 v[120:123], %[h0], s[40:41] sc1\nglobal_load_dwordx4 v[124:127], %[h1], s[40:41] sc1\ns_add_u32 %[ro], %[ro], 1024\n"
        "s_cmp_eq_u32 %[ro], 14336\ns_cselect_b32 %[ro], 0, %[ro]\ns_waitcnt vmcnt(10) lgkmcnt(0)\nv_mfma_f32_32x32x16_bf16 v[32:47], v[104:107], v[16:19], v[32:47]\n"
        "ds_read_b64_tr_b16 v[104:105], %[a0] offset:6144\nds_read_b64_tr_b16 v[106:107], %[a0] offset:7168\nv_mfma_f32_32x32x16_bf16 v[48:63], v[108:111], v[16:19], v[48:63]\n"
        "ds_read_b64_tr_b16 v[108:109], %[a1] offset:6144\nds_read_b64_tr_b16 v[110:111], %[a1] offset:7168\nv_mfma_f32_32x32x16_bf16 v[64:79], v[104:107], v[20:23], v[64:79]\n"
        "v_mfma_f32_32x32x16_bf16 v[80:95], v[108:111], v[20:23], v[80:95]\ns_waitcnt vmcnt(6) lgkmcnt(0)\nv_mfma_f32_32x32x16_bf16 v[32:47], v[96:99], v[24:27], v[32:47]\n"
        "ds_read_b64_tr_b16 v[96:97], %[a0] offset:0\nds_read_b64_tr_b16 v[98:99], %[a0] offset:1024\nv_mfma_f32_32x32x16_bf16 v[48:63], v[100:103], v[24:27], v[48:63]\n"
        "ds_read_b64_tr_b16 v[100:101], %[a1] offset:0\nds_read_b64_tr_b16 v[102:103], %[a1] offset:1024\nv_mfma_f32_32x32x16_bf16 v[64:79], v[96:99], v[28:31], v[64:79]\n"
        "v_mfma_f32_32x32x16_bf16 v[80:95], v[100:103], v[28:31], v[80:95]\ns_waitcnt vmcnt(2) lgkmcnt(0)\nv_mfma_f32_32x32x16_bf16 v[32:47], v[104:107], v[112:115], v[32:47]\n"
        "ds_read_b64_tr_b16 v[104:105], %[a0] offset:2048\nds_read_b64_tr_b16 v[106:107], %[a0] offset:3072\nv_mfma_f32_32x32x16_bf16 v[48:63], v[108:111], v[112:115], v[48:63]\n"
        "ds_read_b64_tr_b16 v[108:109], %[a1] offset:2048\nds_read_b64_tr_b16 v[110:111], %[a1] offset:3072\nv_mfma_f32_32x32x16_bf16 v[64:79], v[104:107], v[116:119], v[64:79]\n"
        "v_mfma_f32_32x32x16_bf16 v[80:95], v[108:111], v[116:119], v[80:95]\ns_waitcnt vmcnt(0) lgkmcnt(0)\nv_mfma_f32_32x32x16_bf16 v[32:47], v[104:107], v[120:123], v[32:47]\n"
        "v_mfma_f32_32x32x16_bf16 v[48:63], v[108:111], v[120:123], v[48:63]\nv_mfma_f32_32x32x16_bf16 v[64:79], v[104:107], v[124:127], v[64:79]\n"
        "v_mfma_f32_32x32x16_bf16 v[80:95], v[108:111], v[124:127], v[80:95]\ns_mov_b32 m0, %[sv]\ns_nop 7\ns_nop 7\n"
          : "=&{v[32:47]}"(d00), "=&{v[48:63]}"(d01), "=&{v[64:79]}"(d10), "=&{v[80:95]}"(d11), [ro] "+s"(ro), [sv] "=&s"(sv)
          : [a0] "v"(dw + at), [a1] "v"(dw + (at ^ 64)), [h0] "v"(l16), [h1] "v"(l16 + 114688), [w0] "v"(vo), [w1] "v"(vo + 7168), [dw] "s"(dw), [sh] "s"(sh), [sw] "s"(sw)
          : "v96", "v97", "v98", "v99", "v100", "v101", "v102", "v103", "v104", "v105", "v106", "v107", "v108", "v109", "v110", "v111", "v112", "v113", "v114", "v115", "v116", "v117", "v118", "v119", "v120", "v121", "v122", "v123", "v124", "v125", "v126", "v127", "v16", "v17", "v18", "v19", "v20", "v21", "v22", "v23", "v24", "v25", "v26", "v27", "v28", "v29", "v30", "v31", "s40", "s41", "s42", "s43", "s44", "s45", "s46", "s47", "scc", "memory");
        PRIO(2);
        TS(256 + b, 4);
        const lptr pw = L + w * 2048 + l16, pr = L + kq * 16384 + l16; f32x4 a, c;
#define F2_RED(dA, dB, tt) do { BARL(); _Pragma("unroll") for (int u = 0; u < 4; ++u) { lw128f(pw + (u >> 1) * 16384 + (u & 1) * 1024, dA[4 * u], dA[4 * u + 1], dA[4 * u + 2], dA[4 * u + 3]); \
            lw128f(pw + 32768 + (u >> 1) * 16384 + (u & 1) * 1024, dB[4 * u], dB[4 * u + 1], dB[4 * u + 2], dB[4 * u + 3]); } BARL(); \
            if (t == tt) { a = lr128f(pr); c = lr128f(pr + 1024); _Pragma("unroll") for (int u = 1; u < 8; ++u) { a += lr128f(pr + u * 2048); c += lr128f(pr + u * 2048 + 1024); } } } while (0)
        F2_RED(d00, d01, 0); F2_RED(d10, d11, 1);
#pragma unroll
        for (int i = 0; i < 4; ++i) { gg[i] = a[i] * __builtin_amdgcn_rcpf(1.f + __builtin_amdgcn_exp2f(a[i] * -1.4426950408889634f));
                                      gg[4 + i] = c[i] * __builtin_amdgcn_rcpf(1.f + __builtin_amdgcn_exp2f(c[i] * -1.4426950408889634f)); } }
    BARL();
#pragma unroll
    for (int n = 0; n < 8; ++n) glds16(vo, wo + cg * 128 + (w * 8 + n) * 8192, L0 + (w * 8 + n) * 1024);
    const size_t ob = (size_t)(tg * 64 + t * 32 + (l & 31)) * 1024 + cg * 128 + kq * 32;
    const u32x2 x0 = *(const u32x2*)(x + ob + 8 * h), x1 = *(const u32x2*)(x + ob + 16 + 8 * h);
    BAR(0); TS(256 + b, 5);
    { GAS unsigned* f = (GAS unsigned*)(ws + WS_FL + (tg * 8 + 2 * kq + (l & 1)) * 128);
      for (unsigned sp = 0; sp < SPIN_CAP; ++sp) { if (__builtin_amdgcn_ballot_w64(__hip_atomic_load(f, RLX_AGENT) == 0u) == 0ull) break; __builtin_amdgcn_s_sleep(2); } }
    TS(256 + b, 6);
    bf16x8 of[8];
    { const char* so = ws + WS_O + ((2 * tg + t) * 32 + 8 * kq) * 1024;
#pragma unroll
      for (int s = 0; s < 8; ++s) LD_SC1(of[s], l16, so + s * 1024); }
    f32x16 c0 = splat16(0.f), c1 = c0;
    const lptr a0 = L + kq * 16384 + at, a1 = L + kq * 16384 + (at ^ 64);
#pragma unroll
    for (int s = 0; s < 8; ++s) { const bf16x8 f0 = ltr(a0, s * 2048, s * 2048 + 1024), f1 = ltr(a1, s * 2048, s * 2048 + 1024);
        asm volatile("s_waitcnt vmcnt(%1)" : "+v"(of[s]) : "n"(7 - s) : "memory");
        c0 = MFMA(f0, of[s], c0); c1 = MFMA(f1, of[s], c1); }
    BARL();
    const lptr pw = L + (t * 16 + kq) * 2048 + l16;
#pragma unroll
    for (int u = 0; u < 4; ++u) { lw128f(pw + (u >> 1) * 8192 + (u & 1) * 1024, c0[4 * u], c0[4 * u + 1], c0[4 * u + 2], c0[4 * u + 3]);
                                  lw128f(pw + 16384 + (u >> 1) * 8192 + (u & 1) * 1024, c1[4 * u], c1[4 * u + 1], c1[4 * u + 2], c1[4 * u + 3]); }
    BARL();
    const lptr pr = L + (t * 16 + kq * 4) * 2048 + l16; f32x4 a = lr128f(pr), c = lr128f(pr + 1024);
#pragma unroll
    for (int u = 1; u < 4; ++u) { a += lr128f(pr + u * 2048); c += lr128f(pr + u * 2048 + 1024); }
    const unsigned A0 = pk(blo(x0[0]) + a[0] * gg[0], bhi(x0[0]) + a[1] * gg[1]), A1 = pk(blo(x0[1]) + a[2] * gg[2], bhi(x0[1]) + a[3] * gg[3]);
    const unsigned B0 = pk(blo(x1[0]) + c[0] * gg[4], bhi(x1[0]) + c[1] * gg[5]), B1 = pk(blo(x1[1]) + c[2] * gg[6], bhi(x1[1]) + c[3] * gg[7]);
    auto s0 = __builtin_amdgcn_permlane32_swap(A0, B0, false, false), s1 = __builtin_amdgcn_permlane32_swap(A1, B1, false, false);
    const u32x4 ov = {s0[0], s1[0], s0[1], s1[1]};
    *(u32x4*)(out + ob + 16 * h) = ov;
    TS(256 + b, 7);
}

__global__ __launch_bounds__(512, 4) void k2_kernel(const char* x, const char* wq, const char* wo, const char* wg, const char* wu, const char* wd, char* out, char* ws) {
    extern __shared__ char smem[];
    const lptr L = (lptr)smem; const unsigned L0 = (unsigned)(size_t)L;
    const int b = blockIdx.x;
    if (b < 256) attn_role(b, L, L0, wq, ws); else if (!NOFFN) ffn_role(b - 256, L, L0, x, wo, wg, wu, wd, out, ws);
}

extern "C" void kernel_launch(void* const* d_in, const int* in_sizes, int n_in, void* d_out, int out_size, void* d_ws, size_t ws_size, hipStream_t stream) {
    (void)hipFuncSetAttribute((const void*)k1_kernel, hipFuncAttributeMaxDynamicSharedMemorySize, LDS_K1);
    (void)hipFuncSetAttribute((const void*)k2_kernel, hipFuncAttributeMaxDynamicSharedMemorySize, LDS_K2);
    k1_kernel<<<256, 512, LDS_K1, stream>>>((const char*)d_in[0], (const char*)d_in[2], (const char*)d_in[3], (const char*)d_in[1], (const char*)d_in[6], (const char*)d_in[7], (const char*)d_in[8], (const char*)d_in[4], (const char*)d_out, (char*)d_ws);
    k2_kernel<<<512, 512, LDS_K2, stream>>>((const char*)d_in[0], (const char*)d_in[1], (const char*)d_in[4], (const char*)d_in[6], (const char*)d_in[7], (const char*)d_in[8], (char*)d_out, (char*)d_ws);
}
```

```cpp
#define BAL 1
#include <hip/hip_runtime.h>
#include <hip/hip_bf16.h>
#include <stdint.h>

typedef short bf16x8 __attribute__((ext_vector_type(8)));
typedef short s16x4 __attribute__((ext_vector_type(4)));
typedef float f32x16 __attribute__((ext_vector_type(16)));
typedef float f32x4 __attribute__((ext_vector_type(4)));
typedef float f32x2 __attribute__((ext_vector_type(2)));
typedef __bf16 b16x2 __attribute__((ext_vector_type(2)));
typedef unsigned u32x4 __attribute__((ext_vector_type(4)));
typedef unsigned u32x2 __attribute__((ext_vector_type(2)));
#define LAS __attribute__((address_space(3)))
#define GAS __attribute__((address_space(1)))
#define DI __device__ __forceinline__
#define MFMA(a, b, c) __builtin_amdgcn_mfma_f32_32x32x16_bf16((a), (b), (c), 0, 0, 0)
#define RLX_AGENT __ATOMIC_RELAXED, __HIP_MEMORY_SCOPE_AGENT
typedef LAS char* lptr;

constexpr unsigned WS_XN = 0, WS_KV = 2u << 20, WS_H = 4u << 20, WS_O = 12u << 20, WS_FL = 16u << 20;
constexpr int FL_H = 256;
constexpr int LDS_K2 = 81920, LDS_K1 = 65792;
constexpr unsigned SPIN_CAP = 1u << 20;
#define NOFFN 0
#define P_LOOP 0
#define P_LOOP2 0
#define P_ATT 1
#define TOG 0
#define TLBW 0
#define PRIO(n) __builtin_amdgcn_s_setprio(n)
#define TS(blk, i)

DI unsigned pk(float a, float b) { f32x2 v = {a, b}; return __builtin_bit_cast(unsigned, __builtin_convertvector(v, b16x2)); }
DI bf16x8 pack8(const f32x16& x, int o) { u32x4 r = {pk(x[o], x[o + 1]), pk(x[o + 2], x[o + 3]), pk(x[o + 4], x[o + 5]), pk(x[o + 6], x[o + 7])}; return __builtin_bit_cast(bf16x8, r); }
DI float blo(unsigned u) { return __uint_as_float(u << 16); }
DI float bhi(unsigned u) { return __uint_as_float(u & 0xffff0000u); }
DI bf16x8 lr128(lptr p) { return *(const LAS bf16x8*)p; }
DI f32x4 lr128f(lptr p) { return *(const LAS f32x4*)p; }
DI void lw128f(lptr p, float a, float b, float c, float d) { f32x4 v = {a, b, c, d}; *(LAS f32x4*)p = v; }
DI bf16x8 ltr(lptr p, int o0, int o1) {
    s16x4 a = __builtin_amdgcn_ds_read_tr16_b64_v4i16((LAS s16x4*)(p + o0)), b = __builtin_amdgcn_ds_read_tr16_b64_v4i16((LAS s16x4*)(p + o1));
    return __builtin_shufflevector(a, b, 0, 1, 2, 3, 4, 5, 6, 7); }
DI void glds16(unsigned voff, const void* sbase, unsigned lds) {
    unsigned sv; asm volatile("s_mov_b32 %0, m0\n\ts_mov_b32 m0, %3\n\ts_nop 0\n\tglobal_load_lds_dwordx4 %1, %2\n\ts_mov_b32 m0, %0" : "=&s"(sv) : "v"(voff), "s"(sbase), "s"(lds) : "memory"); }
DI const char* uni(const char* p) { const unsigned long long a = (unsigned long long)p; return (const char*)(((unsigned long long)(unsigned)__builtin_amdgcn_readfirstlane((unsigned)(a >> 32)) << 32) | (unsigned)__builtin_amdgcn_readfirstlane((unsigned)a)); }
#define BAR(N) asm volatile("s_waitcnt vmcnt(" #N ") lgkmcnt(0)\n\ts_barrier" ::: "memory")
#define BARL() asm volatile("s_waitcnt lgkmcnt(0)\n\ts_barrier" ::: "memory")
DI void st_sc1(unsigned voff, void* sbase, bf16x8 v) { asm volatile("global_store_dwordx4 %0, %1, %2 sc1" ::"v"(voff), "v"(v), "s"(sbase) : "memory"); }
#define LD_SC1(dst, voff, sbase) asm volatile("global_load_dwordx4 %0, %1, %2 sc1" : "=v"(dst) : "v"(voff), "s"(sbase) : "memory")
#define touch(voff, sbase) asm volatile("global_load_dword %0, %1, %2" : "+v"(jk) : "v"(voff), "s"(sbase) : "memory")
#define TOUCH_END() asm volatile("s_waitcnt vmcnt(0)" : "+v"(jk)::"memory")
template <int C> DI float dpp(float x) { return __builtin_bit_cast(float, __builtin_amdgcn_mov_dpp(__builtin_bit_cast(int, x), C, 0xf, 0xf, true)); }
DI float sum32(float x) {
    x += dpp<0xB1>(x); x += dpp<0x4E>(x); x += dpp<0x141>(x); x += dpp<0x140>(x);
    auto s = __builtin_amdgcn_permlane16_swap(__float_as_uint(x), __float_as_uint(x), false, false);
    return __uint_as_float(s[0]) + __uint_as_float(s[1]); }
DI float xhalf_max(float m) { auto r = __builtin_amdgcn_permlane32_swap(__float_as_uint(m), __float_as_uint(m), false, false); return fmaxf(__uint_as_float(r[0]), __uint_as_float(r[1])); }
DI float max16(const f32x16& s, float m) {
#pragma unroll
    for (int i = 0; i < 16; i += 2) m = fmaxf(fmaxf(m, s[i]), s[i + 1]);
    return m; }
DI f32x16 splat16(float v) { f32x16 r;
#pragma unroll
    for (int i = 0; i < 16; ++i) r[i] = v;
    return r; }

__global__ __launch_bounds__(512) void k1_kernel(const char* x, const char* wk, const char* wv, const char* wq, const char* wg, const char* wu, const char* wd, const char* wo, const char* out, char* ws) {
    extern __shared__ char smem[];
    const lptr L = (lptr)smem; const unsigned L0 = (unsigned)(size_t)L;
    const int b = blockIdx.x, tid = threadIdx.x, l = tid & 63, w = __builtin_amdgcn_readfirstlane(tid >> 6), r = l & 31, h = l >> 5;
    TS(512 + b, 0);
    if (b >= 128) {
        const int j = (b - 128) >> 3, fq = (b & 7) >> 1;
        const int n = tid & 255, row = n / 7; unsigned jk = 0;
        if (w < 4) touch((j * 256 + n) * 128, wq);
        if (n < 224) touch((j * 32 + row) * 3584 + fq * 896 + (n - row * 7) * 128, w < 4 ? wg : wu);
        if (TLBW && w == 7) {
            if (l < 7) touch((j * 7 + l) << 16, ws + WS_H);
            if (l < 2) { touch((j * 2 + l) << 16, ws + WS_O); touch((j * 2 + l) << 16, out); }
            if (l < 2 && j < 14) touch((j * 2 + l) << 16, wd);
            if (l == 0 && j < 8) touch(j << 16, wo); }
        TOUCH_END(); TS(512 + b, 2); return; }
    constexpr int WK = 0, WV = 16384 + 128, XL = 33024, RED = 49408;
    const int tok0 = 256 * (b & 7) + 16 * (b >> 3), tl = 2 * w + h;
    const char* xr = x + (size_t)(tok0 + tl) * 1024 + r * 16;
    const u32x4 xa = *(const u32x4*)xr, xb = *(const u32x4*)(xr + 512);
    glds16(l * 16, wk + w * 2048, L0 + WK + w * 2048); glds16(l * 16, wk + w * 2048 + 1024, L0 + WK + w * 2048 + 1024);
    glds16(l * 16, wv + w * 2048, L0 + WV + w * 2048); glds16(l * 16, wv + w * 2048 + 1024, L0 + WV + w * 2048 + 1024);
    if (tid < 3) { const int f = b * 3 + tid; if (f < FL_H + 8) *(unsigned*)(ws + WS_FL + f * 128) = 0u; }
    float v[16];
#pragma unroll
    for (int i = 0; i < 4; ++i) { v[2 * i] = blo(xa[i]); v[2 * i + 1] = bhi(xa[i]); v[8 + 2 * i] = blo(xb[i]); v[9 + 2 * i] = bhi(xb[i]); }
    float s = 0.f;
#pragma unroll
    for (int i = 0; i < 16; ++i) s += v[i];
    const float mean = sum32(s) * (1.f / 512.f); float q2 = 0.f;
#pragma unroll
    for (int i = 0; i < 16; ++i) { v[i] -= mean; q2 += v[i] * v[i]; }
    const float rstd = __builtin_amdgcn_rsqf(sum32(q2) * (1.f / 512.f) + 1e-5f);
    u32x4 ya, yb;
#pragma unroll
    for (int i = 0; i < 4; ++i) { ya[i] = pk(v[2 * i] * rstd, v[2 * i + 1] * rstd); yb[i] = pk(v[8 + 2 * i] * rstd, v[9 + 2 * i] * rstd); }
    { const lptr p = L + XL + r * 256 + ((tl ^ (r & 15)) << 4); *(LAS u32x4*)p = ya; *(LAS u32x4*)(p + 8192) = yb; }
    BAR(0); TS(512 + b, 1);
    { const int c = tid >> 4, tk = tid & 15; const lptr p = L + XL + c * 256 + ((tk ^ (c & 15)) << 4);
      char* g = ws + WS_XN + (tok0 >> 5) * 32768 + c * 512 + ((tok0 & 16) + tk) * 16;
      *(u32x4*)g = *(LAS u32x4*)p; *(u32x4*)(g + 16384) = *(LAS u32x4*)(p + 8192); }
    f32x16 aK = splat16(0.f), aV = splat16(0.f);
    const lptr wp = L + ((l & 16) ? WV : WK) + (64 * w + 8 * h + ((l >> 2) & 3)) * 32 + (l & 3) * 8;
#pragma unroll
    for (int s4 = 0; s4 < 4; ++s4) { const int c = 8 * w + 2 * s4 + h;
        const bf16x8 xf = lr128(L + XL + c * 256 + (((l ^ c) & 15) << 4)), wf = ltr(wp, s4 * 512, s4 * 512 + 128);
        aK = MFMA(wf, xf, aK); aV = MFMA(xf, wf, aV); }
    { const bool vv = l & 16; const lptr p = L + RED + w * 2048 + l * 16;
      lw128f(p, vv ? aV[0] : aK[0], vv ? aV[1] : aK[1], vv ? aV[2] : aK[2], vv ? aV[3] : aK[3]);
      lw128f(p + 1024, vv ? aV[4] : aK[4], vv ? aV[5] : aK[5], vv ? aV[6] : aK[6], vv ? aV[7] : aK[7]); }
    BARL();
    if (w == 0) { f32x4 a = lr128f(L + RED + l * 16), c = lr128f(L + RED + 1024 + l * 16);
#pragma unroll
        for (int i = 1; i < 8; ++i) { a += lr128f(L + RED + i * 2048 + l * 16); c += lr128f(L + RED + i * 2048 + 1024 + l * 16); }
        const u32x4 o = {pk(a[0], a[1]), pk(a[2], a[3]), pk(c[0], c[1]), pk(c[2], c[3])}; const int sh = (tok0 >> 4) & 1;
        const int off = (l & 16) ? 1024 + sh * 512 + h * 256 + (l & 15) * 16 : (16 * sh + (l & 15) + 32 * h) * 16;
        *(u32x4*)(ws + WS_KV + (tok0 >> 5) * 2048 + off) = o; }
    TS(512 + b, 2);
}

DI void attn_role(int b, const lptr L, unsigned L0, const char* wq, char* ws) {
    const int tid = threadIdx.x, l = tid & 63, w = __builtin_amdgcn_readfirstlane(tid >> 6), h = l >> 5, t = w & 1, kq = w >> 1;
    const int i5 = b >> 3, tg = (b & 7) * 4 + (i5 & 3), hg = i5 >> 2, TT = 2 * tg + t;
    constexpr int SL = 8320, TS_ = 2080;
    const unsigned l16 = l * 16;
    PRIO(3); TS(b, 0);
    bf16x8 xf[8];
    { const char* xp = ws + WS_XN + (TT * 32 + 8 * kq) * 1024 + l16;
#pragma unroll
      for (int s = 0; s < 8; ++s) xf[s] = *(const bf16x8*)(xp + s * 1024); }
    { const unsigned vo = (l >> 3) * 1024 + (((l & 7) ^ (((l >> 4) & 1) << 2)) << 4);
#pragma unroll
      for (int n = 0; n < 8; ++n) glds16(vo, wq + hg * 128 + (w * 8 + n) * 8192, L0 + (w * 8 + n) * 1024); }
    const char* kvs = ws + WS_KV + w * 1024; const unsigned kvd = L0 + (w >> 1) * TS_ + (w & 1) * 1024;
    glds16(l16, kvs, kvd + 8 * SL);
    BAR(1); TS(b, 1);
    bf16x8 qf;
    { const int q = (l >> 2) & 3, p = l & 3, ch = ((l >> 4) & 1) * 2 + (p >> 1);
      const int a0 = kq * 16384 + (8 * h + q) * 128 + ((ch ^ ((q >> 1) << 2)) << 4) + 8 * (p & 1);
      f32x16 c0 = splat16(0.f), c1 = splat16(0.f);
#pragma unroll
      for (int s = 0; s < 8; ++s) { c0 = MFMA(ltr(L + a0, s * 2048, s * 2048 + 512), xf[s], c0); c1 = MFMA(ltr(L + (a0 ^ 64), s * 2048, s * 2048 + 512), xf[s], c1); }
      BARL();
      glds16(l16, kvs + 8192, kvd); glds16(l16, kvs + 2 * 8192, kvd + SL);
      if (tid < 24) { const unsigned v = (tid & 1) ? 0u : 0x3F803F80u; const u32x4 one = {v, v, v, v}; *(LAS u32x4*)(L + ((tid >> 1) + (tid < 16 ? 0 : 24)) * TS_ + 2048 + (tid & 1) * 16) = one; }
      f32x4 a, c;
#pragma unroll
      for (int j = 0; j < 4; ++j) { const f32x16& s = j < 2 ? c0 : c1; const int o = 8 * (j & 1);
          if (j == kq) { a = {s[o], s[o + 1], s[o + 2], s[o + 3]}; c = {s[o + 4], s[o + 5], s[o + 6], s[o + 7]}; }
          else { const lptr pw = L + 2 * SL + ((t * 4 + j) * 3 + ((kq - j - 1) & 3)) * 2048 + l16; lw128f(pw, s[o], s[o + 1], s[o + 2], s[o + 3]); lw128f(pw + 1024, s[o + 4], s[o + 5], s[o + 6], s[o + 7]); } }
      BAR(2);
      const lptr pr = L + 2 * SL + (t * 4 + kq) * 6144 + l16;
#pragma unroll
      for (int u = 0; u < 3; ++u) { a += lr128f(pr + u * 2048); c += lr128f(pr + u * 2048 + 1024); }
      const float sc = 0.25f * 1.4426950408889634f; a *= sc; c *= sc;
      const u32x4 qq = {pk(a[0], a[1]), pk(a[2], a[3]), pk(c[0], c[1]), pk(c[2], c[3])}; qf = __builtin_bit_cast(bf16x8, qq); }
    f32x16 ns; const bf16x8 kf = lr128(L + 8 * SL + l16); bf16x8 k0 = lr128(L + 8 * SL + TS_ + l16), k1 = lr128(L + 8 * SL + 2 * TS_ + l16);
    { const f32x16 z = splat16(0.f); f32x16 s0 = MFMA(kf, qf, z), s1 = MFMA(k0, qf, z), s2 = MFMA(k1, qf, z), s3 = MFMA(lr128(L + 8 * SL + 3 * TS_ + l16), qf, z);
      asm volatile("" : "+v"(s0), "+v"(s1), "+v"(s2), "+v"(s3));
      const float m = fmaxf(max16(s1, max16(s0, -3.0e38f)), max16(s3, max16(s2, -3.0e38f)));
      ns = splat16(-(xhalf_max(m) + 90.f)); }
    f32x16 o = splat16(0.f);
    { const unsigned bk = L0 + l16, b0 = L0 + ((l & 16) ? 1024 + h * 256 + (l & 15) * 16 : (l & 11) ? 2064 : 2048), b1 = (l & 16) ? b0 + 512 : b0;
      f32x16 sA = MFMA(kf, qf, ns);
      bf16x8 v0 = lr128(L + 8 * SL + (b0 - L0)), v1 = lr128(L + 8 * SL + (b1 - L0)), vb = {0, 0, 0, 0, 0, 0, 0, 0}, pb = vb;
      unsigned aK = bk + 8 * SL, a0 = b0 + 8 * SL, a1 = b1 + 8 * SL, vo = l16 + 3 * 8192, so = 8 * SL, sd = 2 * SL, sn, c = 0, sv;
      BAR(1); TS(b, 2);
      if (tid < 48) { const unsigned v = (tid & 1) ? 0u : 0x3F803F80u; const u32x4 one = {v, v, v, v}; *(LAS u32x4*)(L + ((tid >> 1) + 8) * TS_ + 2048 + (tid & 1) * 16) = one; }
      unsigned hi = w >> 2, pc = 0; if (BAL == 1) PRIO(2); else PRIO(P_ATT);
      asm volatile(
        "s_mov_b32 %[sv], m0\ns_nop 7\ns_nop 7\n9:\ns_add_u32 %[sn], %[so], 8320\ns_cmp_eq_u32 %[so], 66560\ns_cselect_b32 %[sn], 0, %[sn]\n"
        "v_mfma_f32_32x32x16_bf16 v[48:63], v[100:103], v[96:99], v[80:95]\nds_read_b128 v[100:103], v124 offset:6240\nv_exp_f32 v32, v32\nv_exp_f32 v33, v33\nv_exp_f32 v34, v34\n"
        "v_exp_f32 v35, v35\nv_exp_f32 v36, v36\nv_exp_f32 v37, v37\nv_mfma_f32_32x32x16_bf16 v[64:79], v[24:27], v[120:123], v[64:79]\nds_read_b128 v[24:27], v126 offset:2080\n"
        "v_exp_f32 v38, v38\nv_exp_f32 v39, v39\nv_cvt_pk_bf16_f32 v116, v32, v33\nv_cvt_pk_bf16_f32 v117, v34, v35\nv_cvt_pk_bf16_f32 v118, v36, v37\n"
        "v_cvt_pk_bf16_f32 v119, v38, v39\nv_exp_f32 v40, v40\nv_exp_f32 v41, v41\nv_exp_f32 v42, v42\ns_waitcnt lgkmcnt(2)\n"
        "v_mfma_f32_32x32x16_bf16 v[64:79], v[108:111], v[116:119], v[64:79]\nds_read_b128 v[108:111], v125 offset:2080\nv_add_u32 v127, %[sn], %[bk]\nv_add_u32 v31, %[sn], %[b0]\n"
        "v_add_u32 v30, %[sn], %[b1]\nv_exp_f32 v43, v43\nv_exp_f32 v44, v44\nv_exp_f32 v45, v45\nv_exp_f32 v46, v46\nv_exp_f32 v47, v47\nv_cvt_pk_bf16_f32 v120, v40, v41\n"
        "v_cvt_pk_bf16_f32 v121, v42, v43\nv_cvt_pk_bf16_f32 v122, v44, v45\nv_cvt_pk_bf16_f32 v123, v46, v47\nv_mfma_f32_32x32x16_bf16 v[32:47], v[104:107], v[96:99], v[80:95]\n"
        "ds_read_b128 v[104:107], v127 offset:0\nv_exp_f32 v48, v48\nv_exp_f32 v49, v49\nv_exp_f32 v50, v50\nv_exp_f32 v51, v51\nv_exp_f32 v52, v52\nv_exp_f32 v53, v53\n"
        "v_mfma_f32_32x32x16_bf16 v[64:79], v[112:115], v[120:123], v[64:79]\nds_read_b128 v[112:115], v126 offset:4160\nv_exp_f32 v54, v54\nv_exp_f32 v55, v55\n"
        "v_cvt_pk_bf16_f32 v116, v48, v49\nv_cvt_pk_bf16_f32 v117, v50, v51\nv_cvt_pk_bf16_f32 v118, v52, v53\nv_cvt_pk_bf16_f32 v119, v54, v55\nv_exp_f32 v56, v56\n"
        "v_exp_f32 v57, v57\nv_exp_f32 v58, v58\ns_waitcnt lgkmcnt(2)\nv_mfma_f32_32x32x16_bf16 v[64:79], v[108:111], v[116:119], v[64:79]\n"
        "ds_read_b128 v[108:111], v125 offset:4160\nv_exp_f32 v59, v59\nv_exp_f32 v60, v60\nv_exp_f32 v61, v61\nv_exp_f32 v62, v62\nv_exp_f32 v63, v63\n"
        "v_cvt_pk_bf16_f32 v120, v56, v57\nv_cvt_pk_bf16_f32 v121, v58, v59\nv_cvt_pk_bf16_f32 v122, v60, v61\nv_cvt_pk_bf16_f32 v123, v62, v63\n"
        "v_mfma_f32_32x32x16_bf16 v[48:63], v[100:103], v[96:99], v[80:95]\nds_read_b128 v[100:103], v127 offset:2080\nv_exp_f32 v32, v32\nv_exp_f32 v33, v33\nv_exp_f32 v34, v34\n"
        "v_exp_f32 v35, v35\nv_exp_f32 v36, v36\nv_exp_f32 v37, v37\nv_mfma_f32_32x32x16_bf16 v[64:79], v[24:27], v[120:123], v[64:79]\nds_read_b128 v[24:27], v126 offset:6240\n"
        "v_exp_f32 v38, v38\nv_exp_f32 v39, v39\nv_cvt_pk_bf16_f32 v116, v32, v33\nv_cvt_pk_bf16_f32 v117, v34, v35\nv_cvt_pk_bf16_f32 v118, v36, v37\n"
        "v_cvt_pk_bf16_f32 v119, v38, v39\nv_exp_f32 v40, v40\nv_exp_f32 v41, v41\nv_exp_f32 v42, v42\ns_waitcnt lgkmcnt(2)\n"
        "v_mfma_f32_32x32x16_bf16 v[64:79], v[108:111], v[116:119], v[64:79]\nds_read_b128 v[108:111], v125 offset:6240\ns_cmp_eq_u32 %[hi], 0\ns_cbranch_scc0 15f\ns_setprio 1\n"
        "15:\nv_exp_f32 v43, v43\nv_exp_f32 v44, v44\nv_exp_f32 v45, v45\nv_exp_f32 v46, v46\nv_exp_f32 v47, v47\nv_cvt_pk_bf16_f32 v120, v40, v41\n"
        "v_cvt_pk_bf16_f32 v121, v42, v43\nv_cvt_pk_bf16_f32 v122, v44, v45\nv_cvt_pk_bf16_f32 v123, v46, v47\nv_mfma_f32_32x32x16_bf16 v[32:47], v[104:107], v[96:99], v[80:95]\n"
        "ds_read_b128 v[104:107], v127 offset:4160\nv_exp_f32 v48, v48\nv_exp_f32 v49, v49\nv_exp_f32 v50, v50\nv_exp_f32 v51, v51\nv_exp_f32 v52, v52\nv_exp_f32 v53, v53\n"
        "v_mfma_f32_32x32x16_bf16 v[64:79], v[112:115], v[120:123], v[64:79]\nds_read_b128 v[112:115], v30 offset:0\nv_exp_f32 v54, v54\nv_exp_f32 v55, v55\n"
        "v_cvt_pk_bf16_f32 v116, v48, v49\nv_cvt_pk_bf16_f32 v117, v50, v51\nv_cvt_pk_bf16_f32 v118, v52, v53\nv_cvt_pk_bf16_f32 v119, v54, v55\nv_exp_f32 v56, v56\n"
        "v_exp_f32 v57, v57\nv_exp_f32 v58, v58\ns_waitcnt lgkmcnt(2)\nv_mfma_f32_32x32x16_bf16 v[64:79], v[108:111], v[116:119], v[64:79]\nds_read_b128 v[108:111], v31 offset:0\n"
        "s_cmp_eq_u32 %[hi], 0\ns_cbranch_scc0 14f\ns_setprio 3\n14:\nv_exp_f32 v59, v59\nv_exp_f32 v60, v60\nv_exp_f32 v61, v61\nv_exp_f32 v62, v62\nv_exp_f32 v63, v63\n"
        "v_cvt_pk_bf16_f32 v120, v56, v57\nv_cvt_pk_bf16_f32 v121, v58, v59\nv_cvt_pk_bf16_f32 v122, v60, v61\nv_cvt_pk_bf16_f32 v123, v62, v63\ns_cmp_gt_u32 %[c], 7\n"
        "s_cbranch_scc1 12f\ns_waitcnt vmcnt(0) lgkmcnt(0)\ns_barrier\ns_cmp_eq_u32 %[c], 7\ns_cbranch_scc1 12f\ns_add_u32 m0, %[sd], %[kd]\ns_add_u32 %[sd], %[sd], 8320\n"
        "global_load_lds_dwordx4 %[vo], %[src]\ns_cmp_eq_u32 %[sd], 74880\ns_cselect_b32 %[sd], 0, %[sd]\nv_add_u32 %[vo], 0x2000, %[vo]\ns_cmp_eq_u32 %[c], 6\ns_cbranch_scc1 12f\n"
        "s_add_u32 m0, %[sd], %[kd]\ns_add_u32 %[sd], %[sd], 8320\nglobal_load_lds_dwordx4 %[vo], %[src]\ns_cmp_eq_u32 %[sd], 74880\ns_cselect_b32 %[sd], 0, %[sd]\n"
        "v_add_u32 %[vo], 0x2000, %[vo]\n12:\ns_add_u32 %[c], %[c], 1\ns_mov_b32 %[so], %[sn]\ns_add_u32 %[sn], %[so], 8320\ns_cmp_eq_u32 %[so], 66560\n"
        "s_cselect_b32 %[sn], 0, %[sn]\nv_mfma_f32_32x32x16_bf16 v[48:63], v[100:103], v[96:99], v[80:95]\nds_read_b128 v[100:103], v127 offset:6240\nv_exp_f32 v32, v32\n"
        "v_exp_f32 v33, v33\nv_exp_f32 v34, v34\nv_exp_f32 v35, v35\nv_exp_f32 v36, v36\nv_exp_f32 v37, v37\nv_mfma_f32_32x32x16_bf16 v[64:79], v[24:27], v[120:123], v[64:79]\n"
        "ds_read_b128 v[24:27], v30 offset:2080\nv_exp_f32 v38, v38\nv_exp_f32 v39, v39\nv_cvt_pk_bf16_f32 v116, v32, v33\nv_cvt_pk_bf16_f32 v117, v34, v35\n"
        "v_cvt_pk_bf16_f32 v118, v36, v37\nv_cvt_pk_bf16_f32 v119, v38, v39\nv_exp_f32 v40, v40\nv_exp_f32 v41, v41\nv_exp_f32 v42, v42\ns_waitcnt lgkmcnt(2)\n"
        "v_mfma_f32_32x32x16_bf16 v[64:79], v[108:111], v[116:119], v[64:79]\nds_read_b128 v[108:111], v31 offset:2080\nv_add_u32 v124, %[sn], %[bk]\nv_add_u32 v125, %[sn], %[b0]\n"
        "v_add_u32 v126, %[sn], %[b1]\nv_exp_f32 v43, v43\nv_exp_f32 v44, v44\nv_exp_f32 v45, v45\nv_exp_f32 v46, v46\nv_exp_f32 v47, v47\nv_cvt_pk_bf16_f32 v120, v40, v41\n"
        "v_cvt_pk_bf16_f32 v121, v42, v43\nv_cvt_pk_bf16_f32 v122, v44, v45\nv_cvt_pk_bf16_f32 v123, v46, v47\nv_mfma_f32_32x32x16_bf16 v[32:47], v[104:107], v[96:99], v[80:95]\n"
        "ds_read_b128 v[104:107], v124 offset:0\nv_exp_f32 v48, v48\nv_exp_f32 v49, v49\nv_exp_f32 v50, v50\nv_exp_f32 v51, v51\nv_exp_f32 v52, v52\nv_exp_f32 v53, v53\n"
        "v_mfma_f32_32x32x16_bf16 v[64:79], v[112:115], v[120:123], v[64:79]\nds_read_b128 v[112:115], v30 offset:4160\nv_exp_f32 v54, v54\nv_exp_f32 v55, v55\n"
        "v_cvt_pk_bf16_f32 v116, v48, v49\nv_cvt_pk_bf16_f32 v117, v50, v51\nv_cvt_pk_bf16_f32 v118, v52, v53\nv_cvt_pk_bf16_f32 v119, v54, v55\nv_exp_f32 v56, v56\n"
        "v_exp_f32 v57, v57\nv_exp_f32 v58, v58\ns_waitcnt lgkmcnt(2)\nv_mfma_f32_32x32x16_bf16 v[64:79], v[108:111], v[116:119], v[64:79]\n"
        "ds_read_b128 v[108:111], v31 offset:4160\nv_exp_f32 v59, v59\nv_exp_f32 v60, v60\nv_exp_f32 v61, v61\nv_exp_f32 v62, v62\nv_exp_f32 v63, v63\n"
        "v_cvt_pk_bf16_f32 v120, v56, v57\nv_cvt_pk_bf16_f32 v121, v58, v59\nv_cvt_pk_bf16_f32 v122, v60, v61\nv_cvt_pk_bf16_f32 v123, v62, v63\n"
        "v_mfma_f32_32x32x16_bf16 v[48:63], v[100:103], v[96:99], v[80:95]\nds_read_b128 v[100:103], v124 offset:2080\nv_exp_f32 v32, v32\nv_exp_f32 v33, v33\nv_exp_f32 v34, v34\n"
        "v_exp_f32 v35, v35\nv_exp_f32 v36, v36\nv_exp_f32 v37, v37\nv_mfma_f32_32x32x16_bf16 v[64:79], v[24:27], v[120:123], v[64:79]\nds_read_b128 v[24:27], v30 offset:6240\n"
        "v_exp_f32 v38, v38\nv_exp_f32 v39, v39\nv_cvt_pk_bf16_f32 v116, v32, v33\nv_cvt_pk_bf16_f32 v117, v34, v35\nv_cvt_pk_bf16_f32 v118, v36, v37\n"
        "v_cvt_pk_bf16_f32 v119, v38, v39\nv_exp_f32 v40, v40\nv_exp_f32 v41, v41\nv_exp_f32 v42, v42\ns_waitcnt lgkmcnt(2)\n"
        "v_mfma_f32_32x32x16_bf16 v[64:79], v[108:111], v[116:119], v[64:79]\nds_read_b128 v[108:111], v31 offset:6240\ns_cmp_eq_u32 %[hi], 0\ns_cbranch_scc0 25f\ns_setprio 1\n25:\n"
        "v_exp_f32 v43, v43\nv_exp_f32 v44, v44\nv_exp_f32 v45, v45\nv_exp_f32 v46, v46\nv_exp_f32 v47, v47\nv_cvt_pk_bf16_f32 v120, v40, v41\nv_cvt_pk_bf16_f32 v121, v42, v43\n"
        "v_cvt_pk_bf16_f32 v122, v44, v45\nv_cvt_pk_bf16_f32 v123, v46, v47\nv_mfma_f32_32x32x16_bf16 v[32:47], v[104:107], v[96:99], v[80:95]\n"
        "ds_read_b128 v[104:107], v124 offset:4160\nv_exp_f32 v48, v48\nv_exp_f32 v49, v49\nv_exp_f32 v50, v50\nv_exp_f32 v51, v51\nv_exp_f32 v52, v52\nv_exp_f32 v53, v53\n"
        "v_mfma_f32_32x32x16_bf16 v[64:79], v[112:115], v[120:123], v[64:79]\nds_read_b128 v[112:115], v126 offset:0\nv_exp_f32 v54, v54\nv_exp_f32 v55, v55\n"
        "v_cvt_pk_bf16_f32 v116, v48, v49\nv_cvt_pk_bf16_f32 v117, v50, v51\nv_cvt_pk_bf16_f32 v118, v52, v53\nv_cvt_pk_bf16_f32 v119, v54, v55\nv_exp_f32 v56, v56\n"
        "v_exp_f32 v57, v57\nv_exp_f32 v58, v58\ns_waitcnt lgkmcnt(2)\nv_mfma_f32_32x32x16_bf16 v[64:79], v[108:111], v[116:119], v[64:79]\nds_read_b128 v[108:111], v125 offset:0\n"
        "s_cmp_eq_u32 %[hi], 0\ns_cbranch_scc0 24f\ns_setprio 3\n24:\nv_exp_f32 v59, v59\nv_exp_f32 v60, v60\nv_exp_f32 v61, v61\nv_exp_f32 v62, v62\nv_exp_f32 v63, v63\n"
        "v_cvt_pk_bf16_f32 v120, v56, v57\nv_cvt_pk_bf16_f32 v121, v58, v59\nv_cvt_pk_bf16_f32 v122, v60, v61\nv_cvt_pk_bf16_f32 v123, v62, v63\ns_cmp_gt_u32 %[c], 7\n"
        "s_cbranch_scc1 22f\ns_waitcnt vmcnt(0) lgkmcnt(0)\ns_barrier\ns_cmp_eq_u32 %[c], 7\ns_cbranch_scc1 22f\ns_add_u32 m0, %[sd], %[kd]\ns_add_u32 %[sd], %[sd], 8320\n"
        "global_load_lds_dwordx4 %[vo], %[src]\ns_cmp_eq_u32 %[sd], 74880\ns_cselect_b32 %[sd], 0, %[sd]\nv_add_u32 %[vo], 0x2000, %[vo]\ns_cmp_eq_u32 %[c], 6\ns_cbranch_scc1 22f\n"
        "s_add_u32 m0, %[sd], %[kd]\ns_add_u32 %[sd], %[sd], 8320\nglobal_load_lds_dwordx4 %[vo], %[src]\ns_cmp_eq_u32 %[sd], 74880\ns_cselect_b32 %[sd], 0, %[sd]\n"
        "v_add_u32 %[vo], 0x2000, %[vo]\n22:\ns_add_u32 %[c], %[c], 1\ns_mov_b32 %[so], %[sn]\ns_cmp_lt_u32 %[c], 16\ns_cbranch_scc1 9b\ns_waitcnt lgkmcnt(0)\n"
        "v_mfma_f32_32x32x16_bf16 v[64:79], v[24:27], v[120:123], v[64:79]\ns_mov_b32 m0, %[sv]\ns_nop 7\ns_nop 7\n"
        : "+{v[32:47]}"(sA), "+{v[64:79]}"(o), "+{v[100:103]}"(k0), "+{v[104:107]}"(k1), "+{v[108:111]}"(v0), "+{v[112:115]}"(v1), "+{v[24:27]}"(vb), "+{v[120:123]}"(pb),
          "+{v124}"(aK), "+{v125}"(a0), "+{v126}"(a1), [so] "+s"(so), [sd] "+s"(sd), [sn] "=&s"(sn), [c] "+s"(c), [sv] "=&s"(sv), [vo] "+v"(vo), [hi] "+s"(hi)
        : "{v[80:95]}"(ns), "{v[96:99]}"(qf), [bk] "v"(bk), [b0] "v"(b0), [b1] "v"(b1), [kd] "s"(kvd), [src] "s"(kvs)
        : "v48", "v49", "v50", "v51", "v52", "v53", "v54", "v55", "v56", "v57", "v58", "v59", "v60", "v61", "v62", "v63", "v116", "v117", "v118", "v119", "v127", "v30", "v31", "scc", "memory"
 );

      PRIO(3); }
    TS(b, 3);
    int m_; asm volatile("v_mbcnt_lo_u32_b32 %0, -1, 0\n\tv_mbcnt_hi_u32_b32 %0, -1, %0" : "=v"(m_));
    const unsigned m16 = m_ * 16;

    float bad = o[0];
#pragma unroll
    for (int i = 8; i < 16; ++i) bad = fmaxf(bad, fabsf(o[i]));
    if (__builtin_amdgcn_ballot_w64(!(bad < 1.0e33f)) != 0ull || __builtin_amdgcn_ballot_w64(!(o[0] > 0.f)) != 0ull) {
        const char* kv = ws + WS_KV; float m = -3.0e38f; const f32x16 z = splat16(0.f);
#pragma unroll 1
        for (int u = 0; u < 64; ++u) m = max16(MFMA(*(const bf16x8*)(kv + u * 2048 + m16), qf, z), m);
        ns = splat16(-xhalf_max(m)); o = z;
        const u32x4 one = {0x3F803F80u, 0x3F803F80u, 0x3F803F80u, 0x3F803F80u}; const int vo = 1024 + (m_ >> 5) * 256 + (m_ & 15) * 16;
#pragma unroll 1
        for (int u = 0; u < 64; ++u) { f32x16 s = MFMA(*(const bf16x8*)(kv + u * 2048 + m16), qf, ns);
            u32x4 v0 = *(const u32x4*)(kv + u * 2048 + vo), v1 = *(const u32x4*)(kv + u * 2048 + vo + 512);
            if (!(m_ & 16)) { v0 = one; v1 = one; }
#pragma unroll
            for (int i = 0; i < 16; ++i) s[i] = __builtin_amdgcn_exp2f(s[i]);
            o = MFMA(__builtin_bit_cast(bf16x8, v0), pack8(s, 0), o); o = MFMA(__builtin_bit_cast(bf16x8, v1), pack8(s, 8), o); } }
    const float inv = 1.f / o[0];
#pragma unroll
    for (int i = 8; i < 16; ++i) o[i] *= inv;
    st_sc1(m16, ws + WS_O + (TT * 32 + 4 * hg + kq) * 1024, pack8(o, 8));
    BAR(0);
    if (w == 0 && m_ == 0) __hip_atomic_store((GAS unsigned*)(ws + WS_FL + (tg * 8 + hg) * 128), 1u, RLX_AGENT);
    TS(b, 4);
}

DI void ffn_role(int b, const lptr L, unsigned L0, const char* x, const char* wo, const char* wg, const char* wu, const char* wd, char* out, char* ws) {
    const int tid = threadIdx.x, l = tid & 63, w = __builtin_amdgcn_readfirstlane(tid >> 6), h = l >> 5;
    const int xcd = b & 7, i5 = b >> 3, q = (l >> 2) & 3, p = l & 3; const unsigned l16 = l * 16;
    PRIO(2);
    TS(256 + b, 0);
    if (i5 < 28) {
        const int TB = (xcd & 1) * 4 + (i5 & 3), fb = (xcd >> 1) * 7 + (i5 >> 2), ft = w & 1, tk = w >> 1;
        const unsigned vw = (l >> 3) * 3584 + (((l & 7) ^ (((l >> 4) & 1) << 2)) << 4);
        const char* sw = ((w & 4) ? wu : wg) + (w & 3) * 8 * 3584 + fb * 128;
        const char* sx = ws + WS_XN + (TB * 8 + w) * 32768; const unsigned dw = L0 + w * 1024, dx = L0 + 8192 + w * 2048;
#define F1_ISSUE(st) do { glds16(vw, sw + (st) * 114688, dw + ((st) % 3) * 24576); glds16(l16, sx + (st) * 2048, dx + ((st) % 3) * 24576); glds16(l16, sx + (st) * 2048 + 1024, dx + ((st) % 3) * 24576 + 1024); } while (0)
        F1_ISSUE(0); F1_ISSUE(1); F1_ISSUE(2);
        unsigned jk = 0, sv; const unsigned tv = (i5 * 512 + tid) * 128;
        const unsigned pa = L0 + (((8 * h + q) * 128 + (((((l >> 4) & 1) * 2 + (p >> 1)) ^ ((q >> 1) << 2)) << 4) + 8 * (p & 1)) ^ (ft * 64)), pb = L0 + 8192 + tk * 4096 + l16;
        f32x16 g0, g1, u0, u1;
        PRIO(P_LOOP);
        asm volatile(
        "s_mov_b32 %[sv], m0\ns_mov_b64 s[40:41], %[sw]\ns_mov_b64 s[42:43], %[sx]\ns_waitcnt vmcnt(6)\ns_barrier\nds_read_b64_tr_b16 v[96:97], %[pa] offset:0\n"
        "ds_read_b64_tr_b16 v[98:99], %[pa] offset:512\nds_read_b128 v[104:107], %[pb] offset:0\nds_read_b64_tr_b16 v[100:101], %[pa] offset:4096\n"
        "ds_read_b64_tr_b16 v[102:103], %[pa] offset:4608\nds_read_b128 v[108:111], %[pb] offset:2048\ns_waitcnt lgkmcnt(0)\n"
        "v_mfma_f32_32x32x16_bf16 v[32:47], v[96:99], v[104:107], 0\nds_read_b64_tr_b16 v[112:113], %[pa] offset:2048\nds_read_b64_tr_b16 v[114:115], %[pa] offset:2560\n"
        "v_mfma_f32_32x32x16_bf16 v[48:63], v[96:99], v[108:111], 0\nds_read_b128 v[120:123], %[pb] offset:1024\nds_read_b64_tr_b16 v[116:117], %[pa] offset:6144\n"
        "v_mfma_f32_32x32x16_bf16 v[64:79], v[100:103], v[104:107], 0\nds_read_b64_tr_b16 v[118:119], %[pa] offset:6656\nds_read_b128 v[124:127], %[pb] offset:3072\n"
        "v_mfma_f32_32x32x16_bf16 v[80:95], v[100:103], v[108:111], 0\ns_waitcnt vmcnt(3) lgkmcnt(0)\ns_barrier\n"
        "v_mfma_f32_32x32x16_bf16 v[32:47], v[112:115], v[120:123], v[32:47]\ns_add_u32 m0, %[dw], 0\ns_add_u32 s40, s40, 0x1c000\ns_addc_u32 s41, s41, 0\n"
        "global_load_lds_dwordx4 %[vw], s[40:41]\nds_read_b64_tr_b16 v[96:97], %[pa] offset:24576\nds_read_b64_tr_b16 v[98:99], %[pa] offset:25088\n"
        "v_mfma_f32_32x32x16_bf16 v[48:63], v[112:115], v[124:127], v[48:63]\ns_add_u32 m0, %[dx], 0\ns_add_u32 s42, s42, 0x800\ns_addc_u32 s43, s43, 0\n"
        "global_load_lds_dwordx4 %[vx], s[42:43]\nds_read_b128 v[104:107], %[pb] offset:24576\nds_read_b64_tr_b16 v[100:101], %[pa] offset:28672\n"
        "v_mfma_f32_32x32x16_bf16 v[64:79], v[116:119], v[120:123], v[64:79]\nglobal_load_lds_dwordx4 %[vx], s[42:43] offset:1024\n"
        "ds_read_b64_tr_b16 v[102:103], %[pa] offset:29184\nds_read_b128 v[108:111], %[pb] offset:26624\nv_mfma_f32_32x32x16_bf16 v[80:95], v[116:119], v[124:127], v[80:95]\n"
        "s_waitcnt lgkmcnt(0)\nv_mfma_f32_32x32x16_bf16 v[32:47], v[96:99], v[104:107], v[32:47]\nds_read_b64_tr_b16 v[112:113], %[pa] offset:26624\n"
        "ds_read_b64_tr_b16 v[114:115], %[pa] offset:27136\nv_mfma_f32_32x32x16_bf16 v[48:63], v[96:99], v[108:111], v[48:63]\nds_read_b128 v[120:123], %[pb] offset:25600\n"
        "ds_read_b64_tr_b16 v[116:117], %[pa] offset:30720\nv_mfma_f32_32x32x16_bf16 v[64:79], v[100:103], v[104:107], v[64:79]\nds_read_b64_tr_b16 v[118:119], %[pa] offset:31232\n"
        "ds_read_b128 v[124:127], %[pb] offset:27648\nv_mfma_f32_32x32x16_bf16 v[80:95], v[100:103], v[108:111], v[80:95]\ns_waitcnt vmcnt(3) lgkmcnt(0)\ns_barrier\n"
        "v_mfma_f32_32x32x16_bf16 v[32:47], v[112:115], v[120:123], v[32:47]\ns_add_u32 m0, %[dw], 24576\ns_add_u32 s40, s40, 0x1c000\ns_addc_u32 s41, s41, 0\n"
        "global_load_lds_dwordx4 %[vw], s[40:41]\nds_read_b64_tr_b16 v[96:97], %[pa] offset:49152\nds_read_b64_tr_b16 v[98:99], %[pa] offset:49664\n"
        "v_mfma_f32_32x32x16_bf16 v[48:63], v[112:115], v[124:127], v[48:63]\ns_add_u32 m0, %[dx], 24576\ns_add_u32 s42, s42, 0x800\ns_addc_u32 s43, s43, 0\n"
        "global_load_lds_dwordx4 %[vx], s[42:43]\nds_read_b128 v[104:107], %[pb] offset:49152\nds_read_b64_tr_b16 v[100:101], %[pa] offset:53248\n"
        "v_mfma_f32_32x32x16_bf16 v[64:79], v[116:119], v[120:123], v[64:79]\nglobal_load_lds_dwordx4 %[vx], s[42:43] offset:1024\n"
        "ds_read_b64_tr_b16 v[102:103], %[pa] offset:53760\nds_read_b128 v[108:111], %[pb] offset:51200\nv_mfma_f32_32x32x16_bf16 v[80:95], v[116:119], v[124:127], v[80:95]\n"
        "s_waitcnt lgkmcnt(0)\nv_mfma_f32_32x32x16_bf16 v[32:47], v[96:99], v[104:107], v[32:47]\nds_read_b64_tr_b16 v[112:113], %[pa] offset:51200\n"
        "ds_read_b64_tr_b16 v[114:115], %[pa] offset:51712\nv_mfma_f32_32x32x16_bf16 v[48:63], v[96:99], v[108:111], v[48:63]\nds_read_b128 v[120:123], %[pb] offset:50176\n"
        "ds_read_b64_tr_b16 v[116:117], %[pa] offset:55296\nv_mfma_f32_32x32x16_bf16 v[64:79], v[100:103], v[104:107], v[64:79]\nds_read_b64_tr_b16 v[118:119], %[pa] offset:55808\n"
        "ds_read_b128 v[124:127], %[pb] offset:52224\nv_mfma_f32_32x32x16_bf16 v[80:95], v[100:103], v[108:111], v[80:95]\ns_waitcnt vmcnt(3) lgkmcnt(0)\ns_barrier\n"
        "v_mfma_f32_32x32x16_bf16 v[32:47], v[112:115], v[120:123], v[32:47]\ns_add_u32 m0, %[dw], 49152\ns_add_u32 s40, s40, 0x1c000\ns_addc_u32 s41, s41, 0\n"
        "global_load_lds_dwordx4 %[vw], s[40:41]\nds_read_b64_tr_b16 v[96:97], %[pa] offset:0\nds_read_b64_tr_b16 v[98:99], %[pa] offset:512\n"
        "v_mfma_f32_32x32x16_bf16 v[48:63], v[112:115], v[124:127], v[48:63]\ns_add_u32 m0, %[dx], 49152\ns_add_u32 s42, s42, 0x800\ns_addc_u32 s43, s43, 0\n"
        "global_load_lds_dwordx4 %[vx], s[42:43]\nds_read_b128 v[104:107], %[pb] offset:0\nds_read_b64_tr_b16 v[100:101], %[pa] offset:4096\n"
        "v_mfma_f32_32x32x16_bf16 v[64:79], v[116:119], v[120:123], v[64:79]\nglobal_load_lds_dwordx4 %[vx], s[42:43] offset:1024\nglobal_load_dword %[jk], %[tv], %[wd]\n"
        "ds_read_b64_tr_b16 v[102:103], %[pa] offset:4608\nds_read_b128 v[108:111], %[pb] offset:2048\nv_mfma_f32_32x32x16_bf16 v[80:95], v[116:119], v[124:127], v[80:95]\n"
        "s_waitcnt lgkmcnt(0)\nv_mfma_f32_32x32x16_bf16 v[32:47], v[96:99], v[104:107], v[32:47]\nds_read_b64_tr_b16 v[112:113], %[pa] offset:2048\n"
        "ds_read_b64_tr_b16 v[114:115], %[pa] offset:2560\nv_mfma_f32_32x32x16_bf16 v[48:63], v[96:99], v[108:111], v[48:63]\nds_read_b128 v[120:123], %[pb] offset:1024\n"
        "ds_read_b64_tr_b16 v[116:117], %[pa] offset:6144\nv_mfma_f32_32x32x16_bf16 v[64:79], v[100:103], v[104:107], v[64:79]\nds_read_b64_tr_b16 v[118:119], %[pa] offset:6656\n"
        "ds_read_b128 v[124:127], %[pb] offset:3072\nv_mfma_f32_32x32x16_bf16 v[80:95], v[100:103], v[108:111], v[80:95]\ns_waitcnt vmcnt(4) lgkmcnt(0)\ns_barrier\n"
        "v_mfma_f32_32x32x16_bf16 v[32:47], v[112:115], v[120:123], v[32:47]\ns_add_u32 m0, %[dw], 0\ns_add_u32 s40, s40, 0x1c000\ns_addc_u32 s41, s41, 0\n"
        "global_load_lds_dwordx4 %[vw], s[40:41]\nds_read_b64_tr_b16 v[96:97], %[pa] offset:24576\nds_read_b64_tr_b16 v[98:99], %[pa] offset:25088\n"
        "v_mfma_f32_32x32x16_bf16 v[48:63], v[112:115], v[124:127], v[48:63]\ns_add_u32 m0, %[dx], 0\ns_add_u32 s42, s42, 0x800\ns_addc_u32 s43, s43, 0\n"
        "global_load_lds_dwordx4 %[vx], s[42:43]\nds_read_b128 v[104:107], %[pb] offset:24576\nds_read_b64_tr_b16 v[100:101], %[pa] offset:28672\n"
        "v_mfma_f32_32x32x16_bf16 v[64:79], v[116:119], v[120:123], v[64:79]\nglobal_load_lds_dwordx4 %[vx], s[42:43] offset:1024\n"
        "ds_read_b64_tr_b16 v[102:103], %[pa] offset:29184\nds_read_b128 v[108:111], %[pb] offset:26624\nv_mfma_f32_32x32x16_bf16 v[80:95], v[116:119], v[124:127], v[80:95]\n"
        "s_waitcnt lgkmcnt(0)\nv_mfma_f32_32x32x16_bf16 v[32:47], v[96:99], v[104:107], v[32:47]\nds_read_b64_tr_b16 v[112:113], %[pa] offset:26624\n"
        "ds_read_b64_tr_b16 v[114:115], %[pa] offset:27136\nv_mfma_f32_32x32x16_bf16 v[48:63], v[96:99], v[108:111], v[48:63]\nds_read_b128 v[120:123], %[pb] offset:25600\n"
        "ds_read_b64_tr_b16 v[116:117], %[pa] offset:30720\nv_mfma_f32_32x32x16_bf16 v[64:79], v[100:103], v[104:107], v[64:79]\nds_read_b64_tr_b16 v[118:119], %[pa] offset:31232\n"
        "ds_read_b128 v[124:127], %[pb] offset:27648\nv_mfma_f32_32x32x16_bf16 v[80:95], v[100:103], v[108:111], v[80:95]\ns_waitcnt vmcnt(4) lgkmcnt(0)\ns_barrier\n"
        "v_mfma_f32_32x32x16_bf16 v[32:47], v[112:115], v[120:123], v[32:47]\ns_add_u32 m0, %[dw], 24576\ns_add_u32 s40, s40, 0x1c000\ns_addc_u32 s41, s41, 0\n"
        "global_load_lds_dwordx4 %[vw], s[40:41]\nds_read_b64_tr_b16 v[96:97], %[pa] offset:49152\nds_read_b64_tr_b16 v[98:99], %[pa] offset:49664\n"
        "v_mfma_f32_32x32x16_bf16 v[48:63], v[112:115], v[124:127], v[48:63]\ns_add_u32 m0, %[dx], 24576\ns_add_u32 s42, s42, 0x800\ns_addc_u32 s43, s43, 0\n"
        "global_load_lds_dwordx4 %[vx], s[42:43]\nds_read_b128 v[104:107], %[pb] offset:49152\nds_read_b64_tr_b16 v[100:101], %[pa] offset:53248\n"
        "v_mfma_f32_32x32x16_bf16 v[64:79], v[116:119], v[120:123], v[64:79]\nglobal_load_lds_dwordx4 %[vx], s[42:43] offset:1024\n"
        "ds_read_b64_tr_b16 v[102:103], %[pa] offset:53760\nds_read_b128 v[108:111], %[pb] offset:51200\nv_mfma_f32_32x32x16_bf16 v[80:95], v[116:119], v[124:127], v[80:95]\n"
        "s_waitcnt lgkmcnt(0)\nv_mfma_f32_32x32x16_bf16 v[32:47], v[96:99], v[104:107], v[32:47]\nds_read_b64_tr_b16 v[112:113], %[pa] offset:51200\n"
        "ds_read_b64_tr_b16 v[114:115], %[pa] offset:51712\nv_mfma_f32_32x32x16_bf16 v[48:63], v[96:99], v[108:111], v[48:63]\nds_read_b128 v[120:123], %[pb] offset:50176\n"
        "ds_read_b64_tr_b16 v[116:117], %[pa] offset:55296\nv_mfma_f32_32x32x16_bf16 v[64:79], v[100:103], v[104:107], v[64:79]\nds_read_b64_tr_b16 v[118:119], %[pa] offset:55808\n"
        "ds_read_b128 v[124:127], %[pb] offset:52224\nv_mfma_f32_32x32x16_bf16 v[80:95], v[100:103], v[108:111], v[80:95]\ns_waitcnt vmcnt(3) lgkmcnt(0)\ns_barrier\n"
        "v_mfma_f32_32x32x16_bf16 v[32:47], v[112:115], v[120:123], v[32:47]\ns_add_u32 m0, %[dw], 49152\ns_add_u32 s40, s40, 0x1c000\ns_addc_u32 s41, s41, 0\n"
        "global_load_lds_dwordx4 %[vw], s[40:41]\nds_read_b64_tr_b16 v[96:97], %[pa] offset:0\nds_read_b64_tr_b16 v[98:99], %[pa] offset:512\n"
        "v_mfma_f32_32x32x16_bf16 v[48:63], v[112:115], v[124:127], v[48:63]\ns_add_u32 m0, %[dx], 49152\ns_add_u32 s42, s42, 0x800\ns_addc_u32 s43, s43, 0\n"
        "global_load_lds_dwordx4 %[vx], s[42:43]\nds_read_b128 v[104:107], %[pb] offset:0\nds_read_b64_tr_b16 v[100:101], %[pa] offset:4096\n"
        "v_mfma_f32_32x32x16_bf16 v[64:79], v[116:119], v[120:123], v[64:79]\nglobal_load_lds_dwordx4 %[vx], s[42:43] offset:1024\nds_read_b64_tr_b16 v[102:103], %[pa] offset:4608\n"
        "ds_read_b128 v[108:111], %[pb] offset:2048\nv_mfma_f32_32x32x16_bf16 v[80:95], v[116:119], v[124:127], v[80:95]\ns_waitcnt lgkmcnt(0)\n"
        "v_mfma_f32_32x32x16_bf16 v[32:47], v[96:99], v[104:107], v[32:47]\nds_read_b64_tr_b16 v[112:113], %[pa] offset:2048\nds_read_b64_tr_b16 v[114:115], %[pa] offset:2560\n"
        "v_mfma_f32_32x32x16_bf16 v[48:63], v[96:99], v[108:111], v[48:63]\nds_read_b128 v[120:123], %[pb] offset:1024\nds_read_b64_tr_b16 v[116:117], %[pa] offset:6144\n"
        "v_mfma_f32_32x32x16_bf16 v[64:79], v[100:103], v[104:107], v[64:79]\nds_read_b64_tr_b16 v[118:119], %[pa] offset:6656\nds_read_b128 v[124:127], %[pb] offset:3072\n"
        "v_mfma_f32_32x32x16_bf16 v[80:95], v[100:103], v[108:111], v[80:95]\ns_waitcnt vmcnt(3) lgkmcnt(0)\ns_barrier\n"
        "v_mfma_f32_32x32x16_bf16 v[32:47], v[112:115], v[120:123], v[32:47]\ns_add_u32 m0, %[dw], 0\ns_add_u32 s40, s40, 0x1c000\ns_addc_u32 s41, s41, 0\n"
        "global_load_lds_dwordx4 %[vw], s[40:41]\nds_read_b64_tr_b16 v[96:97], %[pa] offset:24576\nds_read_b64_tr_b16 v[98:99], %[pa] offset:25088\n"
        "v_mfma_f32_32x32x16_bf16 v[48:63], v[112:115], v[124:127], v[48:63]\ns_add_u32 m0, %[dx], 0\ns_add_u32 s42, s42, 0x800\ns_addc_u32 s43, s43, 0\n"
        "global_load_lds_dwordx4 %[vx], s[42:43]\nds_read_b128 v[104:107], %[pb] offset:24576\nds_read_b64_tr_b16 v[100:101], %[pa] offset:28672\n"
        "v_mfma_f32_32x32x16_bf16 v[64:79], v[116:119], v[120:123], v[64:79]\nglobal_load_lds_dwordx4 %[vx], s[42:43] offset:1024\n"
        "ds_read_b64_tr_b16 v[102:103], %[pa] offset:29184\nds_read_b128 v[108:111], %[pb] offset:26624\nv_mfma_f32_32x32x16_bf16 v[80:95], v[116:119], v[124:127], v[80:95]\n"
        "s_waitcnt lgkmcnt(0)\nv_mfma_f32_32x32x16_bf16 v[32:47], v[96:99], v[104:107], v[32:47]\nds_read_b64_tr_b16 v[112:113], %[pa] offset:26624\n"
        "ds_read_b64_tr_b16 v[114:115], %[pa] offset:27136\nv_mfma_f32_32x32x16_bf16 v[48:63], v[96:99], v[108:111], v[48:63]\nds_read_b128 v[120:123], %[pb] offset:25600\n"
        "ds_read_b64_tr_b16 v[116:117], %[pa] offset:30720\nv_mfma_f32_32x32x16_bf16 v[64:79], v[100:103], v[104:107], v[64:79]\nds_read_b64_tr_b16 v[118:119], %[pa] offset:31232\n"
        "ds_read_b128 v[124:127], %[pb] offset:27648\nv_mfma_f32_32x32x16_bf16 v[80:95], v[100:103], v[108:111], v[80:95]\ns_waitcnt vmcnt(3) lgkmcnt(0)\ns_barrier\n"
        "v_mfma_f32_32x32x16_bf16 v[32:47], v[112:115], v[120:123], v[32:47]\ns_add_u32 m0, %[dw], 24576\ns_add_u32 s40, s40, 0x1c000\ns_addc_u32 s41, s41, 0\n"
        "global_load_lds_dwordx4 %[vw], s[40:41]\nds_read_b64_tr_b16 v[96:97], %[pa] offset:49152\nds_read_b64_tr_b16 v[98:99], %[pa] offset:49664\n"
        "v_mfma_f32_32x32x16_bf16 v[48:63], v[112:115], v[124:127], v[48:63]\ns_add_u32 m0, %[dx], 24576\ns_add_u32 s42, s42, 0x800\ns_addc_u32 s43, s43, 0\n"
        "global_load_lds_dwordx4 %[vx], s[42:43]\nds_read_b128 v[104:107], %[pb] offset:49152\nds_read_b64_tr_b16 v[100:101], %[pa] offset:53248\n"
        "v_mfma_f32_32x32x16_bf16 v[64:79], v[116:119], v[120:123], v[64:79]\nglobal_load_lds_dwordx4 %[vx], s[42:43] offset:1024\n"
        "ds_read_b64_tr_b16 v[102:103], %[pa] offset:53760\nds_read_b128 v[108:111], %[pb] offset:51200\nv_mfma_f32_32x32x16_bf16 v[80:95], v[116:119], v[124:127], v[80:95]\n"
        "s_waitcnt lgkmcnt(0)\nv_mfma_f32_32x32x16_bf16 v[32:47], v[96:99], v[104:107], v[32:47]\nds_read_b64_tr_b16 v[112:113], %[pa] offset:51200\n"
        "ds_read_b64_tr_b16 v[114:115], %[pa] offset:51712\nv_mfma_f32_32x32x16_bf16 v[48:63], v[96:99], v[108:111], v[48:63]\nds_read_b128 v[120:123], %[pb] offset:50176\n"
        "ds_read_b64_tr_b16 v[116:117], %[pa] offset:55296\nv_mfma_f32_32x32x16_bf16 v[64:79], v[100:103], v[104:107], v[64:79]\nds_read_b64_tr_b16 v[118:119], %[pa] offset:55808\n"
        "ds_read_b128 v[124:127], %[pb] offset:52224\nv_mfma_f32_32x32x16_bf16 v[80:95], v[100:103], v[108:111], v[80:95]\ns_waitcnt vmcnt(3) lgkmcnt(0)\ns_barrier\n"
        "v_mfma_f32_32x32x16_bf16 v[32:47], v[112:115], v[120:123], v[32:47]\ns_add_u32 m0, %[dw], 49152\ns_add_u32 s40, s40, 0x1c000\ns_addc_u32 s41, s41, 0\n"
        "global_load_lds_dwordx4 %[vw], s[40:41]\nds_read_b64_tr_b16 v[96:97], %[pa] offset:0\nds_read_b64_tr_b16 v[98:99], %[pa] offset:512\n"
        "v_mfma_f32_32x32x16_bf16 v[48:63], v[112:115], v[124:127], v[48:63]\ns_add_u32 m0, %[dx], 49152\ns_add_u32 s42, s42, 0x800\ns_addc_u32 s43, s43, 0\n"
        "global_load_lds_dwordx4 %[vx], s[42:43]\nds_read_b128 v[104:107], %[pb] offset:0\nds_read_b64_tr_b16 v[100:101], %[pa] offset:4096\n"
        "v_mfma_f32_32x32x16_bf16 v[64:79], v[116:119], v[120:123], v[64:79]\nglobal_load_lds_dwordx4 %[vx], s[42:43] offset:1024\nds_read_b64_tr_b16 v[102:103], %[pa] offset:4608\n"
        "ds_read_b128 v[108:111], %[pb] offset:2048\nv_mfma_f32_32x32x16_bf16 v[80:95], v[116:119], v[124:127], v[80:95]\ns_waitcnt lgkmcnt(0)\n"
        "v_mfma_f32_32x32x16_bf16 v[32:47], v[96:99], v[104:107], v[32:47]\nds_read_b64_tr_b16 v[112:113], %[pa] offset:2048\nds_read_b64_tr_b16 v[114:115], %[pa] offset:2560\n"
        "v_mfma_f32_32x32x16_bf16 v[48:63], v[96:99], v[108:111], v[48:63]\nds_read_b128 v[120:123], %[pb] offset:1024\nds_read_b64_tr_b16 v[116:117], %[pa] offset:6144\n"
        "v_mfma_f32_32x32x16_bf16 v[64:79], v[100:103], v[104:107], v[64:79]\nds_read_b64_tr_b16 v[118:119], %[pa] offset:6656\nds_read_b128 v[124:127], %[pb] offset:3072\n"
        "v_mfma_f32_32x32x16_bf16 v[80:95], v[100:103], v[108:111], v[80:95]\ns_waitcnt vmcnt(3) lgkmcnt(0)\ns_barrier\n"
        "v_mfma_f32_32x32x16_bf16 v[32:47], v[112:115], v[120:123], v[32:47]\ns_add_u32 m0, %[dw], 0\ns_add_u32 s40, s40, 0x1c000\ns_addc_u32 s41, s41, 0\n"
        "global_load_lds_dwordx4 %[vw], s[40:41]\nds_read_b64_tr_b16 v[96:97], %[pa] offset:24576\nds_read_b64_tr_b16 v[98:99], %[pa] offset:25088\n"
        "v_mfma_f32_32x32x16_bf16 v[48:63], v[112:115], v[124:127], v[48:63]\ns_add_u32 m0, %[dx], 0\ns_add_u32 s42, s42, 0x800\ns_addc_u32 s43, s43, 0\n"
        "global_load_lds_dwordx4 %[vx], s[42:43]\nds_read_b128 v[104:107], %[pb] offset:24576\nds_read_b64_tr_b16 v[100:101], %[pa] offset:28672\n"
        "v_mfma_f32_32x32x16_bf16 v[64:79], v[116:119], v[120:123], v[64:79]\nglobal_load_lds_dwordx4 %[vx], s[42:43] offset:1024\n"
        "ds_read_b64_tr_b16 v[102:103], %[pa] offset:29184\nds_read_b128 v[108:111], %[pb] offset:26624\nv_mfma_f32_32x32x16_bf16 v[80:95], v[116:119], v[124:127], v[80:95]\n"
        "s_waitcnt lgkmcnt(0)\nv_mfma_f32_32x32x16_bf16 v[32:47], v[96:99], v[104:107], v[32:47]\nds_read_b64_tr_b16 v[112:113], %[pa] offset:26624\n"
        "ds_read_b64_tr_b16 v[114:115], %[pa] offset:27136\nv_mfma_f32_32x32x16_bf16 v[48:63], v[96:99], v[108:111], v[48:63]\nds_read_b128 v[120:123], %[pb] offset:25600\n"
        "ds_read_b64_tr_b16 v[116:117], %[pa] offset:30720\nv_mfma_f32_32x32x16_bf16 v[64:79], v[100:103], v[104:107], v[64:79]\nds_read_b64_tr_b16 v[118:119], %[pa] offset:31232\n"
        "ds_read_b128 v[124:127], %[pb] offset:27648\nv_mfma_f32_32x32x16_bf16 v[80:95], v[100:103], v[108:111], v[80:95]\ns_waitcnt vmcnt(3) lgkmcnt(0)\ns_barrier\n"
        "v_mfma_f32_32x32x16_bf16 v[32:47], v[112:115], v[120:123], v[32:47]\ns_add_u32 m0, %[dw], 24576\ns_add_u32 s40, s40, 0x1c000\ns_addc_u32 s41, s41, 0\n"
        "global_load_lds_dwordx4 %[vw], s[40:41]\nds_read_b64_tr_b16 v[96:97], %[pa] offset:49152\nds_read_b64_tr_b16 v[98:99], %[pa] offset:49664\n"
        "v_mfma_f32_32x32x16_bf16 v[48:63], v[112:115], v[124:127], v[48:63]\ns_add_u32 m0, %[dx], 24576\ns_add_u32 s42, s42, 0x800\ns_addc_u32 s43, s43, 0\n"
        "global_load_lds_dwordx4 %[vx], s[42:43]\nds_read_b128 v[104:107], %[pb] offset:49152\nds_read_b64_tr_b16 v[100:101], %[pa] offset:53248\n"
        "v_mfma_f32_32x32x16_bf16 v[64:79], v[116:119], v[120:123], v[64:79]\nglobal_load_lds_dwordx4 %[vx], s[42:43] offset:1024\n"
        "ds_read_b64_tr_b16 v[102:103], %[pa] offset:53760\nds_read_b128 v[108:111], %[pb] offset:51200\nv_mfma_f32_32x32x16_bf16 v[80:95], v[116:119], v[124:127], v[80:95]\n"
        "s_waitcnt lgkmcnt(0)\nv_mfma_f32_32x32x16_bf16 v[32:47], v[96:99], v[104:107], v[32:47]\nds_read_b64_tr_b16 v[112:113], %[pa] offset:51200\n"
        "ds_read_b64_tr_b16 v[114:115], %[pa] offset:51712\nv_mfma_f32_32x32x16_bf16 v[48:63], v[96:99], v[108:111], v[48:63]\nds_read_b128 v[120:123], %[pb] offset:50176\n"
        "ds_read_b64_tr_b16 v[116:117], %[pa] offset:55296\nv_mfma_f32_32x32x16_bf16 v[64:79], v[100:103], v[104:107], v[64:79]\nds_read_b64_tr_b16 v[118:119], %[pa] offset:55808\n"
        "ds_read_b128 v[124:127], %[pb] offset:52224\nv_mfma_f32_32x32x16_bf16 v[80:95], v[100:103], v[108:111], v[80:95]\ns_waitcnt vmcnt(3) lgkmcnt(0)\ns_barrier\n"
        "v_mfma_f32_32x32x16_bf16 v[32:47], v[112:115], v[120:123], v[32:47]\ns_add_u32 m0, %[dw], 49152\ns_add_u32 s40, s40, 0x1c000\ns_addc_u32 s41, s41, 0\n"
        "global_load_lds_dwordx4 %[vw], s[40:41]\nds_read_b64_tr_b16 v[96:97], %[pa] offset:0\nds_read_b64_tr_b16 v[98:99], %[pa] offset:512\n"
        "v_mfma_f32_32x32x16_bf16 v[48:63], v[112:115], v[124:127], v[48:63]\ns_add_u32 m0, %[dx], 49152\ns_add_u32 s42, s42, 0x800\ns_addc_u32 s43, s43, 0\n"
        "global_load_lds_dwordx4 %[vx], s[42:43]\nds_read_b128 v[104:107], %[pb] offset:0\nds_read_b64_tr_b16 v[100:101], %[pa] offset:4096\n"
        "v_mfma_f32_32x32x16_bf16 v[64:79], v[116:119], v[120:123], v[64:79]\nglobal_load_lds_dwordx4 %[vx], s[42:43] offset:1024\nds_read_b64_tr_b16 v[102:103], %[pa] offset:4608\n"
        "ds_read_b128 v[108:111], %[pb] offset:2048\nv_mfma_f32_32x32x16_bf16 v[80:95], v[116:119], v[124:127], v[80:95]\ns_waitcnt lgkmcnt(0)\n"
        "v_mfma_f32_32x32x16_bf16 v[32:47], v[96:99], v[104:107], v[32:47]\nds_read_b64_tr_b16 v[112:113], %[pa] offset:2048\nds_read_b64_tr_b16 v[114:115], %[pa] offset:2560\n"
        "v_mfma_f32_32x32x16_bf16 v[48:63], v[96:99], v[108:111], v[48:63]\nds_read_b128 v[120:123], %[pb] offset:1024\nds_read_b64_tr_b16 v[116:117], %[pa] offset:6144\n"
        "v_mfma_f32_32x32x16_bf16 v[64:79], v[100:103], v[104:107], v[64:79]\nds_read_b64_tr_b16 v[118:119], %[pa] offset:6656\nds_read_b128 v[124:127], %[pb] offset:3072\n"
        "v_mfma_f32_32x32x16_bf16 v[80:95], v[100:103], v[108:111], v[80:95]\ns_waitcnt vmcnt(3) lgkmcnt(0)\ns_barrier\n"
        "v_mfma_f32_32x32x16_bf16 v[32:47], v[112:115], v[120:123], v[32:47]\ns_add_u32 m0, %[dw], 0\ns_add_u32 s40, s40, 0x1c000\ns_addc_u32 s41, s41, 0\n"
        "global_load_lds_dwordx4 %[vw], s[40:41]\nds_read_b64_tr_b16 v[96:97], %[pa] offset:24576\nds_read_b64_tr_b16 v[98:99], %[pa] offset:25088\n"
        "v_mfma_f32_32x32x16_bf16 v[48:63], v[112:115], v[124:127], v[48:63]\ns_add_u32 m0, %[dx], 0\ns_add_u32 s42, s42, 0x800\ns_addc_u32 s43, s43, 0\n"
        "global_load_lds_dwordx4 %[vx], s[42:43]\nds_read_b128 v[104:107], %[pb] offset:24576\nds_read_b64_tr_b16 v[100:101], %[pa] offset:28672\n"
        "v_mfma_f32_32x32x16_bf16 v[64:79], v[116:119], v[120:123], v[64:79]\nglobal_load_lds_dwordx4 %[vx], s[42:43] offset:1024\n"
        "ds_read_b64_tr_b16 v[102:103], %[pa] offset:29184\nds_read_b128 v[108:111], %[pb] offset:26624\nv_mfma_f32_32x32x16_bf16 v[80:95], v[116:119], v[124:127], v[80:95]\n"
        "s_waitcnt lgkmcnt(0)\nv_mfma_f32_32x32x16_bf16 v[32:47], v[96:99], v[104:107], v[32:47]\nds_read_b64_tr_b16 v[112:113], %[pa] offset:26624\n"
        "ds_read_b64_tr_b16 v[114:115], %[pa] offset:27136\nv_mfma_f32_32x32x16_bf16 v[48:63], v[96:99], v[108:111], v[48:63]\nds_read_b128 v[120:123], %[pb] offset:25600\n"
        "ds_read_b64_tr_b16 v[116:117], %[pa] offset:30720\nv_mfma_f32_32x32x16_bf16 v[64:79], v[100:103], v[104:107], v[64:79]\nds_read_b64_tr_b16 v[118:119], %[pa] offset:31232\n"
        "ds_read_b128 v[124:127], %[pb] offset:27648\nv_mfma_f32_32x32x16_bf16 v[80:95], v[100:103], v[108:111], v[80:95]\ns_waitcnt vmcnt(3) lgkmcnt(0)\ns_barrier\n"
        "v_mfma_f32_32x32x16_bf16 v[32:47], v[112:115], v[120:123], v[32:47]\nds_read_b64_tr_b16 v[96:97], %[pa] offset:49152\nds_read_b64_tr_b16 v[98:99], %[pa] offset:49664\n"
        "v_mfma_f32_32x32x16_bf16 v[48:63], v[112:115], v[124:127], v[48:63]\nds_read_b128 v[104:107], %[pb] offset:49152\nds_read_b64_tr_b16 v[100:101], %[pa] offset:53248\n"
        "v_mfma_f32_32x32x16_bf16 v[64:79], v[116:119], v[120:123], v[64:79]\nds_read_b64_tr_b16 v[102:103], %[pa] offset:53760\nds_read_b128 v[108:111], %[pb] offset:51200\n"
        "v_mfma_f32_32x32x16_bf16 v[80:95], v[116:119], v[124:127], v[80:95]\ns_waitcnt lgkmcnt(0)\nv_mfma_f32_32x32x16_bf16 v[32:47], v[96:99], v[104:107], v[32:47]\n"
        "ds_read_b64_tr_b16 v[112:113], %[pa] offset:51200\nds_read_b64_tr_b16 v[114:115], %[pa] offset:51712\nv_mfma_f32_32x32x16_bf16 v[48:63], v[96:99], v[108:111], v[48:63]\n"
        "ds_read_b128 v[120:123], %[pb] offset:50176\nds_read_b64_tr_b16 v[116:117], %[pa] offset:55296\nv_mfma_f32_32x32x16_bf16 v[64:79], v[100:103], v[104:107], v[64:79]\n"
        "ds_read_b64_tr_b16 v[118:119], %[pa] offset:55808\nds_read_b128 v[124:127], %[pb] offset:52224\nv_mfma_f32_32x32x16_bf16 v[80:95], v[100:103], v[108:111], v[80:95]\n"
        "s_waitcnt vmcnt(0) lgkmcnt(0)\ns_barrier\nv_mfma_f32_32x32x16_bf16 v[32:47], v[112:115], v[120:123], v[32:47]\nds_read_b64_tr_b16 v[96:97], %[pa] offset:0\n"
        "ds_read_b64_tr_b16 v[98:99], %[pa] offset:512\nv_mfma_f32_32x32x16_bf16 v[48:63], v[112:115], v[124:127], v[48:63]\nds_read_b128 v[104:107], %[pb] offset:0\n"
        "ds_read_b64_tr_b16 v[100:101], %[pa] offset:4096\nv_mfma_f32_32x32x16_bf16 v[64:79], v[116:119], v[120:123], v[64:79]\nds_read_b64_tr_b16 v[102:103], %[pa] offset:4608\n"
        "ds_read_b128 v[108:111], %[pb] offset:2048\nv_mfma_f32_32x32x16_bf16 v[80:95], v[116:119], v[124:127], v[80:95]\ns_waitcnt lgkmcnt(0)\n"
        "v_mfma_f32_32x32x16_bf16 v[32:47], v[96:99], v[104:107], v[32:47]\nds_read_b64_tr_b16 v[112:113], %[pa] offset:2048\nds_read_b64_tr_b16 v[114:115], %[pa] offset:2560\n"
        "v_mfma_f32_32x32x16_bf16 v[48:63], v[96:99], v[108:111], v[48:63]\nds_read_b128 v[120:123], %[pb] offset:1024\nds_read_b64_tr_b16 v[116:117], %[pa] offset:6144\n"
        "v_mfma_f32_32x32x16_bf16 v[64:79], v[100:103], v[104:107], v[64:79]\nds_read_b64_tr_b16 v[118:119], %[pa] offset:6656\nds_read_b128 v[124:127], %[pb] offset:3072\n"
        "v_mfma_f32_32x32x16_bf16 v[80:95], v[100:103], v[108:111], v[80:95]\ns_waitcnt lgkmcnt(0)\nv_mfma_f32_32x32x16_bf16 v[32:47], v[112:115], v[120:123], v[32:47]\n"
        "v_mfma_f32_32x32x16_bf16 v[48:63], v[112:115], v[124:127], v[48:63]\nv_mfma_f32_32x32x16_bf16 v[64:79], v[116:119], v[120:123], v[64:79]\n"
        "v_mfma_f32_32x32x16_bf16 v[80:95], v[116:119], v[124:127], v[80:95]\ns_mov_b32 m0, %[sv]\ns_nop 7\ns_nop 7\n"
          : "=&{v[32:47]}"(g0), "=&{v[48:63]}"(g1), "=&{v[64:79]}"(u0), "=&{v[80:95]}"(u1), [jk] "+v"(jk), [sv] "=&s"(sv)
          : [pa] "v"(pa), [pb] "v"(pb), [vw] "v"(vw), [vx] "v"(l16), [tv] "v"(tv), [dw] "s"(dw), [dx] "s"(dx), [sw] "s"(sw + 2 * 114688), [sx] "s"(sx + 2 * 2048), [wd] "s"(wd)
          : "v96", "v97", "v98", "v99", "v100", "v101", "v102", "v103", "v104", "v105", "v106", "v107", "v108", "v109", "v110", "v111", "v112", "v113", "v114", "v115", "v116", "v117", "v118", "v119", "v120", "v121", "v122", "v123", "v124", "v125", "v126", "v127", "s40", "s41", "s42", "s43", "scc", "memory");
        PRIO(2);
#pragma unroll
        for (int i = 0; i < 16; ++i) { g0[i] = fmaxf(g0[i], 0.f) * u0[i]; g1[i] = fmaxf(g1[i], 0.f) * u1[i]; }
        TS(256 + b, 1);
        char* hp = ws + WS_H + ((TB * 8 + 2 * tk) * 112 + fb * 4 + ft * 2) * 1024;
        st_sc1(l16, hp, pack8(g0, 0)); st_sc1(l16, hp + 1024, pack8(g0, 8)); st_sc1(l16, hp + 114688, pack8(g1, 0)); st_sc1(l16, hp + 115712, pack8(g1, 8));
        BAR(0);
        if (tid == 0) __hip_atomic_fetch_add((GAS unsigned*)(ws + WS_FL + (FL_H + TB) * 128), 1u, RLX_AGENT);
    } else {
        const int j = i5 - 28; unsigned jk = 0;
        __builtin_amdgcn_s_sleep(127);
        touch((j * 1024 + tid) * 128, wo); TOUCH_END(); touch((j * 1024 + 512 + tid) * 128, wo); TOUCH_END();
    }
    TS(256 + b, 2);
    const int tg = xcd * 4 + (i5 & 3), cg = i5 >> 2, t = w & 1, kq = w >> 1;
    const unsigned vo = (l >> 3) * 1024 + (((l & 7) ^ (((l >> 4) & 1) << 2)) << 4);
    const int at = (4 * h + q) * 128 + (((((l >> 4) & 1) * 2 + (p >> 1)) ^ ((q >> 1) << 2)) << 4) + 8 * (p & 1);
    float gg[8];
    {   const char* sw = wd + cg * 128 + w * 14 * 16384; const unsigned dw = L0 + w * 8192;
        const int r0 = 2 * cg - 14;
#define ROT(n) ((n) + r0 < 0 ? (n) + r0 + 14 : (n) + r0)
#define F2_W(n) do { const char* s_ = sw + ROT(n) * 16384; glds16(vo, s_, dw + ((n) & 3) * 2048); glds16(vo, s_ + 8192, dw + ((n) & 3) * 2048 + 1024); } while (0)
        F2_W(0); F2_W(1); F2_W(2); F2_W(3);
        { GAS unsigned* f = (GAS unsigned*)(ws + WS_FL + (FL_H + xcd) * 128);
          for (unsigned sp = 0; sp < SPIN_CAP; ++sp) { if (__builtin_amdgcn_readfirstlane(__hip_atomic_load(f, RLX_AGENT)) >= 28u) break; __builtin_amdgcn_s_sleep(4); } }
        TS(256 + b, 3);
        const char* sh = uni(ws + WS_H + (2 * tg * 112 + w * 14) * 1024);
        f32x16 d00, d01, d10, d11; unsigned ro = ROT(0) * 1024, sv;
        PRIO(P_LOOP2);
        asm volatile(
        "s_mov_b32 %[sv], m0\ns_mov_b64 s[44:45], %[sh]\ns_mov_b64 s[46:47], %[sw]\ns_add_u32 s40, s44, %[ro]\ns_addc_u32 s41, s45, 0\n"
        "global_load_dwordx4 v[112:115], %[h0], s[40:41] sc1\nglobal_load_dwordx4 v[116:119], %[h1], s[40:41] sc1\ns_add_u32 %[ro], %[ro], 1024\ns_cmp_eq_u32 %[ro], 14336\n"
        "s_cselect_b32 %[ro], 0, %[ro]\ns_add_u32 s40, s44, %[ro]\ns_addc_u32 s41, s45, 0\nglobal_load_dwordx4 v[120:123], %[h0], s[40:41] sc1\n"
        "global_load_dwordx4 v[124:127], %[h1], s[40:41] sc1\ns_add_u32 %[ro], %[ro], 1024\ns_cmp_eq_u32 %[ro], 14336\ns_cselect_b32 %[ro], 0, %[ro]\ns_add_u32 s40, s44, %[ro]\n"
        "s_addc_u32 s41, s45, 0\nglobal_load_dwordx4 v[16:19], %[h0], s[40:41] sc1\nglobal_load_dwordx4 v[20:23], %[h1], s[40:41] sc1\ns_add_u32 %[ro], %[ro], 1024\n"
        "s_cmp_eq_u32 %[ro], 14336\ns_cselect_b32 %[ro], 0, %[ro]\ns_add_u32 s40, s44, %[ro]\ns_addc_u32 s41, s45, 0\nglobal_load_dwordx4 v[24:27], %[h0], s[40:41] sc1\n"
        "global_load_dwordx4 v[28:31], %[h1], s[40:41] sc1\ns_add_u32 %[ro], %[ro], 1024\ns_cmp_eq_u32 %[ro], 14336\ns_cselect_b32 %[ro], 0, %[ro]\ns_waitcnt vmcnt(8)\n"
        "ds_read_b64_tr_b16 v[96:97], %[a0] offset:0\nds_read_b64_tr_b16 v[98:99], %[a0] offset:1024\nds_read_b64_tr_b16 v[100:101], %[a1] offset:0\n"
        "ds_read_b64_tr_b16 v[102:103], %[a1] offset:1024\ns_waitcnt vmcnt(6) lgkmcnt(0)\nv_mfma_f32_32x32x16_bf16 v[32:47], v[104:107], v[112:115], 0\n"
        "ds_read_b64_tr_b16 v[104:105], %[a0] offset:2048\nds_read_b64_tr_b16 v[106:107], %[a0] offset:3072\ns_lshl_b32 s42, %[ro], 4\ns_add_u32 s42, s46, s42\n"
        "s_addc_u32 s43, s47, 0\ns_add_u32 m0, %[dw], 0\nv_mfma_f32_32x32x16_bf16 v[48:63], v[108:111], v[112:115], 0\nds_read_b64_tr_b16 v[108:109], %[a1] offset:2048\n"
        "ds_read_b64_tr_b16 v[110:111], %[a1] offset:3072\nglobal_load_lds_dwordx4 %[w0], s[42:43]\nv_mfma_f32_32x32x16_bf16 v[64:79], v[104:107], v[116:119], 0\n"
        "global_load_lds_dwordx4 %[w1], s[42:43] offset:1024\nv_mfma_f32_32x32x16_bf16 v[80:95], v[108:111], v[116:119], 0\ns_add_u32 s40, s44, %[ro]\ns_addc_u32 s41, s45, 0\n"
        "global_load_dwordx4 v[112:115], %[h0], s[40:41] sc1\nglobal_load_dwordx4 v[116:119], %[h1], s[40:41] sc1\ns_add_u32 %[ro], %[ro], 1024\ns_cmp_eq_u32 %[ro], 14336\n"
        "s_cselect_b32 %[ro], 0, %[ro]\ns_waitcnt vmcnt(8) lgkmcnt(0)\nv_mfma_f32_32x32x16_bf16 v[32:47], v[96:99], v[120:123], v[32:47]\n"
        "ds_read_b64_tr_b16 v[96:97], %[a0] offset:4096\nds_read_b64_tr_b16 v[98:99], %[a0] offset:5120\ns_lshl_b32 s42, %[ro], 4\ns_add_u32 s42, s46, s42\ns_addc_u32 s43, s47, 0\n"
        "s_add_u32 m0, %[dw], 2048\nv_mfma_f32_32x32x16_bf16 v[48:63], v[100:103], v[120:123], v[48:63]\nds_read_b64_tr_b16 v[100:101], %[a1] offset:4096\n"
        "ds_read_b64_tr_b16 v[102:103], %[a1] offset:5120\nglobal_load_lds_dwordx4 %[w0], s[42:43]\nv_mfma_f32_32x32x16_bf16 v[64:79], v[96:99], v[124:127], v[64:79]\n"
        "global_load_lds_dwordx4 %[w1], s[42:43] offset:1024\nv_mfma_f32_32x32x16_bf16 v[80:95], v[100:103], v[124:127], v[80:95]\ns_add_u32 s40, s44, %[ro]\n"
        "s_addc_u32 s41, s45, 0\nglobal_load_dwordx4 v[120:123], %[h0], s[40:41] sc1\nglobal_load_dwordx4 v[124:127], %[h1], s[40:41] sc1\ns_add_u32 %[ro], %[ro], 1024\n"
        "s_cmp_eq_u32 %[ro], 14336\ns_cselect_b32 %[ro], 0, %[ro]\ns_waitcnt vmcnt(10) lgkmcnt(0)\nv_mfma_f32_32x32x16_bf16 v[32:47], v[104:107], v[16:19], v[32:47]\n"
        "ds_read_b64_tr_b16 v[104:105], %[a0] offset:6144\nds_read_b64_tr_b16 v[106:107], %[a0] offset:7168\ns_lshl_b32 s42, %[ro], 4\ns_add_u32 s42, s46, s42\n"
        "s_addc_u32 s43, s47, 0\ns_add_u32 m0, %[dw], 4096\nv_mfma_f32_32x32x16_bf16 v[48:63], v[108:111], v[16:19], v[48:63]\nds_read_b64_tr_b16 v[108:109], %[a1] offset:6144\n"
        "ds_read_b64_tr_b16 v[110:111], %[a1] offset:7168\nglobal_load_lds_dwordx4 %[w0], s[42:43]\nv_mfma_f32_32x32x16_bf16 v[64:79], v[104:107], v[20:23], v[64:79]\n"
        "global_load_lds_dwordx4 %[w1], s[42:43] offset:1024\nv_mfma_f32_32x32x16_bf16 v[80:95], v[108:111], v[20:23], v[80:95]\ns_add_u32 s40, s44, %[ro]\ns_addc_u32 s41, s45, 0\n"
        "global_load_dwordx4 v[16:19], %[h0], s[40:41] sc1\nglobal_load_dwordx4 v[20:23], %[h1], s[40:41] sc1\ns_add_u32 %[ro], %[ro], 1024\ns_cmp_eq_u32 %[ro], 14336\n"
        "s_cselect_b32 %[ro], 0, %[ro]\ns_waitcnt vmcnt(10) lgkmcnt(0)\nv_mfma_f32_32x32x16_bf16 v[32:47], v[96:99], v[24:27], v[32:47]\n"
        "ds_read_b64_tr_b16 v[96:97], %[a0] offset:0\nds_read_b64_tr_b16 v[98:99], %[a0] offset:1024\ns_lshl_b32 s42, %[ro], 4\ns_add_u32 s42, s46, s42\ns_addc_u32 s43, s47, 0\n"
        "s_add_u32 m0, %[dw], 6144\nv_mfma_f32_32x32x16_bf16 v[48:63], v[100:103], v[24:27], v[48:63]\nds_read_b64_tr_b16 v[100:101], %[a1] offset:0\n"
        "ds_read_b64_tr_b16 v[102:103], %[a1] offset:1024\nglobal_load_lds_dwordx4 %[w0], s[42:43]\nv_mfma_f32_32x32x16_bf16 v[64:79], v[96:99], v[28:31], v[64:79]\n"
        "global_load_lds_dwordx4 %[w1], s[42:43] offset:1024\nv_mfma_f32_32x32x16_bf16 v[80:95], v[100:103], v[28:31], v[80:95]\ns_add_u32 s40, s44, %[ro]\ns_addc_u32 s41, s45, 0\n"
        "global_load_dwordx4 v[24:27], %[h0], s[40:41] sc1\nglobal_load_dwordx4 v[28:31], %[h1], s[40:41] sc1\ns_add_u32 %[ro], %[ro], 1024\ns_cmp_eq_u32 %[ro], 14336\n"
        "s_cselect_b32 %[ro], 0, %[ro]\ns_waitcnt vmcnt(10) lgkmcnt(0)\nv_mfma_f32_32x32x16_bf16 v[32:47], v[104:107], v[112:115], v[32:47]\n"
        "ds_read_b64_tr_b16 v[104:105], %[a0] offset:2048\nds_read_b64_tr_b16 v[106:107], %[a0] offset:3072\ns_lshl_b32 s42, %[ro], 4\ns_add_u32 s42, s46, s42\n"
        "s_addc_u32 s43, s47, 0\ns_add_u32 m0, %[dw], 0\nv_mfma_f32_32x32x16_bf16 v[48:63], v[108:111], v[112:115], v[48:63]\nds_read_b64_tr_b16 v[108:109], %[a1] offset:2048\n"
        "ds_read_b64_tr_b16 v[110:111], %[a1] offset:3072\nglobal_load_lds_dwordx4 %[w0], s[42:43]\nv_mfma_f32_32x32x16_bf16 v[64:79], v[104:107], v[116:119], v[64:79]\n"
        "global_load_lds_dwordx4 %[w1], s[42:43] offset:1024\nv_mfma_f32_32x32x16_bf16 v[80:95], v[108:111], v[116:119], v[80:95]\ns_add_u32 s40, s44, %[ro]\n"
        "s_addc_u32 s41, s45, 0\nglobal_load_dwordx4 v[112:115], %[h0], s[40:41] sc1\nglobal_load_dwordx4 v[116:119], %[h1], s[40:41] sc1\ns_add_u32 %[ro], %[ro], 1024\n"
        "s_cmp_eq_u32 %[ro], 14336\ns_cselect_b32 %[ro], 0, %[ro]\ns_waitcnt vmcnt(10) lgkmcnt(0)\nv_mfma_f32_32x32x16_bf16 v[32:47], v[96:99], v[120:123], v[32:47]\n"
        "ds_read_b64_tr_b16 v[96:97], %[a0] offset:4096\nds_read_b64_tr_b16 v[98:99], %[a0] offset:5120\ns_lshl_b32 s42, %[ro], 4\ns_add_u32 s42, s46, s42\ns_addc_u32 s43, s47, 0\n"
        "s_add_u32 m0, %[dw], 2048\nv_mfma_f32_32x32x16_bf16 v[48:63], v[100:103], v[120:123], v[48:63]\nds_read_b64_tr_b16 v[100:101], %[a1] offset:4096\n"
        "ds_read_b64_tr_b16 v[102:103], %[a1] offset:5120\nglobal_load_lds_dwordx4 %[w0], s[42:43]\nv_mfma_f32_32x32x16_bf16 v[64:79], v[96:99], v[124:127], v[64:79]\n"
        "global_load_lds_dwordx4 %[w1], s[42:43] offset:1024\nv_mfma_f32_32x32x16_bf16 v[80:95], v[100:103], v[124:127], v[80:95]\ns_add_u32 s40, s44, %[ro]\n"
        "s_addc_u32 s41, s45, 0\nglobal_load_dwordx4 v[120:123], %[h0], s[40:41] sc1\nglobal_load_dwordx4 v[124:127], %[h1], s[40:41] sc1\ns_add_u32 %[ro], %[ro], 1024\n"
        "s_cmp_eq_u32 %[ro], 14336\ns_cselect_b32 %[ro], 0, %[ro]\ns_waitcnt vmcnt(10) lgkmcnt(0)\nv_mfma_f32_32x32x16_bf16 v[32:47], v[104:107], v[16:19], v[32:47]\n"
        "ds_read_b64_tr_b16 v[104:105], %[a0] offset:6144\nds_read_b64_tr_b16 v[106:107], %[a0] offset:7168\ns_lshl_b32 s42, %[ro], 4\ns_add_u32 s42, s46, s42\n"
        "s_addc_u32 s43, s47, 0\ns_add_u32 m0, %[dw], 4096\nv_mfma_f32_32x32x16_bf16 v[48:63], v[108:111], v[16:19], v[48:63]\nds_read_b64_tr_b16 v[108:109], %[a1] offset:6144\n"
        "ds_read_b64_tr_b16 v[110:111], %[a1] offset:7168\nglobal_load_lds_dwordx4 %[w0], s[42:43]\nv_mfma_f32_32x32x16_bf16 v[64:79], v[104:107], v[20:23], v[64:79]\n"
        "global_load_lds_dwordx4 %[w1], s[42:43] offset:1024\nv_mfma_f32_32x32x16_bf16 v[80:95], v[108:111], v[20:23], v[80:95]\ns_add_u32 s40, s44, %[ro]\ns_addc_u32 s41, s45, 0\n"
        "global_load_dwordx4 v[16:19], %[h0], s[40:41] sc1\nglobal_load_dwordx4 v[20:23], %[h1], s[40:41] sc1\ns_add_u32 %[ro], %[ro], 1024\ns_cmp_eq_u32 %[ro], 14336\n"
        "s_cselect_b32 %[ro], 0, %[ro]\ns_waitcnt vmcnt(10) lgkmcnt(0)\nv_mfma_f32_32x32x16_bf16 v[32:47], v[96:99], v[24:27], v[32:47]\n"
        "ds_read_b64_tr_b16 v[96:97], %[a0] offset:0\nds_read_b64_tr_b16 v[98:99], %[a0] offset:1024\ns_lshl_b32 s42, %[ro], 4\ns_add_u32 s42, s46, s42\ns_addc_u32 s43, s47, 0\n"
        "s_add_u32 m0, %[dw], 6144\nv_mfma_f32_32x32x16_bf16 v[48:63], v[100:103], v[24:27], v[48:63]\nds_read_b64_tr_b16 v[100:101], %[a1] offset:0\n"
        "ds_read_b64_tr_b16 v[102:103], %[a1] offset:1024\nglobal_load_lds_dwordx4 %[w0], s[42:43]\nv_mfma_f32_32x32x16_bf16 v[64:79], v[96:99], v[28:31], v[64:79]\n"
        "global_load_lds_dwordx4 %[w1], s[42:43] offset:1024\nv_mfma_f32_32x32x16_bf16 v[80:95], v[100:103], v[28:31], v[80:95]\ns_add_u32 s40, s44, %[ro]\ns_addc_u32 s41, s45, 0\n"
        "global_load_dwordx4 v[24:27], %[h0], s[40:41] sc1\nglobal_load_dwordx4 v[28:31], %[h1], s[40:41] sc1\ns_add_u32 %[ro], %[ro], 1024\ns_cmp_eq_u32 %[ro], 14336\n"
        "s_cselect_b32 %[ro], 0, %[ro]\ns_waitcnt vmcnt(10) lgkmcnt(0)\nv_mfma_f32_32x32x16_bf16 v[32:47], v[104:107], v[112:115], v[32:47]\n"
        "ds_read_b64_tr_b16 v[104:105], %[a0] offset:2048\nds_read_b64_tr_b16 v[106:107], %[a0] offset:3072\ns_lshl_b32 s42, %[ro], 4\ns_add_u32 s42, s46, s42\n"
        "s_addc_u32 s43, s47, 0\ns_add_u32 m0, %[dw], 0\nv_mfma_f32_32x32x16_bf16 v[48:63], v[108:111], v[112:115], v[48:63]\nds_read_b64_tr_b16 v[108:109], %[a1] offset:2048\n"
        "ds_read_b64_tr_b16 v[110:111], %[a1] offset:3072\nglobal_load_lds_dwordx4 %[w0], s[42:43]\nv_mfma_f32_32x32x16_bf16 v[64:79], v[104:107], v[116:119], v[64:79]\n"
        "global_load_lds_dwordx4 %[w1], s[42:43] offset:1024\nv_mfma_f32_32x32x16_bf16 v[80:95], v[108:111], v[116:119], v[80:95]\ns_add_u32 s40, s44, %[ro]\n"
        "s_addc_u32 s41, s45, 0\nglobal_load_dwordx4 v[112:115], %[h0], s[40:41] sc1\nglobal_load_dwordx4 v[116:119], %[h1], s[40:41] sc1\ns_add_u32 %[ro], %[ro], 1024\n"
        "s_cmp_eq_u32 %[ro], 14336\ns_cselect_b32 %[ro], 0, %[ro]\ns_waitcnt vmcnt(10) lgkmcnt(0)\nv_mfma_f32_32x32x16_bf16 v[32:47], v[96:99], v[120:123], v[32:47]\n"
        "ds_read_b64_tr_b16 v[96:97], %[a0] offset:4096\nds_read_b64_tr_b16 v[98:99], %[a0] offset:5120\ns_lshl_b32 s42, %[ro], 4\ns_add_u32 s42, s46, s42\ns_addc_u32 s43, s47, 0\n"
        "s_add_u32 m0, %[dw], 2048\nv_mfma_f32_32x32x16_bf16 v[48:63], v[100:103], v[120:123], v[48:63]\nds_read_b64_tr_b16 v[100:101], %[a1] offset:4096\n"
        "ds_read_b64_tr_b16 v[102:103], %[a1] offset:5120\nglobal_load_lds_dwordx4 %[w0], s[42:43]\nv_mfma_f32_32x32x16_bf16 v[64:79], v[96:99], v[124:127], v[64:79]\n"
        "global_load_lds_dwordx4 %[w1], s[42:43] offset:1024\nv_mfma_f32_32x32x16_bf16 v[80:95], v[100:103], v[124:127], v[80:95]\ns_add_u32 s40, s44, %[ro]\n"
        "s_addc_u32 s41, s45, 0\nglobal_load_dwordx4 v[120:123], %[h0], s[40:41] sc1\nglobal_load_dwordx4 v[124:127], %[h1], s[40:41] sc1\ns_add_u32 %[ro], %[ro], 1024\n"
        "s_cmp_eq_u32 %[ro], 14336\ns_cselect_b32 %[ro], 0, %[ro]\ns_waitcnt vmcnt(10) lgkmcnt(0)\nv_mfma_f32_32x32x16_bf16 v[32:47], v[104:107], v[16:19], v[32:47]\n"
        "ds_read_b64_tr_b16 v[104:105], %[a0] offset:6144\nds_read_b64_tr_b16 v[106:107], %[a0] offset:7168\nv_mfma_f32_32x32x16_bf16 v[48:63], v[108:111], v[16:19], v[48:63]\n"
        "ds_read_b64_tr_b16 v[108:109], %[a1] offset:6144\nds_read_b64_tr_b16 v[110:111], %[a1] offset:7168\nv_mfma_f32_32x32x16_bf16 v[64:79], v[104:107], v[20:23], v[64:79]\n"
        "v_mfma_f32_32x32x16_bf16 v[80:95], v[108:111], v[20:23], v[80:95]\ns_waitcnt vmcnt(6) lgkmcnt(0)\nv_mfma_f32_32x32x16_bf16 v[32:47], v[96:99], v[24:27], v[32:47]\n"
        "ds_read_b64_tr_b16 v[96:97], %[a0] offset:0\nds_read_b64_tr_b16 v[98:99], %[a0] offset:1024\nv_mfma_f32_32x32x16_bf16 v[48:63], v[100:103], v[24:27], v[48:63]\n"
        "ds_read_b64_tr_b16 v[100:101], %[a1] offset:0\nds_read_b64_tr_b16 v[102:103], %[a1] offset:1024\nv_mfma_f32_32x32x16_bf16 v[64:79], v[96:99], v[28:31], v[64:79]\n"
        "v_mfma_f32_32x32x16_bf16 v[80:95], v[100:103], v[28:31], v[80:95]\ns_waitcnt vmcnt(2) lgkmcnt(0)\nv_mfma_f32_32x32x16_bf16 v[32:47], v[104:107], v[112:115], v[32:47]\n"
        "ds_read_b64_tr_b16 v[104:105], %[a0] offset:2048\nds_read_b64_tr_b16 v[106:107], %[a0] offset:3072\nv_mfma_f32_32x32x16_bf16 v[48:63], v[108:111], v[112:115], v[48:63]\n"
        "ds_read_b64_tr_b16 v[108:109], %[a1] offset:2048\nds_read_b64_tr_b16 v[110:111], %[a1] offset:3072\nv_mfma_f32_32x32x16_bf16 v[64:79], v[104:107], v[116:119], v[64:79]\n"
        "v_mfma_f32_32x32x16_bf16 v[80:95], v[108:111], v[116:119], v[80:95]\ns_waitcnt vmcnt(0) lgkmcnt(0)\nv_mfma_f32_32x32x16_bf16 v[32:47], v[104:107], v[120:123], v[32:47]\n"
        "v_mfma_f32_32x32x16_bf16 v[48:63], v[108:111], v[120:123], v[48:63]\nv_mfma_f32_32x32x16_bf16 v[64:79], v[104:107], v[124:127], v[64:79]\n"
        "v_mfma_f32_32x32x16_bf16 v[80:95], v[108:111], v[124:127], v[80:95]\ns_mov_b32 m0, %[sv]\ns_nop 7\ns_nop 7\n"
          : "=&{v[32:47]}"(d00), "=&{v[48:63]}"(d01), "=&{v[64:79]}"(d10), "=&{v[80:95]}"(d11), [ro] "+s"(ro), [sv] "=&s"(sv)
          : [a0] "v"(dw + at), [a1] "v"(dw + (at ^ 64)), [h0] "v"(l16), [h1] "v"(l16 + 114688), [w0] "v"(vo), [w1] "v"(vo + 7168), [dw] "s"(dw), [sh] "s"(sh), [sw] "s"(sw)
          : "v96", "v97", "v98", "v99", "v100", "v101", "v102", "v103", "v104", "v105", "v106", "v107", "v108", "v109", "v110", "v111", "v112", "v113", "v114", "v115", "v116", "v117", "v118", "v119", "v120", "v121", "v122", "v123", "v124", "v125", "v126", "v127", "v16", "v17", "v18", "v19", "v20", "v21", "v22", "v23", "v24", "v25", "v26", "v27", "v28", "v29", "v30", "v31", "s40", "s41", "s42", "s43", "s44", "s45", "s46", "s47", "scc", "memory");
        PRIO(2);
        TS(256 + b, 4);
        const lptr pw = L + w * 2048 + l16, pr = L + kq * 16384 + l16; f32x4 a, c;
#define F2_RED(dA, dB, tt) do { BARL(); _Pragma("unroll") for (int u = 0; u < 4; ++u) { lw128f(pw + (u >> 1) * 16384 + (u & 1) * 1024, dA[4 * u], dA[4 * u + 1], dA[4 * u + 2], dA[4 * u + 3]); \
            lw128f(pw + 32768 + (u >> 1) * 16384 + (u & 1) * 1024, dB[4 * u], dB[4 * u + 1], dB[4 * u + 2], dB[4 * u + 3]); } BARL(); \
            if (t == tt) { a = lr128f(pr); c = lr128f(pr + 1024); _Pragma("unroll") for (int u = 1; u < 8; ++u) { a += lr128f(pr + u * 2048); c += lr128f(pr + u * 2048 + 1024); } } } while (0)
        F2_RED(d00, d01, 0); F2_RED(d10, d11, 1);
#pragma unroll
        for (int i = 0; i < 4; ++i) { gg[i] = a[i] * __builtin_amdgcn_rcpf(1.f + __builtin_amdgcn_exp2f(a[i] * -1.4426950408889634f));
                                      gg[4 + i] = c[i] * __builtin_amdgcn_rcpf(1.f + __builtin_amdgcn_exp2f(c[i] * -1.4426950408889634f)); } }
    BARL();
#pragma unroll
    for (int n = 0; n < 8; ++n) glds16(vo, wo + cg * 128 + (w * 8 + n) * 8192, L0 + (w * 8 + n) * 1024);
    const size_t ob = (size_t)(tg * 64 + t * 32 + (l & 31)) * 1024 + cg * 128 + kq * 32;
    const u32x2 x0 = *(const u32x2*)(x + ob + 8 * h), x1 = *(const u32x2*)(x + ob + 16 + 8 * h);
    BAR(0); TS(256 + b, 5);
    { GAS unsigned* f = (GAS unsigned*)(ws + WS_FL + (tg * 8 + 2 * kq + (l & 1)) * 128);
      for (unsigned sp = 0; sp < SPIN_CAP; ++sp) { if (__builtin_amdgcn_ballot_w64(__hip_atomic_load(f, RLX_AGENT) == 0u) == 0ull) break; __builtin_amdgcn_s_sleep(2); } }
    TS(256 + b, 6);
    bf16x8 of[8];
    { const char* so = ws + WS_O + ((2 * tg + t) * 32 + 8 * kq) * 1024;
#pragma unroll
      for (int s = 0; s < 8; ++s) LD_SC1(of[s], l16, so + s * 1024); }
    f32x16 c0 = splat16(0.f), c1 = c0;
    const lptr a0 = L + kq * 16384 + at, a1 = L + kq * 16384 + (at ^ 64);
#pragma unroll
    for (int s = 0; s < 8; ++s) { const bf16x8 f0 = ltr(a0, s * 2048, s * 2048 + 1024), f1 = ltr(a1, s * 2048, s * 2048 + 1024);
        asm volatile("s_waitcnt vmcnt(%1)" : "+v"(of[s]) : "n"(7 - s) : "memory");
        c0 = MFMA(f0, of[s], c0); c1 = MFMA(f1, of[s], c1); }
    BARL();
    const lptr pw = L + (t * 16 + kq) * 2048 + l16;
#pragma unroll
    for (int u = 0; u < 4; ++u) { lw128f(pw + (u >> 1) * 8192 + (u & 1) * 1024, c0[4 * u], c0[4 * u + 1], c0[4 * u + 2], c0[4 * u + 3]);
                                  lw128f(pw + 16384 + (u >> 1) * 8192 + (u & 1) * 1024, c1[4 * u], c1[4 * u + 1], c1[4 * u + 2], c1[4 * u + 3]); }
    BARL();
    const lptr pr = L + (t * 16 + kq * 4) * 2048 + l16; f32x4 a = lr128f(pr), c = lr128f(pr + 1024);
#pragma unroll
    for (int u = 1; u < 4; ++u) { a += lr128f(pr + u * 2048); c += lr128f(pr + u * 2048 + 1024); }
    const unsigned A0 = pk(blo(x0[0]) + a[0] * gg[0], bhi(x0[0]) + a[1] * gg[1]), A1 = pk(blo(x0[1]) + a[2] * gg[2], bhi(x0[1]) + a[3] * gg[3]);
    const unsigned B0 = pk(blo(x1[0]) + c[0] * gg[4], bhi(x1[0]) + c[1] * gg[5]), B1 = pk(blo(x1[1]) + c[2] * gg[6], bhi(x1[1]) + c[3] * gg[7]);
    auto s0 = __builtin_amdgcn_permlane32_swap(A0, B0, false, false), s1 = __builtin_amdgcn_permlane32_swap(A1, B1, false, false);
    const u32x4 ov = {s0[0], s1[0], s0[1], s1[1]};
    *(u32x4*)(out + ob + 16 * h) = ov;
    TS(256 + b, 7);
}

__global__ __launch_bounds__(512, 4) void k2_kernel(const char* x, const char* wq, const char* wo, const char* wg, const char* wu, const char* wd, char* out, char* ws) {
    extern __shared__ char smem[];
    const lptr L = (lptr)smem; const unsigned L0 = (unsigned)(size_t)L;
    const int b = blockIdx.x;
    if (b < 256) attn_role(b, L, L0, wq, ws); else if (!NOFFN) ffn_role(b - 256, L, L0, x, wo, wg, wu, wd, out, ws);
}

extern "C" void kernel_launch(void* const* d_in, const int* in_sizes, int n_in, void* d_out, int out_size, void* d_ws, size_t ws_size, hipStream_t stream) {
    (void)hipFuncSetAttribute((const void*)k1_kernel, hipFuncAttributeMaxDynamicSharedMemorySize, LDS_K1);
    (void)hipFuncSetAttribute((const void*)k2_kernel, hipFuncAttributeMaxDynamicSharedMemorySize, LDS_K2);
    k1_kernel<<<256, 512, LDS_K1, stream>>>((const char*)d_in[0], (const char*)d_in[2], (const char*)d_in[3], (const char*)d_in[1], (const char*)d_in[6], (const char*)d_in[7], (const char*)d_in[8], (const char*)d_in[4], (const char*)d_out, (char*)d_ws);
    k2_kernel<<<512, 512, LDS_K2, stream>>>((const char*)d_in[0], (const char*)d_in[1], (const char*)d_in[4], (const char*)d_in[6], (const char*)d_in[7], (const char*)d_in[8], (char*)d_out, (char*)d_ws);
}
```

```cpp
#define WT 1
#include <hip/hip_runtime.h>
#include <hip/hip_bf16.h>
#include <stdint.h>

typedef short bf16x8 __attribute__((ext_vector_type(8)));
typedef short s16x4 __attribute__((ext_vector_type(4)));
typedef float f32x16 __attribute__((ext_vector_type(16)));
typedef float f32x4 __attribute__((ext_vector_type(4)));
typedef float f32x2 __attribute__((ext_vector_type(2)));
typedef __bf16 b16x2 __attribute__((ext_vector_type(2)));
typedef unsigned u32x4 __attribute__((ext_vector_type(4)));
typedef unsigned u32x2 __attribute__((ext_vector_type(2)));
#define LAS __attribute__((address_space(3)))
#define GAS __attribute__((address_space(1)))
#define DI __device__ __forceinline__
#define MFMA(a, b, c) __builtin_amdgcn_mfma_f32_32x32x16_bf16((a), (b), (c), 0, 0, 0)
#define RLX_AGENT __ATOMIC_RELAXED, __HIP_MEMORY_SCOPE_AGENT
typedef LAS char* lptr;

constexpr unsigned WS_XN = 0, WS_KV = 2u << 20, WS_H = 4u << 20, WS_O = 12u << 20, WS_FL = 16u << 20;
constexpr int FL_H = 256;
constexpr int LDS_K2 = 81920, LDS_K1 = 65792;
constexpr unsigned SPIN_CAP = 1u << 20;
#define NOFFN 0
#define P_LOOP 0
#define P_LOOP2 0
#define P_ATT 1
#define TOG 0
#define BAL 1
#define P_EPI 3
#define TK 1
#define TX 0
#define TLBW 0
#define PRIO(n) __builtin_amdgcn_s_setprio(n)
#define TS(blk, i)

DI unsigned pk(float a, float b) { f32x2 v = {a, b}; return __builtin_bit_cast(unsigned, __builtin_convertvector(v, b16x2)); }
DI bf16x8 pack8(const f32x16& x, int o) { u32x4 r = {pk(x[o], x[o + 1]), pk(x[o + 2], x[o + 3]), pk(x[o + 4], x[o + 5]), pk(x[o + 6], x[o + 7])}; return __builtin_bit_cast(bf16x8, r); }
DI float blo(unsigned u) { return __uint_as_float(u << 16); }
DI float bhi(unsigned u) { return __uint_as_float(u & 0xffff0000u); }
DI bf16x8 lr128(lptr p) { return *(const LAS bf16x8*)p; }
DI f32x4 lr128f(lptr p) { return *(const LAS f32x4*)p; }
DI void lw128f(lptr p, float a, float b, float c, float d) { f32x4 v = {a, b, c, d}; *(LAS f32x4*)p = v; }
DI bf16x8 ltr(lptr p, int o0, int o1) {
    s16x4 a = __builtin_amdgcn_ds_read_tr16_b64_v4i16((LAS s16x4*)(p + o0)), b = __builtin_amdgcn_ds_read_tr16_b64_v4i16((LAS s16x4*)(p + o1));
    return __builtin_shufflevector(a, b, 0, 1, 2, 3, 4, 5, 6, 7); }
DI void glds16(unsigned voff, const void* sbase, unsigned lds) {
    unsigned sv; asm volatile("s_mov_b32 %0, m0\n\ts_mov_b32 m0, %3\n\ts_nop 0\n\tglobal_load_lds_dwordx4 %1, %2\n\ts_mov_b32 m0, %0" : "=&s"(sv) : "v"(voff), "s"(sbase), "s"(lds) : "memory"); }
DI const char* uni(const char* p) { const unsigned long long a = (unsigned long long)p; return (const char*)(((unsigned long long)(unsigned)__builtin_amdgcn_readfirstlane((unsigned)(a >> 32)) << 32) | (unsigned)__builtin_amdgcn_readfirstlane((unsigned)a)); }
#define BAR(N) asm volatile("s_waitcnt vmcnt(" #N ") lgkmcnt(0)\n\ts_barrier" ::: "memory")
#define BARL() asm volatile("s_waitcnt lgkmcnt(0)\n\ts_barrier" ::: "memory")
DI void st_sc1(unsigned voff, void* sbase, bf16x8 v) { asm volatile("global_store_dwordx4 %0, %1, %2 sc1" ::"v"(voff), "v"(v), "s"(sbase) : "memory"); }
#define LD_SC1(dst, voff, sbase) asm volatile("global_load_dwordx4 %0, %1, %2 sc1" : "=v"(dst) : "v"(voff), "s"(sbase) : "memory")
#define touch(voff, sbase) asm volatile("global_load_dword %0, %1, %2" : "+v"(jk) : "v"(voff), "s"(sbase) : "memory")
#define TOUCH_END() asm volatile("s_waitcnt vmcnt(0)" : "+v"(jk)::"memory")
template <int C> DI float dpp(float x) { return __builtin_bit_cast(float, __builtin_amdgcn_mov_dpp(__builtin_bit_cast(int, x), C, 0xf, 0xf, true)); }
DI float sum32(float x) {
    x += dpp<0xB1>(x); x += dpp<0x4E>(x); x += dpp<0x141>(x); x += dpp<0x140>(x);
    auto s = __builtin_amdgcn_permlane16_swap(__float_as_uint(x), __float_as_uint(x), false, false);
    return __uint_as_float(s[0]) + __uint_as_float(s[1]); }
DI float xhalf_max(float m) { auto r = __builtin_amdgcn_permlane32_swap(__float_as_uint(m), __float_as_uint(m), false, false); return fmaxf(__uint_as_float(r[0]), __uint_as_float(r[1])); }
DI float max16(const f32x16& s, float m) {
#pragma unroll
    for (int i = 0; i < 16; i += 2) m = fmaxf(fmaxf(m, s[i]), s[i + 1]);
    return m; }
DI f32x16 splat16(float v) { f32x16 r;
#pragma unroll
    for (int i = 0; i < 16; ++i) r[i] = v;
    return r; }

__global__ __launch_bounds__(512) void k1_kernel(const char* x, const char* wk, const char* wv, const char* wq, const char* wg, const char* wu, const char* wd, const char* wo, const char* out, char* ws) {
    extern __shared__ char smem[];
    const lptr L = (lptr)smem; const unsigned L0 = (unsigned)(size_t)L;
    const int b = blockIdx.x, tid = threadIdx.x, l = tid & 63, w = __builtin_amdgcn_readfirstlane(tid >> 6), r = l & 31, h = l >> 5;
    TS(512 + b, 0);
    if (b >= 128) {
        const int j = (b - 128) >> 3, fq = b & 3;
        const int n = tid & 255, row = n / 7; unsigned jk = 0;
        if (w < 4) touch((j * 256 + n) * 128, wq);
        if (n < 224) touch((j * 32 + row) * 3584 + fq * 896 + (n - row * 7) * 128, w < 4 ? wg : wu);
        if (TLBW && w == 7) {
            if (l < 7) touch((j * 7 + l) << 16, ws + WS_H);
            if (l < 2) { touch((j * 2 + l) << 16, ws + WS_O); touch((j * 2 + l) << 16, out); }
            if (l < 2 && j < 14) touch((j * 2 + l) << 16, wd);
            if (l == 0 && j < 8) touch(j << 16, wo); }
        TOUCH_END(); TS(512 + b, 2); return; }
    constexpr int WK = 0, WV = 16384 + 128, XL = 33024, RED = 49408;
    const int tok0 = 256 * (b & 7) + 16 * (b >> 3), tl = 2 * w + h;
    const char* xr = x + (size_t)(tok0 + tl) * 1024 + r * 16;
    const u32x4 xa = *(const u32x4*)xr, xb = *(const u32x4*)(xr + 512);
    glds16(l * 16, wk + w * 2048, L0 + WK + w * 2048); glds16(l * 16, wk + w * 2048 + 1024, L0 + WK + w * 2048 + 1024);
    glds16(l * 16, wv + w * 2048, L0 + WV + w * 2048); glds16(l * 16, wv + w * 2048 + 1024, L0 + WV + w * 2048 + 1024);
    if (tid < 3) { const int f = b * 3 + tid; if (f < FL_H + 8) *(unsigned*)(ws + WS_FL + f * 128) = 0u; }
    float v[16];
#pragma unroll
    for (int i = 0; i < 4; ++i) { v[2 * i] = blo(xa[i]); v[2 * i + 1] = bhi(xa[i]); v[8 + 2 * i] = blo(xb[i]); v[9 + 2 * i] = bhi(xb[i]); }
    float s = 0.f;
#pragma unroll
    for (int i = 0; i < 16; ++i) s += v[i];
    const float mean = sum32(s) * (1.f / 512.f); float q2 = 0.f;
#pragma unroll
    for (int i = 0; i < 16; ++i) { v[i] -= mean; q2 += v[i] * v[i]; }
    const float rstd = __builtin_amdgcn_rsqf(sum32(q2) * (1.f / 512.f) + 1e-5f);
    u32x4 ya, yb;
#pragma unroll
    for (int i = 0; i < 4; ++i) { ya[i] = pk(v[2 * i] * rstd, v[2 * i + 1] * rstd); yb[i] = pk(v[8 + 2 * i] * rstd, v[9 + 2 * i] * rstd); }
    { const lptr p = L + XL + r * 256 + ((tl ^ (r & 15)) << 4); *(LAS u32x4*)p = ya; *(LAS u32x4*)(p + 8192) = yb; }
    BAR(0); TS(512 + b, 1);
    { const int c = tid >> 4, tk = tid & 15; const lptr p = L + XL + c * 256 + ((tk ^ (c & 15)) << 4);
      char* g = ws + WS_XN + (tok0 >> 5) * 32768 + c * 512 + ((tok0 & 16) + tk) * 16;
      if (WT) { const u32x4 u0 = *(LAS u32x4*)p, u1 = *(LAS u32x4*)(p + 8192); asm volatile("global_store_dwordx4 %0, %1, off sc0 sc1\n\tglobal_store_dwordx4 %2, %3, off sc0 sc1" ::"v"(g), "v"(u0), "v"(g + 16384), "v"(u1) : "memory"); }
      else { *(u32x4*)g = *(LAS u32x4*)p; *(u32x4*)(g + 16384) = *(LAS u32x4*)(p + 8192); } }
    f32x16 aK = splat16(0.f), aV = splat16(0.f);
    const lptr wp = L + ((l & 16) ? WV : WK) + (64 * w + 8 * h + ((l >> 2) & 3)) * 32 + (l & 3) * 8;
#pragma unroll
    for (int s4 = 0; s4 < 4; ++s4) { const int c = 8 * w + 2 * s4 + h;
        const bf16x8 xf = lr128(L + XL + c * 256 + (((l ^ c) & 15) << 4)), wf = ltr(wp, s4 * 512, s4 * 512 + 128);
        aK = MFMA(wf, xf, aK); aV = MFMA(xf, wf, aV); }
    { const bool vv = l & 16; const lptr p = L + RED + w * 2048 + l * 16;
      lw128f(p, vv ? aV[0] : aK[0], vv ? aV[1] : aK[1], vv ? aV[2] : aK[2], vv ? aV[3] : aK[3]);
      lw128f(p + 1024, vv ? aV[4] : aK[4], vv ? aV[5] : aK[5], vv ? aV[6] : aK[6], vv ? aV[7] : aK[7]); }
    BARL();
    if (w == 0) { f32x4 a = lr128f(L + RED + l * 16), c = lr128f(L + RED + 1024 + l * 16);
#pragma unroll
        for (int i = 1; i < 8; ++i) { a += lr128f(L + RED + i * 2048 + l * 16); c += lr128f(L + RED + i * 2048 + 1024 + l * 16); }
        const u32x4 o = {pk(a[0], a[1]), pk(a[2], a[3]), pk(c[0], c[1]), pk(c[2], c[3])}; const int sh = (tok0 >> 4) & 1;
        const int off = (l & 16) ? 1024 + sh * 512 + h * 256 + (l & 15) * 16 : (16 * sh + (l & 15) + 32 * h) * 16;
        if (WT) asm volatile("global_store_dwordx4 %0, %1, off sc0 sc1" ::"v"(ws + WS_KV + (tok0 >> 5) * 2048 + off), "v"(o) : "memory"); else *(u32x4*)(ws + WS_KV + (tok0 >> 5) * 2048 + off) = o; }
    TS(512 + b, 2);
}

DI void attn_role(int b, const lptr L, unsigned L0, const char* wq, char* ws) {
    const int tid = threadIdx.x, l = tid & 63, w = __builtin_amdgcn_readfirstlane(tid >> 6), h = l >> 5, t = w & 1, kq = w >> 1;
    const int i5 = b >> 3, tg = (b & 7) * 4 + (i5 & 3), hg = i5 >> 2, TT = 2 * tg + t;
    constexpr int SL = 8320, TS_ = 2080;
    const unsigned l16 = l * 16;
    PRIO(3); TS(b, 0);
    bf16x8 xf[8]; unsigned jk = 0;
    if (TK && w == 7 && l < 32) touch((i5 * 32 + l) * 128, ws + WS_KV);
    { const char* xp = ws + WS_XN + (TT * 32 + 8 * kq) * 1024 + l16;
#pragma unroll
      for (int s = 0; s < 8; ++s) xf[s] = *(const bf16x8*)(xp + s * 1024); }
    { const unsigned vo = (l >> 3) * 1024 + (((l & 7) ^ (((l >> 4) & 1) << 2)) << 4);
#pragma unroll
      for (int n = 0; n < 8; ++n) glds16(vo, wq + hg * 128 + (w * 8 + n) * 8192, L0 + (w * 8 + n) * 1024); }
    const char* kvs = ws + WS_KV + w * 1024; const unsigned kvd = L0 + (w >> 1) * TS_ + (w & 1) * 1024;
    glds16(l16, kvs, kvd + 8 * SL);
    BAR(1); asm volatile("" : "+v"(jk)); TS(b, 1);
    bf16x8 qf;
    { const int q = (l >> 2) & 3, p = l & 3, ch = ((l >> 4) & 1) * 2 + (p >> 1);
      const int a0 = kq * 16384 + (8 * h + q) * 128 + ((ch ^ ((q >> 1) << 2)) << 4) + 8 * (p & 1);
      f32x16 c0 = splat16(0.f), c1 = splat16(0.f);
#pragma unroll
      for (int s = 0; s < 8; ++s) { c0 = MFMA(ltr(L + a0, s * 2048, s * 2048 + 512), xf[s], c0); c1 = MFMA(ltr(L + (a0 ^ 64), s * 2048, s * 2048 + 512), xf[s], c1); }
      BARL();
      glds16(l16, kvs + 8192, kvd); glds16(l16, kvs + 2 * 8192, kvd + SL);
      if (tid < 24) { const unsigned v = (tid & 1) ? 0u : 0x3F803F80u; const u32x4 one = {v, v, v, v}; *(LAS u32x4*)(L + ((tid >> 1) + (tid < 16 ? 0 : 24)) * TS_ + 2048 + (tid & 1) * 16) = one; }
      f32x4 a, c;
#pragma unroll
      for (int j = 0; j < 4; ++j) { const f32x16& s = j < 2 ? c0 : c1; const int o = 8 * (j & 1);
          if (j == kq) { a = {s[o], s[o + 1], s[o + 2], s[o + 3]}; c = {s[o + 4], s[o + 5], s[o + 6], s[o + 7]}; }
          else { const lptr pw = L + 2 * SL + ((t * 4 + j) * 3 + ((kq - j - 1) & 3)) * 2048 + l16; lw128f(pw, s[o], s[o + 1], s[o + 2], s[o + 3]); lw128f(pw + 1024, s[o + 4], s[o + 5], s[o + 6], s[o + 7]); } }
      BAR(2);
      const lptr pr = L + 2 * SL + (t * 4 + kq) * 6144 + l16;
#pragma unroll
      for (int u = 0; u < 3; ++u) { a += lr128f(pr + u * 2048); c += lr128f(pr + u * 2048 + 1024); }
      const float sc = 0.25f * 1.4426950408889634f; a *= sc; c *= sc;
      const u32x4 qq = {pk(a[0], a[1]), pk(a[2], a[3]), pk(c[0], c[1]), pk(c[2], c[3])}; qf = __builtin_bit_cast(bf16x8, qq); }
    f32x16 ns; const bf16x8 kf = lr128(L + 8 * SL + l16); bf16x8 k0 = lr128(L + 8 * SL + TS_ + l16), k1 = lr128(L + 8 * SL + 2 * TS_ + l16);
    { const f32x16 z = splat16(0.f); f32x16 s0 = MFMA(kf, qf, z), s1 = MFMA(k0, qf, z), s2 = MFMA(k1, qf, z), s3 = MFMA(lr128(L + 8 * SL + 3 * TS_ + l16), qf, z);
      asm volatile("" : "+v"(s0), "+v"(s1), "+v"(s2), "+v"(s3));
      const float m = fmaxf(max16(s1, max16(s0, -3.0e38f)), max16(s3, max16(s2, -3.0e38f)));
      ns = splat16(-(xhalf_max(m) + 90.f)); }
    f32x16 o = splat16(0.f);
    { const unsigned bk = L0 + l16, b0 = L0 + ((l & 16) ? 1024 + h * 256 + (l & 15) * 16 : (l & 11) ? 2064 : 2048), b1 = (l & 16) ? b0 + 512 : b0;
      f32x16 sA = MFMA(kf, qf, ns);
      bf16x8 v0 = lr128(L + 8 * SL + (b0 - L0)), v1 = lr128(L + 8 * SL + (b1 - L0)), vb = {0, 0, 0, 0, 0, 0, 0, 0}, pb = vb;
      unsigned aK = bk + 8 * SL, a0 = b0 + 8 * SL, a1 = b1 + 8 * SL, vo = l16 + 3 * 8192, so = 8 * SL, sd = 2 * SL, sn, c = 0, sv;
      BAR(1); TS(b, 2);
      if (tid < 48) { const unsigned v = (tid & 1) ? 0u : 0x3F803F80u; const u32x4 one = {v, v, v, v}; *(LAS u32x4*)(L + ((tid >> 1) + 8) * TS_ + 2048 + (tid & 1) * 16) = one; }
      unsigned hi = w >> 2, pc = 0; PRIO(P_ATT);
      asm volatile(
        "s_mov_b32 %[sv], m0\ns_nop 7\ns_nop 7\n9:\ns_add_u32 %[sn], %[so], 8320\ns_cmp_eq_u32 %[so], 66560\ns_cselect_b32 %[sn], 0, %[sn]\n"
        "v_mfma_f32_32x32x16_bf16 v[48:63], v[100:103], v[96:99], v[80:95]\nds_read_b128 v[100:103], v124 offset:6240\nv_exp_f32 v32, v32\nv_exp_f32 v33, v33\nv_exp_f32 v34, v34\n"
        "v_exp_f32 v35, v35\nv_exp_f32 v36, v36\nv_exp_f32 v37, v37\nv_mfma_f32_32x32x16_bf16 v[64:79], v[24:27], v[120:123], v[64:79]\nds_read_b128 v[24:27], v126 offset:2080\n"
        "v_exp_f32 v38, v38\nv_exp_f32 v39, v39\nv_cvt_pk_bf16_f32 v116, v32, v33\nv_cvt_pk_bf16_f32 v117, v34, v35\nv_cvt_pk_bf16_f32 v118, v36, v37\n"
        "v_cvt_pk_bf16_f32 v119, v38, v39\nv_exp_f32 v40, v40\nv_exp_f32 v41, v41\nv_exp_f32 v42, v42\ns_waitcnt lgkmcnt(2)\n"
        "v_mfma_f32_32x32x16_bf16 v[64:79], v[108:111], v[116:119], v[64:79]\nds_read_b128 v[108:111], v125 offset:2080\nv_add_u32 v127, %[sn], %[bk]\nv_add_u32 v31, %[sn], %[b0]\n"
        "v_add_u32 v30, %[sn], %[b1]\nv_exp_f32 v43, v43\nv_exp_f32 v44, v44\nv_exp_f32 v45, v45\nv_exp_f32 v46, v46\nv_exp_f32 v47, v47\nv_cvt_pk_bf16_f32 v120, v40, v41\n"
        "v_cvt_pk_bf16_f32 v121, v42, v43\nv_cvt_pk_bf16_f32 v122, v44, v45\nv_cvt_pk_bf16_f32 v123, v46, v47\nv_mfma_f32_32x32x16_bf16 v[32:47], v[104:107], v[96:99], v[80:95]\n"
        "ds_read_b128 v[104:107], v127 offset:0\nv_exp_f32 v48, v48\nv_exp_f32 v49, v49\nv_exp_f32 v50, v50\nv_exp_f32 v51, v51\nv_exp_f32 v52, v52\nv_exp_f32 v53, v53\n"
        "v_mfma_f32_32x32x16_bf16 v[64:79], v[112:115], v[120:123], v[64:79]\nds_read_b128 v[112:115], v126 offset:4160\nv_exp_f32 v54, v54\nv_exp_f32 v55, v55\n"
        "v_cvt_pk_bf16_f32 v116, v48, v49\nv_cvt_pk_bf16_f32 v117, v50, v51\nv_cvt_pk_bf16_f32 v118, v52, v53\nv_cvt_pk_bf16_f32 v119, v54, v55\nv_exp_f32 v56, v56\n"
        "v_exp_f32 v57, v57\nv_exp_f32 v58, v58\ns_waitcnt lgkmcnt(2)\nv_mfma_f32_32x32x16_bf16 v[64:79], v[108:111], v[116:119], v[64:79]\n"
        "ds_read_b128 v[108:111], v125 offset:4160\nv_exp_f32 v59, v59\nv_exp_f32 v60, v60\nv_exp_f32 v61, v61\nv_exp_f32 v62, v62\nv_exp_f32 v63, v63\n"
        "v_cvt_pk_bf16_f32 v120, v56, v57\nv_cvt_pk_bf16_f32 v121, v58, v59\nv_cvt_pk_bf16_f32 v122, v60, v61\nv_cvt_pk_bf16_f32 v123, v62, v63\n"
        "v_mfma_f32_32x32x16_bf16 v[48:63], v[100:103], v[96:99], v[80:95]\nds_read_b128 v[100:103], v127 offset:2080\nv_exp_f32 v32, v32\nv_exp_f32 v33, v33\nv_exp_f32 v34, v34\n"
        "v_exp_f32 v35, v35\nv_exp_f32 v36, v36\nv_exp_f32 v37, v37\nv_mfma_f32_32x32x16_bf16 v[64:79], v[24:27], v[120:123], v[64:79]\nds_read_b128 v[24:27], v126 offset:6240\n"
        "v_exp_f32 v38, v38\nv_exp_f32 v39, v39\nv_cvt_pk_bf16_f32 v116, v32, v33\nv_cvt_pk_bf16_f32 v117, v34, v35\nv_cvt_pk_bf16_f32 v118, v36, v37\n"
        "v_cvt_pk_bf16_f32 v119, v38, v39\nv_exp_f32 v40, v40\nv_exp_f32 v41, v41\nv_exp_f32 v42, v42\ns_waitcnt lgkmcnt(2)\n"
        "v_mfma_f32_32x32x16_bf16 v[64:79], v[108:111], v[116:119], v[64:79]\nds_read_b128 v[108:111], v125 offset:6240\ns_cmp_eq_u32 %[hi], 0\ns_cbranch_scc0 15f\ns_setprio 0\n"
        "15:\nv_exp_f32 v43, v43\nv_exp_f32 v44, v44\nv_exp_f32 v45, v45\nv_exp_f32 v46, v46\nv_exp_f32 v47, v47\nv_cvt_pk_bf16_f32 v120, v40, v41\n"
        "v_cvt_pk_bf16_f32 v121, v42, v43\nv_cvt_pk_bf16_f32 v122, v44, v45\nv_cvt_pk_bf16_f32 v123, v46, v47\nv_mfma_f32_32x32x16_bf16 v[32:47], v[104:107], v[96:99], v[80:95]\n"
        "ds_read_b128 v[104:107], v127 offset:4160\nv_exp_f32 v48, v48\nv_exp_f32 v49, v49\nv_exp_f32 v50, v50\nv_exp_f32 v51, v51\nv_exp_f32 v52, v52\nv_exp_f32 v53, v53\n"
        "v_mfma_f32_32x32x16_bf16 v[64:79], v[112:115], v[120:123], v[64:79]\nds_read_b128 v[112:115], v30 offset:0\nv_exp_f32 v54, v54\nv_exp_f32 v55, v55\n"
        "v_cvt_pk_bf16_f32 v116, v48, v49\nv_cvt_pk_bf16_f32 v117, v50, v51\nv_cvt_pk_bf16_f32 v118, v52, v53\nv_cvt_pk_bf16_f32 v119, v54, v55\nv_exp_f32 v56, v56\n"
        "v_exp_f32 v57, v57\nv_exp_f32 v58, v58\ns_waitcnt lgkmcnt(2)\nv_mfma_f32_32x32x16_bf16 v[64:79], v[108:111], v[116:119], v[64:79]\nds_read_b128 v[108:111], v31 offset:0\n"
        "s_cmp_eq_u32 %[hi], 0\ns_cbranch_scc0 14f\ns_setprio 2\n14:\nv_exp_f32 v59, v59\nv_exp_f32 v60, v60\nv_exp_f32 v61, v61\nv_exp_f32 v62, v62\nv_exp_f32 v63, v63\n"
        "v_cvt_pk_bf16_f32 v120, v56, v57\nv_cvt_pk_bf16_f32 v121, v58, v59\nv_cvt_pk_bf16_f32 v122, v60, v61\nv_cvt_pk_bf16_f32 v123, v62, v63\ns_cmp_gt_u32 %[c], 7\n"
        "s_cbranch_scc1 12f\ns_waitcnt vmcnt(0) lgkmcnt(0)\ns_barrier\ns_cmp_eq_u32 %[c], 7\ns_cbranch_scc1 12f\ns_add_u32 m0, %[sd], %[kd]\ns_add_u32 %[sd], %[sd], 8320\n"
        "global_load_lds_dwordx4 %[vo], %[src]\ns_cmp_eq_u32 %[sd], 74880\ns_cselect_b32 %[sd], 0, %[sd]\nv_add_u32 %[vo], 0x2000, %[vo]\ns_cmp_eq_u32 %[c], 6\ns_cbranch_scc1 12f\n"
        "s_add_u32 m0, %[sd], %[kd]\ns_add_u32 %[sd], %[sd], 8320\nglobal_load_lds_dwordx4 %[vo], %[src]\ns_cmp_eq_u32 %[sd], 74880\ns_cselect_b32 %[sd], 0, %[sd]\n"
        "v_add_u32 %[vo], 0x2000, %[vo]\n12:\ns_add_u32 %[c], %[c], 1\ns_mov_b32 %[so], %[sn]\ns_add_u32 %[sn], %[so], 8320\ns_cmp_eq_u32 %[so], 66560\n"
        "s_cselect_b32 %[sn], 0, %[sn]\nv_mfma_f32_32x32x16_bf16 v[48:63], v[100:103], v[96:99], v[80:95]\nds_read_b128 v[100:103], v127 offset:6240\nv_exp_f32 v32, v32\n"
        "v_exp_f32 v33, v33\nv_exp_f32 v34, v34\nv_exp_f32 v35, v35\nv_exp_f32 v36, v36\nv_exp_f32 v37, v37\nv_mfma_f32_32x32x16_bf16 v[64:79], v[24:27], v[120:123], v[64:79]\n"
        "ds_read_b128 v[24:27], v30 offset:2080\nv_exp_f32 v38, v38\nv_exp_f32 v39, v39\nv_cvt_pk_bf16_f32 v116, v32, v33\nv_cvt_pk_bf16_f32 v117, v34, v35\n"
        "v_cvt_pk_bf16_f32 v118, v36, v37\nv_cvt_pk_bf16_f32 v119, v38, v39\nv_exp_f32 v40, v40\nv_exp_f32 v41, v41\nv_exp_f32 v42, v42\ns_waitcnt lgkmcnt(2)\n"
        "v_mfma_f32_32x32x16_bf16 v[64:79], v[108:111], v[116:119], v[64:79]\nds_read_b128 v[108:111], v31 offset:2080\nv_add_u32 v124, %[sn], %[bk]\nv_add_u32 v125, %[sn], %[b0]\n"
        "v_add_u32 v126, %[sn], %[b1]\nv_exp_f32 v43, v43\nv_exp_f32 v44, v44\nv_exp_f32 v45, v45\nv_exp_f32 v46, v46\nv_exp_f32 v47, v47\nv_cvt_pk_bf16_f32 v120, v40, v41\n"
        "v_cvt_pk_bf16_f32 v121, v42, v43\nv_cvt_pk_bf16_f32 v122, v44, v45\nv_cvt_pk_bf16_f32 v123, v46, v47\nv_mfma_f32_32x32x16_bf16 v[32:47], v[104:107], v[96:99], v[80:95]\n"
        "ds_read_b128 v[104:107], v124 offset:0\nv_exp_f32 v48, v48\nv_exp_f32 v49, v49\nv_exp_f32 v50, v50\nv_exp_f32 v51, v51\nv_exp_f32 v52, v52\nv_exp_f32 v53, v53\n"
        "v_mfma_f32_32x32x16_bf16 v[64:79], v[112:115], v[120:123], v[64:79]\nds_read_b128 v[112:115], v30 offset:4160\nv_exp_f32 v54, v54\nv_exp_f32 v55, v55\n"
        "v_cvt_pk_bf16_f32 v116, v48, v49\nv_cvt_pk_bf16_f32 v117, v50, v51\nv_cvt_pk_bf16_f32 v118, v52, v53\nv_cvt_pk_bf16_f32 v119, v54, v55\nv_exp_f32 v56, v56\n"
        "v_exp_f32 v57, v57\nv_exp_f32 v58, v58\ns_waitcnt lgkmcnt(2)\nv_mfma_f32_32x32x16_bf16 v[64:79], v[108:111], v[116:119], v[64:79]\n"
        "ds_read_b128 v[108:111], v31 offset:4160\nv_exp_f32 v59, v59\nv_exp_f32 v60, v60\nv_exp_f32 v61, v61\nv_exp_f32 v62, v62\nv_exp_f32 v63, v63\n"
        "v_cvt_pk_bf16_f32 v120, v56, v57\nv_cvt_pk_bf16_f32 v121, v58, v59\nv_cvt_pk_bf16_f32 v122, v60, v61\nv_cvt_pk_bf16_f32 v123, v62, v63\n"
        "v_mfma_f32_32x32x16_bf16 v[48:63], v[100:103], v[96:99], v[80:95]\nds_read_b128 v[100:103], v124 offset:2080\nv_exp_f32 v32, v32\nv_exp_f32 v33, v33\nv_exp_f32 v34, v34\n"
        "v_exp_f32 v35, v35\nv_exp_f32 v36, v36\nv_exp_f32 v37, v37\nv_mfma_f32_32x32x16_bf16 v[64:79], v[24:27], v[120:123], v[64:79]\nds_read_b128 v[24:27], v30 offset:6240\n"
        "v_exp_f32 v38, v38\nv_exp_f32 v39, v39\nv_cvt_pk_bf16_f32 v116, v32, v33\nv_cvt_pk_bf16_f32 v117, v34, v35\nv_cvt_pk_bf16_f32 v118, v36, v37\n"
        "v_cvt_pk_bf16_f32 v119, v38, v39\nv_exp_f32 v40, v40\nv_exp_f32 v41, v41\nv_exp_f32 v42, v42\ns_waitcnt lgkmcnt(2)\n"
        "v_mfma_f32_32x32x16_bf16 v[64:79], v[108:111], v[116:119], v[64:79]\nds_read_b128 v[108:111], v31 offset:6240\ns_cmp_eq_u32 %[hi], 0\ns_cbranch_scc0 25f\ns_setprio 0\n25:\n"
        "v_exp_f32 v43, v43\nv_exp_f32 v44, v44\nv_exp_f32 v45, v45\nv_exp_f32 v46, v46\nv_exp_f32 v47, v47\nv_cvt_pk_bf16_f32 v120, v40, v41\nv_cvt_pk_bf16_f32 v121, v42, v43\n"
        "v_cvt_pk_bf16_f32 v122, v44, v45\nv_cvt_pk_bf16_f32 v123, v46, v47\nv_mfma_f32_32x32x16_bf16 v[32:47], v[104:107], v[96:99], v[80:95]\n"
        "ds_read_b128 v[104:107], v124 offset:4160\nv_exp_f32 v48, v48\nv_exp_f32 v49, v49\nv_exp_f32 v50, v50\nv_exp_f32 v51, v51\nv_exp_f32 v52, v52\nv_exp_f32 v53, v53\n"
        "v_mfma_f32_32x32x16_bf16 v[64:79], v[112:115], v[120:123], v[64:79]\nds_read_b128 v[112:115], v126 offset:0\nv_exp_f32 v54, v54\nv_exp_f32 v55, v55\n"
        "v_cvt_pk_bf16_f32 v116, v48, v49\nv_cvt_pk_bf16_f32 v117, v50, v51\nv_cvt_pk_bf16_f32 v118, v52, v53\nv_cvt_pk_bf16_f32 v119, v54, v55\nv_exp_f32 v56, v56\n"
        "v_exp_f32 v57, v57\nv_exp_f32 v58, v58\ns_waitcnt lgkmcnt(2)\nv_mfma_f32_32x32x16_bf16 v[64:79], v[108:111], v[116:119], v[64:79]\nds_read_b128 v[108:111], v125 offset:0\n"
        "s_cmp_eq_u32 %[hi], 0\ns_cbranch_scc0 24f\ns_setprio 2\n24:\nv_exp_f32 v59, v59\nv_exp_f32 v60, v60\nv_exp_f32 v61, v61\nv_exp_f32 v62, v62\nv_exp_f32 v63, v63\n"
        "v_cvt_pk_bf16_f32 v120, v56, v57\nv_cvt_pk_bf16_f32 v121, v58, v59\nv_cvt_pk_bf16_f32 v122, v60, v61\nv_cvt_pk_bf16_f32 v123, v62, v63\ns_cmp_gt_u32 %[c], 7\n"
        "s_cbranch_scc1 22f\ns_waitcnt vmcnt(0) lgkmcnt(0)\ns_barrier\ns_cmp_eq_u32 %[c], 7\ns_cbranch_scc1 22f\ns_add_u32 m0, %[sd], %[kd]\ns_add_u32 %[sd], %[sd], 8320\n"
        "global_load_lds_dwordx4 %[vo], %[src]\ns_cmp_eq_u32 %[sd], 74880\ns_cselect_b32 %[sd], 0, %[sd]\nv_add_u32 %[vo], 0x2000, %[vo]\ns_cmp_eq_u32 %[c], 6\ns_cbranch_scc1 22f\n"
        "s_add_u32 m0, %[sd], %[kd]\ns_add_u32 %[sd], %[sd], 8320\nglobal_load_lds_dwordx4 %[vo], %[src]\ns_cmp_eq_u32 %[sd], 74880\ns_cselect_b32 %[sd], 0, %[sd]\n"
        "v_add_u32 %[vo], 0x2000, %[vo]\n22:\ns_add_u32 %[c], %[c], 1\ns_mov_b32 %[so], %[sn]\ns_cmp_lt_u32 %[c], 16\ns_cbranch_scc1 9b\ns_waitcnt lgkmcnt(0)\n"
        "v_mfma_f32_32x32x16_bf16 v[64:79], v[24:27], v[120:123], v[64:79]\ns_mov_b32 m0, %[sv]\ns_nop 7\ns_nop 7\n"
        : "+{v[32:47]}"(sA), "+{v[64:79]}"(o), "+{v[100:103]}"(k0), "+{v[104:107]}"(k1), "+{v[108:111]}"(v0), "+{v[112:115]}"(v1), "+{v[24:27]}"(vb), "+{v[120:123]}"(pb),
          "+{v124}"(aK), "+{v125}"(a0), "+{v126}"(a1), [so] "+s"(so), [sd] "+s"(sd), [sn] "=&s"(sn), [c] "+s"(c), [sv] "=&s"(sv), [vo] "+v"(vo), [hi] "+s"(hi)
        : "{v[80:95]}"(ns), "{v[96:99]}"(qf), [bk] "v"(bk), [b0] "v"(b0), [b1] "v"(b1), [kd] "s"(kvd), [src] "s"(kvs)
        : "v48", "v49", "v50", "v51", "v52", "v53", "v54", "v55", "v56", "v57", "v58", "v59", "v60", "v61", "v62", "v63", "v116", "v117", "v118", "v119", "v127", "v30", "v31", "scc", "memory"
 );

      PRIO(3); }
    TS(b, 3);
    int m_; asm volatile("v_mbcnt_lo_u32_b32 %0, -1, 0\n\tv_mbcnt_hi_u32_b32 %0, -1, %0" : "=v"(m_));
    const unsigned m16 = m_ * 16;

    float bad = o[0];
#pragma unroll
    for (int i = 8; i < 16; ++i) bad = fmaxf(bad, fabsf(o[i]));
    if (__builtin_amdgcn_ballot_w64(!(bad < 1.0e33f)) != 0ull || __builtin_amdgcn_ballot_w64(!(o[0] > 0.f)) != 0ull) {
        const char* kv = ws + WS_KV; float m = -3.0e38f; const f32x16 z = splat16(0.f);
#pragma unroll 1
        for (int u = 0; u < 64; ++u) m = max16(MFMA(*(const bf16x8*)(kv + u * 2048 + m16), qf, z), m);
        ns = splat16(-xhalf_max(m)); o = z;
        const u32x4 one = {0x3F803F80u, 0x3F803F80u, 0x3F803F80u, 0x3F803F80u}; const int vo = 1024 + (m_ >> 5) * 256 + (m_ & 15) * 16;
#pragma unroll 1
        for (int u = 0; u < 64; ++u) { f32x16 s = MFMA(*(const bf16x8*)(kv + u * 2048 + m16), qf, ns);
            u32x4 v0 = *(const u32x4*)(kv + u * 2048 + vo), v1 = *(const u32x4*)(kv + u * 2048 + vo + 512);
            if (!(m_ & 16)) { v0 = one; v1 = one; }
#pragma unroll
            for (int i = 0; i < 16; ++i) s[i] = __builtin_amdgcn_exp2f(s[i]);
            o = MFMA(__builtin_bit_cast(bf16x8, v0), pack8(s, 0), o); o = MFMA(__builtin_bit_cast(bf16x8, v1), pack8(s, 8), o); } }
    const float inv = 1.f / o[0];
#pragma unroll
    for (int i = 8; i < 16; ++i) o[i] *= inv;
    st_sc1(m16, ws + WS_O + (TT * 32 + 4 * hg + kq) * 1024, pack8(o, 8));
    BAR(0);
    if (w == 0 && m_ == 0) __hip_atomic_store((GAS unsigned*)(ws + WS_FL + (tg * 8 + hg) * 128), 1u, RLX_AGENT);
    TS(b, 4);
}

DI void ffn_role(int b, const lptr L, unsigned L0, const char* x, const char* wo, const char* wg, const char* wu, const char* wd, char* out, char* ws) {
    const int tid = threadIdx.x, l = tid & 63, w = __builtin_amdgcn_readfirstlane(tid >> 6), h = l >> 5;
    const int xcd = b & 7, i5 = b >> 3, q = (l >> 2) & 3, p = l & 3; const unsigned l16 = l * 16;
    PRIO(P_EPI);
    TS(256 + b, 0);
    unsigned jk = 0;
    if (TX && w < 4) touch((i5 * 256 + tid) * 128, ws + WS_XN + (xcd >> 2) * 1048576);
    if (i5 < 28) {
        const int TB = (xcd >> 2) * 4 + (i5 & 3), fb = (xcd & 3) * 7 + (i5 >> 2), ft = w & 1, tk = w >> 1;
        const unsigned vw = (l >> 3) * 3584 + (((l & 7) ^ (((l >> 4) & 1) << 2)) << 4);
        const char* sw = ((w & 4) ? wu : wg) + (w & 3) * 8 * 3584 + fb * 128;
        const char* sx = ws + WS_XN + (TB * 8 + w) * 32768; const unsigned dw = L0 + w * 1024, dx = L0 + 8192 + w * 2048;
#define F1_ISSUE(st) do { glds16(vw, sw + (st) * 114688, dw + ((st) % 3) * 24576); glds16(l16, sx + (st) * 2048, dx + ((st) % 3) * 24576); glds16(l16, sx + (st) * 2048 + 1024, dx + ((st) % 3) * 24576 + 1024); } while (0)
        F1_ISSUE(0); F1_ISSUE(1); F1_ISSUE(2);
        unsigned sv; const unsigned tv = (i5 * 512 + tid) * 128;
        const unsigned pa = L0 + (((8 * h + q) * 128 + (((((l >> 4) & 1) * 2 + (p >> 1)) ^ ((q >> 1) << 2)) << 4) + 8 * (p & 1)) ^ (ft * 64)), pb = L0 + 8192 + tk * 4096 + l16;
        f32x16 g0, g1, u0, u1;
        PRIO(P_LOOP);
        asm volatile(
        "s_mov_b32 %[sv], m0\ns_mov_b64 s[40:41], %[sw]\ns_mov_b64 s[42:43], %[sx]\ns_waitcnt vmcnt(6)\ns_barrier\nds_read_b64_tr_b16 v[96:97], %[pa] offset:0\n"
        "ds_read_b64_tr_b16 v[98:99], %[pa] offset:512\nds_read_b128 v[104:107], %[pb] offset:0\nds_read_b64_tr_b16 v[100:101], %[pa] offset:4096\n"
        "ds_read_b64_tr_b16 v[102:103], %[pa] offset:4608\nds_read_b128 v[108:111], %[pb] offset:2048\ns_waitcnt lgkmcnt(0)\n"
        "v_mfma_f32_32x32x16_bf16 v[32:47], v[96:99], v[104:107], 0\nds_read_b64_tr_b16 v[112:113], %[pa] offset:2048\nds_read_b64_tr_b16 v[114:115], %[pa] offset:2560\n"
        "v_mfma_f32_32x32x16_bf16 v[48:63], v[96:99], v[108:111], 0\nds_read_b128 v[120:123], %[pb] offset:1024\nds_read_b64_tr_b16 v[116:117], %[pa] offset:6144\n"
        "v_mfma_f32_32x32x16_bf16 v[64:79], v[100:103], v[104:107], 0\nds_read_b64_tr_b16 v[118:119], %[pa] offset:6656\nds_read_b128 v[124:127], %[pb] offset:3072\n"
        "v_mfma_f32_32x32x16_bf16 v[80:95], v[100:103], v[108:111], 0\ns_waitcnt vmcnt(3) lgkmcnt(0)\ns_barrier\n"
        "v_mfma_f32_32x32x16_bf16 v[32:47], v[112:115], v[120:123], v[32:47]\ns_add_u32 m0, %[dw], 0\ns_add_u32 s40, s40, 0x1c000\ns_addc_u32 s41, s41, 0\n"
        "global_load_lds_dwordx4 %[vw], s[40:41]\nds_read_b64_tr_b16 v[96:97], %[pa] offset:24576\nds_read_b64_tr_b16 v[98:99], %[pa] offset:25088\n"
        "v_mfma_f32_32x32x16_bf16 v[48:63], v[112:115], v[124:127], v[48:63]\ns_add_u32 m0, %[dx], 0\ns_add_u32 s42, s42, 0x800\ns_addc_u32 s43, s43, 0\n"
        "global_load_lds_dwordx4 %[vx], s[42:43]\nds_read_b128 v[104:107], %[pb] offset:24576\nds_read_b64_tr_b16 v[100:101], %[pa] offset:28672\n"
        "v_mfma_f32_32x32x16_bf16 v[64:79], v[116:119], v[120:123], v[64:79]\nglobal_load_lds_dwordx4 %[vx], s[42:43] offset:1024\n"
        "ds_read_b64_tr_b16 v[102:103], %[pa] offset:29184\nds_read_b128 v[108:111], %[pb] offset:26624\nv_mfma_f32_32x32x16_bf16 v[80:95], v[116:119], v[124:127], v[80:95]\n"
        "s_waitcnt lgkmcnt(0)\nv_mfma_f32_32x32x16_bf16 v[32:47], v[96:99], v[104:107], v[32:47]\nds_read_b64_tr_b16 v[112:113], %[pa] offset:26624\n"
        "ds_read_b64_tr_b16 v[114:115], %[pa] offset:27136\nv_mfma_f32_32x32x16_bf16 v[48:63], v[96:99], v[108:111], v[48:63]\nds_read_b128 v[120:123], %[pb] offset:25600\n"
        "ds_read_b64_tr_b16 v[116:117], %[pa] offset:30720\nv_mfma_f32_32x32x16_bf16 v[64:79], v[100:103], v[104:107], v[64:79]\nds_read_b64_tr_b16 v[118:119], %[pa] offset:31232\n"
        "ds_read_b128 v[124:127], %[pb] offset:27648\nv_mfma_f32_32x32x16_bf16 v[80:95], v[100:103], v[108:111], v[80:95]\ns_waitcnt vmcnt(3) lgkmcnt(0)\ns_barrier\n"
        "v_mfma_f32_32x32x16_bf16 v[32:47], v[112:115], v[120:123], v[32:47]\ns_add_u32 m0, %[dw], 24576\ns_add_u32 s40, s40, 0x1c000\ns_addc_u32 s41, s41, 0\n"
        "global_load_lds_dwordx4 %[vw], s[40:41]\nds_read_b64_tr_b16 v[96:97], %[pa] offset:49152\nds_read_b64_tr_b16 v[98:99], %[pa] offset:49664\n"
        "v_mfma_f32_32x32x16_bf16 v[48:63], v[112:115], v[124:127], v[48:63]\ns_add_u32 m0, %[dx], 24576\ns_add_u32 s42, s42, 0x800\ns_addc_u32 s43, s43, 0\n"
        "global_load_lds_dwordx4 %[vx], s[42:43]\nds_read_b128 v[104:107], %[pb] offset:49152\nds_read_b64_tr_b16 v[100:101], %[pa] offset:53248\n"
        "v_mfma_f32_32x32x16_bf16 v[64:79], v[116:119], v[120:123], v[64:79]\nglobal_load_lds_dwordx4 %[vx], s[42:43] offset:1024\n"
        "ds_read_b64_tr_b16 v[102:103], %[pa] offset:53760\nds_read_b128 v[108:111], %[pb] offset:51200\nv_mfma_f32_32x32x16_bf16 v[80:95], v[116:119], v[124:127], v[80:95]\n"
        "s_waitcnt lgkmcnt(0)\nv_mfma_f32_32x32x16_bf16 v[32:47], v[96:99], v[104:107], v[32:47]\nds_read_b64_tr_b16 v[112:113], %[pa] offset:51200\n"
        "ds_read_b64_tr_b16 v[114:115], %[pa] offset:51712\nv_mfma_f32_32x32x16_bf16 v[48:63], v[96:99], v[108:111], v[48:63]\nds_read_b128 v[120:123], %[pb] offset:50176\n"
        "ds_read_b64_tr_b16 v[116:117], %[pa] offset:55296\nv_mfma_f32_32x32x16_bf16 v[64:79], v[100:103], v[104:107], v[64:79]\nds_read_b64_tr_b16 v[118:119], %[pa] offset:55808\n"
        "ds_read_b128 v[124:127], %[pb] offset:52224\nv_mfma_f32_32x32x16_bf16 v[80:95], v[100:103], v[108:111], v[80:95]\ns_waitcnt vmcnt(3) lgkmcnt(0)\ns_barrier\n"
        "v_mfma_f32_32x32x16_bf16 v[32:47], v[112:115], v[120:123], v[32:47]\ns_add_u32 m0, %[dw], 49152\ns_add_u32 s40, s40, 0x1c000\ns_addc_u32 s41, s41, 0\n"
        "global_load_lds_dwordx4 %[vw], s[40:41]\nds_read_b64_tr_b16 v[96:97], %[pa] offset:0\nds_read_b64_tr_b16 v[98:99], %[pa] offset:512\n"
        "v_mfma_f32_32x32x16_bf16 v[48:63], v[112:115], v[124:127], v[48:63]\ns_add_u32 m0, %[dx], 49152\ns_add_u32 s42, s42, 0x800\ns_addc_u32 s43, s43, 0\n"
        "global_load_lds_dwordx4 %[vx], s[42:43]\nds_read_b128 v[104:107], %[pb] offset:0\nds_read_b64_tr_b16 v[100:101], %[pa] offset:4096\n"
        "v_mfma_f32_32x32x16_bf16 v[64:79], v[116:119], v[120:123], v[64:79]\nglobal_load_lds_dwordx4 %[vx], s[42:43] offset:1024\nglobal_load_dword %[jk], %[tv], %[wd]\n"
        "ds_read_b64_tr_b16 v[102:103], %[pa] offset:4608\nds_read_b128 v[108:111], %[pb] offset:2048\nv_mfma_f32_32x32x16_bf16 v[80:95], v[116:119], v[124:127], v[80:95]\n"
        "s_waitcnt lgkmcnt(0)\nv_mfma_f32_32x32x16_bf16 v[32:47], v[96:99], v[104:107], v[32:47]\nds_read_b64_tr_b16 v[112:113], %[pa] offset:2048\n"
        "ds_read_b64_tr_b16 v[114:115], %[pa] offset:2560\nv_mfma_f32_32x32x16_bf16 v[48:63], v[96:99], v[108:111], v[48:63]\nds_read_b128 v[120:123], %[pb] offset:1024\n"
        "ds_read_b64_tr_b16 v[116:117], %[pa] offset:6144\nv_mfma_f32_32x32x16_bf16 v[64:79], v[100:103], v[104:107], v[64:79]\nds_read_b64_tr_b16 v[118:119], %[pa] offset:6656\n"
        "ds_read_b128 v[124:127], %[pb] offset:3072\nv_mfma_f32_32x32x16_bf16 v[80:95], v[100:103], v[108:111], v[80:95]\ns_waitcnt vmcnt(4) lgkmcnt(0)\ns_barrier\n"
        "v_mfma_f32_32x32x16_bf16 v[32:47], v[112:115], v[120:123], v[32:47]\ns_add_u32 m0, %[dw], 0\ns_add_u32 s40, s40, 0x1c000\ns_addc_u32 s41, s41, 0\n"
        "global_load_lds_dwordx4 %[vw], s[40:41]\nds_read_b64_tr_b16 v[96:97], %[pa] offset:24576\nds_read_b64_tr_b16 v[98:99], %[pa] offset:25088\n"
        "v_mfma_f32_32x32x16_bf16 v[48:63], v[112:115], v[124:127], v[48:63]\ns_add_u32 m0, %[dx], 0\ns_add_u32 s42, s42, 0x800\ns_addc_u32 s43, s43, 0\n"
        "global_load_lds_dwordx4 %[vx], s[42:43]\nds_read_b128 v[104:107], %[pb] offset:24576\nds_read_b64_tr_b16 v[100:101], %[pa] offset:28672\n"
        "v_mfma_f32_32x32x16_bf16 v[64:79], v[116:119], v[120:123], v[64:79]\nglobal_load_lds_dwordx4 %[vx], s[42:43] offset:1024\n"
        "ds_read_b64_tr_b16 v[102:103], %[pa] offset:29184\nds_read_b128 v[108:111], %[pb] offset:26624\nv_mfma_f32_32x32x16_bf16 v[80:95], v[116:119], v[124:127], v[80:95]\n"
        "s_waitcnt lgkmcnt(0)\nv_mfma_f32_32x32x16_bf16 v[32:47], v[96:99], v[104:107], v[32:47]\nds_read_b64_tr_b16 v[112:113], %[pa] offset:26624\n"
        "ds_read_b64_tr_b16 v[114:115], %[pa] offset:27136\nv_mfma_f32_32x32x16_bf16 v[48:63], v[96:99], v[108:111], v[48:63]\nds_read_b128 v[120:123], %[pb] offset:25600\n"
        "ds_read_b64_tr_b16 v[116:117], %[pa] offset:30720\nv_mfma_f32_32x32x16_bf16 v[64:79], v[100:103], v[104:107], v[64:79]\nds_read_b64_tr_b16 v[118:119], %[pa] offset:31232\n"
        "ds_read_b128 v[124:127], %[pb] offset:27648\nv_mfma_f32_32x32x16_bf16 v[80:95], v[100:103], v[108:111], v[80:95]\ns_waitcnt vmcnt(4) lgkmcnt(0)\ns_barrier\n"
        "v_mfma_f32_32x32x16_bf16 v[32:47], v[112:115], v[120:123], v[32:47]\ns_add_u32 m0, %[dw], 24576\ns_add_u32 s40, s40, 0x1c000\ns_addc_u32 s41, s41, 0\n"
        "global_load_lds_dwordx4 %[vw], s[40:41]\nds_read_b64_tr_b16 v[96:97], %[pa] offset:49152\nds_read_b64_tr_b16 v[98:99], %[pa] offset:49664\n"
        "v_mfma_f32_32x32x16_bf16 v[48:63], v[112:115], v[124:127], v[48:63]\ns_add_u32 m0, %[dx], 24576\ns_add_u32 s42, s42, 0x800\ns_addc_u32 s43, s43, 0\n"
        "global_load_lds_dwordx4 %[vx], s[42:43]\nds_read_b128 v[104:107], %[pb] offset:49152\nds_read_b64_tr_b16 v[100:101], %[pa] offset:53248\n"
        "v_mfma_f32_32x32x16_bf16 v[64:79], v[116:119], v[120:123], v[64:79]\nglobal_load_lds_dwordx4 %[vx], s[42:43] offset:1024\n"
        "ds_read_b64_tr_b16 v[102:103], %[pa] offset:53760\nds_read_b128 v[108:111], %[pb] offset:51200\nv_mfma_f32_32x32x16_bf16 v[80:95], v[116:119], v[124:127], v[80:95]\n"
        "s_waitcnt lgkmcnt(0)\nv_mfma_f32_32x32x16_bf16 v[32:47], v[96:99], v[104:107], v[32:47]\nds_read_b64_tr_b16 v[112:113], %[pa] offset:51200\n"
        "ds_read_b64_tr_b16 v[114:115], %[pa] offset:51712\nv_mfma_f32_32x32x16_bf16 v[48:63], v[96:99], v[108:111], v[48:63]\nds_read_b128 v[120:123], %[pb] offset:50176\n"
        "ds_read_b64_tr_b16 v[116:117], %[pa] offset:55296\nv_mfma_f32_32x32x16_bf16 v[64:79], v[100:103], v[104:107], v[64:79]\nds_read_b64_tr_b16 v[118:119], %[pa] offset:55808\n"
        "ds_read_b128 v[124:127], %[pb] offset:52224\nv_mfma_f32_32x32x16_bf16 v[80:95], v[100:103], v[108:111], v[80:95]\ns_waitcnt vmcnt(3) lgkmcnt(0)\ns_barrier\n"
        "v_mfma_f32_32x32x16_bf16 v[32:47], v[112:115], v[120:123], v[32:47]\ns_add_u32 m0, %[dw], 49152\ns_add_u32 s40, s40, 0x1c000\ns_addc_u32 s41, s41, 0\n"
        "global_load_lds_dwordx4 %[vw], s[40:41]\nds_read_b64_tr_b16 v[96:97], %[pa] offset:0\nds_read_b64_tr_b16 v[98:99], %[pa] offset:512\n"
        "v_mfma_f32_32x32x16_bf16 v[48:63], v[112:115], v[124:127], v[48:63]\ns_add_u32 m0, %[dx], 49152\ns_add_u32 s42, s42, 0x800\ns_addc_u32 s43, s43, 0\n"
        "global_load_lds_dwordx4 %[vx], s[42:43]\nds_read_b128 v[104:107], %[pb] offset:0\nds_read_b64_tr_b16 v[100:101], %[pa] offset:4096\n"
        "v_mfma_f32_32x32x16_bf16 v[64:79], v[116:119], v[120:123], v[64:79]\nglobal_load_lds_dwordx4 %[vx], s[42:43] offset:1024\nds_read_b64_tr_b16 v[102:103], %[pa] offset:4608\n"
        "ds_read_b128 v[108:111], %[pb] offset:2048\nv_mfma_f32_32x32x16_bf16 v[80:95], v[116:119], v[124:127], v[80:95]\ns_waitcnt lgkmcnt(0)\n"
        "v_mfma_f32_32x32x16_bf16 v[32:47], v[96:99], v[104:107], v[32:47]\nds_read_b64_tr_b16 v[112:113], %[pa] offset:2048\nds_read_b64_tr_b16 v[114:115], %[pa] offset:2560\n"
        "v_mfma_f32_32x32x16_bf16 v[48:63], v[96:99], v[108:111], v[48:63]\nds_read_b128 v[120:123], %[pb] offset:1024\nds_read_b64_tr_b16 v[116:117], %[pa] offset:6144\n"
        "v_mfma_f32_32x32x16_bf16 v[64:79], v[100:103], v[104:107], v[64:79]\nds_read_b64_tr_b16 v[118:119], %[pa] offset:6656\nds_read_b128 v[124:127], %[pb] offset:3072\n"
        "v_mfma_f32_32x32x16_bf16 v[80:95], v[100:103], v[108:111], v[80:95]\ns_waitcnt vmcnt(3) lgkmcnt(0)\ns_barrier\n"
        "v_mfma_f32_32x32x16_bf16 v[32:47], v[112:115], v[120:123], v[32:47]\ns_add_u32 m0, %[dw], 0\ns_add_u32 s40, s40, 0x1c000\ns_addc_u32 s41, s41, 0\n"
        "global_load_lds_dwordx4 %[vw], s[40:41]\nds_read_b64_tr_b16 v[96:97], %[pa] offset:24576\nds_read_b64_tr_b16 v[98:99], %[pa] offset:25088\n"
        "v_mfma_f32_32x32x16_bf16 v[48:63], v[112:115], v[124:127], v[48:63]\ns_add_u32 m0, %[dx], 0\ns_add_u32 s42, s42, 0x800\ns_addc_u32 s43, s43, 0\n"
        "global_load_lds_dwordx4 %[vx], s[42:43]\nds_read_b128 v[104:107], %[pb] offset:24576\nds_read_b64_tr_b16 v[100:101], %[pa] offset:28672\n"
        "v_mfma_f32_32x32x16_bf16 v[64:79], v[116:119], v[120:123], v[64:79]\nglobal_load_lds_dwordx4 %[vx], s[42:43] offset:1024\n"
        "ds_read_b64_tr_b16 v[102:103], %[pa] offset:29184\nds_read_b128 v[108:111], %[pb] offset:26624\nv_mfma_f32_32x32x16_bf16 v[80:95], v[116:119], v[124:127], v[80:95]\n"
        "s_waitcnt lgkmcnt(0)\nv_mfma_f32_32x32x16_bf16 v[32:47], v[96:99], v[104:107], v[32:47]\nds_read_b64_tr_b16 v[112:113], %[pa] offset:26624\n"
        "ds_read_b64_tr_b16 v[114:115], %[pa] offset:27136\nv_mfma_f32_32x32x16_bf16 v[48:63], v[96:99], v[108:111], v[48:63]\nds_read_b128 v[120:123], %[pb] offset:25600\n"
        "ds_read_b64_tr_b16 v[116:117], %[pa] offset:30720\nv_mfma_f32_32x32x16_bf16 v[64:79], v[100:103], v[104:107], v[64:79]\nds_read_b64_tr_b16 v[118:119], %[pa] offset:31232\n"
        "ds_read_b128 v[124:127], %[pb] offset:27648\nv_mfma_f32_32x32x16_bf16 v[80:95], v[100:103], v[108:111], v[80:95]\ns_waitcnt vmcnt(3) lgkmcnt(0)\ns_barrier\n"
        "v_mfma_f32_32x32x16_bf16 v[32:47], v[112:115], v[120:123], v[32:47]\ns_add_u32 m0, %[dw], 24576\ns_add_u32 s40, s40, 0x1c000\ns_addc_u32 s41, s41, 0\n"
        "global_load_lds_dwordx4 %[vw], s[40:41]\nds_read_b64_tr_b16 v[96:97], %[pa] offset:49152\nds_read_b64_tr_b16 v[98:99], %[pa] offset:49664\n"
        "v_mfma_f32_32x32x16_bf16 v[48:63], v[112:115], v[124:127], v[48:63]\ns_add_u32 m0, %[dx], 24576\ns_add_u32 s42, s42, 0x800\ns_addc_u32 s43, s43, 0\n"
        "global_load_lds_dwordx4 %[vx], s[42:43]\nds_read_b128 v[104:107], %[pb] offset:49152\nds_read_b64_tr_b16 v[100:101], %[pa] offset:53248\n"
        "v_mfma_f32_32x32x16_bf16 v[64:79], v[116:119], v[120:123], v[64:79]\nglobal_load_lds_dwordx4 %[vx], s[42:43] offset:1024\n"
        "ds_read_b64_tr_b16 v[102:103], %[pa] offset:53760\nds_read_b128 v[108:111], %[pb] offset:51200\nv_mfma_f32_32x32x16_bf16 v[80:95], v[116:119], v[124:127], v[80:95]\n"
        "s_waitcnt lgkmcnt(0)\nv_mfma_f32_32x32x16_bf16 v[32:47], v[96:99], v[104:107], v[32:47]\nds_read_b64_tr_b16 v[112:113], %[pa] offset:51200\n"
        "ds_read_b64_tr_b16 v[114:115], %[pa] offset:51712\nv_mfma_f32_32x32x16_bf16 v[48:63], v[96:99], v[108:111], v[48:63]\nds_read_b128 v[120:123], %[pb] offset:50176\n"
        "ds_read_b64_tr_b16 v[116:117], %[pa] offset:55296\nv_mfma_f32_32x32x16_bf16 v[64:79], v[100:103], v[104:107], v[64:79]\nds_read_b64_tr_b16 v[118:119], %[pa] offset:55808\n"
        "ds_read_b128 v[124:127], %[pb] offset:52224\nv_mfma_f32_32x32x16_bf16 v[80:95], v[100:103], v[108:111], v[80:95]\ns_waitcnt vmcnt(3) lgkmcnt(0)\ns_barrier\n"
        "v_mfma_f32_32x32x16_bf16 v[32:47], v[112:115], v[120:123], v[32:47]\ns_add_u32 m0, %[dw], 49152\ns_add_u32 s40, s40, 0x1c000\ns_addc_u32 s41, s41, 0\n"
        "global_load_lds_dwordx4 %[vw], s[40:41]\nds_read_b64_tr_b16 v[96:97], %[pa] offset:0\nds_read_b64_tr_b16 v[98:99], %[pa] offset:512\n"
        "v_mfma_f32_32x32x16_bf16 v[48:63], v[112:115], v[124:127], v[48:63]\ns_add_u32 m0, %[dx], 49152\ns_add_u32 s42, s42, 0x800\ns_addc_u32 s43, s43, 0\n"
        "global_load_lds_dwordx4 %[vx], s[42:43]\nds_read_b128 v[104:107], %[pb] offset:0\nds_read_b64_tr_b16 v[100:101], %[pa] offset:4096\n"
        "v_mfma_f32_32x32x16_bf16 v[64:79], v[116:119], v[120:123], v[64:79]\nglobal_load_lds_dwordx4 %[vx], s[42:43] offset:1024\nds_read_b64_tr_b16 v[102:103], %[pa] offset:4608\n"
        "ds_read_b128 v[108:111], %[pb] offset:2048\nv_mfma_f32_32x32x16_bf16 v[80:95], v[116:119], v[124:127], v[80:95]\ns_waitcnt lgkmcnt(0)\n"
        "v_mfma_f32_32x32x16_bf16 v[32:47], v[96:99], v[104:107], v[32:47]\nds_read_b64_tr_b16 v[112:113], %[pa] offset:2048\nds_read_b64_tr_b16 v[114:115], %[pa] offset:2560\n"
        "v_mfma_f32_32x32x16_bf16 v[48:63], v[96:99], v[108:111], v[48:63]\nds_read_b128 v[120:123], %[pb] offset:1024\nds_read_b64_tr_b16 v[116:117], %[pa] offset:6144\n"
        "v_mfma_f32_32x32x16_bf16 v[64:79], v[100:103], v[104:107], v[64:79]\nds_read_b64_tr_b16 v[118:119], %[pa] offset:6656\nds_read_b128 v[124:127], %[pb] offset:3072\n"
        "v_mfma_f32_32x32x16_bf16 v[80:95], v[100:103], v[108:111], v[80:95]\ns_waitcnt vmcnt(3) lgkmcnt(0)\ns_barrier\n"
        "v_mfma_f32_32x32x16_bf16 v[32:47], v[112:115], v[120:123], v[32:47]\ns_add_u32 m0, %[dw], 0\ns_add_u32 s40, s40, 0x1c000\ns_addc_u32 s41, s41, 0\n"
        "global_load_lds_dwordx4 %[vw], s[40:41]\nds_read_b64_tr_b16 v[96:97], %[pa] offset:24576\nds_read_b64_tr_b16 v[98:99], %[pa] offset:25088\n"
        "v_mfma_f32_32x32x16_bf16 v[48:63], v[112:115], v[124:127], v[48:63]\ns_add_u32 m0, %[dx], 0\ns_add_u32 s42, s42, 0x800\ns_addc_u32 s43, s43, 0\n"
        "global_load_lds_dwordx4 %[vx], s[42:43]\nds_read_b128 v[104:107], %[pb] offset:24576\nds_read_b64_tr_b16 v[100:101], %[pa] offset:28672\n"
        "v_mfma_f32_32x32x16_bf16 v[64:79], v[116:119], v[120:123], v[64:79]\nglobal_load_lds_dwordx4 %[vx], s[42:43] offset:1024\n"
        "ds_read_b64_tr_b16 v[102:103], %[pa] offset:29184\nds_read_b128 v[108:111], %[pb] offset:26624\nv_mfma_f32_32x32x16_bf16 v[80:95], v[116:119], v[124:127], v[80:95]\n"
        "s_waitcnt lgkmcnt(0)\nv_mfma_f32_32x32x16_bf16 v[32:47], v[96:99], v[104:107], v[32:47]\nds_read_b64_tr_b16 v[112:113], %[pa] offset:26624\n"
        "ds_read_b64_tr_b16 v[114:115], %[pa] offset:27136\nv_mfma_f32_32x32x16_bf16 v[48:63], v[96:99], v[108:111], v[48:63]\nds_read_b128 v[120:123], %[pb] offset:25600\n"
        "ds_read_b64_tr_b16 v[116:117], %[pa] offset:30720\nv_mfma_f32_32x32x16_bf16 v[64:79], v[100:103], v[104:107], v[64:79]\nds_read_b64_tr_b16 v[118:119], %[pa] offset:31232\n"
        "ds_read_b128 v[124:127], %[pb] offset:27648\nv_mfma_f32_32x32x16_bf16 v[80:95], v[100:103], v[108:111], v[80:95]\ns_waitcnt vmcnt(3) lgkmcnt(0)\ns_barrier\n"
        "v_mfma_f32_32x32x16_bf16 v[32:47], v[112:115], v[120:123], v[32:47]\ns_add_u32 m0, %[dw], 24576\ns_add_u32 s40, s40, 0x1c000\ns_addc_u32 s41, s41, 0\n"
        "global_load_lds_dwordx4 %[vw], s[40:41]\nds_read_b64_tr_b16 v[96:97], %[pa] offset:49152\nds_read_b64_tr_b16 v[98:99], %[pa] offset:49664\n"
        "v_mfma_f32_32x32x16_bf16 v[48:63], v[112:115], v[124:127], v[48:63]\ns_add_u32 m0, %[dx], 24576\ns_add_u32 s42, s42, 0x800\ns_addc_u32 s43, s43, 0\n"
        "global_load_lds_dwordx4 %[vx], s[42:43]\nds_read_b128 v[104:107], %[pb] offset:49152\nds_read_b64_tr_b16 v[100:101], %[pa] offset:53248\n"
        "v_mfma_f32_32x32x16_bf16 v[64:79], v[116:119], v[120:123], v[64:79]\nglobal_load_lds_dwordx4 %[vx], s[42:43] offset:1024\n"
        "ds_read_b64_tr_b16 v[102:103], %[pa] offset:53760\nds_read_b128 v[108:111], %[pb] offset:51200\nv_mfma_f32_32x32x16_bf16 v[80:95], v[116:119], v[124:127], v[80:95]\n"
        "s_waitcnt lgkmcnt(0)\nv_mfma_f32_32x32x16_bf16 v[32:47], v[96:99], v[104:107], v[32:47]\nds_read_b64_tr_b16 v[112:113], %[pa] offset:51200\n"
        "ds_read_b64_tr_b16 v[114:115], %[pa] offset:51712\nv_mfma_f32_32x32x16_bf16 v[48:63], v[96:99], v[108:111], v[48:63]\nds_read_b128 v[120:123], %[pb] offset:50176\n"
        "ds_read_b64_tr_b16 v[116:117], %[pa] offset:55296\nv_mfma_f32_32x32x16_bf16 v[64:79], v[100:103], v[104:107], v[64:79]\nds_read_b64_tr_b16 v[118:119], %[pa] offset:55808\n"
        "ds_read_b128 v[124:127], %[pb] offset:52224\nv_mfma_f32_32x32x16_bf16 v[80:95], v[100:103], v[108:111], v[80:95]\ns_waitcnt vmcnt(3) lgkmcnt(0)\ns_barrier\n"
        "v_mfma_f32_32x32x16_bf16 v[32:47], v[112:115], v[120:123], v[32:47]\ns_add_u32 m0, %[dw], 49152\ns_add_u32 s40, s40, 0x1c000\ns_addc_u32 s41, s41, 0\n"
        "global_load_lds_dwordx4 %[vw], s[40:41]\nds_read_b64_tr_b16 v[96:97], %[pa] offset:0\nds_read_b64_tr_b16 v[98:99], %[pa] offset:512\n"
        "v_mfma_f32_32x32x16_bf16 v[48:63], v[112:115], v[124:127], v[48:63]\ns_add_u32 m0, %[dx], 49152\ns_add_u32 s42, s42, 0x800\ns_addc_u32 s43, s43, 0\n"
        "global_load_lds_dwordx4 %[vx], s[42:43]\nds_read_b128 v[104:107], %[pb] offset:0\nds_read_b64_tr_b16 v[100:101], %[pa] offset:4096\n"
        "v_mfma_f32_32x32x16_bf16 v[64:79], v[116:119], v[120:123], v[64:79]\nglobal_load_lds_dwordx4 %[vx], s[42:43] offset:1024\nds_read_b64_tr_b16 v[102:103], %[pa] offset:4608\n"
        "ds_read_b128 v[108:111], %[pb] offset:2048\nv_mfma_f32_32x32x16_bf16 v[80:95], v[116:119], v[124:127], v[80:95]\ns_waitcnt lgkmcnt(0)\n"
        "v_mfma_f32_32x32x16_bf16 v[32:47], v[96:99], v[104:107], v[32:47]\nds_read_b64_tr_b16 v[112:113], %[pa] offset:2048\nds_read_b64_tr_b16 v[114:115], %[pa] offset:2560\n"
        "v_mfma_f32_32x32x16_bf16 v[48:63], v[96:99], v[108:111], v[48:63]\nds_read_b128 v[120:123], %[pb] offset:1024\nds_read_b64_tr_b16 v[116:117], %[pa] offset:6144\n"
        "v_mfma_f32_32x32x16_bf16 v[64:79], v[100:103], v[104:107], v[64:79]\nds_read_b64_tr_b16 v[118:119], %[pa] offset:6656\nds_read_b128 v[124:127], %[pb] offset:3072\n"
        "v_mfma_f32_32x32x16_bf16 v[80:95], v[100:103], v[108:111], v[80:95]\ns_waitcnt vmcnt(3) lgkmcnt(0)\ns_barrier\n"
        "v_mfma_f32_32x32x16_bf16 v[32:47], v[112:115], v[120:123], v[32:47]\ns_add_u32 m0, %[dw], 0\ns_add_u32 s40, s40, 0x1c000\ns_addc_u32 s41, s41, 0\n"
        "global_load_lds_dwordx4 %[vw], s[40:41]\nds_read_b64_tr_b16 v[96:97], %[pa] offset:24576\nds_read_b64_tr_b16 v[98:99], %[pa] offset:25088\n"
        "v_mfma_f32_32x32x16_bf16 v[48:63], v[112:115], v[124:127], v[48:63]\ns_add_u32 m0, %[dx], 0\ns_add_u32 s42, s42, 0x800\ns_addc_u32 s43, s43, 0\n"
        "global_load_lds_dwordx4 %[vx], s[42:43]\nds_read_b128 v[104:107], %[pb] offset:24576\nds_read_b64_tr_b16 v[100:101], %[pa] offset:28672\n"
        "v_mfma_f32_32x32x16_bf16 v[64:79], v[116:119], v[120:123], v[64:79]\nglobal_load_lds_dwordx4 %[vx], s[42:43] offset:1024\n"
        "ds_read_b64_tr_b16 v[102:103], %[pa] offset:29184\nds_read_b128 v[108:111], %[pb] offset:26624\nv_mfma_f32_32x32x16_bf16 v[80:95], v[116:119], v[124:127], v[80:95]\n"
        "s_waitcnt lgkmcnt(0)\nv_mfma_f32_32x32x16_bf16 v[32:47], v[96:99], v[104:107], v[32:47]\nds_read_b64_tr_b16 v[112:113], %[pa] offset:26624\n"
        "ds_read_b64_tr_b16 v[114:115], %[pa] offset:27136\nv_mfma_f32_32x32x16_bf16 v[48:63], v[96:99], v[108:111], v[48:63]\nds_read_b128 v[120:123], %[pb] offset:25600\n"
        "ds_read_b64_tr_b16 v[116:117], %[pa] offset:30720\nv_mfma_f32_32x32x16_bf16 v[64:79], v[100:103], v[104:107], v[64:79]\nds_read_b64_tr_b16 v[118:119], %[pa] offset:31232\n"
        "ds_read_b128 v[124:127], %[pb] offset:27648\nv_mfma_f32_32x32x16_bf16 v[80:95], v[100:103], v[108:111], v[80:95]\ns_waitcnt vmcnt(3) lgkmcnt(0)\ns_barrier\n"
        "v_mfma_f32_32x32x16_bf16 v[32:47], v[112:115], v[120:123], v[32:47]\nds_read_b64_tr_b16 v[96:97], %[pa] offset:49152\nds_read_b64_tr_b16 v[98:99], %[pa] offset:49664\n"
        "v_mfma_f32_32x32x16_bf16 v[48:63], v[112:115], v[124:127], v[48:63]\nds_read_b128 v[104:107], %[pb] offset:49152\nds_read_b64_tr_b16 v[100:101], %[pa] offset:53248\n"
        "v_mfma_f32_32x32x16_bf16 v[64:79], v[116:119], v[120:123], v[64:79]\nds_read_b64_tr_b16 v[102:103], %[pa] offset:53760\nds_read_b128 v[108:111], %[pb] offset:51200\n"
        "v_mfma_f32_32x32x16_bf16 v[80:95], v[116:119], v[124:127], v[80:95]\ns_waitcnt lgkmcnt(0)\nv_mfma_f32_32x32x16_bf16 v[32:47], v[96:99], v[104:107], v[32:47]\n"
        "ds_read_b64_tr_b16 v[112:113], %[pa] offset:51200\nds_read_b64_tr_b16 v[114:115], %[pa] offset:51712\nv_mfma_f32_32x32x16_bf16 v[48:63], v[96:99], v[108:111], v[48:63]\n"
        "ds_read_b128 v[120:123], %[pb] offset:50176\nds_read_b64_tr_b16 v[116:117], %[pa] offset:55296\nv_mfma_f32_32x32x16_bf16 v[64:79], v[100:103], v[104:107], v[64:79]\n"
        "ds_read_b64_tr_b16 v[118:119], %[pa] offset:55808\nds_read_b128 v[124:127], %[pb] offset:52224\nv_mfma_f32_32x32x16_bf16 v[80:95], v[100:103], v[108:111], v[80:95]\n"
        "s_waitcnt vmcnt(0) lgkmcnt(0)\ns_barrier\nv_mfma_f32_32x32x16_bf16 v[32:47], v[112:115], v[120:123], v[32:47]\nds_read_b64_tr_b16 v[96:97], %[pa] offset:0\n"
        "ds_read_b64_tr_b16 v[98:99], %[pa] offset:512\nv_mfma_f32_32x32x16_bf16 v[48:63], v[112:115], v[124:127], v[48:63]\nds_read_b128 v[104:107], %[pb] offset:0\n"
        "ds_read_b64_tr_b16 v[100:101], %[pa] offset:4096\nv_mfma_f32_32x32x16_bf16 v[64:79], v[116:119], v[120:123], v[64:79]\nds_read_b64_tr_b16 v[102:103], %[pa] offset:4608\n"
        "ds_read_b128 v[108:111], %[pb] offset:2048\nv_mfma_f32_32x32x16_bf16 v[80:95], v[116:119], v[124:127], v[80:95]\ns_waitcnt lgkmcnt(0)\n"
        "v_mfma_f32_32x32x16_bf16 v[32:47], v[96:99], v[104:107], v[32:47]\nds_read_b64_tr_b16 v[112:113], %[pa] offset:2048\nds_read_b64_tr_b16 v[114:115], %[pa] offset:2560\n"
        "v_mfma_f32_32x32x16_bf16 v[48:63], v[96:99], v[108:111], v[48:63]\nds_read_b128 v[120:123], %[pb] offset:1024\nds_read_b64_tr_b16 v[116:117], %[pa] offset:6144\n"
        "v_mfma_f32_32x32x16_bf16 v[64:79], v[100:103], v[104:107], v[64:79]\nds_read_b64_tr_b16 v[118:119], %[pa] offset:6656\nds_read_b128 v[124:127], %[pb] offset:3072\n"
        "v_mfma_f32_32x32x16_bf16 v[80:95], v[100:103], v[108:111], v[80:95]\ns_waitcnt lgkmcnt(0)\nv_mfma_f32_32x32x16_bf16 v[32:47], v[112:115], v[120:123], v[32:47]\n"
        "v_mfma_f32_32x32x16_bf16 v[48:63], v[112:115], v[124:127], v[48:63]\nv_mfma_f32_32x32x16_bf16 v[64:79], v[116:119], v[120:123], v[64:79]\n"
        "v_mfma_f32_32x32x16_bf16 v[80:95], v[116:119], v[124:127], v[80:95]\ns_mov_b32 m0, %[sv]\ns_nop 7\ns_nop 7\n"
          : "=&{v[32:47]}"(g0), "=&{v[48:63]}"(g1), "=&{v[64:79]}"(u0), "=&{v[80:95]}"(u1), [jk] "+v"(jk), [sv] "=&s"(sv)
          : [pa] "v"(pa), [pb] "v"(pb), [vw] "v"(vw), [vx] "v"(l16), [tv] "v"(tv), [dw] "s"(dw), [dx] "s"(dx), [sw] "s"(sw + 2 * 114688), [sx] "s"(sx + 2 * 2048), [wd] "s"(wd)
          : "v96", "v97", "v98", "v99", "v100", "v101", "v102", "v103", "v104", "v105", "v106", "v107", "v108", "v109", "v110", "v111", "v112", "v113", "v114", "v115", "v116", "v117", "v118", "v119", "v120", "v121", "v122", "v123", "v124", "v125", "v126", "v127", "s40", "s41", "s42", "s43", "scc", "memory");
        PRIO(P_EPI);
#pragma unroll
        for (int i = 0; i < 16; ++i) { g0[i] = fmaxf(g0[i], 0.f) * u0[i]; g1[i] = fmaxf(g1[i], 0.f) * u1[i]; }
        TS(256 + b, 1);
        char* hp = ws + WS_H + ((TB * 8 + 2 * tk) * 112 + fb * 4 + ft * 2) * 1024;
        st_sc1(l16, hp, pack8(g0, 0)); st_sc1(l16, hp + 1024, pack8(g0, 8)); st_sc1(l16, hp + 114688, pack8(g1, 0)); st_sc1(l16, hp + 115712, pack8(g1, 8));
        BAR(0);
        if (tid == 0) __hip_atomic_fetch_add((GAS unsigned*)(ws + WS_FL + (FL_H + TB) * 128), 1u, RLX_AGENT);
    } else {
        const int j = i5 - 28; TOUCH_END();
        __builtin_amdgcn_s_sleep(127);
        touch((j * 1024 + tid) * 128, wo); TOUCH_END(); touch((j * 1024 + 512 + tid) * 128, wo); TOUCH_END();
    }
    TS(256 + b, 2);
    const int tg = xcd * 4 + (i5 & 3), cg = i5 >> 2, t = w & 1, kq = w >> 1;
    const unsigned vo = (l >> 3) * 1024 + (((l & 7) ^ (((l >> 4) & 1) << 2)) << 4);
    const int at = (4 * h + q) * 128 + (((((l >> 4) & 1) * 2 + (p >> 1)) ^ ((q >> 1) << 2)) << 4) + 8 * (p & 1);
    float gg[8]; u32x2 x0, x1;
    const unsigned ob = (tg * 64 + t * 32 + (l & 31)) * 1024 + cg * 128 + kq * 32;
    {   const char* sw = wd + cg * 128 + w * 14 * 16384; const unsigned dw = L0 + w * 8192;
        const int r0 = 2 * cg - 14;
#define ROT(n) ((n) + r0 < 0 ? (n) + r0 + 14 : (n) + r0)
#define F2_W(n) do { const char* s_ = sw + ROT(n) * 16384; glds16(vo, s_, dw + ((n) & 3) * 2048); glds16(vo, s_ + 8192, dw + ((n) & 3) * 2048 + 1024); } while (0)
        F2_W(0); F2_W(1); F2_W(2); F2_W(3);
        { GAS unsigned* f = (GAS unsigned*)(ws + WS_FL + (FL_H + xcd) * 128);
          for (unsigned sp = 0; sp < SPIN_CAP; ++sp) { if (__builtin_amdgcn_readfirstlane(__hip_atomic_load(f, RLX_AGENT)) >= 28u) break; __builtin_amdgcn_s_sleep(4); } }
        TS(256 + b, 3);
        const char* sh = uni(ws + WS_H + (2 * tg * 112 + w * 14) * 1024);
        f32x16 d00, d01, d10, d11; unsigned ro = ROT(0) * 1024, sv;
        PRIO(P_LOOP2);
        asm volatile(
        "s_mov_b32 %[sv], m0\ns_mov_b64 s[44:45], %[sh]\ns_mov_b64 s[46:47], %[sw]\ns_add_u32 s40, s44, %[ro]\ns_addc_u32 s41, s45, 0\n"
        "global_load_dwordx4 v[112:115], %[h0], s[40:41] sc1\nglobal_load_dwordx4 v[116:119], %[h1], s[40:41] sc1\ns_add_u32 %[ro], %[ro], 1024\ns_cmp_eq_u32 %[ro], 14336\n"
        "s_cselect_b32 %[ro], 0, %[ro]\ns_add_u32 s40, s44, %[ro]\ns_addc_u32 s41, s45, 0\nglobal_load_dwordx4 v[120:123], %[h0], s[40:41] sc1\n"
        "global_load_dwordx4 v[124:127], %[h1], s[40:41] sc1\ns_add_u32 %[ro], %[ro], 1024\ns_cmp_eq_u32 %[ro], 14336\ns_cselect_b32 %[ro], 0, %[ro]\ns_add_u32 s40, s44, %[ro]\n"
        "s_addc_u32 s41, s45, 0\nglobal_load_dwordx4 v[16:19], %[h0], s[40:41] sc1\nglobal_load_dwordx4 v[20:23], %[h1], s[40:41] sc1\ns_add_u32 %[ro], %[ro], 1024\n"
        "s_cmp_eq_u32 %[ro], 14336\ns_cselect_b32 %[ro], 0, %[ro]\ns_add_u32 s40, s44, %[ro]\ns_addc_u32 s41, s45, 0\nglobal_load_dwordx4 v[24:27], %[h0], s[40:41] sc1\n"
        "global_load_dwordx4 v[28:31], %[h1], s[40:41] sc1\ns_add_u32 %[ro], %[ro], 1024\ns_cmp_eq_u32 %[ro], 14336\ns_cselect_b32 %[ro], 0, %[ro]\ns_waitcnt vmcnt(8)\n"
        "ds_read_b64_tr_b16 v[96:97], %[a0] offset:0\nds_read_b64_tr_b16 v[98:99], %[a0] offset:1024\nds_read_b64_tr_b16 v[100:101], %[a1] offset:0\n"
        "ds_read_b64_tr_b16 v[102:103], %[a1] offset:1024\ns_waitcnt vmcnt(6) lgkmcnt(0)\nv_mfma_f32_32x32x16_bf16 v[32:47], v[104:107], v[112:115], 0\n"
        "ds_read_b64_tr_b16 v[104:105], %[a0] offset:2048\nds_read_b64_tr_b16 v[106:107], %[a0] offset:3072\ns_lshl_b32 s42, %[ro], 4\ns_add_u32 s42, s46, s42\n"
        "s_addc_u32 s43, s47, 0\ns_add_u32 m0, %[dw], 0\nv_mfma_f32_32x32x16_bf16 v[48:63], v[108:111], v[112:115], 0\nds_read_b64_tr_b16 v[108:109], %[a1] offset:2048\n"
        "ds_read_b64_tr_b16 v[110:111], %[a1] offset:3072\nglobal_load_lds_dwordx4 %[w0], s[42:43]\nv_mfma_f32_32x32x16_bf16 v[64:79], v[104:107], v[116:119], 0\n"
        "global_load_lds_dwordx4 %[w1], s[42:43] offset:1024\nv_mfma_f32_32x32x16_bf16 v[80:95], v[108:111], v[116:119], 0\ns_add_u32 s40, s44, %[ro]\ns_addc_u32 s41, s45, 0\n"
        "global_load_dwordx4 v[112:115], %[h0], s[40:41] sc1\nglobal_load_dwordx4 v[116:119], %[h1], s[40:41] sc1\ns_add_u32 %[ro], %[ro], 1024\ns_cmp_eq_u32 %[ro], 14336\n"
        "s_cselect_b32 %[ro], 0, %[ro]\ns_waitcnt vmcnt(8) lgkmcnt(0)\nv_mfma_f32_32x32x16_bf16 v[32:47], v[96:99], v[120:123], v[32:47]\n"
        "ds_read_b64_tr_b16 v[96:97], %[a0] offset:4096\nds_read_b64_tr_b16 v[98:99], %[a0] offset:5120\ns_lshl_b32 s42, %[ro], 4\ns_add_u32 s42, s46, s42\ns_addc_u32 s43, s47, 0\n"
        "s_add_u32 m0, %[dw], 2048\nv_mfma_f32_32x32x16_bf16 v[48:63], v[100:103], v[120:123], v[48:63]\nds_read_b64_tr_b16 v[100:101], %[a1] offset:4096\n"
        "ds_read_b64_tr_b16 v[102:103], %[a1] offset:5120\nglobal_load_lds_dwordx4 %[w0], s[42:43]\nv_mfma_f32_32x32x16_bf16 v[64:79], v[96:99], v[124:127], v[64:79]\n"
        "global_load_lds_dwordx4 %[w1], s[42:43] offset:1024\nv_mfma_f32_32x32x16_bf16 v[80:95], v[100:103], v[124:127], v[80:95]\ns_add_u32 s40, s44, %[ro]\n"
        "s_addc_u32 s41, s45, 0\nglobal_load_dwordx4 v[120:123], %[h0], s[40:41] sc1\nglobal_load_dwordx4 v[124:127], %[h1], s[40:41] sc1\ns_add_u32 %[ro], %[ro], 1024\n"
        "s_cmp_eq_u32 %[ro], 14336\ns_cselect_b32 %[ro], 0, %[ro]\ns_waitcnt vmcnt(10) lgkmcnt(0)\nv_mfma_f32_32x32x16_bf16 v[32:47], v[104:107], v[16:19], v[32:47]\n"
        "ds_read_b64_tr_b16 v[104:105], %[a0] offset:6144\nds_read_b64_tr_b16 v[106:107], %[a0] offset:7168\ns_lshl_b32 s42, %[ro], 4\ns_add_u32 s42, s46, s42\n"
        "s_addc_u32 s43, s47, 0\ns_add_u32 m0, %[dw], 4096\nv_mfma_f32_32x32x16_bf16 v[48:63], v[108:111], v[16:19], v[48:63]\nds_read_b64_tr_b16 v[108:109], %[a1] offset:6144\n"
        "ds_read_b64_tr_b16 v[110:111], %[a1] offset:7168\nglobal_load_lds_dwordx4 %[w0], s[42:43]\nv_mfma_f32_32x32x16_bf16 v[64:79], v[104:107], v[20:23], v[64:79]\n"
        "global_load_lds_dwordx4 %[w1], s[42:43] offset:1024\nv_mfma_f32_32x32x16_bf16 v[80:95], v[108:111], v[20:23], v[80:95]\ns_add_u32 s40, s44, %[ro]\ns_addc_u32 s41, s45, 0\n"
        "global_load_dwordx4 v[16:19], %[h0], s[40:41] sc1\nglobal_load_dwordx4 v[20:23], %[h1], s[40:41] sc1\ns_add_u32 %[ro], %[ro], 1024\ns_cmp_eq_u32 %[ro], 14336\n"
        "s_cselect_b32 %[ro], 0, %[ro]\ns_waitcnt vmcnt(10) lgkmcnt(0)\nv_mfma_f32_32x32x16_bf16 v[32:47], v[96:99], v[24:27], v[32:47]\n"
        "ds_read_b64_tr_b16 v[96:97], %[a0] offset:0\nds_read_b64_tr_b16 v[98:99], %[a0] offset:1024\ns_lshl_b32 s42, %[ro], 4\ns_add_u32 s42, s46, s42\ns_addc_u32 s43, s47, 0\n"
        "s_add_u32 m0, %[dw], 6144\nv_mfma_f32_32x32x16_bf16 v[48:63], v[100:103], v[24:27], v[48:63]\nds_read_b64_tr_b16 v[100:101], %[a1] offset:0\n"
        "ds_read_b64_tr_b16 v[102:103], %[a1] offset:1024\nglobal_load_lds_dwordx4 %[w0], s[42:43]\nv_mfma_f32_32x32x16_bf16 v[64:79], v[96:99], v[28:31], v[64:79]\n"
        "global_load_lds_dwordx4 %[w1], s[42:43] offset:1024\nv_mfma_f32_32x32x16_bf16 v[80:95], v[100:103], v[28:31], v[80:95]\ns_add_u32 s40, s44, %[ro]\ns_addc_u32 s41, s45, 0\n"
        "global_load_dwordx4 v[24:27], %[h0], s[40:41] sc1\nglobal_load_dwordx4 v[28:31], %[h1], s[40:41] sc1\ns_add_u32 %[ro], %[ro], 1024\ns_cmp_eq_u32 %[ro], 14336\n"
        "s_cselect_b32 %[ro], 0, %[ro]\ns_waitcnt vmcnt(10) lgkmcnt(0)\nv_mfma_f32_32x32x16_bf16 v[32:47], v[104:107], v[112:115], v[32:47]\n"
        "ds_read_b64_tr_b16 v[104:105], %[a0] offset:2048\nds_read_b64_tr_b16 v[106:107], %[a0] offset:3072\ns_lshl_b32 s42, %[ro], 4\ns_add_u32 s42, s46, s42\n"
        "s_addc_u32 s43, s47, 0\ns_add_u32 m0, %[dw], 0\nv_mfma_f32_32x32x16_bf16 v[48:63], v[108:111], v[112:115], v[48:63]\nds_read_b64_tr_b16 v[108:109], %[a1] offset:2048\n"
        "ds_read_b64_tr_b16 v[110:111], %[a1] offset:3072\nglobal_load_lds_dwordx4 %[w0], s[42:43]\nv_mfma_f32_32x32x16_bf16 v[64:79], v[104:107], v[116:119], v[64:79]\n"
        "global_load_lds_dwordx4 %[w1], s[42:43] offset:1024\nv_mfma_f32_32x32x16_bf16 v[80:95], v[108:111], v[116:119], v[80:95]\ns_add_u32 s40, s44, %[ro]\n"
        "s_addc_u32 s41, s45, 0\nglobal_load_dwordx4 v[112:115], %[h0], s[40:41] sc1\nglobal_load_dwordx4 v[116:119], %[h1], s[40:41] sc1\ns_add_u32 %[ro], %[ro], 1024\n"
        "s_cmp_eq_u32 %[ro], 14336\ns_cselect_b32 %[ro], 0, %[ro]\ns_waitcnt vmcnt(10) lgkmcnt(0)\nv_mfma_f32_32x32x16_bf16 v[32:47], v[96:99], v[120:123], v[32:47]\n"
        "ds_read_b64_tr_b16 v[96:97], %[a0] offset:4096\nds_read_b64_tr_b16 v[98:99], %[a0] offset:5120\ns_lshl_b32 s42, %[ro], 4\ns_add_u32 s42, s46, s42\ns_addc_u32 s43, s47, 0\n"
        "s_add_u32 m0, %[dw], 2048\nv_mfma_f32_32x32x16_bf16 v[48:63], v[100:103], v[120:123], v[48:63]\nds_read_b64_tr_b16 v[100:101], %[a1] offset:4096\n"
        "ds_read_b64_tr_b16 v[102:103], %[a1] offset:5120\nglobal_load_lds_dwordx4 %[w0], s[42:43]\nv_mfma_f32_32x32x16_bf16 v[64:79], v[96:99], v[124:127], v[64:79]\n"
        "global_load_lds_dwordx4 %[w1], s[42:43] offset:1024\nv_mfma_f32_32x32x16_bf16 v[80:95], v[100:103], v[124:127], v[80:95]\ns_add_u32 s40, s44, %[ro]\n"
        "s_addc_u32 s41, s45, 0\nglobal_load_dwordx4 v[120:123], %[h0], s[40:41] sc1\nglobal_load_dwordx4 v[124:127], %[h1], s[40:41] sc1\ns_add_u32 %[ro], %[ro], 1024\n"
        "s_cmp_eq_u32 %[ro], 14336\ns_cselect_b32 %[ro], 0, %[ro]\ns_waitcnt vmcnt(10) lgkmcnt(0)\nv_mfma_f32_32x32x16_bf16 v[32:47], v[104:107], v[16:19], v[32:47]\n"
        "ds_read_b64_tr_b16 v[104:105], %[a0] offset:6144\nds_read_b64_tr_b16 v[106:107], %[a0] offset:7168\ns_lshl_b32 s42, %[ro], 4\ns_add_u32 s42, s46, s42\n"
        "s_addc_u32 s43, s47, 0\ns_add_u32 m0, %[dw], 4096\nv_mfma_f32_32x32x16_bf16 v[48:63], v[108:111], v[16:19], v[48:63]\nds_read_b64_tr_b16 v[108:109], %[a1] offset:6144\n"
        "ds_read_b64_tr_b16 v[110:111], %[a1] offset:7168\nglobal_load_lds_dwordx4 %[w0], s[42:43]\nv_mfma_f32_32x32x16_bf16 v[64:79], v[104:107], v[20:23], v[64:79]\n"
        "global_load_lds_dwordx4 %[w1], s[42:43] offset:1024\nv_mfma_f32_32x32x16_bf16 v[80:95], v[108:111], v[20:23], v[80:95]\ns_add_u32 s40, s44, %[ro]\ns_addc_u32 s41, s45, 0\n"
        "global_load_dwordx4 v[16:19], %[h0], s[40:41] sc1\nglobal_load_dwordx4 v[20:23], %[h1], s[40:41] sc1\ns_add_u32 %[ro], %[ro], 1024\ns_cmp_eq_u32 %[ro], 14336\n"
        "s_cselect_b32 %[ro], 0, %[ro]\ns_waitcnt vmcnt(10) lgkmcnt(0)\nv_mfma_f32_32x32x16_bf16 v[32:47], v[96:99], v[24:27], v[32:47]\n"
        "ds_read_b64_tr_b16 v[96:97], %[a0] offset:0\nds_read_b64_tr_b16 v[98:99], %[a0] offset:1024\ns_lshl_b32 s42, %[ro], 4\ns_add_u32 s42, s46, s42\ns_addc_u32 s43, s47, 0\n"
        "s_add_u32 m0, %[dw], 6144\nv_mfma_f32_32x32x16_bf16 v[48:63], v[100:103], v[24:27], v[48:63]\nds_read_b64_tr_b16 v[100:101], %[a1] offset:0\n"
        "ds_read_b64_tr_b16 v[102:103], %[a1] offset:1024\nglobal_load_lds_dwordx4 %[w0], s[42:43]\nv_mfma_f32_32x32x16_bf16 v[64:79], v[96:99], v[28:31], v[64:79]\n"
        "global_load_lds_dwordx4 %[w1], s[42:43] offset:1024\nv_mfma_f32_32x32x16_bf16 v[80:95], v[100:103], v[28:31], v[80:95]\ns_add_u32 s40, s44, %[ro]\ns_addc_u32 s41, s45, 0\n"
        "global_load_dwordx4 v[24:27], %[h0], s[40:41] sc1\nglobal_load_dwordx4 v[28:31], %[h1], s[40:41] sc1\ns_add_u32 %[ro], %[ro], 1024\ns_cmp_eq_u32 %[ro], 14336\n"
        "s_cselect_b32 %[ro], 0, %[ro]\ns_waitcnt vmcnt(10) lgkmcnt(0)\nv_mfma_f32_32x32x16_bf16 v[32:47], v[104:107], v[112:115], v[32:47]\n"
        "ds_read_b64_tr_b16 v[104:105], %[a0] offset:2048\nds_read_b64_tr_b16 v[106:107], %[a0] offset:3072\ns_lshl_b32 s42, %[ro], 4\ns_add_u32 s42, s46, s42\n"
        "s_addc_u32 s43, s47, 0\ns_add_u32 m0, %[dw], 0\nv_mfma_f32_32x32x16_bf16 v[48:63], v[108:111], v[112:115], v[48:63]\nds_read_b64_tr_b16 v[108:109], %[a1] offset:2048\n"
        "ds_read_b64_tr_b16 v[110:111], %[a1] offset:3072\nglobal_load_lds_dwordx4 %[w0], s[42:43]\nv_mfma_f32_32x32x16_bf16 v[64:79], v[104:107], v[116:119], v[64:79]\n"
        "global_load_lds_dwordx4 %[w1], s[42:43] offset:1024\nv_mfma_f32_32x32x16_bf16 v[80:95], v[108:111], v[116:119], v[80:95]\ns_add_u32 s40, s44, %[ro]\n"
        "s_addc_u32 s41, s45, 0\nglobal_load_dwordx4 v[112:115], %[h0], s[40:41] sc1\nglobal_load_dwordx4 v[116:119], %[h1], s[40:41] sc1\ns_add_u32 %[ro], %[ro], 1024\n"
        "s_cmp_eq_u32 %[ro], 14336\ns_cselect_b32 %[ro], 0, %[ro]\ns_waitcnt vmcnt(10) lgkmcnt(0)\nv_mfma_f32_32x32x16_bf16 v[32:47], v[96:99], v[120:123], v[32:47]\n"
        "ds_read_b64_tr_b16 v[96:97], %[a0] offset:4096\nds_read_b64_tr_b16 v[98:99], %[a0] offset:5120\ns_lshl_b32 s42, %[ro], 4\ns_add_u32 s42, s46, s42\ns_addc_u32 s43, s47, 0\n"
        "s_add_u32 m0, %[dw], 2048\nv_mfma_f32_32x32x16_bf16 v[48:63], v[100:103], v[120:123], v[48:63]\nds_read_b64_tr_b16 v[100:101], %[a1] offset:4096\n"
        "ds_read_b64_tr_b16 v[102:103], %[a1] offset:5120\nglobal_load_lds_dwordx4 %[w0], s[42:43]\nv_mfma_f32_32x32x16_bf16 v[64:79], v[96:99], v[124:127], v[64:79]\n"
        "global_load_lds_dwordx4 %[w1], s[42:43] offset:1024\nv_mfma_f32_32x32x16_bf16 v[80:95], v[100:103], v[124:127], v[80:95]\ns_add_u32 s40, s44, %[ro]\n"
        "s_addc_u32 s41, s45, 0\nglobal_load_dwordx4 v[120:123], %[h0], s[40:41] sc1\nglobal_load_dwordx4 v[124:127], %[h1], s[40:41] sc1\ns_add_u32 %[ro], %[ro], 1024\n"
        "s_cmp_eq_u32 %[ro], 14336\ns_cselect_b32 %[ro], 0, %[ro]\ns_waitcnt vmcnt(10) lgkmcnt(0)\nv_mfma_f32_32x32x16_bf16 v[32:47], v[104:107], v[16:19], v[32:47]\n"
        "ds_read_b64_tr_b16 v[104:105], %[a0] offset:6144\nds_read_b64_tr_b16 v[106:107], %[a0] offset:7168\nv_mfma_f32_32x32x16_bf16 v[48:63], v[108:111], v[16:19], v[48:63]\n"
        "ds_read_b64_tr_b16 v[108:109], %[a1] offset:6144\nds_read_b64_tr_b16 v[110:111], %[a1] offset:7168\nv_mfma_f32_32x32x16_bf16 v[64:79], v[104:107], v[20:23], v[64:79]\n"
        "v_mfma_f32_32x32x16_bf16 v[80:95], v[108:111], v[20:23], v[80:95]\ns_waitcnt vmcnt(6) lgkmcnt(0)\nv_mfma_f32_32x32x16_bf16 v[32:47], v[96:99], v[24:27], v[32:47]\n"
        "ds_read_b64_tr_b16 v[96:97], %[a0] offset:0\nds_read_b64_tr_b16 v[98:99], %[a0] offset:1024\nv_mfma_f32_32x32x16_bf16 v[48:63], v[100:103], v[24:27], v[48:63]\n"
        "ds_read_b64_tr_b16 v[100:101], %[a1] offset:0\nds_read_b64_tr_b16 v[102:103], %[a1] offset:1024\nv_mfma_f32_32x32x16_bf16 v[64:79], v[96:99], v[28:31], v[64:79]\n"
        "v_mfma_f32_32x32x16_bf16 v[80:95], v[100:103], v[28:31], v[80:95]\ns_waitcnt vmcnt(2) lgkmcnt(0)\nv_mfma_f32_32x32x16_bf16 v[32:47], v[104:107], v[112:115], v[32:47]\n"
        "ds_read_b64_tr_b16 v[104:105], %[a0] offset:2048\nds_read_b64_tr_b16 v[106:107], %[a0] offset:3072\nv_mfma_f32_32x32x16_bf16 v[48:63], v[108:111], v[112:115], v[48:63]\n"
        "ds_read_b64_tr_b16 v[108:109], %[a1] offset:2048\nds_read_b64_tr_b16 v[110:111], %[a1] offset:3072\nv_mfma_f32_32x32x16_bf16 v[64:79], v[104:107], v[116:119], v[64:79]\n"
        "v_mfma_f32_32x32x16_bf16 v[80:95], v[108:111], v[116:119], v[80:95]\ns_waitcnt vmcnt(0) lgkmcnt(0)\nv_mfma_f32_32x32x16_bf16 v[32:47], v[104:107], v[120:123], v[32:47]\n"
        "v_mfma_f32_32x32x16_bf16 v[48:63], v[108:111], v[120:123], v[48:63]\nv_mfma_f32_32x32x16_bf16 v[64:79], v[104:107], v[124:127], v[64:79]\n"
        "v_mfma_f32_32x32x16_bf16 v[80:95], v[108:111], v[124:127], v[80:95]\ns_mov_b32 m0, %[sv]\ns_nop 7\ns_nop 7\n"
          : "=&{v[32:47]}"(d00), "=&{v[48:63]}"(d01), "=&{v[64:79]}"(d10), "=&{v[80:95]}"(d11), [ro] "+s"(ro), [sv] "=&s"(sv)
          : [a0] "v"(dw + at), [a1] "v"(dw + (at ^ 64)), [h0] "v"(l16), [h1] "v"(l16 + 114688), [w0] "v"(vo), [w1] "v"(vo + 7168), [dw] "s"(dw), [sh] "s"(sh), [sw] "s"(sw)
          : "v96", "v97", "v98", "v99", "v100", "v101", "v102", "v103", "v104", "v105", "v106", "v107", "v108", "v109", "v110", "v111", "v112", "v113", "v114", "v115", "v116", "v117", "v118", "v119", "v120", "v121", "v122", "v123", "v124", "v125", "v126", "v127", "v16", "v17", "v18", "v19", "v20", "v21", "v22", "v23", "v24", "v25", "v26", "v27", "v28", "v29", "v30", "v31", "s40", "s41", "s42", "s43", "s44", "s45", "s46", "s47", "scc", "memory");
        PRIO(P_EPI);
        TS(256 + b, 4);
        asm volatile("global_load_dwordx2 %0, %2, %3\n\tglobal_load_dwordx2 %1, %2, %3 offset:16" : "=&v"(x0), "=&v"(x1) : "v"(ob + 8 * h), "s"(x) : "memory");
        const lptr pw = L + w * 2048 + l16, pr = L + kq * 16384 + l16; f32x4 a, c;
#define F2_RED(dA, dB, tt) do { BARL(); _Pragma("unroll") for (int u = 0; u < 4; ++u) { lw128f(pw + (u >> 1) * 16384 + (u & 1) * 1024, dA[4 * u], dA[4 * u + 1], dA[4 * u + 2], dA[4 * u + 3]); \
            lw128f(pw + 32768 + (u >> 1) * 16384 + (u & 1) * 1024, dB[4 * u], dB[4 * u + 1], dB[4 * u + 2], dB[4 * u + 3]); } BARL(); \
            if (t == tt) { a = lr128f(pr); c = lr128f(pr + 1024); _Pragma("unroll") for (int u = 1; u < 8; ++u) { a += lr128f(pr + u * 2048); c += lr128f(pr + u * 2048 + 1024); } } } while (0)
        F2_RED(d00, d01, 0); F2_RED(d10, d11, 1);
#pragma unroll
        for (int i = 0; i < 4; ++i) { gg[i] = a[i] * __builtin_amdgcn_rcpf(1.f + __builtin_amdgcn_exp2f(a[i] * -1.4426950408889634f));
                                      gg[4 + i] = c[i] * __builtin_amdgcn_rcpf(1.f + __builtin_amdgcn_exp2f(c[i] * -1.4426950408889634f)); } }
    BARL(); TS(256 + b, 15);
#pragma unroll
    for (int n = 0; n < 8; ++n) glds16(vo, wo + cg * 128 + (w * 8 + n) * 8192, L0 + (w * 8 + n) * 1024);
    BAR(0); asm volatile("" : "+v"(x0), "+v"(x1)); TS(256 + b, 5);
    { GAS unsigned* f = (GAS unsigned*)(ws + WS_FL + (tg * 8 + 2 * kq + (l & 1)) * 128);
      for (unsigned sp = 0; sp < SPIN_CAP; ++sp) { if (__builtin_amdgcn_ballot_w64(__hip_atomic_load(f, RLX_AGENT) == 0u) == 0ull) break; __builtin_amdgcn_s_sleep(2); } }
    TS(256 + b, 6);
    bf16x8 of[8];
    { const char* so = ws + WS_O + ((2 * tg + t) * 32 + 8 * kq) * 1024;
#pragma unroll
      for (int s = 0; s < 8; ++s) LD_SC1(of[s], l16, so + s * 1024); }
    f32x16 c0 = splat16(0.f), c1 = c0;
    const lptr a0 = L + kq * 16384 + at, a1 = L + kq * 16384 + (at ^ 64);
#pragma unroll
    for (int s = 0; s < 8; ++s) { const bf16x8 f0 = ltr(a0, s * 2048, s * 2048 + 1024), f1 = ltr(a1, s * 2048, s * 2048 + 1024);
        asm volatile("s_waitcnt vmcnt(%1)" : "+v"(of[s]) : "n"(7 - s) : "memory");
        c0 = MFMA(f0, of[s], c0); c1 = MFMA(f1, of[s], c1); }
    BARL();
    const lptr pw = L + (t * 16 + kq) * 2048 + l16;
#pragma unroll
    for (int u = 0; u < 4; ++u) { lw128f(pw + (u >> 1) * 8192 + (u & 1) * 1024, c0[4 * u], c0[4 * u + 1], c0[4 * u + 2], c0[4 * u + 3]);
                                  lw128f(pw + 16384 + (u >> 1) * 8192 + (u & 1) * 1024, c1[4 * u], c1[4 * u + 1], c1[4 * u + 2], c1[4 * u + 3]); }
    BARL();
    const lptr pr = L + (t * 16 + kq * 4) * 2048 + l16; f32x4 a = lr128f(pr), c = lr128f(pr + 1024);
#pragma unroll
    for (int u = 1; u < 4; ++u) { a += lr128f(pr + u * 2048); c += lr128f(pr + u * 2048 + 1024); }
    const unsigned A0 = pk(blo(x0[0]) + a[0] * gg[0], bhi(x0[0]) + a[1] * gg[1]), A1 = pk(blo(x0[1]) + a[2] * gg[2], bhi(x0[1]) + a[3] * gg[3]);
    const unsigned B0 = pk(blo(x1[0]) + c[0] * gg[4], bhi(x1[0]) + c[1] * gg[5]), B1 = pk(blo(x1[1]) + c[2] * gg[6], bhi(x1[1]) + c[3] * gg[7]);
    auto s0 = __builtin_amdgcn_permlane32_swap(A0, B0, false, false), s1 = __builtin_amdgcn_permlane32_swap(A1, B1, false, false);
    const u32x4 ov = {s0[0], s1[0], s0[1], s1[1]};
    if (WT & 2) asm volatile("global_store_dwordx4 %0, %1, %2 sc0 sc1" ::"v"(ob + 16 * h), "v"(ov), "s"(out) : "memory"); else *(u32x4*)(out + ob + 16 * h) = ov;
    TS(256 + b, 7);
}

__global__ __launch_bounds__(512, 4) void k2_kernel(const char* x, const char* wq, const char* wo, const char* wg, const char* wu, const char* wd, char* out, char* ws) {
    extern __shared__ char smem[];
    const lptr L = (lptr)smem; const unsigned L0 = (unsigned)(size_t)L;
    const int b = blockIdx.x;
    if (b < 256) attn_role(b, L, L0, wq, ws); else if (!NOFFN) ffn_role(b - 256, L, L0, x, wo, wg, wu, wd, out, ws);
}

extern "C" void kernel_launch(void* const* d_in, const int* in_sizes, int n_in, void* d_out, int out_size, void* d_ws, size_t ws_size, hipStream_t stream) {
    (void)hipFuncSetAttribute((const void*)k1_kernel, hipFuncAttributeMaxDynamicSharedMemorySize, LDS_K1);
    (void)hipFuncSetAttribute((const void*)k2_kernel, hipFuncAttributeMaxDynamicSharedMemorySize, LDS_K2);
    k1_kernel<<<256, 512, LDS_K1, stream>>>((const char*)d_in[0], (const char*)d_in[2], (const char*)d_in[3], (const char*)d_in[1], (const char*)d_in[6], (const char*)d_in[7], (const char*)d_in[8], (const char*)d_in[4], (const char*)d_out, (char*)d_ws);
    k2_kernel<<<512, 512, LDS_K2, stream>>>((const char*)d_in[0], (const char*)d_in[1], (const char*)d_in[4], (const char*)d_in[6], (const char*)d_in[7], (const char*)d_in[8], (char*)d_out, (char*)d_ws);
}
```

```cpp
#include <hip/hip_runtime.h>
#include <hip/hip_bf16.h>
#include <stdint.h>

typedef short bf16x8 __attribute__((ext_vector_type(8)));
typedef short s16x4 __attribute__((ext_vector_type(4)));
typedef float f32x16 __attribute__((ext_vector_type(16)));
typedef float f32x4 __attribute__((ext_vector_type(4)));
typedef float f32x2 __attribute__((ext_vector_type(2)));
typedef __bf16 b16x2 __attribute__((ext_vector_type(2)));
typedef unsigned u32x4 __attribute__((ext_vector_type(4)));
typedef unsigned u32x2 __attribute__((ext_vector_type(2)));
#define LAS __attribute__((address_space(3)))
#define GAS __attribute__((address_space(1)))
#define DI __device__ __forceinline__
#define MFMA(a, b, c) __builtin_amdgcn_mfma_f32_32x32x16_bf16((a), (b), (c), 0, 0, 0)
#define RLX_AGENT __ATOMIC_RELAXED, __HIP_MEMORY_SCOPE_AGENT
typedef LAS char* lptr;

constexpr unsigned WS_XN = 0, WS_KV = 2u << 20, WS_H = 4u << 20, WS_O = 12u << 20, WS_FL = 16u << 20;
constexpr int FL_O = 0, FL_H = 256, FL_X = 512, FL_A = 640, FL_E = 1152;
constexpr int LDS_K2 = 81920;
constexpr unsigned SPIN_CAP = 1u << 20;
#define NOFFN 0
#define P_LOOP 0
#define P_LOOP2 0
#define P_ATT 1
#define TOG 0
#define BAL 1
#define P_EPI 3
#define TK 1
#define TX 0
#define WT 0
#define DLY 48
#define PSL 4
#define TLBW 0
#define PRIO(n) __builtin_amdgcn_s_setprio(n)
#define TS(blk, i)

DI unsigned pk(float a, float b) { f32x2 v = {a, b}; return __builtin_bit_cast(unsigned, __builtin_convertvector(v, b16x2)); }
DI bf16x8 pack8(const f32x16& x, int o) { u32x4 r = {pk(x[o], x[o + 1]), pk(x[o + 2], x[o + 3]), pk(x[o + 4], x[o + 5]), pk(x[o + 6], x[o + 7])}; return __builtin_bit_cast(bf16x8, r); }
DI float blo(unsigned u) { return __uint_as_float(u << 16); }
DI float bhi(unsigned u) { return __uint_as_float(u & 0xffff0000u); }
DI bf16x8 lr128(lptr p) { return *(const LAS bf16x8*)p; }
DI f32x4 lr128f(lptr p) { return *(const LAS f32x4*)p; }
DI void lw128f(lptr p, float a, float b, float c, float d) { f32x4 v = {a, b, c, d}; *(LAS f32x4*)p = v; }
DI bf16x8 ltr(lptr p, int o0, int o1) {
    s16x4 a = __builtin_amdgcn_ds_read_tr16_b64_v4i16((LAS s16x4*)(p + o0)), b = __builtin_amdgcn_ds_read_tr16_b64_v4i16((LAS s16x4*)(p + o1));
    return __builtin_shufflevector(a, b, 0, 1, 2, 3, 4, 5, 6, 7); }
DI void glds16(unsigned voff, const void* sbase, unsigned lds) {
    unsigned sv; asm volatile("s_mov_b32 %0, m0\n\ts_mov_b32 m0, %3\n\ts_nop 0\n\tglobal_load_lds_dwordx4 %1, %2\n\ts_mov_b32 m0, %0" : "=&s"(sv) : "v"(voff), "s"(sbase), "s"(lds) : "memory"); }
DI void glds16c(unsigned voff, const void* sbase, unsigned lds) {
    unsigned sv; asm volatile("s_mov_b32 %0, m0\n\ts_mov_b32 m0, %3\n\ts_nop 0\n\tglobal_load_lds_dwordx4 %1, %2 sc1\n\ts_mov_b32 m0, %0" : "=&s"(sv) : "v"(voff), "s"(sbase), "s"(lds) : "memory"); }
DI const char* uni(const char* p) { const unsigned long long a = (unsigned long long)p; return (const char*)(((unsigned long long)(unsigned)__builtin_amdgcn_readfirstlane((unsigned)(a >> 32)) << 32) | (unsigned)__builtin_amdgcn_readfirstlane((unsigned)a)); }
#define BAR(N) asm volatile("s_waitcnt vmcnt(" #N ") lgkmcnt(0)\n\ts_barrier" ::: "memory")
#define BARL() asm volatile("s_waitcnt lgkmcnt(0)\n\ts_barrier" ::: "memory")
DI void st_sc1(unsigned voff, void* sbase, bf16x8 v) { asm volatile("global_store_dwordx4 %0, %1, %2 sc1\n\ts_nop 1" ::"v"(voff), "v"(v), "s"(sbase) : "memory"); }
#define LD_SC1(dst, voff, sbase) asm volatile("global_load_dwordx4 %0, %1, %2 sc1" : "=v"(dst) : "v"(voff), "s"(sbase) : "memory")
DI bf16x8 ldc(const char* p) { bf16x8 v; asm volatile("global_load_dwordx4 %0, %1, off sc1\n\ts_waitcnt vmcnt(0)" : "=v"(v) : "v"(p) : "memory"); return v; }
#define touch(voff, sbase) asm volatile("global_load_dword %0, %1, %2" : "+v"(jk) : "v"(voff), "s"(sbase) : "memory")
#define TOUCH_END() asm volatile("s_waitcnt vmcnt(0)" : "+v"(jk)::"memory")
template <int C> DI float dpp(float x) { return __builtin_bit_cast(float, __builtin_amdgcn_mov_dpp(__builtin_bit_cast(int, x), C, 0xf, 0xf, true)); }
DI float sum32(float x) {
    x += dpp<0xB1>(x); x += dpp<0x4E>(x); x += dpp<0x141>(x); x += dpp<0x140>(x);
    auto s = __builtin_amdgcn_permlane16_swap(__float_as_uint(x), __float_as_uint(x), false, false);
    return __uint_as_float(s[0]) + __uint_as_float(s[1]); }
DI float xhalf_max(float m) { auto r = __builtin_amdgcn_permlane32_swap(__float_as_uint(m), __float_as_uint(m), false, false); return fmaxf(__uint_as_float(r[0]), __uint_as_float(r[1])); }
DI float max16(const f32x16& s, float m) {
#pragma unroll
    for (int i = 0; i < 16; i += 2) m = fmaxf(fmaxf(m, s[i]), s[i + 1]);
    return m; }
DI f32x16 splat16(float v) { f32x16 r;
#pragma unroll
    for (int i = 0; i < 16; ++i) r[i] = v;
    return r; }

typedef unsigned long long u64;
DI u64 mk_tag(u64 e) { e = (u64)uni((const char*)e); return e * 0x9E3779B97F4A7C15ull + 0x632BE59BD9B4E019ull; }
DI void set_flag(char* ws, int f, u64 tag) { __hip_atomic_store((GAS u64*)(ws + WS_FL + f * 128), tag, RLX_AGENT); }
DI void wait_flags(char* ws, int f, u64 tag) {
    GAS u64* p = (GAS u64*)(ws + WS_FL + f * 128);
    for (unsigned sp = 0; sp < SPIN_CAP; ++sp) { if (__builtin_amdgcn_ballot_w64(__hip_atomic_load(p, RLX_AGENT) != tag) == 0ull) break; __builtin_amdgcn_s_sleep(PSL); } }

template <class F> DI void ln_kv(int tok0, const lptr L, unsigned L0, const char* x, const char* wk, const char* wv, char* ws, F mid) {
    const int tid = threadIdx.x, l = tid & 63, w = __builtin_amdgcn_readfirstlane(tid >> 6), r = l & 31, h = l >> 5;
    constexpr int WK = 8192, WV = 32768, XL = 57344, RED = 8192;
    const int tl = 2 * w + h;
    const char* xr = x + (size_t)(tok0 + tl) * 1024 + r * 16;
    const u32x4 xa = *(const u32x4*)xr, xb = *(const u32x4*)(xr + 512);
    glds16(l * 16, wk + w * 2048, L0 + WK + w * 2048); glds16(l * 16, wk + w * 2048 + 1024, L0 + WK + w * 2048 + 1024);
    glds16(l * 16, wv + w * 2048, L0 + WV + w * 2048); glds16(l * 16, wv + w * 2048 + 1024, L0 + WV + w * 2048 + 1024);
    mid();
    float v[16];
#pragma unroll
    for (int i = 0; i < 4; ++i) { v[2 * i] = blo(xa[i]); v[2 * i + 1] = bhi(xa[i]); v[8 + 2 * i] = blo(xb[i]); v[9 + 2 * i] = bhi(xb[i]); }
    float s = 0.f;
#pragma unroll
    for (int i = 0; i < 16; ++i) s += v[i];
    const float mean = sum32(s) * (1.f / 512.f); float q2 = 0.f;
#pragma unroll
    for (int i = 0; i < 16; ++i) { v[i] -= mean; q2 += v[i] * v[i]; }
    const float rstd = __builtin_amdgcn_rsqf(sum32(q2) * (1.f / 512.f) + 1e-5f);
    u32x4 ya, yb;
#pragma unroll
    for (int i = 0; i < 4; ++i) { ya[i] = pk(v[2 * i] * rstd, v[2 * i + 1] * rstd); yb[i] = pk(v[8 + 2 * i] * rstd, v[9 + 2 * i] * rstd); }
    { const lptr p = L + XL + r * 256 + ((tl ^ (r & 15)) << 4); *(LAS u32x4*)p = ya; *(LAS u32x4*)(p + 8192) = yb; }
    BAR(0); TS(256 + blockIdx.x, 1);
    { const int c = tid >> 4, tk = tid & 15; const lptr p = L + XL + c * 256 + ((tk ^ (c & 15)) << 4);
      char* g = ws + WS_XN + (tok0 >> 5) * 32768; const unsigned o = c * 512 + ((tok0 & 16) + tk) * 16;
      st_sc1(o, g, lr128(p)); st_sc1(o, g + 16384, lr128(p + 8192)); }
    f32x16 aK = splat16(0.f), aV = splat16(0.f);
    const lptr wp = L + ((l & 16) ? WV : WK) + (64 * w + 8 * h + ((l >> 2) & 3)) * 32 + (l & 3) * 8;
#pragma unroll
    for (int s4 = 0; s4 < 4; ++s4) { const int c = 8 * w + 2 * s4 + h;
        const bf16x8 xf = lr128(L + XL + c * 256 + (((l ^ c) & 15) << 4)), wf = ltr(wp, s4 * 512, s4 * 512 + 128);
        aK = MFMA(wf, xf, aK); aV = MFMA(xf, wf, aV); }
    { const bool vv = l & 16; const lptr p = L + RED + w * 2048 + l * 16;
      lw128f(p, vv ? aV[0] : aK[0], vv ? aV[1] : aK[1], vv ? aV[2] : aK[2], vv ? aV[3] : aK[3]);
      lw128f(p + 1024, vv ? aV[4] : aK[4], vv ? aV[5] : aK[5], vv ? aV[6] : aK[6], vv ? aV[7] : aK[7]); }
    BARL();
    if (w == 0) { f32x4 a = lr128f(L + RED + l * 16), c = lr128f(L + RED + 1024 + l * 16);
#pragma unroll
        for (int i = 1; i < 8; ++i) { a += lr128f(L + RED + i * 2048 + l * 16); c += lr128f(L + RED + i * 2048 + 1024 + l * 16); }
        const u32x4 o = {pk(a[0], a[1]), pk(a[2], a[3]), pk(c[0], c[1]), pk(c[2], c[3])}; const int sh = (tok0 >> 4) & 1;
        const unsigned off = (l & 16) ? 1024 + sh * 512 + h * 256 + (l & 15) * 16 : (16 * sh + (l & 15) + 32 * h) * 16;
        st_sc1(off, ws + WS_KV + (tok0 >> 5) * 2048, __builtin_bit_cast(bf16x8, o)); }
    TS(256 + blockIdx.x, 2);
    BAR(0);
}

DI void attn_role(int b, const lptr L, unsigned L0, const char* wq, char* ws) {
    int tid = threadIdx.x; asm volatile("" : "+v"(tid));
    const int l = tid & 63, w = __builtin_amdgcn_readfirstlane(tid >> 6), h = l >> 5, t = w & 1, kq = w >> 1;
    const int i5 = b >> 3, tg = (b & 7) * 4 + (i5 & 1) + (i5 >> 4) * 2, hg = (i5 >> 1) & 7, TT = 2 * tg + t;
    constexpr int SL = 8320, TS_ = 2080;
    const unsigned l16 = l * 16;
    if (i5 < 16) __builtin_amdgcn_s_sleep(DLY);
    const u64 ep = __hip_atomic_load((GAS u64*)(ws + WS_FL + FL_E * 128), RLX_AGENT);

    { const unsigned vo = (l >> 3) * 1024 + (((l & 7) ^ (((l >> 4) & 1) << 2)) << 4);
#pragma unroll
      for (int n = 0; n < 8; ++n) glds16(vo, wq + hg * 128 + (w * 8 + n) * 8192, L0 + (w * 8 + n) * 1024); }
    const u64 tag = mk_tag(ep);
    wait_flags(ws, FL_X + w * 16 + (l & 15), tag);
    BARL(); TS(b, 5);
    if (tid == 0) set_flag(ws, FL_A + b, tag);
    bf16x8 xf[8];
    { const char* xp = uni(ws + WS_XN + (TT * 32 + 8 * kq) * 1024);
#pragma unroll
      for (int s = 0; s < 8; ++s) LD_SC1(xf[s], l16, xp + s * 1024); }
    const char* kvs = uni(ws + WS_KV + w * 1024); const unsigned kvd = L0 + (w >> 1) * TS_ + (w & 1) * 1024;
    glds16c(l16, kvs, kvd + 8 * SL);
    asm volatile("s_waitcnt vmcnt(1)" : "+v"(xf[0]), "+v"(xf[1]), "+v"(xf[2]), "+v"(xf[3]), "+v"(xf[4]), "+v"(xf[5]), "+v"(xf[6]), "+v"(xf[7])::"memory"); TS(b, 1);
    bf16x8 qf;
    { const int q = (l >> 2) & 3, p = l & 3, ch = ((l >> 4) & 1) * 2 + (p >> 1);
      const int a0 = kq * 16384 + (8 * h + q) * 128 + ((ch ^ ((q >> 1) << 2)) << 4) + 8 * (p & 1);
      f32x16 c0 = splat16(0.f), c1 = splat16(0.f);
#pragma unroll
      for (int s = 0; s < 8; ++s) { c0 = MFMA(ltr(L + a0, s * 2048, s * 2048 + 512), xf[s], c0); c1 = MFMA(ltr(L + (a0 ^ 64), s * 2048, s * 2048 + 512), xf[s], c1); }
      BARL();
      glds16c(l16, kvs + 8192, kvd); glds16c(l16, kvs + 2 * 8192, kvd + SL);
      if (tid < 24) { const unsigned v = (tid & 1) ? 0u : 0x3F803F80u; const u32x4 one = {v, v, v, v}; *(LAS u32x4*)(L + ((tid >> 1) + (tid < 16 ? 0 : 24)) * TS_ + 2048 + (tid & 1) * 16) = one; }
      f32x4 a, c;
#pragma unroll
      for (int j = 0; j < 4; ++j) { const f32x16& s = j < 2 ? c0 : c1; const int o = 8 * (j & 1);
          if (j == kq) { a = {s[o], s[o + 1], s[o + 2], s[o + 3]}; c = {s[o + 4], s[o + 5], s[o + 6], s[o + 7]}; }
          else { const lptr pw = L + 2 * SL + ((t * 4 + j) * 3 + ((kq - j - 1) & 3)) * 2048 + l16; lw128f(pw, s[o], s[o + 1], s[o + 2], s[o + 3]); lw128f(pw + 1024, s[o + 4], s[o + 5], s[o + 6], s[o + 7]); } }
      BAR(2);
      const lptr pr = L + 2 * SL + (t * 4 + kq) * 6144 + l16;
#pragma unroll
      for (int u = 0; u < 3; ++u) { a += lr128f(pr + u * 2048); c += lr128f(pr + u * 2048 + 1024); }
      const float sc = 0.25f * 1.4426950408889634f; a *= sc; c *= sc;
      const u32x4 qq = {pk(a[0], a[1]), pk(a[2], a[3]), pk(c[0], c[1]), pk(c[2], c[3])}; qf = __builtin_bit_cast(bf16x8, qq); }
    f32x16 ns; const bf16x8 kf = lr128(L + 8 * SL + l16); bf16x8 k0 = lr128(L + 8 * SL + TS_ + l16), k1 = lr128(L + 8 * SL + 2 * TS_ + l16);
    { const f32x16 z = splat16(0.f); f32x16 s0 = MFMA(kf, qf, z), s1 = MFMA(k0, qf, z), s2 = MFMA(k1, qf, z), s3 = MFMA(lr128(L + 8 * SL + 3 * TS_ + l16), qf, z);
      asm volatile("" : "+v"(s0), "+v"(s1), "+v"(s2), "+v"(s3));
      const float m = fmaxf(max16(s1, max16(s0, -3.0e38f)), max16(s3, max16(s2, -3.0e38f)));
      ns = splat16(-(xhalf_max(m) + 90.f)); }
    f32x16 o = splat16(0.f);
    { const unsigned bk = L0 + l16, b0 = L0 + ((l & 16) ? 1024 + h * 256 + (l & 15) * 16 : (l & 11) ? 2064 : 2048), b1 = (l & 16) ? b0 + 512 : b0;
      f32x16 sA = MFMA(kf, qf, ns);
      bf16x8 v0 = lr128(L + 8 * SL + (b0 - L0)), v1 = lr128(L + 8 * SL + (b1 - L0)), vb = {0, 0, 0, 0, 0, 0, 0, 0}, pb = vb;
      unsigned aK = bk + 8 * SL, a0 = b0 + 8 * SL, a1 = b1 + 8 * SL, vo = l16 + 3 * 8192, so = 8 * SL, sd = 2 * SL, sn, c = 0, sv;
      BAR(1); TS(b, 2);
      if (tid < 48) { const unsigned v = (tid & 1) ? 0u : 0x3F803F80u; const u32x4 one = {v, v, v, v}; *(LAS u32x4*)(L + ((tid >> 1) + 8) * TS_ + 2048 + (tid & 1) * 16) = one; }
      unsigned hi = w >> 2, pc = 0; PRIO(P_ATT);
      asm volatile(
        "s_mov_b32 %[sv], m0\ns_nop 7\ns_nop 7\n9:\ns_add_u32 %[sn], %[so], 8320\ns_cmp_eq_u32 %[so], 66560\ns_cselect_b32 %[sn], 0, %[sn]\n"
        "v_mfma_f32_32x32x16_bf16 v[48:63], v[100:103], v[96:99], v[80:95]\nds_read_b128 v[100:103], v124 offset:6240\nv_exp_f32 v32, v32\nv_exp_f32 v33, v33\nv_exp_f32 v34, v34\n"
        "v_exp_f32 v35, v35\nv_exp_f32 v36, v36\nv_exp_f32 v37, v37\nv_mfma_f32_32x32x16_bf16 v[64:79], v[24:27], v[120:123], v[64:79]\nds_read_b128 v[24:27], v126 offset:2080\n"
        "v_exp_f32 v38, v38\nv_exp_f32 v39, v39\nv_cvt_pk_bf16_f32 v116, v32, v33\nv_cvt_pk_bf16_f32 v117, v34, v35\nv_cvt_pk_bf16_f32 v118, v36, v37\n"
        "v_cvt_pk_bf16_f32 v119, v38, v39\nv_exp_f32 v40, v40\nv_exp_f32 v41, v41\nv_exp_f32 v42, v42\ns_waitcnt lgkmcnt(2)\n"
        "v_mfma_f32_32x32x16_bf16 v[64:79], v[108:111], v[116:119], v[64:79]\nds_read_b128 v[108:111], v125 offset:2080\nv_add_u32 v127, %[sn], %[bk]\nv_add_u32 v31, %[sn], %[b0]\n"
        "v_add_u32 v30, %[sn], %[b1]\nv_exp_f32 v43, v43\nv_exp_f32 v44, v44\nv_exp_f32 v45, v45\nv_exp_f32 v46, v46\nv_exp_f32 v47, v47\nv_cvt_pk_bf16_f32 v120, v40, v41\n"
        "v_cvt_pk_bf16_f32 v121, v42, v43\nv_cvt_pk_bf16_f32 v122, v44, v45\nv_cvt_pk_bf16_f32 v123, v46, v47\nv_mfma_f32_32x32x16_bf16 v[32:47], v[104:107], v[96:99], v[80:95]\n"
        "ds_read_b128 v[104:107], v127 offset:0\nv_exp_f32 v48, v48\nv_exp_f32 v49, v49\nv_exp_f32 v50, v50\nv_exp_f32 v51, v51\nv_exp_f32 v52, v52\nv_exp_f32 v53, v53\n"
        "v_mfma_f32_32x32x16_bf16 v[64:79], v[112:115], v[120:123], v[64:79]\nds_read_b128 v[112:115], v126 offset:4160\nv_exp_f32 v54, v54\nv_exp_f32 v55, v55\n"
        "v_cvt_pk_bf16_f32 v116, v48, v49\nv_cvt_pk_bf16_f32 v117, v50, v51\nv_cvt_pk_bf16_f32 v118, v52, v53\nv_cvt_pk_bf16_f32 v119, v54, v55\nv_exp_f32 v56, v56\n"
        "v_exp_f32 v57, v57\nv_exp_f32 v58, v58\ns_waitcnt lgkmcnt(2)\nv_mfma_f32_32x32x16_bf16 v[64:79], v[108:111], v[116:119], v[64:79]\n"
        "ds_read_b128 v[108:111], v125 offset:4160\nv_exp_f32 v59, v59\nv_exp_f32 v60, v60\nv_exp_f32 v61, v61\nv_exp_f32 v62, v62\nv_exp_f32 v63, v63\n"
        "v_cvt_pk_bf16_f32 v120, v56, v57\nv_cvt_pk_bf16_f32 v121, v58, v59\nv_cvt_pk_bf16_f32 v122, v60, v61\nv_cvt_pk_bf16_f32 v123, v62, v63\n"
        "v_mfma_f32_32x32x16_bf16 v[48:63], v[100:103], v[96:99], v[80:95]\nds_read_b128 v[100:103], v127 offset:2080\nv_exp_f32 v32, v32\nv_exp_f32 v33, v33\nv_exp_f32 v34, v34\n"
        "v_exp_f32 v35, v35\nv_exp_f32 v36, v36\nv_exp_f32 v37, v37\nv_mfma_f32_32x32x16_bf16 v[64:79], v[24:27], v[120:123], v[64:79]\nds_read_b128 v[24:27], v126 offset:6240\n"
        "v_exp_f32 v38, v38\nv_exp_f32 v39, v39\nv_cvt_pk_bf16_f32 v116, v32, v33\nv_cvt_pk_bf16_f32 v117, v34, v35\nv_cvt_pk_bf16_f32 v118, v36, v37\n"
        "v_cvt_pk_bf16_f32 v119, v38, v39\nv_exp_f32 v40, v40\nv_exp_f32 v41, v41\nv_exp_f32 v42, v42\ns_waitcnt lgkmcnt(2)\n"
        "v_mfma_f32_32x32x16_bf16 v[64:79], v[108:111], v[116:119], v[64:79]\nds_read_b128 v[108:111], v125 offset:6240\ns_cmp_eq_u32 %[hi], 0\ns_cbranch_scc0 15f\ns_setprio 0\n"
        "15:\nv_exp_f32 v43, v43\nv_exp_f32 v44, v44\nv_exp_f32 v45, v45\nv_exp_f32 v46, v46\nv_exp_f32 v47, v47\nv_cvt_pk_bf16_f32 v120, v40, v41\n"
        "v_cvt_pk_bf16_f32 v121, v42, v43\nv_cvt_pk_bf16_f32 v122, v44, v45\nv_cvt_pk_bf16_f32 v123, v46, v47\nv_mfma_f32_32x32x16_bf16 v[32:47], v[104:107], v[96:99], v[80:95]\n"
        "ds_read_b128 v[104:107], v127 offset:4160\nv_exp_f32 v48, v48\nv_exp_f32 v49, v49\nv_exp_f32 v50, v50\nv_exp_f32 v51, v51\nv_exp_f32 v52, v52\nv_exp_f32 v53, v53\n"
        "v_mfma_f32_32x32x16_bf16 v[64:79], v[112:115], v[120:123], v[64:79]\nds_read_b128 v[112:115], v30 offset:0\nv_exp_f32 v54, v54\nv_exp_f32 v55, v55\n"
        "v_cvt_pk_bf16_f32 v116, v48, v49\nv_cvt_pk_bf16_f32 v117, v50, v51\nv_cvt_pk_bf16_f32 v118, v52, v53\nv_cvt_pk_bf16_f32 v119, v54, v55\nv_exp_f32 v56, v56\n"
        "v_exp_f32 v57, v57\nv_exp_f32 v58, v58\ns_waitcnt lgkmcnt(2)\nv_mfma_f32_32x32x16_bf16 v[64:79], v[108:111], v[116:119], v[64:79]\nds_read_b128 v[108:111], v31 offset:0\n"
        "s_cmp_eq_u32 %[hi], 0\ns_cbranch_scc0 14f\ns_setprio 2\n14:\nv_exp_f32 v59, v59\nv_exp_f32 v60, v60\nv_exp_f32 v61, v61\nv_exp_f32 v62, v62\nv_exp_f32 v63, v63\n"
        "v_cvt_pk_bf16_f32 v120, v56, v57\nv_cvt_pk_bf16_f32 v121, v58, v59\nv_cvt_pk_bf16_f32 v122, v60, v61\nv_cvt_pk_bf16_f32 v123, v62, v63\ns_cmp_gt_u32 %[c], 7\n"
        "s_cbranch_scc1 12f\ns_waitcnt vmcnt(0) lgkmcnt(0)\ns_barrier\ns_cmp_eq_u32 %[c], 7\ns_cbranch_scc1 12f\ns_add_u32 m0, %[sd], %[kd]\ns_add_u32 %[sd], %[sd], 8320\n"
        "global_load_lds_dwordx4 %[vo], %[src] sc1\ns_cmp_eq_u32 %[sd], 74880\ns_cselect_b32 %[sd], 0, %[sd]\nv_add_u32 %[vo], 0x2000, %[vo]\ns_cmp_eq_u32 %[c], 6\n"
        "s_cbranch_scc1 12f\ns_add_u32 m0, %[sd], %[kd]\ns_add_u32 %[sd], %[sd], 8320\nglobal_load_lds_dwordx4 %[vo], %[src] sc1\ns_cmp_eq_u32 %[sd], 74880\n"
        "s_cselect_b32 %[sd], 0, %[sd]\nv_add_u32 %[vo], 0x2000, %[vo]\n12:\ns_add_u32 %[c], %[c], 1\ns_mov_b32 %[so], %[sn]\ns_add_u32 %[sn], %[so], 8320\n"
        "s_cmp_eq_u32 %[so], 66560\ns_cselect_b32 %[sn], 0, %[sn]\nv_mfma_f32_32x32x16_bf16 v[48:63], v[100:103], v[96:99], v[80:95]\nds_read_b128 v[100:103], v127 offset:6240\n"
        "v_exp_f32 v32, v32\nv_exp_f32 v33, v33\nv_exp_f32 v34, v34\nv_exp_f32 v35, v35\nv_exp_f32 v36, v36\nv_exp_f32 v37, v37\n"
        "v_mfma_f32_32x32x16_bf16 v[64:79], v[24:27], v[120:123], v[64:79]\nds_read_b128 v[24:27], v30 offset:2080\nv_exp_f32 v38, v38\nv_exp_f32 v39, v39\n"
        "v_cvt_pk_bf16_f32 v116, v32, v33\nv_cvt_pk_bf16_f32 v117, v34, v35\nv_cvt_pk_bf16_f32 v118, v36, v37\nv_cvt_pk_bf16_f32 v119, v38, v39\nv_exp_f32 v40, v40\n"
        "v_exp_f32 v41, v41\nv_exp_f32 v42, v42\ns_waitcnt lgkmcnt(2)\nv_mfma_f32_32x32x16_bf16 v[64:79], v[108:111], v[116:119], v[64:79]\n"
        "ds_read_b128 v[108:111], v31 offset:2080\nv_add_u32 v124, %[sn], %[bk]\nv_add_u32 v125, %[sn], %[b0]\nv_add_u32 v126, %[sn], %[b1]\nv_exp_f32 v43, v43\nv_exp_f32 v44, v44\n"
        "v_exp_f32 v45, v45\nv_exp_f32 v46, v46\nv_exp_f32 v47, v47\nv_cvt_pk_bf16_f32 v120, v40, v41\nv_cvt_pk_bf16_f32 v121, v42, v43\nv_cvt_pk_bf16_f32 v122, v44, v45\n"
        "v_cvt_pk_bf16_f32 v123, v46, v47\nv_mfma_f32_32x32x16_bf16 v[32:47], v[104:107], v[96:99], v[80:95]\nds_read_b128 v[104:107], v124 offset:0\nv_exp_f32 v48, v48\n"
        "v_exp_f32 v49, v49\nv_exp_f32 v50, v50\nv_exp_f32 v51, v51\nv_exp_f32 v52, v52\nv_exp_f32 v53, v53\nv_mfma_f32_32x32x16_bf16 v[64:79], v[112:115], v[120:123], v[64:79]\n"
        "ds_read_b128 v[112:115], v30 offset:4160\nv_exp_f32 v54, v54\nv_exp_f32 v55, v55\nv_cvt_pk_bf16_f32 v116, v48, v49\nv_cvt_pk_bf16_f32 v117, v50, v51\n"
        "v_cvt_pk_bf16_f32 v118, v52, v53\nv_cvt_pk_bf16_f32 v119, v54, v55\nv_exp_f32 v56, v56\nv_exp_f32 v57, v57\nv_exp_f32 v58, v58\ns_waitcnt lgkmcnt(2)\n"
        "v_mfma_f32_32x32x16_bf16 v[64:79], v[108:111], v[116:119], v[64:79]\nds_read_b128 v[108:111], v31 offset:4160\nv_exp_f32 v59, v59\nv_exp_f32 v60, v60\nv_exp_f32 v61, v61\n"
        "v_exp_f32 v62, v62\nv_exp_f32 v63, v63\nv_cvt_pk_bf16_f32 v120, v56, v57\nv_cvt_pk_bf16_f32 v121, v58, v59\nv_cvt_pk_bf16_f32 v122, v60, v61\n"
        "v_cvt_pk_bf16_f32 v123, v62, v63\nv_mfma_f32_32x32x16_bf16 v[48:63], v[100:103], v[96:99], v[80:95]\nds_read_b128 v[100:103], v124 offset:2080\nv_exp_f32 v32, v32\n"
        "v_exp_f32 v33, v33\nv_exp_f32 v34, v34\nv_exp_f32 v35, v35\nv_exp_f32 v36, v36\nv_exp_f32 v37, v37\nv_mfma_f32_32x32x16_bf16 v[64:79], v[24:27], v[120:123], v[64:79]\n"
        "ds_read_b128 v[24:27], v30 offset:6240\nv_exp_f32 v38, v38\nv_exp_f32 v39, v39\nv_cvt_pk_bf16_f32 v116, v32, v33\nv_cvt_pk_bf16_f32 v117, v34, v35\n"
        "v_cvt_pk_bf16_f32 v118, v36, v37\nv_cvt_pk_bf16_f32 v119, v38, v39\nv_exp_f32 v40, v40\nv_exp_f32 v41, v41\nv_exp_f32 v42, v42\ns_waitcnt lgkmcnt(2)\n"
        "v_mfma_f32_32x32x16_bf16 v[64:79], v[108:111], v[116:119], v[64:79]\nds_read_b128 v[108:111], v31 offset:6240\ns_cmp_eq_u32 %[hi], 0\ns_cbranch_scc0 25f\ns_setprio 0\n25:\n"
        "v_exp_f32 v43, v43\nv_exp_f32 v44, v44\nv_exp_f32 v45, v45\nv_exp_f32 v46, v46\nv_exp_f32 v47, v47\nv_cvt_pk_bf16_f32 v120, v40, v41\nv_cvt_pk_bf16_f32 v121, v42, v43\n"
        "v_cvt_pk_bf16_f32 v122, v44, v45\nv_cvt_pk_bf16_f32 v123, v46, v47\nv_mfma_f32_32x32x16_bf16 v[32:47], v[104:107], v[96:99], v[80:95]\n"
        "ds_read_b128 v[104:107], v124 offset:4160\nv_exp_f32 v48, v48\nv_exp_f32 v49, v49\nv_exp_f32 v50, v50\nv_exp_f32 v51, v51\nv_exp_f32 v52, v52\nv_exp_f32 v53, v53\n"
        "v_mfma_f32_32x32x16_bf16 v[64:79], v[112:115], v[120:123], v[64:79]\nds_read_b128 v[112:115], v126 offset:0\nv_exp_f32 v54, v54\nv_exp_f32 v55, v55\n"
        "v_cvt_pk_bf16_f32 v116, v48, v49\nv_cvt_pk_bf16_f32 v117, v50, v51\nv_cvt_pk_bf16_f32 v118, v52, v53\nv_cvt_pk_bf16_f32 v119, v54, v55\nv_exp_f32 v56, v56\n"
        "v_exp_f32 v57, v57\nv_exp_f32 v58, v58\ns_waitcnt lgkmcnt(2)\nv_mfma_f32_32x32x16_bf16 v[64:79], v[108:111], v[116:119], v[64:79]\nds_read_b128 v[108:111], v125 offset:0\n"
        "s_cmp_eq_u32 %[hi], 0\ns_cbranch_scc0 24f\ns_setprio 2\n24:\nv_exp_f32 v59, v59\nv_exp_f32 v60, v60\nv_exp_f32 v61, v61\nv_exp_f32 v62, v62\nv_exp_f32 v63, v63\n"
        "v_cvt_pk_bf16_f32 v120, v56, v57\nv_cvt_pk_bf16_f32 v121, v58, v59\nv_cvt_pk_bf16_f32 v122, v60, v61\nv_cvt_pk_bf16_f32 v123, v62, v63\ns_cmp_gt_u32 %[c], 7\n"
        "s_cbranch_scc1 22f\ns_waitcnt vmcnt(0) lgkmcnt(0)\ns_barrier\ns_cmp_eq_u32 %[c], 7\ns_cbranch_scc1 22f\ns_add_u32 m0, %[sd], %[kd]\ns_add_u32 %[sd], %[sd], 8320\n"
        "global_load_lds_dwordx4 %[vo], %[src] sc1\ns_cmp_eq_u32 %[sd], 74880\ns_cselect_b32 %[sd], 0, %[sd]\nv_add_u32 %[vo], 0x2000, %[vo]\ns_cmp_eq_u32 %[c], 6\n"
        "s_cbranch_scc1 22f\ns_add_u32 m0, %[sd], %[kd]\ns_add_u32 %[sd], %[sd], 8320\nglobal_load_lds_dwordx4 %[vo], %[src] sc1\ns_cmp_eq_u32 %[sd], 74880\n"
        "s_cselect_b32 %[sd], 0, %[sd]\nv_add_u32 %[vo], 0x2000, %[vo]\n22:\ns_add_u32 %[c], %[c], 1\ns_mov_b32 %[so], %[sn]\ns_cmp_lt_u32 %[c], 16\ns_cbranch_scc1 9b\n"
        "s_waitcnt lgkmcnt(0)\nv_mfma_f32_32x32x16_bf16 v[64:79], v[24:27], v[120:123], v[64:79]\ns_mov_b32 m0, %[sv]\ns_nop 7\ns_nop 7\n"
        : "+{v[32:47]}"(sA), "+{v[64:79]}"(o), "+{v[100:103]}"(k0), "+{v[104:107]}"(k1), "+{v[108:111]}"(v0), "+{v[112:115]}"(v1), "+{v[24:27]}"(vb), "+{v[120:123]}"(pb),
          "+{v124}"(aK), "+{v125}"(a0), "+{v126}"(a1), [so] "+s"(so), [sd] "+s"(sd), [sn] "=&s"(sn), [c] "+s"(c), [sv] "=&s"(sv), [vo] "+v"(vo), [hi] "+s"(hi)
        : "{v[80:95]}"(ns), "{v[96:99]}"(qf), [bk] "v"(bk), [b0] "v"(b0), [b1] "v"(b1), [kd] "s"(kvd), [src] "s"(kvs)
        : "v48", "v49", "v50", "v51", "v52", "v53", "v54", "v55", "v56", "v57", "v58", "v59", "v60", "v61", "v62", "v63", "v116", "v117", "v118", "v119", "v127", "v30", "v31", "scc", "memory"
 );

      PRIO(3); }
    TS(b, 3);
    int m_; asm volatile("v_mbcnt_lo_u32_b32 %0, -1, 0\n\tv_mbcnt_hi_u32_b32 %0, -1, %0" : "=v"(m_));
    const unsigned m16 = m_ * 16;

    float bad = o[0];
#pragma unroll
    for (int i = 8; i < 16; ++i) bad = fmaxf(bad, fabsf(o[i]));
    if (__builtin_amdgcn_ballot_w64(!(bad < 1.0e33f)) != 0ull || __builtin_amdgcn_ballot_w64(!(o[0] > 0.f)) != 0ull) {
        const char* kv = ws + WS_KV; float m = -3.0e38f; const f32x16 z = splat16(0.f);
#pragma unroll 1
        for (int u = 0; u < 64; ++u) m = max16(MFMA(ldc(kv + u * 2048 + m16), qf, z), m);
        ns = splat16(-xhalf_max(m)); o = z;
        const u32x4 one = {0x3F803F80u, 0x3F803F80u, 0x3F803F80u, 0x3F803F80u}; const int vo = 1024 + (m_ >> 5) * 256 + (m_ & 15) * 16;
#pragma unroll 1
        for (int u = 0; u < 64; ++u) { f32x16 s = MFMA(ldc(kv + u * 2048 + m16), qf, ns);
            u32x4 v0 = __builtin_bit_cast(u32x4, ldc(kv + u * 2048 + vo)), v1 = __builtin_bit_cast(u32x4, ldc(kv + u * 2048 + vo + 512));
            if (!(m_ & 16)) { v0 = one; v1 = one; }
#pragma unroll
            for (int i = 0; i < 16; ++i) s[i] = __builtin_amdgcn_exp2f(s[i]);
            o = MFMA(__builtin_bit_cast(bf16x8, v0), pack8(s, 0), o); o = MFMA(__builtin_bit_cast(bf16x8, v1), pack8(s, 8), o); } }
    const float inv = 1.f / o[0];
#pragma unroll
    for (int i = 8; i < 16; ++i) o[i] *= inv;
    st_sc1(m16, ws + WS_O + (TT * 32 + 4 * hg + kq) * 1024, pack8(o, 8));
    BAR(0);
    if (w == 0 && m_ == 0) set_flag(ws, FL_O + tg * 8 + hg, tag);
    TS(b, 4);
}

DI void ffn_role(int b, const lptr L, unsigned L0, const char* x, const char* wk, const char* wv, const char* wo, const char* wg, const char* wu, const char* wd, char* out, char* ws) {
    const int tid = threadIdx.x, l = tid & 63, w = __builtin_amdgcn_readfirstlane(tid >> 6), h = l >> 5;
    const int xcd = b & 7, i5 = b >> 3, q = (l >> 2) & 3, p = l & 3; const unsigned l16 = l * 16;
    u64 ep; unsigned jk = 0;
    const int TB = (xcd >> 2) * 4 + (i5 & 3), fb = (xcd & 3) * 7 + (i5 >> 2), ft = w & 1, tk = w >> 1;
    const unsigned vw = (l >> 3) * 3584 + (((l & 7) ^ (((l >> 4) & 1) << 2)) << 4), dw = L0 + w * 1024, dx = L0 + 8192 + w * 2048;
    const char* sw = ((w & 4) ? wu : wg) + (w & 3) * 8 * 3584 + fb * 128;
    if (i5 < 16) {
        ln_kv(xcd * 256 + i5 * 16, L, L0, x, wk, wv, ws, [&] { ep = __hip_atomic_load((GAS u64*)(ws + WS_FL + FL_E * 128), RLX_AGENT); });
    } else {
        ep = __hip_atomic_load((GAS u64*)(ws + WS_FL + FL_E * 128), RLX_AGENT);
        const int n = tid & 255, row = n / 7;
        if (n < 224) touch(((i5 - 16) * 32 + row) * 3584 + (xcd & 3) * 896 + (n - row * 7) * 128, w < 4 ? wg : wu);
        TOUCH_END(); BARL(); }
    const u64 tag = mk_tag(ep);
    if (tid == 0) { if (i5 < 16) set_flag(ws, FL_X + xcd * 16 + i5, tag); set_flag(ws, FL_A + 256 + b, tag); }
    TS(256 + b, 14);
    if (i5 < 28) {
        glds16(vw, sw, dw); glds16(vw, sw + 114688, dw + 24576); glds16(vw, sw + 2 * 114688, dw + 49152);
        wait_flags(ws, FL_X + TB * 16 + 2 * w + (l & 1), tag);
        const char* sx = uni(ws + WS_XN + (TB * 8 + w) * 32768);
#pragma unroll
        for (int st = 0; st < 3; ++st) { glds16c(l16, sx + st * 2048, dx + st * 24576); glds16c(l16, sx + st * 2048 + 1024, dx + st * 24576 + 1024); }
        unsigned sv; const unsigned tv = (i5 * 512 + tid) * 128;
        const unsigned pa = L0 + (((8 * h + q) * 128 + (((((l >> 4) & 1) * 2 + (p >> 1)) ^ ((q >> 1) << 2)) << 4) + 8 * (p & 1)) ^ (ft * 64)), pb = L0 + 8192 + tk * 4096 + l16;
        f32x16 g0, g1, u0, u1;
        PRIO(P_LOOP);
        asm volatile(
        "s_mov_b32 %[sv], m0\ns_mov_b64 s[40:41], %[sw]\ns_mov_b64 s[42:43], %[sx]\ns_waitcnt vmcnt(4)\ns_barrier\nds_read_b64_tr_b16 v[96:97], %[pa] offset:0\n"
        "ds_read_b64_tr_b16 v[98:99], %[pa] offset:512\nds_read_b128 v[104:107], %[pb] offset:0\nds_read_b64_tr_b16 v[100:101], %[pa] offset:4096\n"
        "ds_read_b64_tr_b16 v[102:103], %[pa] offset:4608\nds_read_b128 v[108:111], %[pb] offset:2048\ns_waitcnt lgkmcnt(0)\n"
        "v_mfma_f32_32x32x16_bf16 v[32:47], v[96:99], v[104:107], 0\nds_read_b64_tr_b16 v[112:113], %[pa] offset:2048\nds_read_b64_tr_b16 v[114:115], %[pa] offset:2560\n"
        "v_mfma_f32_32x32x16_bf16 v[48:63], v[96:99], v[108:111], 0\nds_read_b128 v[120:123], %[pb] offset:1024\nds_read_b64_tr_b16 v[116:117], %[pa] offset:6144\n"
        "v_mfma_f32_32x32x16_bf16 v[64:79], v[100:103], v[104:107], 0\nds_read_b64_tr_b16 v[118:119], %[pa] offset:6656\nds_read_b128 v[124:127], %[pb] offset:3072\n"
        "v_mfma_f32_32x32x16_bf16 v[80:95], v[100:103], v[108:111], 0\ns_waitcnt vmcnt(2) lgkmcnt(0)\ns_barrier\n"
        "v_mfma_f32_32x32x16_bf16 v[32:47], v[112:115], v[120:123], v[32:47]\ns_add_u32 m0, %[dw], 0\ns_add_u32 s40, s40, 0x1c000\ns_addc_u32 s41, s41, 0\n"
        "global_load_lds_dwordx4 %[vw], s[40:41]\nds_read_b64_tr_b16 v[96:97], %[pa] offset:24576\nds_read_b64_tr_b16 v[98:99], %[pa] offset:25088\n"
        "v_mfma_f32_32x32x16_bf16 v[48:63], v[112:115], v[124:127], v[48:63]\ns_add_u32 m0, %[dx], 0\ns_add_u32 s42, s42, 0x800\ns_addc_u32 s43, s43, 0\n"
        "global_load_lds_dwordx4 %[vx], s[42:43] sc1\nds_read_b128 v[104:107], %[pb] offset:24576\nds_read_b64_tr_b16 v[100:101], %[pa] offset:28672\n"
        "v_mfma_f32_32x32x16_bf16 v[64:79], v[116:119], v[120:123], v[64:79]\nglobal_load_lds_dwordx4 %[vx], s[42:43] offset:1024 sc1\n"
        "ds_read_b64_tr_b16 v[102:103], %[pa] offset:29184\nds_read_b128 v[108:111], %[pb] offset:26624\nv_mfma_f32_32x32x16_bf16 v[80:95], v[116:119], v[124:127], v[80:95]\n"
        "s_waitcnt lgkmcnt(0)\nv_mfma_f32_32x32x16_bf16 v[32:47], v[96:99], v[104:107], v[32:47]\nds_read_b64_tr_b16 v[112:113], %[pa] offset:26624\n"
        "ds_read_b64_tr_b16 v[114:115], %[pa] offset:27136\nv_mfma_f32_32x32x16_bf16 v[48:63], v[96:99], v[108:111], v[48:63]\nds_read_b128 v[120:123], %[pb] offset:25600\n"
        "ds_read_b64_tr_b16 v[116:117], %[pa] offset:30720\nv_mfma_f32_32x32x16_bf16 v[64:79], v[100:103], v[104:107], v[64:79]\nds_read_b64_tr_b16 v[118:119], %[pa] offset:31232\n"
        "ds_read_b128 v[124:127], %[pb] offset:27648\nv_mfma_f32_32x32x16_bf16 v[80:95], v[100:103], v[108:111], v[80:95]\ns_waitcnt vmcnt(3) lgkmcnt(0)\ns_barrier\n"
        "v_mfma_f32_32x32x16_bf16 v[32:47], v[112:115], v[120:123], v[32:47]\ns_add_u32 m0, %[dw], 24576\ns_add_u32 s40, s40, 0x1c000\ns_addc_u32 s41, s41, 0\n"
        "global_load_lds_dwordx4 %[vw], s[40:41]\nds_read_b64_tr_b16 v[96:97], %[pa] offset:49152\nds_read_b64_tr_b16 v[98:99], %[pa] offset:49664\n"
        "v_mfma_f32_32x32x16_bf16 v[48:63], v[112:115], v[124:127], v[48:63]\ns_add_u32 m0, %[dx], 24576\ns_add_u32 s42, s42, 0x800\ns_addc_u32 s43, s43, 0\n"
        "global_load_lds_dwordx4 %[vx], s[42:43] sc1\nds_read_b128 v[104:107], %[pb] offset:49152\nds_read_b64_tr_b16 v[100:101], %[pa] offset:53248\n"
        "v_mfma_f32_32x32x16_bf16 v[64:79], v[116:119], v[120:123], v[64:79]\nglobal_load_lds_dwordx4 %[vx], s[42:43] offset:1024 sc1\n"
        "ds_read_b64_tr_b16 v[102:103], %[pa] offset:53760\nds_read_b128 v[108:111], %[pb] offset:51200\nv_mfma_f32_32x32x16_bf16 v[80:95], v[116:119], v[124:127], v[80:95]\n"
        "s_waitcnt lgkmcnt(0)\nv_mfma_f32_32x32x16_bf16 v[32:47], v[96:99], v[104:107], v[32:47]\nds_read_b64_tr_b16 v[112:113], %[pa] offset:51200\n"
        "ds_read_b64_tr_b16 v[114:115], %[pa] offset:51712\nv_mfma_f32_32x32x16_bf16 v[48:63], v[96:99], v[108:111], v[48:63]\nds_read_b128 v[120:123], %[pb] offset:50176\n"
        "ds_read_b64_tr_b16 v[116:117], %[pa] offset:55296\nv_mfma_f32_32x32x16_bf16 v[64:79], v[100:103], v[104:107], v[64:79]\nds_read_b64_tr_b16 v[118:119], %[pa] offset:55808\n"
        "ds_read_b128 v[124:127], %[pb] offset:52224\nv_mfma_f32_32x32x16_bf16 v[80:95], v[100:103], v[108:111], v[80:95]\ns_waitcnt vmcnt(3) lgkmcnt(0)\ns_barrier\n"
        "v_mfma_f32_32x32x16_bf16 v[32:47], v[112:115], v[120:123], v[32:47]\ns_add_u32 m0, %[dw], 49152\ns_add_u32 s40, s40, 0x1c000\ns_addc_u32 s41, s41, 0\n"
        "global_load_lds_dwordx4 %[vw], s[40:41]\nds_read_b64_tr_b16 v[96:97], %[pa] offset:0\nds_read_b64_tr_b16 v[98:99], %[pa] offset:512\n"
        "v_mfma_f32_32x32x16_bf16 v[48:63], v[112:115], v[124:127], v[48:63]\ns_add_u32 m0, %[dx], 49152\ns_add_u32 s42, s42, 0x800\ns_addc_u32 s43, s43, 0\n"
        "global_load_lds_dwordx4 %[vx], s[42:43] sc1\nds_read_b128 v[104:107], %[pb] offset:0\nds_read_b64_tr_b16 v[100:101], %[pa] offset:4096\n"
        "v_mfma_f32_32x32x16_bf16 v[64:79], v[116:119], v[120:123], v[64:79]\nglobal_load_lds_dwordx4 %[vx], s[42:43] offset:1024 sc1\nglobal_load_dword %[jk], %[tv], %[wd]\n"
        "ds_read_b64_tr_b16 v[102:103], %[pa] offset:4608\nds_read_b128 v[108:111], %[pb] offset:2048\nv_mfma_f32_32x32x16_bf16 v[80:95], v[116:119], v[124:127], v[80:95]\n"
        "s_waitcnt lgkmcnt(0)\nv_mfma_f32_32x32x16_bf16 v[32:47], v[96:99], v[104:107], v[32:47]\nds_read_b64_tr_b16 v[112:113], %[pa] offset:2048\n"
        "ds_read_b64_tr_b16 v[114:115], %[pa] offset:2560\nv_mfma_f32_32x32x16_bf16 v[48:63], v[96:99], v[108:111], v[48:63]\nds_read_b128 v[120:123], %[pb] offset:1024\n"
        "ds_read_b64_tr_b16 v[116:117], %[pa] offset:6144\nv_mfma_f32_32x32x16_bf16 v[64:79], v[100:103], v[104:107], v[64:79]\nds_read_b64_tr_b16 v[118:119], %[pa] offset:6656\n"
        "ds_read_b128 v[124:127], %[pb] offset:3072\nv_mfma_f32_32x32x16_bf16 v[80:95], v[100:103], v[108:111], v[80:95]\ns_waitcnt vmcnt(4) lgkmcnt(0)\ns_barrier\n"
        "v_mfma_f32_32x32x16_bf16 v[32:47], v[112:115], v[120:123], v[32:47]\ns_add_u32 m0, %[dw], 0\ns_add_u32 s40, s40, 0x1c000\ns_addc_u32 s41, s41, 0\n"
        "global_load_lds_dwordx4 %[vw], s[40:41]\nds_read_b64_tr_b16 v[96:97], %[pa] offset:24576\nds_read_b64_tr_b16 v[98:99], %[pa] offset:25088\n"
        "v_mfma_f32_32x32x16_bf16 v[48:63], v[112:115], v[124:127], v[48:63]\ns_add_u32 m0, %[dx], 0\ns_add_u32 s42, s42, 0x800\ns_addc_u32 s43, s43, 0\n"
        "global_load_lds_dwordx4 %[vx], s[42:43] sc1\nds_read_b128 v[104:107], %[pb] offset:24576\nds_read_b64_tr_b16 v[100:101], %[pa] offset:28672\n"
        "v_mfma_f32_32x32x16_bf16 v[64:79], v[116:119], v[120:123], v[64:79]\nglobal_load_lds_dwordx4 %[vx], s[42:43] offset:1024 sc1\n"
        "ds_read_b64_tr_b16 v[102:103], %[pa] offset:29184\nds_read_b128 v[108:111], %[pb] offset:26624\nv_mfma_f32_32x32x16_bf16 v[80:95], v[116:119], v[124:127], v[80:95]\n"
        "s_waitcnt lgkmcnt(0)\nv_mfma_f32_32x32x16_bf16 v[32:47], v[96:99], v[104:107], v[32:47]\nds_read_b64_tr_b16 v[112:113], %[pa] offset:26624\n"
        "ds_read_b64_tr_b16 v[114:115], %[pa] offset:27136\nv_mfma_f32_32x32x16_bf16 v[48:63], v[96:99], v[108:111], v[48:63]\nds_read_b128 v[120:123], %[pb] offset:25600\n"
        "ds_read_b64_tr_b16 v[116:117], %[pa] offset:30720\nv_mfma_f32_32x32x16_bf16 v[64:79], v[100:103], v[104:107], v[64:79]\nds_read_b64_tr_b16 v[118:119], %[pa] offset:31232\n"
        "ds_read_b128 v[124:127], %[pb] offset:27648\nv_mfma_f32_32x32x16_bf16 v[80:95], v[100:103], v[108:111], v[80:95]\ns_waitcnt vmcnt(4) lgkmcnt(0)\ns_barrier\n"
        "v_mfma_f32_32x32x16_bf16 v[32:47], v[112:115], v[120:123], v[32:47]\ns_add_u32 m0, %[dw], 24576\ns_add_u32 s40, s40, 0x1c000\ns_addc_u32 s41, s41, 0\n"
        "global_load_lds_dwordx4 %[vw], s[40:41]\nds_read_b64_tr_b16 v[96:97], %[pa] offset:49152\nds_read_b64_tr_b16 v[98:99], %[pa] offset:49664\n"
        "v_mfma_f32_32x32x16_bf16 v[48:63], v[112:115], v[124:127], v[48:63]\ns_add_u32 m0, %[dx], 24576\ns_add_u32 s42, s42, 0x800\ns_addc_u32 s43, s43, 0\n"
        "global_load_lds_dwordx4 %[vx], s[42:43] sc1\nds_read_b128 v[104:107], %[pb] offset:49152\nds_read_b64_tr_b16 v[100:101], %[pa] offset:53248\n"
        "v_mfma_f32_32x32x16_bf16 v[64:79], v[116:119], v[120:123], v[64:79]\nglobal_load_lds_dwordx4 %[vx], s[42:43] offset:1024 sc1\n"
        "ds_read_b64_tr_b16 v[102:103], %[pa] offset:53760\nds_read_b128 v[108:111], %[pb] offset:51200\nv_mfma_f32_32x32x16_bf16 v[80:95], v[116:119], v[124:127], v[80:95]\n"
        "s_waitcnt lgkmcnt(0)\nv_mfma_f32_32x32x16_bf16 v[32:47], v[96:99], v[104:107], v[32:47]\nds_read_b64_tr_b16 v[112:113], %[pa] offset:51200\n"
        "ds_read_b64_tr_b16 v[114:115], %[pa] offset:51712\nv_mfma_f32_32x32x16_bf16 v[48:63], v[96:99], v[108:111], v[48:63]\nds_read_b128 v[120:123], %[pb] offset:50176\n"
        "ds_read_b64_tr_b16 v[116:117], %[pa] offset:55296\nv_mfma_f32_32x32x16_bf16 v[64:79], v[100:103], v[104:107], v[64:79]\nds_read_b64_tr_b16 v[118:119], %[pa] offset:55808\n"
        "ds_read_b128 v[124:127], %[pb] offset:52224\nv_mfma_f32_32x32x16_bf16 v[80:95], v[100:103], v[108:111], v[80:95]\ns_waitcnt vmcnt(3) lgkmcnt(0)\ns_barrier\n"
        "v_mfma_f32_32x32x16_bf16 v[32:47], v[112:115], v[120:123], v[32:47]\ns_add_u32 m0, %[dw], 49152\ns_add_u32 s40, s40, 0x1c000\ns_addc_u32 s41, s41, 0\n"
        "global_load_lds_dwordx4 %[vw], s[40:41]\nds_read_b64_tr_b16 v[96:97], %[pa] offset:0\nds_read_b64_tr_b16 v[98:99], %[pa] offset:512\n"
        "v_mfma_f32_32x32x16_bf16 v[48:63], v[112:115], v[124:127], v[48:63]\ns_add_u32 m0, %[dx], 49152\ns_add_u32 s42, s42, 0x800\ns_addc_u32 s43, s43, 0\n"
        "global_load_lds_dwordx4 %[vx], s[42:43] sc1\nds_read_b128 v[104:107], %[pb] offset:0\nds_read_b64_tr_b16 v[100:101], %[pa] offset:4096\n"
        "v_mfma_f32_32x32x16_bf16 v[64:79], v[116:119], v[120:123], v[64:79]\nglobal_load_lds_dwordx4 %[vx], s[42:43] offset:1024 sc1\n"
        "ds_read_b64_tr_b16 v[102:103], %[pa] offset:4608\nds_read_b128 v[108:111], %[pb] offset:2048\nv_mfma_f32_32x32x16_bf16 v[80:95], v[116:119], v[124:127], v[80:95]\n"
        "s_waitcnt lgkmcnt(0)\nv_mfma_f32_32x32x16_bf16 v[32:47], v[96:99], v[104:107], v[32:47]\nds_read_b64_tr_b16 v[112:113], %[pa] offset:2048\n"
        "ds_read_b64_tr_b16 v[114:115], %[pa] offset:2560\nv_mfma_f32_32x32x16_bf16 v[48:63], v[96:99], v[108:111], v[48:63]\nds_read_b128 v[120:123], %[pb] offset:1024\n"
        "ds_read_b64_tr_b16 v[116:117], %[pa] offset:6144\nv_mfma_f32_32x32x16_bf16 v[64:79], v[100:103], v[104:107], v[64:79]\nds_read_b64_tr_b16 v[118:119], %[pa] offset:6656\n"
        "ds_read_b128 v[124:127], %[pb] offset:3072\nv_mfma_f32_32x32x16_bf16 v[80:95], v[100:103], v[108:111], v[80:95]\ns_waitcnt vmcnt(3) lgkmcnt(0)\ns_barrier\n"
        "v_mfma_f32_32x32x16_bf16 v[32:47], v[112:115], v[120:123], v[32:47]\ns_add_u32 m0, %[dw], 0\ns_add_u32 s40, s40, 0x1c000\ns_addc_u32 s41, s41, 0\n"
        "global_load_lds_dwordx4 %[vw], s[40:41]\nds_read_b64_tr_b16 v[96:97], %[pa] offset:24576\nds_read_b64_tr_b16 v[98:99], %[pa] offset:25088\n"
        "v_mfma_f32_32x32x16_bf16 v[48:63], v[112:115], v[124:127], v[48:63]\ns_add_u32 m0, %[dx], 0\ns_add_u32 s42, s42, 0x800\ns_addc_u32 s43, s43, 0\n"
        "global_load_lds_dwordx4 %[vx], s[42:43] sc1\nds_read_b128 v[104:107], %[pb] offset:24576\nds_read_b64_tr_b16 v[100:101], %[pa] offset:28672\n"
        "v_mfma_f32_32x32x16_bf16 v[64:79], v[116:119], v[120:123], v[64:79]\nglobal_load_lds_dwordx4 %[vx], s[42:43] offset:1024 sc1\n"
        "ds_read_b64_tr_b16 v[102:103], %[pa] offset:29184\nds_read_b128 v[108:111], %[pb] offset:26624\nv_mfma_f32_32x32x16_bf16 v[80:95], v[116:119], v[124:127], v[80:95]\n"
        "s_waitcnt lgkmcnt(0)\nv_mfma_f32_32x32x16_bf16 v[32:47], v[96:99], v[104:107], v[32:47]\nds_read_b64_tr_b16 v[112:113], %[pa] offset:26624\n"
        "ds_read_b64_tr_b16 v[114:115], %[pa] offset:27136\nv_mfma_f32_32x32x16_bf16 v[48:63], v[96:99], v[108:111], v[48:63]\nds_read_b128 v[120:123], %[pb] offset:25600\n"
        "ds_read_b64_tr_b16 v[116:117], %[pa] offset:30720\nv_mfma_f32_32x32x16_bf16 v[64:79], v[100:103], v[104:107], v[64:79]\nds_read_b64_tr_b16 v[118:119], %[pa] offset:31232\n"
        "ds_read_b128 v[124:127], %[pb] offset:27648\nv_mfma_f32_32x32x16_bf16 v[80:95], v[100:103], v[108:111], v[80:95]\ns_waitcnt vmcnt(3) lgkmcnt(0)\ns_barrier\n"
        "v_mfma_f32_32x32x16_bf16 v[32:47], v[112:115], v[120:123], v[32:47]\ns_add_u32 m0, %[dw], 24576\ns_add_u32 s40, s40, 0x1c000\ns_addc_u32 s41, s41, 0\n"
        "global_load_lds_dwordx4 %[vw], s[40:41]\nds_read_b64_tr_b16 v[96:97], %[pa] offset:49152\nds_read_b64_tr_b16 v[98:99], %[pa] offset:49664\n"
        "v_mfma_f32_32x32x16_bf16 v[48:63], v[112:115], v[124:127], v[48:63]\ns_add_u32 m0, %[dx], 24576\ns_add_u32 s42, s42, 0x800\ns_addc_u32 s43, s43, 0\n"
        "global_load_lds_dwordx4 %[vx], s[42:43] sc1\nds_read_b128 v[104:107], %[pb] offset:49152\nds_read_b64_tr_b16 v[100:101], %[pa] offset:53248\n"
        "v_mfma_f32_32x32x16_bf16 v[64:79], v[116:119], v[120:123], v[64:79]\nglobal_load_lds_dwordx4 %[vx], s[42:43] offset:1024 sc1\n"
        "ds_read_b64_tr_b16 v[102:103], %[pa] offset:53760\nds_read_b128 v[108:111], %[pb] offset:51200\nv_mfma_f32_32x32x16_bf16 v[80:95], v[116:119], v[124:127], v[80:95]\n"
        "s_waitcnt lgkmcnt(0)\nv_mfma_f32_32x32x16_bf16 v[32:47], v[96:99], v[104:107], v[32:47]\nds_read_b64_tr_b16 v[112:113], %[pa] offset:51200\n"
        "ds_read_b64_tr_b16 v[114:115], %[pa] offset:51712\nv_mfma_f32_32x32x16_bf16 v[48:63], v[96:99], v[108:111], v[48:63]\nds_read_b128 v[120:123], %[pb] offset:50176\n"
        "ds_read_b64_tr_b16 v[116:117], %[pa] offset:55296\nv_mfma_f32_32x32x16_bf16 v[64:79], v[100:103], v[104:107], v[64:79]\nds_read_b64_tr_b16 v[118:119], %[pa] offset:55808\n"
        "ds_read_b128 v[124:127], %[pb] offset:52224\nv_mfma_f32_32x32x16_bf16 v[80:95], v[100:103], v[108:111], v[80:95]\ns_waitcnt vmcnt(3) lgkmcnt(0)\ns_barrier\n"
        "v_mfma_f32_32x32x16_bf16 v[32:47], v[112:115], v[120:123], v[32:47]\ns_add_u32 m0, %[dw], 49152\ns_add_u32 s40, s40, 0x1c000\ns_addc_u32 s41, s41, 0\n"
        "global_load_lds_dwordx4 %[vw], s[40:41]\nds_read_b64_tr_b16 v[96:97], %[pa] offset:0\nds_read_b64_tr_b16 v[98:99], %[pa] offset:512\n"
        "v_mfma_f32_32x32x16_bf16 v[48:63], v[112:115], v[124:127], v[48:63]\ns_add_u32 m0, %[dx], 49152\ns_add_u32 s42, s42, 0x800\ns_addc_u32 s43, s43, 0\n"
        "global_load_lds_dwordx4 %[vx], s[42:43] sc1\nds_read_b128 v[104:107], %[pb] offset:0\nds_read_b64_tr_b16 v[100:101], %[pa] offset:4096\n"
        "v_mfma_f32_32x32x16_bf16 v[64:79], v[116:119], v[120:123], v[64:79]\nglobal_load_lds_dwordx4 %[vx], s[42:43] offset:1024 sc1\n"
        "ds_read_b64_tr_b16 v[102:103], %[pa] offset:4608\nds_read_b128 v[108:111], %[pb] offset:2048\nv_mfma_f32_32x32x16_bf16 v[80:95], v[116:119], v[124:127], v[80:95]\n"
        "s_waitcnt lgkmcnt(0)\nv_mfma_f32_32x32x16_bf16 v[32:47], v[96:99], v[104:107], v[32:47]\nds_read_b64_tr_b16 v[112:113], %[pa] offset:2048\n"
        "ds_read_b64_tr_b16 v[114:115], %[pa] offset:2560\nv_mfma_f32_32x32x16_bf16 v[48:63], v[96:99], v[108:111], v[48:63]\nds_read_b128 v[120:123], %[pb] offset:1024\n"
        "ds_read_b64_tr_b16 v[116:117], %[pa] offset:6144\nv_mfma_f32_32x32x16_bf16 v[64:79], v[100:103], v[104:107], v[64:79]\nds_read_b64_tr_b16 v[118:119], %[pa] offset:6656\n"
        "ds_read_b128 v[124:127], %[pb] offset:3072\nv_mfma_f32_32x32x16_bf16 v[80:95], v[100:103], v[108:111], v[80:95]\ns_waitcnt vmcnt(3) lgkmcnt(0)\ns_barrier\n"
        "v_mfma_f32_32x32x16_bf16 v[32:47], v[112:115], v[120:123], v[32:47]\ns_add_u32 m0, %[dw], 0\ns_add_u32 s40, s40, 0x1c000\ns_addc_u32 s41, s41, 0\n"
        "global_load_lds_dwordx4 %[vw], s[40:41]\nds_read_b64_tr_b16 v[96:97], %[pa] offset:24576\nds_read_b64_tr_b16 v[98:99], %[pa] offset:25088\n"
        "v_mfma_f32_32x32x16_bf16 v[48:63], v[112:115], v[124:127], v[48:63]\ns_add_u32 m0, %[dx], 0\ns_add_u32 s42, s42, 0x800\ns_addc_u32 s43, s43, 0\n"
        "global_load_lds_dwordx4 %[vx], s[42:43] sc1\nds_read_b128 v[104:107], %[pb] offset:24576\nds_read_b64_tr_b16 v[100:101], %[pa] offset:28672\n"
        "v_mfma_f32_32x32x16_bf16 v[64:79], v[116:119], v[120:123], v[64:79]\nglobal_load_lds_dwordx4 %[vx], s[42:43] offset:1024 sc1\n"
        "ds_read_b64_tr_b16 v[102:103], %[pa] offset:29184\nds_read_b128 v[108:111], %[pb] offset:26624\nv_mfma_f32_32x32x16_bf16 v[80:95], v[116:119], v[124:127], v[80:95]\n"
        "s_waitcnt lgkmcnt(0)\nv_mfma_f32_32x32x16_bf16 v[32:47], v[96:99], v[104:107], v[32:47]\nds_read_b64_tr_b16 v[112:113], %[pa] offset:26624\n"
        "ds_read_b64_tr_b16 v[114:115], %[pa] offset:27136\nv_mfma_f32_32x32x16_bf16 v[48:63], v[96:99], v[108:111], v[48:63]\nds_read_b128 v[120:123], %[pb] offset:25600\n"
        "ds_read_b64_tr_b16 v[116:117], %[pa] offset:30720\nv_mfma_f32_32x32x16_bf16 v[64:79], v[100:103], v[104:107], v[64:79]\nds_read_b64_tr_b16 v[118:119], %[pa] offset:31232\n"
        "ds_read_b128 v[124:127], %[pb] offset:27648\nv_mfma_f32_32x32x16_bf16 v[80:95], v[100:103], v[108:111], v[80:95]\ns_waitcnt vmcnt(3) lgkmcnt(0)\ns_barrier\n"
        "v_mfma_f32_32x32x16_bf16 v[32:47], v[112:115], v[120:123], v[32:47]\ns_add_u32 m0, %[dw], 24576\ns_add_u32 s40, s40, 0x1c000\ns_addc_u32 s41, s41, 0\n"
        "global_load_lds_dwordx4 %[vw], s[40:41]\nds_read_b64_tr_b16 v[96:97], %[pa] offset:49152\nds_read_b64_tr_b16 v[98:99], %[pa] offset:49664\n"
        "v_mfma_f32_32x32x16_bf16 v[48:63], v[112:115], v[124:127], v[48:63]\ns_add_u32 m0, %[dx], 24576\ns_add_u32 s42, s42, 0x800\ns_addc_u32 s43, s43, 0\n"
        "global_load_lds_dwordx4 %[vx], s[42:43] sc1\nds_read_b128 v[104:107], %[pb] offset:49152\nds_read_b64_tr_b16 v[100:101], %[pa] offset:53248\n"
        "v_mfma_f32_32x32x16_bf16 v[64:79], v[116:119], v[120:123], v[64:79]\nglobal_load_lds_dwordx4 %[vx], s[42:43] offset:1024 sc1\n"
        "ds_read_b64_tr_b16 v[102:103], %[pa] offset:53760\nds_read_b128 v[108:111], %[pb] offset:51200\nv_mfma_f32_32x32x16_bf16 v[80:95], v[116:119], v[124:127], v[80:95]\n"
        "s_waitcnt lgkmcnt(0)\nv_mfma_f32_32x32x16_bf16 v[32:47], v[96:99], v[104:107], v[32:47]\nds_read_b64_tr_b16 v[112:113], %[pa] offset:51200\n"
        "ds_read_b64_tr_b16 v[114:115], %[pa] offset:51712\nv_mfma_f32_32x32x16_bf16 v[48:63], v[96:99], v[108:111], v[48:63]\nds_read_b128 v[120:123], %[pb] offset:50176\n"
        "ds_read_b64_tr_b16 v[116:117], %[pa] offset:55296\nv_mfma_f32_32x32x16_bf16 v[64:79], v[100:103], v[104:107], v[64:79]\nds_read_b64_tr_b16 v[118:119], %[pa] offset:55808\n"
        "ds_read_b128 v[124:127], %[pb] offset:52224\nv_mfma_f32_32x32x16_bf16 v[80:95], v[100:103], v[108:111], v[80:95]\ns_waitcnt vmcnt(3) lgkmcnt(0)\ns_barrier\n"
        "v_mfma_f32_32x32x16_bf16 v[32:47], v[112:115], v[120:123], v[32:47]\ns_add_u32 m0, %[dw], 49152\ns_add_u32 s40, s40, 0x1c000\ns_addc_u32 s41, s41, 0\n"
        "global_load_lds_dwordx4 %[vw], s[40:41]\nds_read_b64_tr_b16 v[96:97], %[pa] offset:0\nds_read_b64_tr_b16 v[98:99], %[pa] offset:512\n"
        "v_mfma_f32_32x32x16_bf16 v[48:63], v[112:115], v[124:127], v[48:63]\ns_add_u32 m0, %[dx], 49152\ns_add_u32 s42, s42, 0x800\ns_addc_u32 s43, s43, 0\n"
        "global_load_lds_dwordx4 %[vx], s[42:43] sc1\nds_read_b128 v[104:107], %[pb] offset:0\nds_read_b64_tr_b16 v[100:101], %[pa] offset:4096\n"
        "v_mfma_f32_32x32x16_bf16 v[64:79], v[116:119], v[120:123], v[64:79]\nglobal_load_lds_dwordx4 %[vx], s[42:43] offset:1024 sc1\n"
        "ds_read_b64_tr_b16 v[102:103], %[pa] offset:4608\nds_read_b128 v[108:111], %[pb] offset:2048\nv_mfma_f32_32x32x16_bf16 v[80:95], v[116:119], v[124:127], v[80:95]\n"
        "s_waitcnt lgkmcnt(0)\nv_mfma_f32_32x32x16_bf16 v[32:47], v[96:99], v[104:107], v[32:47]\nds_read_b64_tr_b16 v[112:113], %[pa] offset:2048\n"
        "ds_read_b64_tr_b16 v[114:115], %[pa] offset:2560\nv_mfma_f32_32x32x16_bf16 v[48:63], v[96:99], v[108:111], v[48:63]\nds_read_b128 v[120:123], %[pb] offset:1024\n"
        "ds_read_b64_tr_b16 v[116:117], %[pa] offset:6144\nv_mfma_f32_32x32x16_bf16 v[64:79], v[100:103], v[104:107], v[64:79]\nds_read_b64_tr_b16 v[118:119], %[pa] offset:6656\n"
        "ds_read_b128 v[124:127], %[pb] offset:3072\nv_mfma_f32_32x32x16_bf16 v[80:95], v[100:103], v[108:111], v[80:95]\ns_waitcnt vmcnt(3) lgkmcnt(0)\ns_barrier\n"
        "v_mfma_f32_32x32x16_bf16 v[32:47], v[112:115], v[120:123], v[32:47]\ns_add_u32 m0, %[dw], 0\ns_add_u32 s40, s40, 0x1c000\ns_addc_u32 s41, s41, 0\n"
        "global_load_lds_dwordx4 %[vw], s[40:41]\nds_read_b64_tr_b16 v[96:97], %[pa] offset:24576\nds_read_b64_tr_b16 v[98:99], %[pa] offset:25088\n"
        "v_mfma_f32_32x32x16_bf16 v[48:63], v[112:115], v[124:127], v[48:63]\ns_add_u32 m0, %[dx], 0\ns_add_u32 s42, s42, 0x800\ns_addc_u32 s43, s43, 0\n"
        "global_load_lds_dwordx4 %[vx], s[42:43] sc1\nds_read_b128 v[104:107], %[pb] offset:24576\nds_read_b64_tr_b16 v[100:101], %[pa] offset:28672\n"
        "v_mfma_f32_32x32x16_bf16 v[64:79], v[116:119], v[120:123], v[64:79]\nglobal_load_lds_dwordx4 %[vx], s[42:43] offset:1024 sc1\n"
        "ds_read_b64_tr_b16 v[102:103], %[pa] offset:29184\nds_read_b128 v[108:111], %[pb] offset:26624\nv_mfma_f32_32x32x16_bf16 v[80:95], v[116:119], v[124:127], v[80:95]\n"
        "s_waitcnt lgkmcnt(0)\nv_mfma_f32_32x32x16_bf16 v[32:47], v[96:99], v[104:107], v[32:47]\nds_read_b64_tr_b16 v[112:113], %[pa] offset:26624\n"
        "ds_read_b64_tr_b16 v[114:115], %[pa] offset:27136\nv_mfma_f32_32x32x16_bf16 v[48:63], v[96:99], v[108:111], v[48:63]\nds_read_b128 v[120:123], %[pb] offset:25600\n"
        "ds_read_b64_tr_b16 v[116:117], %[pa] offset:30720\nv_mfma_f32_32x32x16_bf16 v[64:79], v[100:103], v[104:107], v[64:79]\nds_read_b64_tr_b16 v[118:119], %[pa] offset:31232\n"
        "ds_read_b128 v[124:127], %[pb] offset:27648\nv_mfma_f32_32x32x16_bf16 v[80:95], v[100:103], v[108:111], v[80:95]\ns_waitcnt vmcnt(3) lgkmcnt(0)\ns_barrier\n"
        "v_mfma_f32_32x32x16_bf16 v[32:47], v[112:115], v[120:123], v[32:47]\nds_read_b64_tr_b16 v[96:97], %[pa] offset:49152\nds_read_b64_tr_b16 v[98:99], %[pa] offset:49664\n"
        "v_mfma_f32_32x32x16_bf16 v[48:63], v[112:115], v[124:127], v[48:63]\nds_read_b128 v[104:107], %[pb] offset:49152\nds_read_b64_tr_b16 v[100:101], %[pa] offset:53248\n"
        "v_mfma_f32_32x32x16_bf16 v[64:79], v[116:119], v[120:123], v[64:79]\nds_read_b64_tr_b16 v[102:103], %[pa] offset:53760\nds_read_b128 v[108:111], %[pb] offset:51200\n"
        "v_mfma_f32_32x32x16_bf16 v[80:95], v[116:119], v[124:127], v[80:95]\ns_waitcnt lgkmcnt(0)\nv_mfma_f32_32x32x16_bf16 v[32:47], v[96:99], v[104:107], v[32:47]\n"
        "ds_read_b64_tr_b16 v[112:113], %[pa] offset:51200\nds_read_b64_tr_b16 v[114:115], %[pa] offset:51712\nv_mfma_f32_32x32x16_bf16 v[48:63], v[96:99], v[108:111], v[48:63]\n"
        "ds_read_b128 v[120:123], %[pb] offset:50176\nds_read_b64_tr_b16 v[116:117], %[pa] offset:55296\nv_mfma_f32_32x32x16_bf16 v[64:79], v[100:103], v[104:107], v[64:79]\n"
        "ds_read_b64_tr_b16 v[118:119], %[pa] offset:55808\nds_read_b128 v[124:127], %[pb] offset:52224\nv_mfma_f32_32x32x16_bf16 v[80:95], v[100:103], v[108:111], v[80:95]\n"
        "s_waitcnt vmcnt(0) lgkmcnt(0)\ns_barrier\nv_mfma_f32_32x32x16_bf16 v[32:47], v[112:115], v[120:123], v[32:47]\nds_read_b64_tr_b16 v[96:97], %[pa] offset:0\n"
        "ds_read_b64_tr_b16 v[98:99], %[pa] offset:512\nv_mfma_f32_32x32x16_bf16 v[48:63], v[112:115], v[124:127], v[48:63]\nds_read_b128 v[104:107], %[pb] offset:0\n"
        "ds_read_b64_tr_b16 v[100:101], %[pa] offset:4096\nv_mfma_f32_32x32x16_bf16 v[64:79], v[116:119], v[120:123], v[64:79]\nds_read_b64_tr_b16 v[102:103], %[pa] offset:4608\n"
        "ds_read_b128 v[108:111], %[pb] offset:2048\nv_mfma_f32_32x32x16_bf16 v[80:95], v[116:119], v[124:127], v[80:95]\ns_waitcnt lgkmcnt(0)\n"
        "v_mfma_f32_32x32x16_bf16 v[32:47], v[96:99], v[104:107], v[32:47]\nds_read_b64_tr_b16 v[112:113], %[pa] offset:2048\nds_read_b64_tr_b16 v[114:115], %[pa] offset:2560\n"
        "v_mfma_f32_32x32x16_bf16 v[48:63], v[96:99], v[108:111], v[48:63]\nds_read_b128 v[120:123], %[pb] offset:1024\nds_read_b64_tr_b16 v[116:117], %[pa] offset:6144\n"
        "v_mfma_f32_32x32x16_bf16 v[64:79], v[100:103], v[104:107], v[64:79]\nds_read_b64_tr_b16 v[118:119], %[pa] offset:6656\nds_read_b128 v[124:127], %[pb] offset:3072\n"
        "v_mfma_f32_32x32x16_bf16 v[80:95], v[100:103], v[108:111], v[80:95]\ns_waitcnt lgkmcnt(0)\nv_mfma_f32_32x32x16_bf16 v[32:47], v[112:115], v[120:123], v[32:47]\n"
        "v_mfma_f32_32x32x16_bf16 v[48:63], v[112:115], v[124:127], v[48:63]\nv_mfma_f32_32x32x16_bf16 v[64:79], v[116:119], v[120:123], v[64:79]\n"
        "v_mfma_f32_32x32x16_bf16 v[80:95], v[116:119], v[124:127], v[80:95]\ns_mov_b32 m0, %[sv]\ns_nop 7\ns_nop 7\n"
          : "=&{v[32:47]}"(g0), "=&{v[48:63]}"(g1), "=&{v[64:79]}"(u0), "=&{v[80:95]}"(u1), [jk] "+v"(jk), [sv] "=&s"(sv)
          : [pa] "v"(pa), [pb] "v"(pb), [vw] "v"(vw), [vx] "v"(l16), [tv] "v"(tv), [dw] "s"(dw), [dx] "s"(dx), [sw] "s"(sw + 2 * 114688), [sx] "s"(sx + 2 * 2048), [wd] "s"(wd)
          : "v96", "v97", "v98", "v99", "v100", "v101", "v102", "v103", "v104", "v105", "v106", "v107", "v108", "v109", "v110", "v111", "v112", "v113", "v114", "v115", "v116", "v117", "v118", "v119", "v120", "v121", "v122", "v123", "v124", "v125", "v126", "v127", "s40", "s41", "s42", "s43", "scc", "memory");
        PRIO(P_EPI);
#pragma unroll
        for (int i = 0; i < 16; ++i) { g0[i] = fmaxf(g0[i], 0.f) * u0[i]; g1[i] = fmaxf(g1[i], 0.f) * u1[i]; }
        TS(256 + b, 1);
        char* hp = ws + WS_H + ((TB * 8 + 2 * tk) * 112 + fb * 4 + ft * 2) * 1024;
        st_sc1(l16, hp, pack8(g0, 0)); st_sc1(l16, hp + 1024, pack8(g0, 8)); st_sc1(l16, hp + 114688, pack8(g1, 0)); st_sc1(l16, hp + 115712, pack8(g1, 8));
        BAR(0);
        if (tid == 0) set_flag(ws, FL_H + TB * 32 + fb, tag);
    } else {
        const int j = i5 - 28;
        __builtin_amdgcn_s_sleep(127);
        touch((j * 1024 + tid) * 128, wo); TOUCH_END(); touch((j * 1024 + 512 + tid) * 128, wo); TOUCH_END();
        if (b == 255) {
            wait_flags(ws, FL_A + tid, tag); BARL();
            if (tid == 0) set_flag(ws, FL_E, tag); }
    }
    TS(256 + b, 2);
    const int tg = xcd * 4 + (i5 & 3), cg = i5 >> 2, t = w & 1, kq = w >> 1;
    const unsigned vo = (l >> 3) * 1024 + (((l & 7) ^ (((l >> 4) & 1) << 2)) << 4);
    const int at = (4 * h + q) * 128 + (((((l >> 4) & 1) * 2 + (p >> 1)) ^ ((q >> 1) << 2)) << 4) + 8 * (p & 1);
    float gg[8]; u32x2 x0, x1;
    const unsigned ob = (tg * 64 + t * 32 + (l & 31)) * 1024 + cg * 128 + kq * 32;
    {   const char* sw = uni(wd + cg * 128 + w * 14 * 16384); const unsigned dw = L0 + w * 8192;
        const int r0 = 2 * cg - 14;
#define ROT(n) ((n) + r0 < 0 ? (n) + r0 + 14 : (n) + r0)
#define F2_W(n) do { const char* s_ = sw + ROT(n) * 16384; glds16(vo, s_, dw + ((n) & 3) * 2048); glds16(vo, s_ + 8192, dw + ((n) & 3) * 2048 + 1024); } while (0)
        F2_W(0); F2_W(1); F2_W(2); F2_W(3);
        wait_flags(ws, FL_H + xcd * 32 + (w * 7 >> 1) + (l & 3), tag);
        TS(256 + b, 3);
        const char* sh = uni(ws + WS_H + (2 * tg * 112 + w * 14) * 1024);
        f32x16 d00, d01, d10, d11; unsigned ro = ROT(0) * 1024, sv;
        PRIO(P_LOOP2);
        asm volatile(
        "s_mov_b32 %[sv], m0\ns_mov_b64 s[44:45], %[sh]\ns_mov_b64 s[46:47], %[sw]\ns_add_u32 s40, s44, %[ro]\ns_addc_u32 s41, s45, 0\n"
        "global_load_dwordx4 v[112:115], %[h0], s[40:41] sc1\nglobal_load_dwordx4 v[116:119], %[h1], s[40:41] sc1\ns_add_u32 %[ro], %[ro], 1024\ns_cmp_eq_u32 %[ro], 14336\n"
        "s_cselect_b32 %[ro], 0, %[ro]\ns_add_u32 s40, s44, %[ro]\ns_addc_u32 s41, s45, 0\nglobal_load_dwordx4 v[120:123], %[h0], s[40:41] sc1\n"
        "global_load_dwordx4 v[124:127], %[h1], s[40:41] sc1\ns_add_u32 %[ro], %[ro], 1024\ns_cmp_eq_u32 %[ro], 14336\ns_cselect_b32 %[ro], 0, %[ro]\ns_add_u32 s40, s44, %[ro]\n"
        "s_addc_u32 s41, s45, 0\nglobal_load_dwordx4 v[16:19], %[h0], s[40:41] sc1\nglobal_load_dwordx4 v[20:23], %[h1], s[40:41] sc1\ns_add_u32 %[ro], %[ro], 1024\n"
        "s_cmp_eq_u32 %[ro], 14336\ns_cselect_b32 %[ro], 0, %[ro]\ns_add_u32 s40, s44, %[ro]\ns_addc_u32 s41, s45, 0\nglobal_load_dwordx4 v[24:27], %[h0], s[40:41] sc1\n"
        "global_load_dwordx4 v[28:31], %[h1], s[40:41] sc1\ns_add_u32 %[ro], %[ro], 1024\ns_cmp_eq_u32 %[ro], 14336\ns_cselect_b32 %[ro], 0, %[ro]\ns_waitcnt vmcnt(8)\n"
        "ds_read_b64_tr_b16 v[96:97], %[a0] offset:0\nds_read_b64_tr_b16 v[98:99], %[a0] offset:1024\nds_read_b64_tr_b16 v[100:101], %[a1] offset:0\n"
        "ds_read_b64_tr_b16 v[102:103], %[a1] offset:1024\ns_waitcnt vmcnt(6) lgkmcnt(0)\nv_mfma_f32_32x32x16_bf16 v[32:47], v[104:107], v[112:115], 0\n"
        "ds_read_b64_tr_b16 v[104:105], %[a0] offset:2048\nds_read_b64_tr_b16 v[106:107], %[a0] offset:3072\ns_lshl_b32 s42, %[ro], 4\ns_add_u32 s42, s46, s42\n"
        "s_addc_u32 s43, s47, 0\ns_add_u32 m0, %[dw], 0\nv_mfma_f32_32x32x16_bf16 v[48:63], v[108:111], v[112:115], 0\nds_read_b64_tr_b16 v[108:109], %[a1] offset:2048\n"
        "ds_read_b64_tr_b16 v[110:111], %[a1] offset:3072\nglobal_load_lds_dwordx4 %[w0], s[42:43]\nv_mfma_f32_32x32x16_bf16 v[64:79], v[104:107], v[116:119], 0\n"
        "global_load_lds_dwordx4 %[w1], s[42:43] offset:1024\nv_mfma_f32_32x32x16_bf16 v[80:95], v[108:111], v[116:119], 0\ns_add_u32 s40, s44, %[ro]\ns_addc_u32 s41, s45, 0\n"
        "global_load_dwordx4 v[112:115], %[h0], s[40:41] sc1\nglobal_load_dwordx4 v[116:119], %[h1], s[40:41] sc1\ns_add_u32 %[ro], %[ro], 1024\ns_cmp_eq_u32 %[ro], 14336\n"
        "s_cselect_b32 %[ro], 0, %[ro]\ns_waitcnt vmcnt(8) lgkmcnt(0)\nv_mfma_f32_32x32x16_bf16 v[32:47], v[96:99], v[120:123], v[32:47]\n"
        "ds_read_b64_tr_b16 v[96:97], %[a0] offset:4096\nds_read_b64_tr_b16 v[98:99], %[a0] offset:5120\ns_lshl_b32 s42, %[ro], 4\ns_add_u32 s42, s46, s42\ns_addc_u32 s43, s47, 0\n"
        "s_add_u32 m0, %[dw], 2048\nv_mfma_f32_32x32x16_bf16 v[48:63], v[100:103], v[120:123], v[48:63]\nds_read_b64_tr_b16 v[100:101], %[a1] offset:4096\n"
        "ds_read_b64_tr_b16 v[102:103], %[a1] offset:5120\nglobal_load_lds_dwordx4 %[w0], s[42:43]\nv_mfma_f32_32x32x16_bf16 v[64:79], v[96:99], v[124:127], v[64:79]\n"
        "global_load_lds_dwordx4 %[w1], s[42:43] offset:1024\nv_mfma_f32_32x32x16_bf16 v[80:95], v[100:103], v[124:127], v[80:95]\ns_add_u32 s40, s44, %[ro]\n"
        "s_addc_u32 s41, s45, 0\nglobal_load_dwordx4 v[120:123], %[h0], s[40:41] sc1\nglobal_load_dwordx4 v[124:127], %[h1], s[40:41] sc1\ns_add_u32 %[ro], %[ro], 1024\n"
        "s_cmp_eq_u32 %[ro], 14336\ns_cselect_b32 %[ro], 0, %[ro]\ns_waitcnt vmcnt(10) lgkmcnt(0)\nv_mfma_f32_32x32x16_bf16 v[32:47], v[104:107], v[16:19], v[32:47]\n"
        "ds_read_b64_tr_b16 v[104:105], %[a0] offset:6144\nds_read_b64_tr_b16 v[106:107], %[a0] offset:7168\ns_lshl_b32 s42, %[ro], 4\ns_add_u32 s42, s46, s42\n"
        "s_addc_u32 s43, s47, 0\ns_add_u32 m0, %[dw], 4096\nv_mfma_f32_32x32x16_bf16 v[48:63], v[108:111], v[16:19], v[48:63]\nds_read_b64_tr_b16 v[108:109], %[a1] offset:6144\n"
        "ds_read_b64_tr_b16 v[110:111], %[a1] offset:7168\nglobal_load_lds_dwordx4 %[w0], s[42:43]\nv_mfma_f32_32x32x16_bf16 v[64:79], v[104:107], v[20:23], v[64:79]\n"
        "global_load_lds_dwordx4 %[w1], s[42:43] offset:1024\nv_mfma_f32_32x32x16_bf16 v[80:95], v[108:111], v[20:23], v[80:95]\ns_add_u32 s40, s44, %[ro]\ns_addc_u32 s41, s45, 0\n"
        "global_load_dwordx4 v[16:19], %[h0], s[40:41] sc1\nglobal_load_dwordx4 v[20:23], %[h1], s[40:41] sc1\ns_add_u32 %[ro], %[ro], 1024\ns_cmp_eq_u32 %[ro], 14336\n"
        "s_cselect_b32 %[ro], 0, %[ro]\ns_waitcnt vmcnt(10) lgkmcnt(0)\nv_mfma_f32_32x32x16_bf16 v[32:47], v[96:99], v[24:27], v[32:47]\n"
        "ds_read_b64_tr_b16 v[96:97], %[a0] offset:0\nds_read_b64_tr_b16 v[98:99], %[a0] offset:1024\ns_lshl_b32 s42, %[ro], 4\ns_add_u32 s42, s46, s42\ns_addc_u32 s43, s47, 0\n"
        "s_add_u32 m0, %[dw], 6144\nv_mfma_f32_32x32x16_bf16 v[48:63], v[100:103], v[24:27], v[48:63]\nds_read_b64_tr_b16 v[100:101], %[a1] offset:0\n"
        "ds_read_b64_tr_b16 v[102:103], %[a1] offset:1024\nglobal_load_lds_dwordx4 %[w0], s[42:43]\nv_mfma_f32_32x32x16_bf16 v[64:79], v[96:99], v[28:31], v[64:79]\n"
        "global_load_lds_dwordx4 %[w1], s[42:43] offset:1024\nv_mfma_f32_32x32x16_bf16 v[80:95], v[100:103], v[28:31], v[80:95]\ns_add_u32 s40, s44, %[ro]\ns_addc_u32 s41, s45, 0\n"
        "global_load_dwordx4 v[24:27], %[h0], s[40:41] sc1\nglobal_load_dwordx4 v[28:31], %[h1], s[40:41] sc1\ns_add_u32 %[ro], %[ro], 1024\ns_cmp_eq_u32 %[ro], 14336\n"
        "s_cselect_b32 %[ro], 0, %[ro]\ns_waitcnt vmcnt(10) lgkmcnt(0)\nv_mfma_f32_32x32x16_bf16 v[32:47], v[104:107], v[112:115], v[32:47]\n"
        "ds_read_b64_tr_b16 v[104:105], %[a0] offset:2048\nds_read_b64_tr_b16 v[106:107], %[a0] offset:3072\ns_lshl_b32 s42, %[ro], 4\ns_add_u32 s42, s46, s42\n"
        "s_addc_u32 s43, s47, 0\ns_add_u32 m0, %[dw], 0\nv_mfma_f32_32x32x16_bf16 v[48:63], v[108:111], v[112:115], v[48:63]\nds_read_b64_tr_b16 v[108:109], %[a1] offset:2048\n"
        "ds_read_b64_tr_b16 v[110:111], %[a1] offset:3072\nglobal_load_lds_dwordx4 %[w0], s[42:43]\nv_mfma_f32_32x32x16_bf16 v[64:79], v[104:107], v[116:119], v[64:79]\n"
        "global_load_lds_dwordx4 %[w1], s[42:43] offset:1024\nv_mfma_f32_32x32x16_bf16 v[80:95], v[108:111], v[116:119], v[80:95]\ns_add_u32 s40, s44, %[ro]\n"
        "s_addc_u32 s41, s45, 0\nglobal_load_dwordx4 v[112:115], %[h0], s[40:41] sc1\nglobal_load_dwordx4 v[116:119], %[h1], s[40:41] sc1\ns_add_u32 %[ro], %[ro], 1024\n"
        "s_cmp_eq_u32 %[ro], 14336\ns_cselect_b32 %[ro], 0, %[ro]\ns_waitcnt vmcnt(10) lgkmcnt(0)\nv_mfma_f32_32x32x16_bf16 v[32:47], v[96:99], v[120:123], v[32:47]\n"
        "ds_read_b64_tr_b16 v[96:97], %[a0] offset:4096\nds_read_b64_tr_b16 v[98:99], %[a0] offset:5120\ns_lshl_b32 s42, %[ro], 4\ns_add_u32 s42, s46, s42\ns_addc_u32 s43, s47, 0\n"
        "s_add_u32 m0, %[dw], 2048\nv_mfma_f32_32x32x16_bf16 v[48:63], v[100:103], v[120:123], v[48:63]\nds_read_b64_tr_b16 v[100:101], %[a1] offset:4096\n"
        "ds_read_b64_tr_b16 v[102:103], %[a1] offset:5120\nglobal_load_lds_dwordx4 %[w0], s[42:43]\nv_mfma_f32_32x32x16_bf16 v[64:79], v[96:99], v[124:127], v[64:79]\n"
        "global_load_lds_dwordx4 %[w1], s[42:43] offset:1024\nv_mfma_f32_32x32x16_bf16 v[80:95], v[100:103], v[124:127], v[80:95]\ns_add_u32 s40, s44, %[ro]\n"
        "s_addc_u32 s41, s45, 0\nglobal_load_dwordx4 v[120:123], %[h0], s[40:41] sc1\nglobal_load_dwordx4 v[124:127], %[h1], s[40:41] sc1\ns_add_u32 %[ro], %[ro], 1024\n"
        "s_cmp_eq_u32 %[ro], 14336\ns_cselect_b32 %[ro], 0, %[ro]\ns_waitcnt vmcnt(10) lgkmcnt(0)\nv_mfma_f32_32x32x16_bf16 v[32:47], v[104:107], v[16:19], v[32:47]\n"
        "ds_read_b64_tr_b16 v[104:105], %[a0] offset:6144\nds_read_b64_tr_b16 v[106:107], %[a0] offset:7168\ns_lshl_b32 s42, %[ro], 4\ns_add_u32 s42, s46, s42\n"
        "s_addc_u32 s43, s47, 0\ns_add_u32 m0, %[dw], 4096\nv_mfma_f32_32x32x16_bf16 v[48:63], v[108:111], v[16:19], v[48:63]\nds_read_b64_tr_b16 v[108:109], %[a1] offset:6144\n"
        "ds_read_b64_tr_b16 v[110:111], %[a1] offset:7168\nglobal_load_lds_dwordx4 %[w0], s[42:43]\nv_mfma_f32_32x32x16_bf16 v[64:79], v[104:107], v[20:23], v[64:79]\n"
        "global_load_lds_dwordx4 %[w1], s[42:43] offset:1024\nv_mfma_f32_32x32x16_bf16 v[80:95], v[108:111], v[20:23], v[80:95]\ns_add_u32 s40, s44, %[ro]\ns_addc_u32 s41, s45, 0\n"
        "global_load_dwordx4 v[16:19], %[h0], s[40:41] sc1\nglobal_load_dwordx4 v[20:23], %[h1], s[40:41] sc1\ns_add_u32 %[ro], %[ro], 1024\ns_cmp_eq_u32 %[ro], 14336\n"
        "s_cselect_b32 %[ro], 0, %[ro]\ns_waitcnt vmcnt(10) lgkmcnt(0)\nv_mfma_f32_32x32x16_bf16 v[32:47], v[96:99], v[24:27], v[32:47]\n"
        "ds_read_b64_tr_b16 v[96:97], %[a0] offset:0\nds_read_b64_tr_b16 v[98:99], %[a0] offset:1024\ns_lshl_b32 s42, %[ro], 4\ns_add_u32 s42, s46, s42\ns_addc_u32 s43, s47, 0\n"
        "s_add_u32 m0, %[dw], 6144\nv_mfma_f32_32x32x16_bf16 v[48:63], v[100:103], v[24:27], v[48:63]\nds_read_b64_tr_b16 v[100:101], %[a1] offset:0\n"
        "ds_read_b64_tr_b16 v[102:103], %[a1] offset:1024\nglobal_load_lds_dwordx4 %[w0], s[42:43]\nv_mfma_f32_32x32x16_bf16 v[64:79], v[96:99], v[28:31], v[64:79]\n"
        "global_load_lds_dwordx4 %[w1], s[42:43] offset:1024\nv_mfma_f32_32x32x16_bf16 v[80:95], v[100:103], v[28:31], v[80:95]\ns_add_u32 s40, s44, %[ro]\ns_addc_u32 s41, s45, 0\n"
        "global_load_dwordx4 v[24:27], %[h0], s[40:41] sc1\nglobal_load_dwordx4 v[28:31], %[h1], s[40:41] sc1\ns_add_u32 %[ro], %[ro], 1024\ns_cmp_eq_u32 %[ro], 14336\n"
        "s_cselect_b32 %[ro], 0, %[ro]\ns_waitcnt vmcnt(10) lgkmcnt(0)\nv_mfma_f32_32x32x16_bf16 v[32:47], v[104:107], v[112:115], v[32:47]\n"
        "ds_read_b64_tr_b16 v[104:105], %[a0] offset:2048\nds_read_b64_tr_b16 v[106:107], %[a0] offset:3072\ns_lshl_b32 s42, %[ro], 4\ns_add_u32 s42, s46, s42\n"
        "s_addc_u32 s43, s47, 0\ns_add_u32 m0, %[dw], 0\nv_mfma_f32_32x32x16_bf16 v[48:63], v[108:111], v[112:115], v[48:63]\nds_read_b64_tr_b16 v[108:109], %[a1] offset:2048\n"
        "ds_read_b64_tr_b16 v[110:111], %[a1] offset:3072\nglobal_load_lds_dwordx4 %[w0], s[42:43]\nv_mfma_f32_32x32x16_bf16 v[64:79], v[104:107], v[116:119], v[64:79]\n"
        "global_load_lds_dwordx4 %[w1], s[42:43] offset:1024\nv_mfma_f32_32x32x16_bf16 v[80:95], v[108:111], v[116:119], v[80:95]\ns_add_u32 s40, s44, %[ro]\n"
        "s_addc_u32 s41, s45, 0\nglobal_load_dwordx4 v[112:115], %[h0], s[40:41] sc1\nglobal_load_dwordx4 v[116:119], %[h1], s[40:41] sc1\ns_add_u32 %[ro], %[ro], 1024\n"
        "s_cmp_eq_u32 %[ro], 14336\ns_cselect_b32 %[ro], 0, %[ro]\ns_waitcnt vmcnt(10) lgkmcnt(0)\nv_mfma_f32_32x32x16_bf16 v[32:47], v[96:99], v[120:123], v[32:47]\n"
        "ds_read_b64_tr_b16 v[96:97], %[a0] offset:4096\nds_read_b64_tr_b16 v[98:99], %[a0] offset:5120\ns_lshl_b32 s42, %[ro], 4\ns_add_u32 s42, s46, s42\ns_addc_u32 s43, s47, 0\n"
        "s_add_u32 m0, %[dw], 2048\nv_mfma_f32_32x32x16_bf16 v[48:63], v[100:103], v[120:123], v[48:63]\nds_read_b64_tr_b16 v[100:101], %[a1] offset:4096\n"
        "ds_read_b64_tr_b16 v[102:103], %[a1] offset:5120\nglobal_load_lds_dwordx4 %[w0], s[42:43]\nv_mfma_f32_32x32x16_bf16 v[64:79], v[96:99], v[124:127], v[64:79]\n"
        "global_load_lds_dwordx4 %[w1], s[42:43] offset:1024\nv_mfma_f32_32x32x16_bf16 v[80:95], v[100:103], v[124:127], v[80:95]\ns_add_u32 s40, s44, %[ro]\n"
        "s_addc_u32 s41, s45, 0\nglobal_load_dwordx4 v[120:123], %[h0], s[40:41] sc1\nglobal_load_dwordx4 v[124:127], %[h1], s[40:41] sc1\ns_add_u32 %[ro], %[ro], 1024\n"
        "s_cmp_eq_u32 %[ro], 14336\ns_cselect_b32 %[ro], 0, %[ro]\ns_waitcnt vmcnt(10) lgkmcnt(0)\nv_mfma_f32_32x32x16_bf16 v[32:47], v[104:107], v[16:19], v[32:47]\n"
        "ds_read_b64_tr_b16 v[104:105], %[a0] offset:6144\nds_read_b64_tr_b16 v[106:107], %[a0] offset:7168\nv_mfma_f32_32x32x16_bf16 v[48:63], v[108:111], v[16:19], v[48:63]\n"
        "ds_read_b64_tr_b16 v[108:109], %[a1] offset:6144\nds_read_b64_tr_b16 v[110:111], %[a1] offset:7168\nv_mfma_f32_32x32x16_bf16 v[64:79], v[104:107], v[20:23], v[64:79]\n"
        "v_mfma_f32_32x32x16_bf16 v[80:95], v[108:111], v[20:23], v[80:95]\ns_waitcnt vmcnt(6) lgkmcnt(0)\nv_mfma_f32_32x32x16_bf16 v[32:47], v[96:99], v[24:27], v[32:47]\n"
        "ds_read_b64_tr_b16 v[96:97], %[a0] offset:0\nds_read_b64_tr_b16 v[98:99], %[a0] offset:1024\nv_mfma_f32_32x32x16_bf16 v[48:63], v[100:103], v[24:27], v[48:63]\n"
        "ds_read_b64_tr_b16 v[100:101], %[a1] offset:0\nds_read_b64_tr_b16 v[102:103], %[a1] offset:1024\nv_mfma_f32_32x32x16_bf16 v[64:79], v[96:99], v[28:31], v[64:79]\n"
        "v_mfma_f32_32x32x16_bf16 v[80:95], v[100:103], v[28:31], v[80:95]\ns_waitcnt vmcnt(2) lgkmcnt(0)\nv_mfma_f32_32x32x16_bf16 v[32:47], v[104:107], v[112:115], v[32:47]\n"
        "ds_read_b64_tr_b16 v[104:105], %[a0] offset:2048\nds_read_b64_tr_b16 v[106:107], %[a0] offset:3072\nv_mfma_f32_32x32x16_bf16 v[48:63], v[108:111], v[112:115], v[48:63]\n"
        "ds_read_b64_tr_b16 v[108:109], %[a1] offset:2048\nds_read_b64_tr_b16 v[110:111], %[a1] offset:3072\nv_mfma_f32_32x32x16_bf16 v[64:79], v[104:107], v[116:119], v[64:79]\n"
        "v_mfma_f32_32x32x16_bf16 v[80:95], v[108:111], v[116:119], v[80:95]\ns_waitcnt vmcnt(0) lgkmcnt(0)\nv_mfma_f32_32x32x16_bf16 v[32:47], v[104:107], v[120:123], v[32:47]\n"
        "v_mfma_f32_32x32x16_bf16 v[48:63], v[108:111], v[120:123], v[48:63]\nv_mfma_f32_32x32x16_bf16 v[64:79], v[104:107], v[124:127], v[64:79]\n"
        "v_mfma_f32_32x32x16_bf16 v[80:95], v[108:111], v[124:127], v[80:95]\ns_mov_b32 m0, %[sv]\ns_nop 7\ns_nop 7\n"
          : "=&{v[32:47]}"(d00), "=&{v[48:63]}"(d01), "=&{v[64:79]}"(d10), "=&{v[80:95]}"(d11), [ro] "+s"(ro), [sv] "=&s"(sv)
          : [a0] "v"(dw + at), [a1] "v"(dw + (at ^ 64)), [h0] "v"(l16), [h1] "v"(l16 + 114688), [w0] "v"(vo), [w1] "v"(vo + 7168), [dw] "s"(dw), [sh] "s"(sh), [sw] "s"(sw)
          : "v96", "v97", "v98", "v99", "v100", "v101", "v102", "v103", "v104", "v105", "v106", "v107", "v108", "v109", "v110", "v111", "v112", "v113", "v114", "v115", "v116", "v117", "v118", "v119", "v120", "v121", "v122", "v123", "v124", "v125", "v126", "v127", "v16", "v17", "v18", "v19", "v20", "v21", "v22", "v23", "v24", "v25", "v26", "v27", "v28", "v29", "v30", "v31", "s40", "s41", "s42", "s43", "s44", "s45", "s46", "s47", "scc", "memory");
        PRIO(P_EPI);
        TS(256 + b, 4);
        asm volatile("global_load_dwordx2 %0, %2, %3\n\tglobal_load_dwordx2 %1, %2, %3 offset:16" : "=&v"(x0), "=&v"(x1) : "v"(ob + 8 * h), "s"(x) : "memory");
        const lptr pw = L + w * 2048 + l16, pr = L + kq * 16384 + l16; f32x4 a, c;
#define F2_RED(dA, dB, tt) do { BARL(); _Pragma("unroll") for (int u = 0; u < 4; ++u) { lw128f(pw + (u >> 1) * 16384 + (u & 1) * 1024, dA[4 * u], dA[4 * u + 1], dA[4 * u + 2], dA[4 * u + 3]); \
            lw128f(pw + 32768 + (u >> 1) * 16384 + (u & 1) * 1024, dB[4 * u], dB[4 * u + 1], dB[4 * u + 2], dB[4 * u + 3]); } BARL(); \
            if (t == tt) { a = lr128f(pr); c = lr128f(pr + 1024); _Pragma("unroll") for (int u = 1; u < 8; ++u) { a += lr128f(pr + u * 2048); c += lr128f(pr + u * 2048 + 1024); } } } while (0)
        F2_RED(d00, d01, 0); F2_RED(d10, d11, 1);
#pragma unroll
        for (int i = 0; i < 4; ++i) { gg[i] = a[i] * __builtin_amdgcn_rcpf(1.f + __builtin_amdgcn_exp2f(a[i] * -1.4426950408889634f));
                                      gg[4 + i] = c[i] * __builtin_amdgcn_rcpf(1.f + __builtin_amdgcn_exp2f(c[i] * -1.4426950408889634f)); } }
    BARL(); TS(256 + b, 15);
#pragma unroll
    for (int n = 0; n < 8; ++n) glds16(vo, wo + cg * 128 + (w * 8 + n) * 8192, L0 + (w * 8 + n) * 1024);
    BAR(0); asm volatile("" : "+v"(x0), "+v"(x1)); TS(256 + b, 5);
    wait_flags(ws, FL_O + tg * 8 + 2 * kq + (l & 1), tag);
    TS(256 + b, 6);
    bf16x8 of[8];
    { const char* so = uni(ws + WS_O + ((2 * tg + t) * 32 + 8 * kq) * 1024);
#pragma unroll
      for (int s = 0; s < 8; ++s) LD_SC1(of[s], l16, so + s * 1024); }
    f32x16 c0 = splat16(0.f), c1 = c0;
    const lptr a0 = L + kq * 16384 + at, a1 = L + kq * 16384 + (at ^ 64);
#pragma unroll
    for (int s = 0; s < 8; ++s) { const bf16x8 f0 = ltr(a0, s * 2048, s * 2048 + 1024), f1 = ltr(a1, s * 2048, s * 2048 + 1024);
        asm volatile("s_waitcnt vmcnt(%1)" : "+v"(of[s]) : "n"(7 - s) : "memory");
        c0 = MFMA(f0, of[s], c0); c1 = MFMA(f1, of[s], c1); }
    BARL();
    const lptr pw = L + (t * 16 + kq) * 2048 + l16;
#pragma unroll
    for (int u = 0; u < 4; ++u) { lw128f(pw + (u >> 1) * 8192 + (u & 1) * 1024, c0[4 * u], c0[4 * u + 1], c0[4 * u + 2], c0[4 * u + 3]);
                                  lw128f(pw + 16384 + (u >> 1) * 8192 + (u & 1) * 1024, c1[4 * u], c1[4 * u + 1], c1[4 * u + 2], c1[4 * u + 3]); }
    BARL();
    const lptr pr = L + (t * 16 + kq * 4) * 2048 + l16; f32x4 a = lr128f(pr), c = lr128f(pr + 1024);
#pragma unroll
    for (int u = 1; u < 4; ++u) { a += lr128f(pr + u * 2048); c += lr128f(pr + u * 2048 + 1024); }
    const unsigned A0 = pk(blo(x0[0]) + a[0] * gg[0], bhi(x0[0]) + a[1] * gg[1]), A1 = pk(blo(x0[1]) + a[2] * gg[2], bhi(x0[1]) + a[3] * gg[3]);
    const unsigned B0 = pk(blo(x1[0]) + c[0] * gg[4], bhi(x1[0]) + c[1] * gg[5]), B1 = pk(blo(x1[1]) + c[2] * gg[6], bhi(x1[1]) + c[3] * gg[7]);
    auto s0 = __builtin_amdgcn_permlane32_swap(A0, B0, false, false), s1 = __builtin_amdgcn_permlane32_swap(A1, B1, false, false);
    const u32x4 ov = {s0[0], s1[0], s0[1], s1[1]};
    if (WT & 2) asm volatile("global_store_dwordx4 %0, %1, %2 sc0 sc1" ::"v"(ob + 16 * h), "v"(ov), "s"(out) : "memory"); else *(u32x4*)(out + ob + 16 * h) = ov;
    TS(256 + b, 7);
}

__global__ __launch_bounds__(512, 4) void k2_kernel(const char* x, const char* wq, const char* wk, const char* wv, const char* wo, const char* wg, const char* wu, const char* wd, char* out, char* ws) {
    extern __shared__ char smem[];
    const lptr L = (lptr)smem; const unsigned L0 = (unsigned)(size_t)L;
    const int b = blockIdx.x;
    PRIO(3); TS(b, 0);
    if (b < 256) attn_role(b, L, L0, wq, ws); else ffn_role(b - 256, L, L0, x, wk, wv, wo, wg, wu, wd, out, ws);
}

extern "C" void kernel_launch(void* const* d_in, const int* in_sizes, int n_in, void* d_out, int out_size, void* d_ws, size_t ws_size, hipStream_t stream) {
    (void)hipFuncSetAttribute((const void*)k2_kernel, hipFuncAttributeMaxDynamicSharedMemorySize, LDS_K2);
    k2_kernel<<<512, 512, LDS_K2, stream>>>((const char*)d_in[0], (const char*)d_in[1], (const char*)d_in[2], (const char*)d_in[3], (const char*)d_in[4], (const char*)d_in[6], (const char*)d_in[7], (const char*)d_in[8], (char*)d_out, (char*)d_ws);
}
```

```cpp
#include <hip/hip_runtime.h>
#include <hip/hip_bf16.h>
#include <stdint.h>

typedef short bf16x8 __attribute__((ext_vector_type(8)));
typedef short s16x4 __attribute__((ext_vector_type(4)));
typedef float f32x16 __attribute__((ext_vector_type(16)));
typedef float f32x4 __attribute__((ext_vector_type(4)));
typedef float f32x2 __attribute__((ext_vector_type(2)));
typedef __bf16 b16x2 __attribute__((ext_vector_type(2)));
typedef unsigned u32x4 __attribute__((ext_vector_type(4)));
typedef unsigned u32x2 __attribute__((ext_vector_type(2)));
#define LAS __attribute__((address_space(3)))
#define GAS __attribute__((address_space(1)))
#define DI __device__ __forceinline__
#define MFMA(a, b, c) __builtin_amdgcn_mfma_f32_32x32x16_bf16((a), (b), (c), 0, 0, 0)
#define RLX_AGENT __ATOMIC_RELAXED, __HIP_MEMORY_SCOPE_AGENT
typedef LAS char* lptr;

constexpr unsigned WS_XN = 0, WS_KV = 2u << 20, WS_H = 4u << 20, WS_O = 12u << 20, WS_FL = 16u << 20;
constexpr int FL_O = 0, FL_H = 256, FL_X = 512, FL_K = 640, FL_A = 768, FL_E = 1280;
constexpr int LDS_K2 = 81920;
constexpr unsigned SPIN_CAP = 1u << 20;
#define NOFFN 0
#define P_LOOP 0
#define P_LOOP2 0
#define P_ATT 1
#define TOG 0
#define BAL 1
#define P_EPI 3
#define TK 1
#define TX 0
#define WT 0
#define DLY 48
#define PSL 4
#define TLBW 0
#define PRIO(n) __builtin_amdgcn_s_setprio(n)
#define TS(blk, i)

DI unsigned pk(float a, float b) { f32x2 v = {a, b}; return __builtin_bit_cast(unsigned, __builtin_convertvector(v, b16x2)); }
DI bf16x8 pack8(const f32x16& x, int o) { u32x4 r = {pk(x[o], x[o + 1]), pk(x[o + 2], x[o + 3]), pk(x[o + 4], x[o + 5]), pk(x[o + 6], x[o + 7])}; return __builtin_bit_cast(bf16x8, r); }
DI float blo(unsigned u) { return __uint_as_float(u << 16); }
DI float bhi(unsigned u) { return __uint_as_float(u & 0xffff0000u); }
DI bf16x8 lr128(lptr p) { return *(const LAS bf16x8*)p; }
DI f32x4 lr128f(lptr p) { return *(const LAS f32x4*)p; }
DI void lw128f(lptr p, float a, float b, float c, float d) { f32x4 v = {a, b, c, d}; *(LAS f32x4*)p = v; }
DI bf16x8 ltr(lptr p, int o0, int o1) {
    s16x4 a = __builtin_amdgcn_ds_read_tr16_b64_v4i16((LAS s16x4*)(p + o0)), b = __builtin_amdgcn_ds_read_tr16_b64_v4i16((LAS s16x4*)(p + o1));
    return __builtin_shufflevector(a, b, 0, 1, 2, 3, 4, 5, 6, 7); }
DI void glds16(unsigned voff, const void* sbase, unsigned lds) {
    unsigned sv; asm volatile("s_mov_b32 %0, m0\n\ts_mov_b32 m0, %3\n\ts_nop 0\n\tglobal_load_lds_dwordx4 %1, %2\n\ts_mov_b32 m0, %0" : "=&s"(sv) : "v"(voff), "s"(sbase), "s"(lds) : "memory"); }
DI void glds16c(unsigned voff, const void* sbase, unsigned lds) {
    unsigned sv; asm volatile("s_mov_b32 %0, m0\n\ts_mov_b32 m0, %3\n\ts_nop 0\n\tglobal_load_lds_dwordx4 %1, %2 sc1\n\ts_mov_b32 m0, %0" : "=&s"(sv) : "v"(voff), "s"(sbase), "s"(lds) : "memory"); }
DI const char* uni(const char* p) { const unsigned long long a = (unsigned long long)p; return (const char*)(((unsigned long long)(unsigned)__builtin_amdgcn_readfirstlane((unsigned)(a >> 32)) << 32) | (unsigned)__builtin_amdgcn_readfirstlane((unsigned)a)); }
#define BAR(N) asm volatile("s_waitcnt vmcnt(" #N ") lgkmcnt(0)\n\ts_barrier" ::: "memory")
#define BARL() asm volatile("s_waitcnt lgkmcnt(0)\n\ts_barrier" ::: "memory")
DI void st_sc1(unsigned voff, void* sbase, bf16x8 v) { asm volatile("global_store_dwordx4 %0, %1, %2 sc1\n\ts_nop 1" ::"v"(voff), "v"(v), "s"(sbase) : "memory"); }
#define LD_SC1(dst, voff, sbase) asm volatile("global_load_dwordx4 %0, %1, %2 sc1" : "=v"(dst) : "v"(voff), "s"(sbase) : "memory")
DI bf16x8 ldc(const char* p) { bf16x8 v; asm volatile("global_load_dwordx4 %0, %1, off sc1\n\ts_waitcnt vmcnt(0)" : "=v"(v) : "v"(p) : "memory"); return v; }
#define touch(voff, sbase) asm volatile("global_load_dword %0, %1, %2" : "+v"(jk) : "v"(voff), "s"(sbase) : "memory")
#define TOUCH_END() asm volatile("s_waitcnt vmcnt(0)" : "+v"(jk)::"memory")
template <int C> DI float dpp(float x) { return __builtin_bit_cast(float, __builtin_amdgcn_mov_dpp(__builtin_bit_cast(int, x), C, 0xf, 0xf, true)); }
DI float sum32(float x) {
    x += dpp<0xB1>(x); x += dpp<0x4E>(x); x += dpp<0x141>(x); x += dpp<0x140>(x);
    auto s = __builtin_amdgcn_permlane16_swap(__float_as_uint(x), __float_as_uint(x), false, false);
    return __uint_as_float(s[0]) + __uint_as_float(s[1]); }
DI float xhalf_max(float m) { auto r = __builtin_amdgcn_permlane32_swap(__float_as_uint(m), __float_as_uint(m), false, false); return fmaxf(__uint_as_float(r[0]), __uint_as_float(r[1])); }
DI float max16(const f32x16& s, float m) {
#pragma unroll
    for (int i = 0; i < 16; i += 2) m = fmaxf(fmaxf(m, s[i]), s[i + 1]);
    return m; }
DI f32x16 splat16(float v) { f32x16 r;
#pragma unroll
    for (int i = 0; i < 16; ++i) r[i] = v;
    return r; }

typedef unsigned long long u64;
DI u64 mk_tag(u64 e) { e = (u64)uni((const char*)e); return e * 0x9E3779B97F4A7C15ull + 0x632BE59BD9B4E019ull; }
DI void set_flag(char* ws, int f, u64 tag) { __hip_atomic_store((GAS u64*)(ws + WS_FL + f * 128), tag, RLX_AGENT); }
DI void wait_flags(char* ws, int f, u64 tag) {
    GAS u64* p = (GAS u64*)(ws + WS_FL + f * 128);
    for (unsigned sp = 0; sp < SPIN_CAP; ++sp) { if (__builtin_amdgcn_ballot_w64(__hip_atomic_load(p, RLX_AGENT) != tag) == 0ull) break; __builtin_amdgcn_s_sleep(PSL); } }

DI u64 ln_kv(int tok0, const lptr L, unsigned L0, const char* x, const char* wk, const char* wv, char* ws) {
    const int tid = threadIdx.x, l = tid & 63, w = __builtin_amdgcn_readfirstlane(tid >> 6), r = l & 31, h = l >> 5;
    constexpr int WK = 8192, WV = 32768, XL = 57344, RED = 8192;
    const int tl = 2 * w + h;
    u32x4 xa, xb; u64 ep;
    asm volatile("global_load_dwordx4 %0, %2, %3\n\tglobal_load_dwordx4 %1, %2, %3 offset:512" : "=&v"(xa), "=&v"(xb) : "v"((tok0 + tl) * 1024 + r * 16), "s"(x) : "memory");
    glds16(l * 16, wk + w * 2048, L0 + WK + w * 2048); glds16(l * 16, wk + w * 2048 + 1024, L0 + WK + w * 2048 + 1024);
    glds16(l * 16, wv + w * 2048, L0 + WV + w * 2048); glds16(l * 16, wv + w * 2048 + 1024, L0 + WV + w * 2048 + 1024);
    asm volatile("global_load_dwordx2 %0, %3, %4 sc1\n\ts_waitcnt vmcnt(5)" : "=&v"(ep), "+v"(xa), "+v"(xb) : "v"(0), "s"(ws + WS_FL + FL_E * 128) : "memory");
    float v[16];
#pragma unroll
    for (int i = 0; i < 4; ++i) { v[2 * i] = blo(xa[i]); v[2 * i + 1] = bhi(xa[i]); v[8 + 2 * i] = blo(xb[i]); v[9 + 2 * i] = bhi(xb[i]); }
    float s = 0.f;
#pragma unroll
    for (int i = 0; i < 16; ++i) s += v[i];
    const float mean = sum32(s) * (1.f / 512.f); float q2 = 0.f;
#pragma unroll
    for (int i = 0; i < 16; ++i) { v[i] -= mean; q2 += v[i] * v[i]; }
    const float rstd = __builtin_amdgcn_rsqf(sum32(q2) * (1.f / 512.f) + 1e-5f);
    u32x4 ya, yb;
#pragma unroll
    for (int i = 0; i < 4; ++i) { ya[i] = pk(v[2 * i] * rstd, v[2 * i + 1] * rstd); yb[i] = pk(v[8 + 2 * i] * rstd, v[9 + 2 * i] * rstd); }
    { const lptr p = L + XL + r * 256 + ((tl ^ (r & 15)) << 4); *(LAS u32x4*)p = ya; *(LAS u32x4*)(p + 8192) = yb; }
    BAR(0); asm volatile("" : "+v"(ep)); const u64 tag = mk_tag(ep); TS(256 + blockIdx.x, 1);
    { const int c = tid >> 4, tk = tid & 15; const lptr p = L + XL + c * 256 + ((tk ^ (c & 15)) << 4);
      char* g = ws + WS_XN + (tok0 >> 5) * 32768; const unsigned o = c * 512 + ((tok0 & 16) + tk) * 16;
      st_sc1(o, g, lr128(p)); st_sc1(o, g + 16384, lr128(p + 8192)); }
    f32x16 aK = splat16(0.f), aV = splat16(0.f);
    const lptr wp = L + ((l & 16) ? WV : WK) + (64 * w + 8 * h + ((l >> 2) & 3)) * 32 + (l & 3) * 8;
#pragma unroll
    for (int s4 = 0; s4 < 4; ++s4) { const int c = 8 * w + 2 * s4 + h;
        const bf16x8 xf = lr128(L + XL + c * 256 + (((l ^ c) & 15) << 4)), wf = ltr(wp, s4 * 512, s4 * 512 + 128);
        aK = MFMA(wf, xf, aK); aV = MFMA(xf, wf, aV); }
    { const bool vv = l & 16; const lptr p = L + RED + w * 2048 + l * 16;
      lw128f(p, vv ? aV[0] : aK[0], vv ? aV[1] : aK[1], vv ? aV[2] : aK[2], vv ? aV[3] : aK[3]);
      lw128f(p + 1024, vv ? aV[4] : aK[4], vv ? aV[5] : aK[5], vv ? aV[6] : aK[6], vv ? aV[7] : aK[7]); }
    BARL();
    if (w == 0) { f32x4 a = lr128f(L + RED + l * 16), c = lr128f(L + RED + 1024 + l * 16);
#pragma unroll
        for (int i = 1; i < 8; ++i) { a += lr128f(L + RED + i * 2048 + l * 16); c += lr128f(L + RED + i * 2048 + 1024 + l * 16); }
        const u32x4 o = {pk(a[0], a[1]), pk(a[2], a[3]), pk(c[0], c[1]), pk(c[2], c[3])}; const int sh = (tok0 >> 4) & 1;
        const unsigned off = (l & 16) ? 1024 + sh * 512 + h * 256 + (l & 15) * 16 : (16 * sh + (l & 15) + 32 * h) * 16;
        st_sc1(off, ws + WS_KV + (tok0 >> 5) * 2048, __builtin_bit_cast(bf16x8, o)); }
    TS(256 + blockIdx.x, 2);
    if (w == 0) { BAR(1); asm volatile("s_waitcnt vmcnt(0)" ::: "memory"); if (l == 0) set_flag(ws, FL_K + (tok0 >> 4), tag); }
    else { BAR(0); if (tid == 64) set_flag(ws, FL_X + (tok0 >> 4), tag); }
    return tag;
}

DI void attn_role(int b, const lptr L, unsigned L0, const char* wq, char* ws) {
    int tid = threadIdx.x; asm volatile("" : "+v"(tid));
    const int l = tid & 63, w = __builtin_amdgcn_readfirstlane(tid >> 6), h = l >> 5, t = w & 1, kq = w >> 1;
    const int i5 = b >> 3, tg = (b & 7) * 4 + (i5 & 1) + (i5 >> 4) * 2, hg = (i5 >> 1) & 7, TT = 2 * tg + t;
    constexpr int SL = 8320, TS_ = 2080;
    const unsigned l16 = l * 16;
    if (i5 < 16) __builtin_amdgcn_s_sleep(DLY);
    const u64 ep = __hip_atomic_load((GAS u64*)(ws + WS_FL + FL_E * 128), RLX_AGENT);

    { const unsigned vo = (l >> 3) * 1024 + (((l & 7) ^ (((l >> 4) & 1) << 2)) << 4);
#pragma unroll
      for (int n = 0; n < 8; ++n) glds16(vo, wq + hg * 128 + (w * 8 + n) * 8192, L0 + (w * 8 + n) * 1024); }
    const u64 tag = mk_tag(ep);
    wait_flags(ws, FL_X + 2 * TT + (l & 1), tag); TS(b, 5);
    bf16x8 xf[8];
    { const char* xp = uni(ws + WS_XN + (TT * 32 + 8 * kq) * 1024);
#pragma unroll
      for (int s = 0; s < 8; ++s) LD_SC1(xf[s], l16, xp + s * 1024); }
    const char* kvs = uni(ws + WS_KV + w * 1024); const unsigned kvd = L0 + (w >> 1) * TS_ + (w & 1) * 1024;
    const int fk = FL_K + (l >> 1 & 15) * 8 + (w >> 1) * 2 + (l & 1);
    const bool early = __builtin_amdgcn_ballot_w64(__hip_atomic_load((GAS u64*)(ws + WS_FL + fk * 128), RLX_AGENT) != tag) == 0ull;
    asm volatile("s_waitcnt vmcnt(0)\n\ts_barrier" : "+v"(xf[0]), "+v"(xf[1]), "+v"(xf[2]), "+v"(xf[3]), "+v"(xf[4]), "+v"(xf[5]), "+v"(xf[6]), "+v"(xf[7])::"memory"); TS(b, 1);
    if (early) glds16c(l16, kvs, kvd + 8 * SL);
    if (tid == 0) set_flag(ws, FL_A + b, tag);
    bf16x8 qf;
    { const int q = (l >> 2) & 3, p = l & 3, ch = ((l >> 4) & 1) * 2 + (p >> 1);
      const int a0 = kq * 16384 + (8 * h + q) * 128 + ((ch ^ ((q >> 1) << 2)) << 4) + 8 * (p & 1);
      f32x16 c0 = splat16(0.f), c1 = splat16(0.f);
#pragma unroll
      for (int s = 0; s < 8; ++s) { c0 = MFMA(ltr(L + a0, s * 2048, s * 2048 + 512), xf[s], c0); c1 = MFMA(ltr(L + (a0 ^ 64), s * 2048, s * 2048 + 512), xf[s], c1); }
      if (!early) { wait_flags(ws, fk, tag); glds16c(l16, kvs, kvd + 8 * SL); }
      BARL(); TS(b, 6);
      glds16c(l16, kvs + 8192, kvd); glds16c(l16, kvs + 2 * 8192, kvd + SL);
      if (tid < 24) { const unsigned v = (tid & 1) ? 0u : 0x3F803F80u; const u32x4 one = {v, v, v, v}; *(LAS u32x4*)(L + ((tid >> 1) + (tid < 16 ? 0 : 24)) * TS_ + 2048 + (tid & 1) * 16) = one; }
      f32x4 a, c;
#pragma unroll
      for (int j = 0; j < 4; ++j) { const f32x16& s = j < 2 ? c0 : c1; const int o = 8 * (j & 1);
          if (j == kq) { a = {s[o], s[o + 1], s[o + 2], s[o + 3]}; c = {s[o + 4], s[o + 5], s[o + 6], s[o + 7]}; }
          else { const lptr pw = L + 2 * SL + ((t * 4 + j) * 3 + ((kq - j - 1) & 3)) * 2048 + l16; lw128f(pw, s[o], s[o + 1], s[o + 2], s[o + 3]); lw128f(pw + 1024, s[o + 4], s[o + 5], s[o + 6], s[o + 7]); } }
      BAR(2);
      const lptr pr = L + 2 * SL + (t * 4 + kq) * 6144 + l16;
#pragma unroll
      for (int u = 0; u < 3; ++u) { a += lr128f(pr + u * 2048); c += lr128f(pr + u * 2048 + 1024); }
      const float sc = 0.25f * 1.4426950408889634f; a *= sc; c *= sc;
      const u32x4 qq = {pk(a[0], a[1]), pk(a[2], a[3]), pk(c[0], c[1]), pk(c[2], c[3])}; qf = __builtin_bit_cast(bf16x8, qq); }
    f32x16 ns; const bf16x8 kf = lr128(L + 8 * SL + l16); bf16x8 k0 = lr128(L + 8 * SL + TS_ + l16), k1 = lr128(L + 8 * SL + 2 * TS_ + l16);
    { const f32x16 z = splat16(0.f); f32x16 s0 = MFMA(kf, qf, z), s1 = MFMA(k0, qf, z), s2 = MFMA(k1, qf, z), s3 = MFMA(lr128(L + 8 * SL + 3 * TS_ + l16), qf, z);
      asm volatile("" : "+v"(s0), "+v"(s1), "+v"(s2), "+v"(s3));
      const float m = fmaxf(max16(s1, max16(s0, -3.0e38f)), max16(s3, max16(s2, -3.0e38f)));
      ns = splat16(-(xhalf_max(m) + 90.f)); }
    f32x16 o = splat16(0.f);
    { const unsigned bk = L0 + l16, b0 = L0 + ((l & 16) ? 1024 + h * 256 + (l & 15) * 16 : (l & 11) ? 2064 : 2048), b1 = (l & 16) ? b0 + 512 : b0;
      f32x16 sA = MFMA(kf, qf, ns);
      bf16x8 v0 = lr128(L + 8 * SL + (b0 - L0)), v1 = lr128(L + 8 * SL + (b1 - L0)), vb = {0, 0, 0, 0, 0, 0, 0, 0}, pb = vb;
      unsigned aK = bk + 8 * SL, a0 = b0 + 8 * SL, a1 = b1 + 8 * SL, vo = l16 + 3 * 8192, so = 8 * SL, sd = 2 * SL, sn, c = 0, sv;
      BAR(1); TS(b, 2);
      if (tid < 48) { const unsigned v = (tid & 1) ? 0u : 0x3F803F80u; const u32x4 one = {v, v, v, v}; *(LAS u32x4*)(L + ((tid >> 1) + 8) * TS_ + 2048 + (tid & 1) * 16) = one; }
      unsigned hi = w >> 2, pc = 0; PRIO(P_ATT);
      asm volatile(
        "s_mov_b32 %[sv], m0\ns_nop 7\ns_nop 7\n9:\ns_add_u32 %[sn], %[so], 8320\ns_cmp_eq_u32 %[so], 66560\ns_cselect_b32 %[sn], 0, %[sn]\n"
        "v_mfma_f32_32x32x16_bf16 v[48:63], v[100:103], v[96:99], v[80:95]\nds_read_b128 v[100:103], v124 offset:6240\nv_exp_f32 v32, v32\nv_exp_f32 v33, v33\nv_exp_f32 v34, v34\n"
        "v_exp_f32 v35, v35\nv_exp_f32 v36, v36\nv_exp_f32 v37, v37\nv_mfma_f32_32x32x16_bf16 v[64:79], v[24:27], v[120:123], v[64:79]\nds_read_b128 v[24:27], v126 offset:2080\n"
        "v_exp_f32 v38, v38\nv_exp_f32 v39, v39\nv_cvt_pk_bf16_f32 v116, v32, v33\nv_cvt_pk_bf16_f32 v117, v34, v35\nv_cvt_pk_bf16_f32 v118, v36, v37\n"
        "v_cvt_pk_bf16_f32 v119, v38, v39\nv_exp_f32 v40, v40\nv_exp_f32 v41, v41\nv_exp_f32 v42, v42\ns_waitcnt lgkmcnt(2)\n"
        "v_mfma_f32_32x32x16_bf16 v[64:79], v[108:111], v[116:119], v[64:79]\nds_read_b128 v[108:111], v125 offset:2080\nv_add_u32 v127, %[sn], %[bk]\nv_add_u32 v31, %[sn], %[b0]\n"
        "v_add_u32 v30, %[sn], %[b1]\nv_exp_f32 v43, v43\nv_exp_f32 v44, v44\nv_exp_f32 v45, v45\nv_exp_f32 v46, v46\nv_exp_f32 v47, v47\nv_cvt_pk_bf16_f32 v120, v40, v41\n"
        "v_cvt_pk_bf16_f32 v121, v42, v43\nv_cvt_pk_bf16_f32 v122, v44, v45\nv_cvt_pk_bf16_f32 v123, v46, v47\nv_mfma_f32_32x32x16_bf16 v[32:47], v[104:107], v[96:99], v[80:95]\n"
        "ds_read_b128 v[104:107], v127 offset:0\nv_exp_f32 v48, v48\nv_exp_f32 v49, v49\nv_exp_f32 v50, v50\nv_exp_f32 v51, v51\nv_exp_f32 v52, v52\nv_exp_f32 v53, v53\n"
        "v_mfma_f32_32x32x16_bf16 v[64:79], v[112:115], v[120:123], v[64:79]\nds_read_b128 v[112:115], v126 offset:4160\nv_exp_f32 v54, v54\nv_exp_f32 v55, v55\n"
        "v_cvt_pk_bf16_f32 v116, v48, v49\nv_cvt_pk_bf16_f32 v117, v50, v51\nv_cvt_pk_bf16_f32 v118, v52, v53\nv_cvt_pk_bf16_f32 v119, v54, v55\nv_exp_f32 v56, v56\n"
        "v_exp_f32 v57, v57\nv_exp_f32 v58, v58\ns_waitcnt lgkmcnt(2)\nv_mfma_f32_32x32x16_bf16 v[64:79], v[108:111], v[116:119], v[64:79]\n"
        "ds_read_b128 v[108:111], v125 offset:4160\nv_exp_f32 v59, v59\nv_exp_f32 v60, v60\nv_exp_f32 v61, v61\nv_exp_f32 v62, v62\nv_exp_f32 v63, v63\n"
        "v_cvt_pk_bf16_f32 v120, v56, v57\nv_cvt_pk_bf16_f32 v121, v58, v59\nv_cvt_pk_bf16_f32 v122, v60, v61\nv_cvt_pk_bf16_f32 v123, v62, v63\n"
        "v_mfma_f32_32x32x16_bf16 v[48:63], v[100:103], v[96:99], v[80:95]\nds_read_b128 v[100:103], v127 offset:2080\nv_exp_f32 v32, v32\nv_exp_f32 v33, v33\nv_exp_f32 v34, v34\n"
        "v_exp_f32 v35, v35\nv_exp_f32 v36, v36\nv_exp_f32 v37, v37\nv_mfma_f32_32x32x16_bf16 v[64:79], v[24:27], v[120:123], v[64:79]\nds_read_b128 v[24:27], v126 offset:6240\n"
        "v_exp_f32 v38, v38\nv_exp_f32 v39, v39\nv_cvt_pk_bf16_f32 v116, v32, v33\nv_cvt_pk_bf16_f32 v117, v34, v35\nv_cvt_pk_bf16_f32 v118, v36, v37\n"
        "v_cvt_pk_bf16_f32 v119, v38, v39\nv_exp_f32 v40, v40\nv_exp_f32 v41, v41\nv_exp_f32 v42, v42\ns_waitcnt lgkmcnt(2)\n"
        "v_mfma_f32_32x32x16_bf16 v[64:79], v[108:111], v[116:119], v[64:79]\nds_read_b128 v[108:111], v125 offset:6240\ns_cmp_eq_u32 %[hi], 0\ns_cbranch_scc0 15f\ns_setprio 0\n"
        "15:\nv_exp_f32 v43, v43\nv_exp_f32 v44, v44\nv_exp_f32 v45, v45\nv_exp_f32 v46, v46\nv_exp_f32 v47, v47\nv_cvt_pk_bf16_f32 v120, v40, v41\n"
        "v_cvt_pk_bf16_f32 v121, v42, v43\nv_cvt_pk_bf16_f32 v122, v44, v45\nv_cvt_pk_bf16_f32 v123, v46, v47\nv_mfma_f32_32x32x16_bf16 v[32:47], v[104:107], v[96:99], v[80:95]\n"
        "ds_read_b128 v[104:107], v127 offset:4160\nv_exp_f32 v48, v48\nv_exp_f32 v49, v49\nv_exp_f32 v50, v50\nv_exp_f32 v51, v51\nv_exp_f32 v52, v52\nv_exp_f32 v53, v53\n"
        "v_mfma_f32_32x32x16_bf16 v[64:79], v[112:115], v[120:123], v[64:79]\nds_read_b128 v[112:115], v30 offset:0\nv_exp_f32 v54, v54\nv_exp_f32 v55, v55\n"
        "v_cvt_pk_bf16_f32 v116, v48, v49\nv_cvt_pk_bf16_f32 v117, v50, v51\nv_cvt_pk_bf16_f32 v118, v52, v53\nv_cvt_pk_bf16_f32 v119, v54, v55\nv_exp_f32 v56, v56\n"
        "v_exp_f32 v57, v57\nv_exp_f32 v58, v58\ns_waitcnt lgkmcnt(2)\nv_mfma_f32_32x32x16_bf16 v[64:79], v[108:111], v[116:119], v[64:79]\nds_read_b128 v[108:111], v31 offset:0\n"
        "s_cmp_eq_u32 %[hi], 0\ns_cbranch_scc0 14f\ns_setprio 2\n14:\nv_exp_f32 v59, v59\nv_exp_f32 v60, v60\nv_exp_f32 v61, v61\nv_exp_f32 v62, v62\nv_exp_f32 v63, v63\n"
        "v_cvt_pk_bf16_f32 v120, v56, v57\nv_cvt_pk_bf16_f32 v121, v58, v59\nv_cvt_pk_bf16_f32 v122, v60, v61\nv_cvt_pk_bf16_f32 v123, v62, v63\ns_cmp_gt_u32 %[c], 7\n"
        "s_cbranch_scc1 12f\ns_waitcnt vmcnt(0) lgkmcnt(0)\ns_barrier\ns_cmp_eq_u32 %[c], 7\ns_cbranch_scc1 12f\ns_add_u32 m0, %[sd], %[kd]\ns_add_u32 %[sd], %[sd], 8320\n"
        "global_load_lds_dwordx4 %[vo], %[src] sc1\ns_cmp_eq_u32 %[sd], 74880\ns_cselect_b32 %[sd], 0, %[sd]\nv_add_u32 %[vo], 0x2000, %[vo]\ns_cmp_eq_u32 %[c], 6\n"
        "s_cbranch_scc1 12f\ns_add_u32 m0, %[sd], %[kd]\ns_add_u32 %[sd], %[sd], 8320\nglobal_load_lds_dwordx4 %[vo], %[src] sc1\ns_cmp_eq_u32 %[sd], 74880\n"
        "s_cselect_b32 %[sd], 0, %[sd]\nv_add_u32 %[vo], 0x2000, %[vo]\n12:\ns_add_u32 %[c], %[c], 1\ns_mov_b32 %[so], %[sn]\ns_add_u32 %[sn], %[so], 8320\n"
        "s_cmp_eq_u32 %[so], 66560\ns_cselect_b32 %[sn], 0, %[sn]\nv_mfma_f32_32x32x16_bf16 v[48:63], v[100:103], v[96:99], v[80:95]\nds_read_b128 v[100:103], v127 offset:6240\n"
        "v_exp_f32 v32, v32\nv_exp_f32 v33, v33\nv_exp_f32 v34, v34\nv_exp_f32 v35, v35\nv_exp_f32 v36, v36\nv_exp_f32 v37, v37\n"
        "v_mfma_f32_32x32x16_bf16 v[64:79], v[24:27], v[120:123], v[64:79]\nds_read_b128 v[24:27], v30 offset:2080\nv_exp_f32 v38, v38\nv_exp_f32 v39, v39\n"
        "v_cvt_pk_bf16_f32 v116, v32, v33\nv_cvt_pk_bf16_f32 v117, v34, v35\nv_cvt_pk_bf16_f32 v118, v36, v37\nv_cvt_pk_bf16_f32 v119, v38, v39\nv_exp_f32 v40, v40\n"
        "v_exp_f32 v41, v41\nv_exp_f32 v42, v42\ns_waitcnt lgkmcnt(2)\nv_mfma_f32_32x32x16_bf16 v[64:79], v[108:111], v[116:119], v[64:79]\n"
        "ds_read_b128 v[108:111], v31 offset:2080\nv_add_u32 v124, %[sn], %[bk]\nv_add_u32 v125, %[sn], %[b0]\nv_add_u32 v126, %[sn], %[b1]\nv_exp_f32 v43, v43\nv_exp_f32 v44, v44\n"
        "v_exp_f32 v45, v45\nv_exp_f32 v46, v46\nv_exp_f32 v47, v47\nv_cvt_pk_bf16_f32 v120, v40, v41\nv_cvt_pk_bf16_f32 v121, v42, v43\nv_cvt_pk_bf16_f32 v122, v44, v45\n"
        "v_cvt_pk_bf16_f32 v123, v46, v47\nv_mfma_f32_32x32x16_bf16 v[32:47], v[104:107], v[96:99], v[80:95]\nds_read_b128 v[104:107], v124 offset:0\nv_exp_f32 v48, v48\n"
        "v_exp_f32 v49, v49\nv_exp_f32 v50, v50\nv_exp_f32 v51, v51\nv_exp_f32 v52, v52\nv_exp_f32 v53, v53\nv_mfma_f32_32x32x16_bf16 v[64:79], v[112:115], v[120:123], v[64:79]\n"
        "ds_read_b128 v[112:115], v30 offset:4160\nv_exp_f32 v54, v54\nv_exp_f32 v55, v55\nv_cvt_pk_bf16_f32 v116, v48, v49\nv_cvt_pk_bf16_f32 v117, v50, v51\n"
        "v_cvt_pk_bf16_f32 v118, v52, v53\nv_cvt_pk_bf16_f32 v119, v54, v55\nv_exp_f32 v56, v56\nv_exp_f32 v57, v57\nv_exp_f32 v58, v58\ns_waitcnt lgkmcnt(2)\n"
        "v_mfma_f32_32x32x16_bf16 v[64:79], v[108:111], v[116:119], v[64:79]\nds_read_b128 v[108:111], v31 offset:4160\nv_exp_f32 v59, v59\nv_exp_f32 v60, v60\nv_exp_f32 v61, v61\n"
        "v_exp_f32 v62, v62\nv_exp_f32 v63, v63\nv_cvt_pk_bf16_f32 v120, v56, v57\nv_cvt_pk_bf16_f32 v121, v58, v59\nv_cvt_pk_bf16_f32 v122, v60, v61\n"
        "v_cvt_pk_bf16_f32 v123, v62, v63\nv_mfma_f32_32x32x16_bf16 v[48:63], v[100:103], v[96:99], v[80:95]\nds_read_b128 v[100:103], v124 offset:2080\nv_exp_f32 v32, v32\n"
        "v_exp_f32 v33, v33\nv_exp_f32 v34, v34\nv_exp_f32 v35, v35\nv_exp_f32 v36, v36\nv_exp_f32 v37, v37\nv_mfma_f32_32x32x16_bf16 v[64:79], v[24:27], v[120:123], v[64:79]\n"
        "ds_read_b128 v[24:27], v30 offset:6240\nv_exp_f32 v38, v38\nv_exp_f32 v39, v39\nv_cvt_pk_bf16_f32 v116, v32, v33\nv_cvt_pk_bf16_f32 v117, v34, v35\n"
        "v_cvt_pk_bf16_f32 v118, v36, v37\nv_cvt_pk_bf16_f32 v119, v38, v39\nv_exp_f32 v40, v40\nv_exp_f32 v41, v41\nv_exp_f32 v42, v42\ns_waitcnt lgkmcnt(2)\n"
        "v_mfma_f32_32x32x16_bf16 v[64:79], v[108:111], v[116:119], v[64:79]\nds_read_b128 v[108:111], v31 offset:6240\ns_cmp_eq_u32 %[hi], 0\ns_cbranch_scc0 25f\ns_setprio 0\n25:\n"
        "v_exp_f32 v43, v43\nv_exp_f32 v44, v44\nv_exp_f32 v45, v45\nv_exp_f32 v46, v46\nv_exp_f32 v47, v47\nv_cvt_pk_bf16_f32 v120, v40, v41\nv_cvt_pk_bf16_f32 v121, v42, v43\n"
        "v_cvt_pk_bf16_f32 v122, v44, v45\nv_cvt_pk_bf16_f32 v123, v46, v47\nv_mfma_f32_32x32x16_bf16 v[32:47], v[104:107], v[96:99], v[80:95]\n"
        "ds_read_b128 v[104:107], v124 offset:4160\nv_exp_f32 v48, v48\nv_exp_f32 v49, v49\nv_exp_f32 v50, v50\nv_exp_f32 v51, v51\nv_exp_f32 v52, v52\nv_exp_f32 v53, v53\n"
        "v_mfma_f32_32x32x16_bf16 v[64:79], v[112:115], v[120:123], v[64:79]\nds_read_b128 v[112:115], v126 offset:0\nv_exp_f32 v54, v54\nv_exp_f32 v55, v55\n"
        "v_cvt_pk_bf16_f32 v116, v48, v49\nv_cvt_pk_bf16_f32 v117, v50, v51\nv_cvt_pk_bf16_f32 v118, v52, v53\nv_cvt_pk_bf16_f32 v119, v54, v55\nv_exp_f32 v56, v56\n"
        "v_exp_f32 v57, v57\nv_exp_f32 v58, v58\ns_waitcnt lgkmcnt(2)\nv_mfma_f32_32x32x16_bf16 v[64:79], v[108:111], v[116:119], v[64:79]\nds_read_b128 v[108:111], v125 offset:0\n"
        "s_cmp_eq_u32 %[hi], 0\ns_cbranch_scc0 24f\ns_setprio 2\n24:\nv_exp_f32 v59, v59\nv_exp_f32 v60, v60\nv_exp_f32 v61, v61\nv_exp_f32 v62, v62\nv_exp_f32 v63, v63\n"
        "v_cvt_pk_bf16_f32 v120, v56, v57\nv_cvt_pk_bf16_f32 v121, v58, v59\nv_cvt_pk_bf16_f32 v122, v60, v61\nv_cvt_pk_bf16_f32 v123, v62, v63\ns_cmp_gt_u32 %[c], 7\n"
        "s_cbranch_scc1 22f\ns_waitcnt vmcnt(0) lgkmcnt(0)\ns_barrier\ns_cmp_eq_u32 %[c], 7\ns_cbranch_scc1 22f\ns_add_u32 m0, %[sd], %[kd]\ns_add_u32 %[sd], %[sd], 8320\n"
        "global_load_lds_dwordx4 %[vo], %[src] sc1\ns_cmp_eq_u32 %[sd], 74880\ns_cselect_b32 %[sd], 0, %[sd]\nv_add_u32 %[vo], 0x2000, %[vo]\ns_cmp_eq_u32 %[c], 6\n"
        "s_cbranch_scc1 22f\ns_add_u32 m0, %[sd], %[kd]\ns_add_u32 %[sd], %[sd], 8320\nglobal_load_lds_dwordx4 %[vo], %[src] sc1\ns_cmp_eq_u32 %[sd], 74880\n"
        "s_cselect_b32 %[sd], 0, %[sd]\nv_add_u32 %[vo], 0x2000, %[vo]\n22:\ns_add_u32 %[c], %[c], 1\ns_mov_b32 %[so], %[sn]\ns_cmp_lt_u32 %[c], 16\ns_cbranch_scc1 9b\n"
        "s_waitcnt lgkmcnt(0)\nv_mfma_f32_32x32x16_bf16 v[64:79], v[24:27], v[120:123], v[64:79]\ns_mov_b32 m0, %[sv]\ns_nop 7\ns_nop 7\n"
        : "+{v[32:47]}"(sA), "+{v[64:79]}"(o), "+{v[100:103]}"(k0), "+{v[104:107]}"(k1), "+{v[108:111]}"(v0), "+{v[112:115]}"(v1), "+{v[24:27]}"(vb), "+{v[120:123]}"(pb),
          "+{v124}"(aK), "+{v125}"(a0), "+{v126}"(a1), [so] "+s"(so), [sd] "+s"(sd), [sn] "=&s"(sn), [c] "+s"(c), [sv] "=&s"(sv), [vo] "+v"(vo), [hi] "+s"(hi)
        : "{v[80:95]}"(ns), "{v[96:99]}"(qf), [bk] "v"(bk), [b0] "v"(b0), [b1] "v"(b1), [kd] "s"(kvd), [src] "s"(kvs)
        : "v48", "v49", "v50", "v51", "v52", "v53", "v54", "v55", "v56", "v57", "v58", "v59", "v60", "v61", "v62", "v63", "v116", "v117", "v118", "v119", "v127", "v30", "v31", "scc", "memory"
 );

      PRIO(3); }
    TS(b, 3);
    int m_; asm volatile("v_mbcnt_lo_u32_b32 %0, -1, 0\n\tv_mbcnt_hi_u32_b32 %0, -1, %0" : "=v"(m_));
    const unsigned m16 = m_ * 16;

    float bad = o[0];
#pragma unroll
    for (int i = 8; i < 16; ++i) bad = fmaxf(bad, fabsf(o[i]));
    if (__builtin_amdgcn_ballot_w64(!(bad < 1.0e33f)) != 0ull || __builtin_amdgcn_ballot_w64(!(o[0] > 0.f)) != 0ull) {
        const char* kv = ws + WS_KV; float m = -3.0e38f; const f32x16 z = splat16(0.f);
#pragma unroll 1
        for (int u = 0; u < 64; ++u) m = max16(MFMA(ldc(kv + u * 2048 + m16), qf, z), m);
        ns = splat16(-xhalf_max(m)); o = z;
        const u32x4 one = {0x3F803F80u, 0x3F803F80u, 0x3F803F80u, 0x3F803F80u}; const int vo = 1024 + (m_ >> 5) * 256 + (m_ & 15) * 16;
#pragma unroll 1
        for (int u = 0; u < 64; ++u) { f32x16 s = MFMA(ldc(kv + u * 2048 + m16), qf, ns);
            u32x4 v0 = __builtin_bit_cast(u32x4, ldc(kv + u * 2048 + vo)), v1 = __builtin_bit_cast(u32x4, ldc(kv + u * 2048 + vo + 512));
            if (!(m_ & 16)) { v0 = one; v1 = one; }
#pragma unroll
            for (int i = 0; i < 16; ++i) s[i] = __builtin_amdgcn_exp2f(s[i]);
            o = MFMA(__builtin_bit_cast(bf16x8, v0), pack8(s, 0), o); o = MFMA(__builtin_bit_cast(bf16x8, v1), pack8(s, 8), o); } }
    const float inv = 1.f / o[0];
#pragma unroll
    for (int i = 8; i < 16; ++i) o[i] *= inv;
    st_sc1(m16, ws + WS_O + (TT * 32 + 4 * hg + kq) * 1024, pack8(o, 8));
    BAR(0);
    if (w == 0 && m_ == 0) set_flag(ws, FL_O + tg * 8 + hg, tag);
    TS(b, 4);
}

DI void ffn_role(int b, const lptr L, unsigned L0, const char* x, const char* wk, const char* wv, const char* wo, const char* wg, const char* wu, const char* wd, char* out, char* ws) {
    const int tid = threadIdx.x, l = tid & 63, w = __builtin_amdgcn_readfirstlane(tid >> 6), h = l >> 5;
    const int xcd = b & 7, i5 = b >> 3, q = (l >> 2) & 3, p = l & 3; const unsigned l16 = l * 16;
    u64 tag; unsigned jk = 0;
    const int TB = (xcd >> 2) * 4 + (i5 & 3), fb = (xcd & 3) * 7 + (i5 >> 2), ft = w & 1, tk = w >> 1;
    const unsigned vw = (l >> 3) * 3584 + (((l & 7) ^ (((l >> 4) & 1) << 2)) << 4), dw = L0 + w * 1024, dx = L0 + 8192 + w * 2048;
    const char* sw = ((w & 4) ? wu : wg) + (w & 3) * 8 * 3584 + fb * 128;
    if (i5 < 16) {
        tag = ln_kv(xcd * 256 + i5 * 16, L, L0, x, wk, wv, ws);
    } else {
        const u64 ep = __hip_atomic_load((GAS u64*)(ws + WS_FL + FL_E * 128), RLX_AGENT);
        const int n = tid & 255, row = n / 7;
        if (n < 224) touch(((i5 - 16) * 32 + row) * 3584 + (xcd & 3) * 896 + (n - row * 7) * 128, w < 4 ? wg : wu);
        TOUCH_END(); BARL(); tag = mk_tag(ep); }
    if (tid == 128) set_flag(ws, FL_A + 256 + b, tag);
    TS(256 + b, 14);
    if (i5 < 28) {
        glds16(vw, sw, dw); glds16(vw, sw + 114688, dw + 24576); glds16(vw, sw + 2 * 114688, dw + 49152);
        wait_flags(ws, FL_X + TB * 16 + 2 * w + (l & 1), tag);
        const char* sx = uni(ws + WS_XN + (TB * 8 + w) * 32768);
#pragma unroll
        for (int st = 0; st < 3; ++st) { glds16c(l16, sx + st * 2048, dx + st * 24576); glds16c(l16, sx + st * 2048 + 1024, dx + st * 24576 + 1024); }
        unsigned sv; const unsigned tv = (i5 * 512 + tid) * 128;
        const unsigned pa = L0 + (((8 * h + q) * 128 + (((((l >> 4) & 1) * 2 + (p >> 1)) ^ ((q >> 1) << 2)) << 4) + 8 * (p & 1)) ^ (ft * 64)), pb = L0 + 8192 + tk * 4096 + l16;
        f32x16 g0, g1, u0, u1;
        PRIO(P_LOOP);
        asm volatile(
        "s_mov_b32 %[sv], m0\ns_mov_b64 s[40:41], %[sw]\ns_mov_b64 s[42:43], %[sx]\ns_waitcnt vmcnt(4)\ns_barrier\nds_read_b64_tr_b16 v[96:97], %[pa] offset:0\n"
        "ds_read_b64_tr_b16 v[98:99], %[pa] offset:512\nds_read_b128 v[104:107], %[pb] offset:0\nds_read_b64_tr_b16 v[100:101], %[pa] offset:4096\n"
        "ds_read_b64_tr_b16 v[102:103], %[pa] offset:4608\nds_read_b128 v[108:111], %[pb] offset:2048\ns_waitcnt lgkmcnt(0)\n"
        "v_mfma_f32_32x32x16_bf16 v[32:47], v[96:99], v[104:107], 0\nds_read_b64_tr_b16 v[112:113], %[pa] offset:2048\nds_read_b64_tr_b16 v[114:115], %[pa] offset:2560\n"
        "v_mfma_f32_32x32x16_bf16 v[48:63], v[96:99], v[108:111], 0\nds_read_b128 v[120:123], %[pb] offset:1024\nds_read_b64_tr_b16 v[116:117], %[pa] offset:6144\n"
        "v_mfma_f32_32x32x16_bf16 v[64:79], v[100:103], v[104:107], 0\nds_read_b64_tr_b16 v[118:119], %[pa] offset:6656\nds_read_b128 v[124:127], %[pb] offset:3072\n"
        "v_mfma_f32_32x32x16_bf16 v[80:95], v[100:103], v[108:111], 0\ns_waitcnt vmcnt(2) lgkmcnt(0)\ns_barrier\n"
        "v_mfma_f32_32x32x16_bf16 v[32:47], v[112:115], v[120:123], v[32:47]\ns_add_u32 m0, %[dw], 0\ns_add_u32 s40, s40, 0x1c000\ns_addc_u32 s41, s41, 0\n"
        "global_load_lds_dwordx4 %[vw], s[40:41]\nds_read_b64_tr_b16 v[96:97], %[pa] offset:24576\nds_read_b64_tr_b16 v[98:99], %[pa] offset:25088\n"
        "v_mfma_f32_32x32x16_bf16 v[48:63], v[112:115], v[124:127], v[48:63]\ns_add_u32 m0, %[dx], 0\ns_add_u32 s42, s42, 0x800\ns_addc_u32 s43, s43, 0\n"
        "global_load_lds_dwordx4 %[vx], s[42:43] sc1\nds_read_b128 v[104:107], %[pb] offset:24576\nds_read_b64_tr_b16 v[100:101], %[pa] offset:28672\n"
        "v_mfma_f32_32x32x16_bf16 v[64:79], v[116:119], v[120:123], v[64:79]\nglobal_load_lds_dwordx4 %[vx], s[42:43] offset:1024 sc1\n"
        "ds_read_b64_tr_b16 v[102:103], %[pa] offset:29184\nds_read_b128 v[108:111], %[pb] offset:26624\nv_mfma_f32_32x32x16_bf16 v[80:95], v[116:119], v[124:127], v[80:95]\n"
        "s_waitcnt lgkmcnt(0)\nv_mfma_f32_32x32x16_bf16 v[32:47], v[96:99], v[104:107], v[32:47]\nds_read_b64_tr_b16 v[112:113], %[pa] offset:26624\n"
        "ds_read_b64_tr_b16 v[114:115], %[pa] offset:27136\nv_mfma_f32_32x32x16_bf16 v[48:63], v[96:99], v[108:111], v[48:63]\nds_read_b128 v[120:123], %[pb] offset:25600\n"
        "ds_read_b64_tr_b16 v[116:117], %[pa] offset:30720\nv_mfma_f32_32x32x16_bf16 v[64:79], v[100:103], v[104:107], v[64:79]\nds_read_b64_tr_b16 v[118:119], %[pa] offset:31232\n"
        "ds_read_b128 v[124:127], %[pb] offset:27648\nv_mfma_f32_32x32x16_bf16 v[80:95], v[100:103], v[108:111], v[80:95]\ns_waitcnt vmcnt(3) lgkmcnt(0)\ns_barrier\n"
        "v_mfma_f32_32x32x16_bf16 v[32:47], v[112:115], v[120:123], v[32:47]\ns_add_u32 m0, %[dw], 24576\ns_add_u32 s40, s40, 0x1c000\ns_addc_u32 s41, s41, 0\n"
        "global_load_lds_dwordx4 %[vw], s[40:41]\nds_read_b64_tr_b16 v[96:97], %[pa] offset:49152\nds_read_b64_tr_b16 v[98:99], %[pa] offset:49664\n"
        "v_mfma_f32_32x32x16_bf16 v[48:63], v[112:115], v[124:127], v[48:63]\ns_add_u32 m0, %[dx], 24576\ns_add_u32 s42, s42, 0x800\ns_addc_u32 s43, s43, 0\n"
        "global_load_lds_dwordx4 %[vx], s[42:43] sc1\nds_read_b128 v[104:107], %[pb] offset:49152\nds_read_b64_tr_b16 v[100:101], %[pa] offset:53248\n"
        "v_mfma_f32_32x32x16_bf16 v[64:79], v[116:119], v[120:123], v[64:79]\nglobal_load_lds_dwordx4 %[vx], s[42:43] offset:1024 sc1\n"
        "ds_read_b64_tr_b16 v[102:103], %[pa] offset:53760\nds_read_b128 v[108:111], %[pb] offset:51200\nv_mfma_f32_32x32x16_bf16 v[80:95], v[116:119], v[124:127], v[80:95]\n"
        "s_waitcnt lgkmcnt(0)\nv_mfma_f32_32x32x16_bf16 v[32:47], v[96:99], v[104:107], v[32:47]\nds_read_b64_tr_b16 v[112:113], %[pa] offset:51200\n"
        "ds_read_b64_tr_b16 v[114:115], %[pa] offset:51712\nv_mfma_f32_32x32x16_bf16 v[48:63], v[96:99], v[108:111], v[48:63]\nds_read_b128 v[120:123], %[pb] offset:50176\n"
        "ds_read_b64_tr_b16 v[116:117], %[pa] offset:55296\nv_mfma_f32_32x32x16_bf16 v[64:79], v[100:103], v[104:107], v[64:79]\nds_read_b64_tr_b16 v[118:119], %[pa] offset:55808\n"
        "ds_read_b128 v[124:127], %[pb] offset:52224\nv_mfma_f32_32x32x16_bf16 v[80:95], v[100:103], v[108:111], v[80:95]\ns_waitcnt vmcnt(3) lgkmcnt(0)\ns_barrier\n"
        "v_mfma_f32_32x32x16_bf16 v[32:47], v[112:115], v[120:123], v[32:47]\ns_add_u32 m0, %[dw], 49152\ns_add_u32 s40, s40, 0x1c000\ns_addc_u32 s41, s41, 0\n"
        "global_load_lds_dwordx4 %[vw], s[40:41]\nds_read_b64_tr_b16 v[96:97], %[pa] offset:0\nds_read_b64_tr_b16 v[98:99], %[pa] offset:512\n"
        "v_mfma_f32_32x32x16_bf16 v[48:63], v[112:115], v[124:127], v[48:63]\ns_add_u32 m0, %[dx], 49152\ns_add_u32 s42, s42, 0x800\ns_addc_u32 s43, s43, 0\n"
        "global_load_lds_dwordx4 %[vx], s[42:43] sc1\nds_read_b128 v[104:107], %[pb] offset:0\nds_read_b64_tr_b16 v[100:101], %[pa] offset:4096\n"
        "v_mfma_f32_32x32x16_bf16 v[64:79], v[116:119], v[120:123], v[64:79]\nglobal_load_lds_dwordx4 %[vx], s[42:43] offset:1024 sc1\nglobal_load_dword %[jk], %[tv], %[wd]\n"
        "ds_read_b64_tr_b16 v[102:103], %[pa] offset:4608\nds_read_b128 v[108:111], %[pb] offset:2048\nv_mfma_f32_32x32x16_bf16 v[80:95], v[116:119], v[124:127], v[80:95]\n"
        "s_waitcnt lgkmcnt(0)\nv_mfma_f32_32x32x16_bf16 v[32:47], v[96:99], v[104:107], v[32:47]\nds_read_b64_tr_b16 v[112:113], %[pa] offset:2048\n"
        "ds_read_b64_tr_b16 v[114:115], %[pa] offset:2560\nv_mfma_f32_32x32x16_bf16 v[48:63], v[96:99], v[108:111], v[48:63]\nds_read_b128 v[120:123], %[pb] offset:1024\n"
        "ds_read_b64_tr_b16 v[116:117], %[pa] offset:6144\nv_mfma_f32_32x32x16_bf16 v[64:79], v[100:103], v[104:107], v[64:79]\nds_read_b64_tr_b16 v[118:119], %[pa] offset:6656\n"
        "ds_read_b128 v[124:127], %[pb] offset:3072\nv_mfma_f32_32x32x16_bf16 v[80:95], v[100:103], v[108:111], v[80:95]\ns_waitcnt vmcnt(4) lgkmcnt(0)\ns_barrier\n"
        "v_mfma_f32_32x32x16_bf16 v[32:47], v[112:115], v[120:123], v[32:47]\ns_add_u32 m0, %[dw], 0\ns_add_u32 s40, s40, 0x1c000\ns_addc_u32 s41, s41, 0\n"
        "global_load_lds_dwordx4 %[vw], s[40:41]\nds_read_b64_tr_b16 v[96:97], %[pa] offset:24576\nds_read_b64_tr_b16 v[98:99], %[pa] offset:25088\n"
        "v_mfma_f32_32x32x16_bf16 v[48:63], v[112:115], v[124:127], v[48:63]\ns_add_u32 m0, %[dx], 0\ns_add_u32 s42, s42, 0x800\ns_addc_u32 s43, s43, 0\n"
        "global_load_lds_dwordx4 %[vx], s[42:43] sc1\nds_read_b128 v[104:107], %[pb] offset:24576\nds_read_b64_tr_b16 v[100:101], %[pa] offset:28672\n"
        "v_mfma_f32_32x32x16_bf16 v[64:79], v[116:119], v[120:123], v[64:79]\nglobal_load_lds_dwordx4 %[vx], s[42:43] offset:1024 sc1\n"
        "ds_read_b64_tr_b16 v[102:103], %[pa] offset:29184\nds_read_b128 v[108:111], %[pb] offset:26624\nv_mfma_f32_32x32x16_bf16 v[80:95], v[116:119], v[124:127], v[80:95]\n"
        "s_waitcnt lgkmcnt(0)\nv_mfma_f32_32x32x16_bf16 v[32:47], v[96:99], v[104:107], v[32:47]\nds_read_b64_tr_b16 v[112:113], %[pa] offset:26624\n"
        "ds_read_b64_tr_b16 v[114:115], %[pa] offset:27136\nv_mfma_f32_32x32x16_bf16 v[48:63], v[96:99], v[108:111], v[48:63]\nds_read_b128 v[120:123], %[pb] offset:25600\n"
        "ds_read_b64_tr_b16 v[116:117], %[pa] offset:30720\nv_mfma_f32_32x32x16_bf16 v[64:79], v[100:103], v[104:107], v[64:79]\nds_read_b64_tr_b16 v[118:119], %[pa] offset:31232\n"
        "ds_read_b128 v[124:127], %[pb] offset:27648\nv_mfma_f32_32x32x16_bf16 v[80:95], v[100:103], v[108:111], v[80:95]\ns_waitcnt vmcnt(4) lgkmcnt(0)\ns_barrier\n"
        "v_mfma_f32_32x32x16_bf16 v[32:47], v[112:115], v[120:123], v[32:47]\ns_add_u32 m0, %[dw], 24576\ns_add_u32 s40, s40, 0x1c000\ns_addc_u32 s41, s41, 0\n"
        "global_load_lds_dwordx4 %[vw], s[40:41]\nds_read_b64_tr_b16 v[96:97], %[pa] offset:49152\nds_read_b64_tr_b16 v[98:99], %[pa] offset:49664\n"
        "v_mfma_f32_32x32x16_bf16 v[48:63], v[112:115], v[124:127], v[48:63]\ns_add_u32 m0, %[dx], 24576\ns_add_u32 s42, s42, 0x800\ns_addc_u32 s43, s43, 0\n"
        "global_load_lds_dwordx4 %[vx], s[42:43] sc1\nds_read_b128 v[104:107], %[pb] offset:49152\nds_read_b64_tr_b16 v[100:101], %[pa] offset:53248\n"
        "v_mfma_f32_32x32x16_bf16 v[64:79], v[116:119], v[120:123], v[64:79]\nglobal_load_lds_dwordx4 %[vx], s[42:43] offset:1024 sc1\n"
        "ds_read_b64_tr_b16 v[102:103], %[pa] offset:53760\nds_read_b128 v[108:111], %[pb] offset:51200\nv_mfma_f32_32x32x16_bf16 v[80:95], v[116:119], v[124:127], v[80:95]\n"
        "s_waitcnt lgkmcnt(0)\nv_mfma_f32_32x32x16_bf16 v[32:47], v[96:99], v[104:107], v[32:47]\nds_read_b64_tr_b16 v[112:113], %[pa] offset:51200\n"
        "ds_read_b64_tr_b16 v[114:115], %[pa] offset:51712\nv_mfma_f32_32x32x16_bf16 v[48:63], v[96:99], v[108:111], v[48:63]\nds_read_b128 v[120:123], %[pb] offset:50176\n"
        "ds_read_b64_tr_b16 v[116:117], %[pa] offset:55296\nv_mfma_f32_32x32x16_bf16 v[64:79], v[100:103], v[104:107], v[64:79]\nds_read_b64_tr_b16 v[118:119], %[pa] offset:55808\n"
        "ds_read_b128 v[124:127], %[pb] offset:52224\nv_mfma_f32_32x32x16_bf16 v[80:95], v[100:103], v[108:111], v[80:95]\ns_waitcnt vmcnt(3) lgkmcnt(0)\ns_barrier\n"
        "v_mfma_f32_32x32x16_bf16 v[32:47], v[112:115], v[120:123], v[32:47]\ns_add_u32 m0, %[dw], 49152\ns_add_u32 s40, s40, 0x1c000\ns_addc_u32 s41, s41, 0\n"
        "global_load_lds_dwordx4 %[vw], s[40:41]\nds_read_b64_tr_b16 v[96:97], %[pa] offset:0\nds_read_b64_tr_b16 v[98:99], %[pa] offset:512\n"
        "v_mfma_f32_32x32x16_bf16 v[48:63], v[112:115], v[124:127], v[48:63]\ns_add_u32 m0, %[dx], 49152\ns_add_u32 s42, s42, 0x800\ns_addc_u32 s43, s43, 0\n"
        "global_load_lds_dwordx4 %[vx], s[42:43] sc1\nds_read_b128 v[104:107], %[pb] offset:0\nds_read_b64_tr_b16 v[100:101], %[pa] offset:4096\n"
        "v_mfma_f32_32x32x16_bf16 v[64:79], v[116:119], v[120:123], v[64:79]\nglobal_load_lds_dwordx4 %[vx], s[42:43] offset:1024 sc1\n"
        "ds_read_b64_tr_b16 v[102:103], %[pa] offset:4608\nds_read_b128 v[108:111], %[pb] offset:2048\nv_mfma_f32_32x32x16_bf16 v[80:95], v[116:119], v[124:127], v[80:95]\n"
        "s_waitcnt lgkmcnt(0)\nv_mfma_f32_32x32x16_bf16 v[32:47], v[96:99], v[104:107], v[32:47]\nds_read_b64_tr_b16 v[112:113], %[pa] offset:2048\n"
        "ds_read_b64_tr_b16 v[114:115], %[pa] offset:2560\nv_mfma_f32_32x32x16_bf16 v[48:63], v[96:99], v[108:111], v[48:63]\nds_read_b128 v[120:123], %[pb] offset:1024\n"
        "ds_read_b64_tr_b16 v[116:117], %[pa] offset:6144\nv_mfma_f32_32x32x16_bf16 v[64:79], v[100:103], v[104:107], v[64:79]\nds_read_b64_tr_b16 v[118:119], %[pa] offset:6656\n"
        "ds_read_b128 v[124:127], %[pb] offset:3072\nv_mfma_f32_32x32x16_bf16 v[80:95], v[100:103], v[108:111], v[80:95]\ns_waitcnt vmcnt(3) lgkmcnt(0)\ns_barrier\n"
        "v_mfma_f32_32x32x16_bf16 v[32:47], v[112:115], v[120:123], v[32:47]\ns_add_u32 m0, %[dw], 0\ns_add_u32 s40, s40, 0x1c000\ns_addc_u32 s41, s41, 0\n"
        "global_load_lds_dwordx4 %[vw], s[40:41]\nds_read_b64_tr_b16 v[96:97], %[pa] offset:24576\nds_read_b64_tr_b16 v[98:99], %[pa] offset:25088\n"
        "v_mfma_f32_32x32x16_bf16 v[48:63], v[112:115], v[124:127], v[48:63]\ns_add_u32 m0, %[dx], 0\ns_add_u32 s42, s42, 0x800\ns_addc_u32 s43, s43, 0\n"
        "global_load_lds_dwordx4 %[vx], s[42:43] sc1\nds_read_b128 v[104:107], %[pb] offset:24576\nds_read_b64_tr_b16 v[100:101], %[pa] offset:28672\n"
        "v_mfma_f32_32x32x16_bf16 v[64:79], v[116:119], v[120:123], v[64:79]\nglobal_load_lds_dwordx4 %[vx], s[42:43] offset:1024 sc1\n"
        "ds_read_b64_tr_b16 v[102:103], %[pa] offset:29184\nds_read_b128 v[108:111], %[pb] offset:26624\nv_mfma_f32_32x32x16_bf16 v[80:95], v[116:119], v[124:127], v[80:95]\n"
        "s_waitcnt lgkmcnt(0)\nv_mfma_f32_32x32x16_bf16 v[32:47], v[96:99], v[104:107], v[32:47]\nds_read_b64_tr_b16 v[112:113], %[pa] offset:26624\n"
        "ds_read_b64_tr_b16 v[114:115], %[pa] offset:27136\nv_mfma_f32_32x32x16_bf16 v[48:63], v[96:99], v[108:111], v[48:63]\nds_read_b128 v[120:123], %[pb] offset:25600\n"
        "ds_read_b64_tr_b16 v[116:117], %[pa] offset:30720\nv_mfma_f32_32x32x16_bf16 v[64:79], v[100:103], v[104:107], v[64:79]\nds_read_b64_tr_b16 v[118:119], %[pa] offset:31232\n"
        "ds_read_b128 v[124:127], %[pb] offset:27648\nv_mfma_f32_32x32x16_bf16 v[80:95], v[100:103], v[108:111], v[80:95]\ns_waitcnt vmcnt(3) lgkmcnt(0)\ns_barrier\n"
        "v_mfma_f32_32x32x16_bf16 v[32:47], v[112:115], v[120:123], v[32:47]\ns_add_u32 m0, %[dw], 24576\ns_add_u32 s40, s40, 0x1c000\ns_addc_u32 s41, s41, 0\n"
        "global_load_lds_dwordx4 %[vw], s[40:41]\nds_read_b64_tr_b16 v[96:97], %[pa] offset:49152\nds_read_b64_tr_b16 v[98:99], %[pa] offset:49664\n"
        "v_mfma_f32_32x32x16_bf16 v[48:63], v[112:115], v[124:127], v[48:63]\ns_add_u32 m0, %[dx], 24576\ns_add_u32 s42, s42, 0x800\ns_addc_u32 s43, s43, 0\n"
        "global_load_lds_dwordx4 %[vx], s[42:43] sc1\nds_read_b128 v[104:107], %[pb] offset:49152\nds_read_b64_tr_b16 v[100:101], %[pa] offset:53248\n"
        "v_mfma_f32_32x32x16_bf16 v[64:79], v[116:119], v[120:123], v[64:79]\nglobal_load_lds_dwordx4 %[vx], s[42:43] offset:1024 sc1\n"
        "ds_read_b64_tr_b16 v[102:103], %[pa] offset:53760\nds_read_b128 v[108:111], %[pb] offset:51200\nv_mfma_f32_32x32x16_bf16 v[80:95], v[116:119], v[124:127], v[80:95]\n"
        "s_waitcnt lgkmcnt(0)\nv_mfma_f32_32x32x16_bf16 v[32:47], v[96:99], v[104:107], v[32:47]\nds_read_b64_tr_b16 v[112:113], %[pa] offset:51200\n"
        "ds_read_b64_tr_b16 v[114:115], %[pa] offset:51712\nv_mfma_f32_32x32x16_bf16 v[48:63], v[96:99], v[108:111], v[48:63]\nds_read_b128 v[120:123], %[pb] offset:50176\n"
        "ds_read_b64_tr_b16 v[116:117], %[pa] offset:55296\nv_mfma_f32_32x32x16_bf16 v[64:79], v[100:103], v[104:107], v[64:79]\nds_read_b64_tr_b16 v[118:119], %[pa] offset:55808\n"
        "ds_read_b128 v[124:127], %[pb] offset:52224\nv_mfma_f32_32x32x16_bf16 v[80:95], v[100:103], v[108:111], v[80:95]\ns_waitcnt vmcnt(3) lgkmcnt(0)\ns_barrier\n"
        "v_mfma_f32_32x32x16_bf16 v[32:47], v[112:115], v[120:123], v[32:47]\ns_add_u32 m0, %[dw], 49152\ns_add_u32 s40, s40, 0x1c000\ns_addc_u32 s41, s41, 0\n"
        "global_load_lds_dwordx4 %[vw], s[40:41]\nds_read_b64_tr_b16 v[96:97], %[pa] offset:0\nds_read_b64_tr_b16 v[98:99], %[pa] offset:512\n"
        "v_mfma_f32_32x32x16_bf16 v[48:63], v[112:115], v[124:127], v[48:63]\ns_add_u32 m0, %[dx], 49152\ns_add_u32 s42, s42, 0x800\ns_addc_u32 s43, s43, 0\n"
        "global_load_lds_dwordx4 %[vx], s[42:43] sc1\nds_read_b128 v[104:107], %[pb] offset:0\nds_read_b64_tr_b16 v[100:101], %[pa] offset:4096\n"
        "v_mfma_f32_32x32x16_bf16 v[64:79], v[116:119], v[120:123], v[64:79]\nglobal_load_lds_dwordx4 %[vx], s[42:43] offset:1024 sc1\n"
        "ds_read_b64_tr_b16 v[102:103], %[pa] offset:4608\nds_read_b128 v[108:111], %[pb] offset:2048\nv_mfma_f32_32x32x16_bf16 v[80:95], v[116:119], v[124:127], v[80:95]\n"
        "s_waitcnt lgkmcnt(0)\nv_mfma_f32_32x32x16_bf16 v[32:47], v[96:99], v[104:107], v[32:47]\nds_read_b64_tr_b16 v[112:113], %[pa] offset:2048\n"
        "ds_read_b64_tr_b16 v[114:115], %[pa] offset:2560\nv_mfma_f32_32x32x16_bf16 v[48:63], v[96:99], v[108:111], v[48:63]\nds_read_b128 v[120:123], %[pb] offset:1024\n"
        "ds_read_b64_tr_b16 v[116:117], %[pa] offset:6144\nv_mfma_f32_32x32x16_bf16 v[64:79], v[100:103], v[104:107], v[64:79]\nds_read_b64_tr_b16 v[118:119], %[pa] offset:6656\n"
        "ds_read_b128 v[124:127], %[pb] offset:3072\nv_mfma_f32_32x32x16_bf16 v[80:95], v[100:103], v[108:111], v[80:95]\ns_waitcnt vmcnt(3) lgkmcnt(0)\ns_barrier\n"
        "v_mfma_f32_32x32x16_bf16 v[32:47], v[112:115], v[120:123], v[32:47]\ns_add_u32 m0, %[dw], 0\ns_add_u32 s40, s40, 0x1c000\ns_addc_u32 s41, s41, 0\n"
        "global_load_lds_dwordx4 %[vw], s[40:41]\nds_read_b64_tr_b16 v[96:97], %[pa] offset:24576\nds_read_b64_tr_b16 v[98:99], %[pa] offset:25088\n"
        "v_mfma_f32_32x32x16_bf16 v[48:63], v[112:115], v[124:127], v[48:63]\ns_add_u32 m0, %[dx], 0\ns_add_u32 s42, s42, 0x800\ns_addc_u32 s43, s43, 0\n"
        "global_load_lds_dwordx4 %[vx], s[42:43] sc1\nds_read_b128 v[104:107], %[pb] offset:24576\nds_read_b64_tr_b16 v[100:101], %[pa] offset:28672\n"
        "v_mfma_f32_32x32x16_bf16 v[64:79], v[116:119], v[120:123], v[64:79]\nglobal_load_lds_dwordx4 %[vx], s[42:43] offset:1024 sc1\n"
        "ds_read_b64_tr_b16 v[102:103], %[pa] offset:29184\nds_read_b128 v[108:111], %[pb] offset:26624\nv_mfma_f32_32x32x16_bf16 v[80:95], v[116:119], v[124:127], v[80:95]\n"
        "s_waitcnt lgkmcnt(0)\nv_mfma_f32_32x32x16_bf16 v[32:47], v[96:99], v[104:107], v[32:47]\nds_read_b64_tr_b16 v[112:113], %[pa] offset:26624\n"
        "ds_read_b64_tr_b16 v[114:115], %[pa] offset:27136\nv_mfma_f32_32x32x16_bf16 v[48:63], v[96:99], v[108:111], v[48:63]\nds_read_b128 v[120:123], %[pb] offset:25600\n"
        "ds_read_b64_tr_b16 v[116:117], %[pa] offset:30720\nv_mfma_f32_32x32x16_bf16 v[64:79], v[100:103], v[104:107], v[64:79]\nds_read_b64_tr_b16 v[118:119], %[pa] offset:31232\n"
        "ds_read_b128 v[124:127], %[pb] offset:27648\nv_mfma_f32_32x32x16_bf16 v[80:95], v[100:103], v[108:111], v[80:95]\ns_waitcnt vmcnt(3) lgkmcnt(0)\ns_barrier\n"
        "v_mfma_f32_32x32x16_bf16 v[32:47], v[112:115], v[120:123], v[32:47]\ns_add_u32 m0, %[dw], 24576\ns_add_u32 s40, s40, 0x1c000\ns_addc_u32 s41, s41, 0\n"
        "global_load_lds_dwordx4 %[vw], s[40:41]\nds_read_b64_tr_b16 v[96:97], %[pa] offset:49152\nds_read_b64_tr_b16 v[98:99], %[pa] offset:49664\n"
        "v_mfma_f32_32x32x16_bf16 v[48:63], v[112:115], v[124:127], v[48:63]\ns_add_u32 m0, %[dx], 24576\ns_add_u32 s42, s42, 0x800\ns_addc_u32 s43, s43, 0\n"
        "global_load_lds_dwordx4 %[vx], s[42:43] sc1\nds_read_b128 v[104:107], %[pb] offset:49152\nds_read_b64_tr_b16 v[100:101], %[pa] offset:53248\n"
        "v_mfma_f32_32x32x16_bf16 v[64:79], v[116:119], v[120:123], v[64:79]\nglobal_load_lds_dwordx4 %[vx], s[42:43] offset:1024 sc1\n"
        "ds_read_b64_tr_b16 v[102:103], %[pa] offset:53760\nds_read_b128 v[108:111], %[pb] offset:51200\nv_mfma_f32_32x32x16_bf16 v[80:95], v[116:119], v[124:127], v[80:95]\n"
        "s_waitcnt lgkmcnt(0)\nv_mfma_f32_32x32x16_bf16 v[32:47], v[96:99], v[104:107], v[32:47]\nds_read_b64_tr_b16 v[112:113], %[pa] offset:51200\n"
        "ds_read_b64_tr_b16 v[114:115], %[pa] offset:51712\nv_mfma_f32_32x32x16_bf16 v[48:63], v[96:99], v[108:111], v[48:63]\nds_read_b128 v[120:123], %[pb] offset:50176\n"
        "ds_read_b64_tr_b16 v[116:117], %[pa] offset:55296\nv_mfma_f32_32x32x16_bf16 v[64:79], v[100:103], v[104:107], v[64:79]\nds_read_b64_tr_b16 v[118:119], %[pa] offset:55808\n"
        "ds_read_b128 v[124:127], %[pb] offset:52224\nv_mfma_f32_32x32x16_bf16 v[80:95], v[100:103], v[108:111], v[80:95]\ns_waitcnt vmcnt(3) lgkmcnt(0)\ns_barrier\n"
        "v_mfma_f32_32x32x16_bf16 v[32:47], v[112:115], v[120:123], v[32:47]\ns_add_u32 m0, %[dw], 49152\ns_add_u32 s40, s40, 0x1c000\ns_addc_u32 s41, s41, 0\n"
        "global_load_lds_dwordx4 %[vw], s[40:41]\nds_read_b64_tr_b16 v[96:97], %[pa] offset:0\nds_read_b64_tr_b16 v[98:99], %[pa] offset:512\n"
        "v_mfma_f32_32x32x16_bf16 v[48:63], v[112:115], v[124:127], v[48:63]\ns_add_u32 m0, %[dx], 49152\ns_add_u32 s42, s42, 0x800\ns_addc_u32 s43, s43, 0\n"
        "global_load_lds_dwordx4 %[vx], s[42:43] sc1\nds_read_b128 v[104:107], %[pb] offset:0\nds_read_b64_tr_b16 v[100:101], %[pa] offset:4096\n"
        "v_mfma_f32_32x32x16_bf16 v[64:79], v[116:119], v[120:123], v[64:79]\nglobal_load_lds_dwordx4 %[vx], s[42:43] offset:1024 sc1\n"
        "ds_read_b64_tr_b16 v[102:103], %[pa] offset:4608\nds_read_b128 v[108:111], %[pb] offset:2048\nv_mfma_f32_32x32x16_bf16 v[80:95], v[116:119], v[124:127], v[80:95]\n"
        "s_waitcnt lgkmcnt(0)\nv_mfma_f32_32x32x16_bf16 v[32:47], v[96:99], v[104:107], v[32:47]\nds_read_b64_tr_b16 v[112:113], %[pa] offset:2048\n"
        "ds_read_b64_tr_b16 v[114:115], %[pa] offset:2560\nv_mfma_f32_32x32x16_bf16 v[48:63], v[96:99], v[108:111], v[48:63]\nds_read_b128 v[120:123], %[pb] offset:1024\n"
        "ds_read_b64_tr_b16 v[116:117], %[pa] offset:6144\nv_mfma_f32_32x32x16_bf16 v[64:79], v[100:103], v[104:107], v[64:79]\nds_read_b64_tr_b16 v[118:119], %[pa] offset:6656\n"
        "ds_read_b128 v[124:127], %[pb] offset:3072\nv_mfma_f32_32x32x16_bf16 v[80:95], v[100:103], v[108:111], v[80:95]\ns_waitcnt vmcnt(3) lgkmcnt(0)\ns_barrier\n"
        "v_mfma_f32_32x32x16_bf16 v[32:47], v[112:115], v[120:123], v[32:47]\ns_add_u32 m0, %[dw], 0\ns_add_u32 s40, s40, 0x1c000\ns_addc_u32 s41, s41, 0\n"
        "global_load_lds_dwordx4 %[vw], s[40:41]\nds_read_b64_tr_b16 v[96:97], %[pa] offset:24576\nds_read_b64_tr_b16 v[98:99], %[pa] offset:25088\n"
        "v_mfma_f32_32x32x16_bf16 v[48:63], v[112:115], v[124:127], v[48:63]\ns_add_u32 m0, %[dx], 0\ns_add_u32 s42, s42, 0x800\ns_addc_u32 s43, s43, 0\n"
        "global_load_lds_dwordx4 %[vx], s[42:43] sc1\nds_read_b128 v[104:107], %[pb] offset:24576\nds_read_b64_tr_b16 v[100:101], %[pa] offset:28672\n"
        "v_mfma_f32_32x32x16_bf16 v[64:79], v[116:119], v[120:123], v[64:79]\nglobal_load_lds_dwordx4 %[vx], s[42:43] offset:1024 sc1\n"
        "ds_read_b64_tr_b16 v[102:103], %[pa] offset:29184\nds_read_b128 v[108:111], %[pb] offset:26624\nv_mfma_f32_32x32x16_bf16 v[80:95], v[116:119], v[124:127], v[80:95]\n"
        "s_waitcnt lgkmcnt(0)\nv_mfma_f32_32x32x16_bf16 v[32:47], v[96:99], v[104:107], v[32:47]\nds_read_b64_tr_b16 v[112:113], %[pa] offset:26624\n"
        "ds_read_b64_tr_b16 v[114:115], %[pa] offset:27136\nv_mfma_f32_32x32x16_bf16 v[48:63], v[96:99], v[108:111], v[48:63]\nds_read_b128 v[120:123], %[pb] offset:25600\n"
        "ds_read_b64_tr_b16 v[116:117], %[pa] offset:30720\nv_mfma_f32_32x32x16_bf16 v[64:79], v[100:103], v[104:107], v[64:79]\nds_read_b64_tr_b16 v[118:119], %[pa] offset:31232\n"
        "ds_read_b128 v[124:127], %[pb] offset:27648\nv_mfma_f32_32x32x16_bf16 v[80:95], v[100:103], v[108:111], v[80:95]\ns_waitcnt vmcnt(3) lgkmcnt(0)\ns_barrier\n"
        "v_mfma_f32_32x32x16_bf16 v[32:47], v[112:115], v[120:123], v[32:47]\nds_read_b64_tr_b16 v[96:97], %[pa] offset:49152\nds_read_b64_tr_b16 v[98:99], %[pa] offset:49664\n"
        "v_mfma_f32_32x32x16_bf16 v[48:63], v[112:115], v[124:127], v[48:63]\nds_read_b128 v[104:107], %[pb] offset:49152\nds_read_b64_tr_b16 v[100:101], %[pa] offset:53248\n"
        "v_mfma_f32_32x32x16_bf16 v[64:79], v[116:119], v[120:123], v[64:79]\nds_read_b64_tr_b16 v[102:103], %[pa] offset:53760\nds_read_b128 v[108:111], %[pb] offset:51200\n"
        "v_mfma_f32_32x32x16_bf16 v[80:95], v[116:119], v[124:127], v[80:95]\ns_waitcnt lgkmcnt(0)\nv_mfma_f32_32x32x16_bf16 v[32:47], v[96:99], v[104:107], v[32:47]\n"
        "ds_read_b64_tr_b16 v[112:113], %[pa] offset:51200\nds_read_b64_tr_b16 v[114:115], %[pa] offset:51712\nv_mfma_f32_32x32x16_bf16 v[48:63], v[96:99], v[108:111], v[48:63]\n"
        "ds_read_b128 v[120:123], %[pb] offset:50176\nds_read_b64_tr_b16 v[116:117], %[pa] offset:55296\nv_mfma_f32_32x32x16_bf16 v[64:79], v[100:103], v[104:107], v[64:79]\n"
        "ds_read_b64_tr_b16 v[118:119], %[pa] offset:55808\nds_read_b128 v[124:127], %[pb] offset:52224\nv_mfma_f32_32x32x16_bf16 v[80:95], v[100:103], v[108:111], v[80:95]\n"
        "s_waitcnt vmcnt(0) lgkmcnt(0)\ns_barrier\nv_mfma_f32_32x32x16_bf16 v[32:47], v[112:115], v[120:123], v[32:47]\nds_read_b64_tr_b16 v[96:97], %[pa] offset:0\n"
        "ds_read_b64_tr_b16 v[98:99], %[pa] offset:512\nv_mfma_f32_32x32x16_bf16 v[48:63], v[112:115], v[124:127], v[48:63]\nds_read_b128 v[104:107], %[pb] offset:0\n"
        "ds_read_b64_tr_b16 v[100:101], %[pa] offset:4096\nv_mfma_f32_32x32x16_bf16 v[64:79], v[116:119], v[120:123], v[64:79]\nds_read_b64_tr_b16 v[102:103], %[pa] offset:4608\n"
        "ds_read_b128 v[108:111], %[pb] offset:2048\nv_mfma_f32_32x32x16_bf16 v[80:95], v[116:119], v[124:127], v[80:95]\ns_waitcnt lgkmcnt(0)\n"
        "v_mfma_f32_32x32x16_bf16 v[32:47], v[96:99], v[104:107], v[32:47]\nds_read_b64_tr_b16 v[112:113], %[pa] offset:2048\nds_read_b64_tr_b16 v[114:115], %[pa] offset:2560\n"
        "v_mfma_f32_32x32x16_bf16 v[48:63], v[96:99], v[108:111], v[48:63]\nds_read_b128 v[120:123], %[pb] offset:1024\nds_read_b64_tr_b16 v[116:117], %[pa] offset:6144\n"
        "v_mfma_f32_32x32x16_bf16 v[64:79], v[100:103], v[104:107], v[64:79]\nds_read_b64_tr_b16 v[118:119], %[pa] offset:6656\nds_read_b128 v[124:127], %[pb] offset:3072\n"
        "v_mfma_f32_32x32x16_bf16 v[80:95], v[100:103], v[108:111], v[80:95]\ns_waitcnt lgkmcnt(0)\nv_mfma_f32_32x32x16_bf16 v[32:47], v[112:115], v[120:123], v[32:47]\n"
        "v_mfma_f32_32x32x16_bf16 v[48:63], v[112:115], v[124:127], v[48:63]\nv_mfma_f32_32x32x16_bf16 v[64:79], v[116:119], v[120:123], v[64:79]\n"
        "v_mfma_f32_32x32x16_bf16 v[80:95], v[116:119], v[124:127], v[80:95]\ns_mov_b32 m0, %[sv]\ns_nop 7\ns_nop 7\n"
          : "=&{v[32:47]}"(g0), "=&{v[48:63]}"(g1), "=&{v[64:79]}"(u0), "=&{v[80:95]}"(u1), [jk] "+v"(jk), [sv] "=&s"(sv)
          : [pa] "v"(pa), [pb] "v"(pb), [vw] "v"(vw), [vx] "v"(l16), [tv] "v"(tv), [dw] "s"(dw), [dx] "s"(dx), [sw] "s"(sw + 2 * 114688), [sx] "s"(sx + 2 * 2048), [wd] "s"(wd)
          : "v96", "v97", "v98", "v99", "v100", "v101", "v102", "v103", "v104", "v105", "v106", "v107", "v108", "v109", "v110", "v111", "v112", "v113", "v114", "v115", "v116", "v117", "v118", "v119", "v120", "v121", "v122", "v123", "v124", "v125", "v126", "v127", "s40", "s41", "s42", "s43", "scc", "memory");
        PRIO(P_EPI);
#pragma unroll
        for (int i = 0; i < 16; ++i) { g0[i] = fmaxf(g0[i], 0.f) * u0[i]; g1[i] = fmaxf(g1[i], 0.f) * u1[i]; }
        TS(256 + b, 1);
        char* hp = ws + WS_H + ((TB * 8 + 2 * tk) * 112 + fb * 4 + ft * 2) * 1024;
        st_sc1(l16, hp, pack8(g0, 0)); st_sc1(l16, hp + 1024, pack8(g0, 8)); st_sc1(l16, hp + 114688, pack8(g1, 0)); st_sc1(l16, hp + 115712, pack8(g1, 8));
        BAR(0);
        if (tid == 0) set_flag(ws, FL_H + TB * 32 + fb, tag);
    } else {
        const int j = i5 - 28;
        __builtin_amdgcn_s_sleep(127);
        touch((j * 1024 + tid) * 128, wo); TOUCH_END(); touch((j * 1024 + 512 + tid) * 128, wo); TOUCH_END();
        if (b == 255) {
            wait_flags(ws, FL_A + tid, tag); BARL();
            if (tid == 0) set_flag(ws, FL_E, tag); }
    }
    TS(256 + b, 2);
    const int tg = xcd * 4 + (i5 & 3), cg = i5 >> 2, t = w & 1, kq = w >> 1;
    const unsigned vo = (l >> 3) * 1024 + (((l & 7) ^ (((l >> 4) & 1) << 2)) << 4);
    const int at = (4 * h + q) * 128 + (((((l >> 4) & 1) * 2 + (p >> 1)) ^ ((q >> 1) << 2)) << 4) + 8 * (p & 1);
    float gg[8]; u32x2 x0, x1;
    const unsigned ob = (tg * 64 + t * 32 + (l & 31)) * 1024 + cg * 128 + kq * 32;
    {   const char* sw = uni(wd + cg * 128 + w * 14 * 16384); const unsigned dw = L0 + w * 8192;
        const int r0 = 2 * cg - 14;
#define ROT(n) ((n) + r0 < 0 ? (n) + r0 + 14 : (n) + r0)
#define F2_W(n) do { const char* s_ = sw + ROT(n) * 16384; glds16(vo, s_, dw + ((n) & 3) * 2048); glds16(vo, s_ + 8192, dw + ((n) & 3) * 2048 + 1024); } while (0)
        F2_W(0); F2_W(1); F2_W(2); F2_W(3);
        wait_flags(ws, FL_H + xcd * 32 + (w * 7 >> 1) + (l & 3), tag);
        TS(256 + b, 3);
        const char* sh = uni(ws + WS_H + (2 * tg * 112 + w * 14) * 1024);
        f32x16 d00, d01, d10, d11; unsigned ro = ROT(0) * 1024, sv;
        PRIO(P_LOOP2);
        asm volatile(
        "s_mov_b32 %[sv], m0\ns_mov_b64 s[44:45], %[sh]\ns_mov_b64 s[46:47], %[sw]\ns_add_u32 s40, s44, %[ro]\ns_addc_u32 s41, s45, 0\n"
        "global_load_dwordx4 v[112:115], %[h0], s[40:41] sc1\nglobal_load_dwordx4 v[116:119], %[h1], s[40:41] sc1\ns_add_u32 %[ro], %[ro], 1024\ns_cmp_eq_u32 %[ro], 14336\n"
        "s_cselect_b32 %[ro], 0, %[ro]\ns_add_u32 s40, s44, %[ro]\ns_addc_u32 s41, s45, 0\nglobal_load_dwordx4 v[120:123], %[h0], s[40:41] sc1\n"
        "global_load_dwordx4 v[124:127], %[h1], s[40:41] sc1\ns_add_u32 %[ro], %[ro], 1024\ns_cmp_eq_u32 %[ro], 14336\ns_cselect_b32 %[ro], 0, %[ro]\ns_add_u32 s40, s44, %[ro]\n"
        "s_addc_u32 s41, s45, 0\nglobal_load_dwordx4 v[16:19], %[h0], s[40:41] sc1\nglobal_load_dwordx4 v[20:23], %[h1], s[40:41] sc1\ns_add_u32 %[ro], %[ro], 1024\n"
        "s_cmp_eq_u32 %[ro], 14336\ns_cselect_b32 %[ro], 0, %[ro]\ns_add_u32 s40, s44, %[ro]\ns_addc_u32 s41, s45, 0\nglobal_load_dwordx4 v[24:27], %[h0], s[40:41] sc1\n"
        "global_load_dwordx4 v[28:31], %[h1], s[40:41] sc1\ns_add_u32 %[ro], %[ro], 1024\ns_cmp_eq_u32 %[ro], 14336\ns_cselect_b32 %[ro], 0, %[ro]\ns_waitcnt vmcnt(8)\n"
        "ds_read_b64_tr_b16 v[96:97], %[a0] offset:0\nds_read_b64_tr_b16 v[98:99], %[a0] offset:1024\nds_read_b64_tr_b16 v[100:101], %[a1] offset:0\n"
        "ds_read_b64_tr_b16 v[102:103], %[a1] offset:1024\ns_waitcnt vmcnt(6) lgkmcnt(0)\nv_mfma_f32_32x32x16_bf16 v[32:47], v[104:107], v[112:115], 0\n"
        "ds_read_b64_tr_b16 v[104:105], %[a0] offset:2048\nds_read_b64_tr_b16 v[106:107], %[a0] offset:3072\ns_lshl_b32 s42, %[ro], 4\ns_add_u32 s42, s46, s42\n"
        "s_addc_u32 s43, s47, 0\ns_add_u32 m0, %[dw], 0\nv_mfma_f32_32x32x16_bf16 v[48:63], v[108:111], v[112:115], 0\nds_read_b64_tr_b16 v[108:109], %[a1] offset:2048\n"
        "ds_read_b64_tr_b16 v[110:111], %[a1] offset:3072\nglobal_load_lds_dwordx4 %[w0], s[42:43]\nv_mfma_f32_32x32x16_bf16 v[64:79], v[104:107], v[116:119], 0\n"
        "global_load_lds_dwordx4 %[w1], s[42:43] offset:1024\nv_mfma_f32_32x32x16_bf16 v[80:95], v[108:111], v[116:119], 0\ns_add_u32 s40, s44, %[ro]\ns_addc_u32 s41, s45, 0\n"
        "global_load_dwordx4 v[112:115], %[h0], s[40:41] sc1\nglobal_load_dwordx4 v[116:119], %[h1], s[40:41] sc1\ns_add_u32 %[ro], %[ro], 1024\ns_cmp_eq_u32 %[ro], 14336\n"
        "s_cselect_b32 %[ro], 0, %[ro]\ns_waitcnt vmcnt(8) lgkmcnt(0)\nv_mfma_f32_32x32x16_bf16 v[32:47], v[96:99], v[120:123], v[32:47]\n"
        "ds_read_b64_tr_b16 v[96:97], %[a0] offset:4096\nds_read_b64_tr_b16 v[98:99], %[a0] offset:5120\ns_lshl_b32 s42, %[ro], 4\ns_add_u32 s42, s46, s42\ns_addc_u32 s43, s47, 0\n"
        "s_add_u32 m0, %[dw], 2048\nv_mfma_f32_32x32x16_bf16 v[48:63], v[100:103], v[120:123], v[48:63]\nds_read_b64_tr_b16 v[100:101], %[a1] offset:4096\n"
        "ds_read_b64_tr_b16 v[102:103], %[a1] offset:5120\nglobal_load_lds_dwordx4 %[w0], s[42:43]\nv_mfma_f32_32x32x16_bf16 v[64:79], v[96:99], v[124:127], v[64:79]\n"
        "global_load_lds_dwordx4 %[w1], s[42:43] offset:1024\nv_mfma_f32_32x32x16_bf16 v[80:95], v[100:103], v[124:127], v[80:95]\ns_add_u32 s40, s44, %[ro]\n"
        "s_addc_u32 s41, s45, 0\nglobal_load_dwordx4 v[120:123], %[h0], s[40:41] sc1\nglobal_load_dwordx4 v[124:127], %[h1], s[40:41] sc1\ns_add_u32 %[ro], %[ro], 1024\n"
        "s_cmp_eq_u32 %[ro], 14336\ns_cselect_b32 %[ro], 0, %[ro]\ns_waitcnt vmcnt(10) lgkmcnt(0)\nv_mfma_f32_32x32x16_bf16 v[32:47], v[104:107], v[16:19], v[32:47]\n"
        "ds_read_b64_tr_b16 v[104:105], %[a0] offset:6144\nds_read_b64_tr_b16 v[106:107], %[a0] offset:7168\ns_lshl_b32 s42, %[ro], 4\ns_add_u32 s42, s46, s42\n"
        "s_addc_u32 s43, s47, 0\ns_add_u32 m0, %[dw], 4096\nv_mfma_f32_32x32x16_bf16 v[48:63], v[108:111], v[16:19], v[48:63]\nds_read_b64_tr_b16 v[108:109], %[a1] offset:6144\n"
        "ds_read_b64_tr_b16 v[110:111], %[a1] offset:7168\nglobal_load_lds_dwordx4 %[w0], s[42:43]\nv_mfma_f32_32x32x16_bf16 v[64:79], v[104:107], v[20:23], v[64:79]\n"
        "global_load_lds_dwordx4 %[w1], s[42:43] offset:1024\nv_mfma_f32_32x32x16_bf16 v[80:95], v[108:111], v[20:23], v[80:95]\ns_add_u32 s40, s44, %[ro]\ns_addc_u32 s41, s45, 0\n"
        "global_load_dwordx4 v[16:19], %[h0], s[40:41] sc1\nglobal_load_dwordx4 v[20:23], %[h1], s[40:41] sc1\ns_add_u32 %[ro], %[ro], 1024\ns_cmp_eq_u32 %[ro], 14336\n"
        "s_cselect_b32 %[ro], 0, %[ro]\ns_waitcnt vmcnt(10) lgkmcnt(0)\nv_mfma_f32_32x32x16_bf16 v[32:47], v[96:99], v[24:27], v[32:47]\n"
        "ds_read_b64_tr_b16 v[96:97], %[a0] offset:0\nds_read_b64_tr_b16 v[98:99], %[a0] offset:1024\ns_lshl_b32 s42, %[ro], 4\ns_add_u32 s42, s46, s42\ns_addc_u32 s43, s47, 0\n"
        "s_add_u32 m0, %[dw], 6144\nv_mfma_f32_32x32x16_bf16 v[48:63], v[100:103], v[24:27], v[48:63]\nds_read_b64_tr_b16 v[100:101], %[a1] offset:0\n"
        "ds_read_b64_tr_b16 v[102:103], %[a1] offset:1024\nglobal_load_lds_dwordx4 %[w0], s[42:43]\nv_mfma_f32_32x32x16_bf16 v[64:79], v[96:99], v[28:31], v[64:79]\n"
        "global_load_lds_dwordx4 %[w1], s[42:43] offset:1024\nv_mfma_f32_32x32x16_bf16 v[80:95], v[100:103], v[28:31], v[80:95]\ns_add_u32 s40, s44, %[ro]\ns_addc_u32 s41, s45, 0\n"
        "global_load_dwordx4 v[24:27], %[h0], s[40:41] sc1\nglobal_load_dwordx4 v[28:31], %[h1], s[40:41] sc1\ns_add_u32 %[ro], %[ro], 1024\ns_cmp_eq_u32 %[ro], 14336\n"
        "s_cselect_b32 %[ro], 0, %[ro]\ns_waitcnt vmcnt(10) lgkmcnt(0)\nv_mfma_f32_32x32x16_bf16 v[32:47], v[104:107], v[112:115], v[32:47]\n"
        "ds_read_b64_tr_b16 v[104:105], %[a0] offset:2048\nds_read_b64_tr_b16 v[106:107], %[a0] offset:3072\ns_lshl_b32 s42, %[ro], 4\ns_add_u32 s42, s46, s42\n"
        "s_addc_u32 s43, s47, 0\ns_add_u32 m0, %[dw], 0\nv_mfma_f32_32x32x16_bf16 v[48:63], v[108:111], v[112:115], v[48:63]\nds_read_b64_tr_b16 v[108:109], %[a1] offset:2048\n"
        "ds_read_b64_tr_b16 v[110:111], %[a1] offset:3072\nglobal_load_lds_dwordx4 %[w0], s[42:43]\nv_mfma_f32_32x32x16_bf16 v[64:79], v[104:107], v[116:119], v[64:79]\n"
        "global_load_lds_dwordx4 %[w1], s[42:43] offset:1024\nv_mfma_f32_32x32x16_bf16 v[80:95], v[108:111], v[116:119], v[80:95]\ns_add_u32 s40, s44, %[ro]\n"
        "s_addc_u32 s41, s45, 0\nglobal_load_dwordx4 v[112:115], %[h0], s[40:41] sc1\nglobal_load_dwordx4 v[116:119], %[h1], s[40:41] sc1\ns_add_u32 %[ro], %[ro], 1024\n"
        "s_cmp_eq_u32 %[ro], 14336\ns_cselect_b32 %[ro], 0, %[ro]\ns_waitcnt vmcnt(10) lgkmcnt(0)\nv_mfma_f32_32x32x16_bf16 v[32:47], v[96:99], v[120:123], v[32:47]\n"
        "ds_read_b64_tr_b16 v[96:97], %[a0] offset:4096\nds_read_b64_tr_b16 v[98:99], %[a0] offset:5120\ns_lshl_b32 s42, %[ro], 4\ns_add_u32 s42, s46, s42\ns_addc_u32 s43, s47, 0\n"
        "s_add_u32 m0, %[dw], 2048\nv_mfma_f32_32x32x16_bf16 v[48:63], v[100:103], v[120:123], v[48:63]\nds_read_b64_tr_b16 v[100:101], %[a1] offset:4096\n"
        "ds_read_b64_tr_b16 v[102:103], %[a1] offset:5120\nglobal_load_lds_dwordx4 %[w0], s[42:43]\nv_mfma_f32_32x32x16_bf16 v[64:79], v[96:99], v[124:127], v[64:79]\n"
        "global_load_lds_dwordx4 %[w1], s[42:43] offset:1024\nv_mfma_f32_32x32x16_bf16 v[80:95], v[100:103], v[124:127], v[80:95]\ns_add_u32 s40, s44, %[ro]\n"
        "s_addc_u32 s41, s45, 0\nglobal_load_dwordx4 v[120:123], %[h0], s[40:41] sc1\nglobal_load_dwordx4 v[124:127], %[h1], s[40:41] sc1\ns_add_u32 %[ro], %[ro], 1024\n"
        "s_cmp_eq_u32 %[ro], 14336\ns_cselect_b32 %[ro], 0, %[ro]\ns_waitcnt vmcnt(10) lgkmcnt(0)\nv_mfma_f32_32x32x16_bf16 v[32:47], v[104:107], v[16:19], v[32:47]\n"
        "ds_read_b64_tr_b16 v[104:105], %[a0] offset:6144\nds_read_b64_tr_b16 v[106:107], %[a0] offset:7168\ns_lshl_b32 s42, %[ro], 4\ns_add_u32 s42, s46, s42\n"
        "s_addc_u32 s43, s47, 0\ns_add_u32 m0, %[dw], 4096\nv_mfma_f32_32x32x16_bf16 v[48:63], v[108:111], v[16:19], v[48:63]\nds_read_b64_tr_b16 v[108:109], %[a1] offset:6144\n"
        "ds_read_b64_tr_b16 v[110:111], %[a1] offset:7168\nglobal_load_lds_dwordx4 %[w0], s[42:43]\nv_mfma_f32_32x32x16_bf16 v[64:79], v[104:107], v[20:23], v[64:79]\n"
        "global_load_lds_dwordx4 %[w1], s[42:43] offset:1024\nv_mfma_f32_32x32x16_bf16 v[80:95], v[108:111], v[20:23], v[80:95]\ns_add_u32 s40, s44, %[ro]\ns_addc_u32 s41, s45, 0\n"
        "global_load_dwordx4 v[16:19], %[h0], s[40:41] sc1\nglobal_load_dwordx4 v[20:23], %[h1], s[40:41] sc1\ns_add_u32 %[ro], %[ro], 1024\ns_cmp_eq_u32 %[ro], 14336\n"
        "s_cselect_b32 %[ro], 0, %[ro]\ns_waitcnt vmcnt(10) lgkmcnt(0)\nv_mfma_f32_32x32x16_bf16 v[32:47], v[96:99], v[24:27], v[32:47]\n"
        "ds_read_b64_tr_b16 v[96:97], %[a0] offset:0\nds_read_b64_tr_b16 v[98:99], %[a0] offset:1024\ns_lshl_b32 s42, %[ro], 4\ns_add_u32 s42, s46, s42\ns_addc_u32 s43, s47, 0\n"
        "s_add_u32 m0, %[dw], 6144\nv_mfma_f32_32x32x16_bf16 v[48:63], v[100:103], v[24:27], v[48:63]\nds_read_b64_tr_b16 v[100:101], %[a1] offset:0\n"
        "ds_read_b64_tr_b16 v[102:103], %[a1] offset:1024\nglobal_load_lds_dwordx4 %[w0], s[42:43]\nv_mfma_f32_32x32x16_bf16 v[64:79], v[96:99], v[28:31], v[64:79]\n"
        "global_load_lds_dwordx4 %[w1], s[42:43] offset:1024\nv_mfma_f32_32x32x16_bf16 v[80:95], v[100:103], v[28:31], v[80:95]\ns_add_u32 s40, s44, %[ro]\ns_addc_u32 s41, s45, 0\n"
        "global_load_dwordx4 v[24:27], %[h0], s[40:41] sc1\nglobal_load_dwordx4 v[28:31], %[h1], s[40:41] sc1\ns_add_u32 %[ro], %[ro], 1024\ns_cmp_eq_u32 %[ro], 14336\n"
        "s_cselect_b32 %[ro], 0, %[ro]\ns_waitcnt vmcnt(10) lgkmcnt(0)\nv_mfma_f32_32x32x16_bf16 v[32:47], v[104:107], v[112:115], v[32:47]\n"
        "ds_read_b64_tr_b16 v[104:105], %[a0] offset:2048\nds_read_b64_tr_b16 v[106:107], %[a0] offset:3072\ns_lshl_b32 s42, %[ro], 4\ns_add_u32 s42, s46, s42\n"
        "s_addc_u32 s43, s47, 0\ns_add_u32 m0, %[dw], 0\nv_mfma_f32_32x32x16_bf16 v[48:63], v[108:111], v[112:115], v[48:63]\nds_read_b64_tr_b16 v[108:109], %[a1] offset:2048\n"
        "ds_read_b64_tr_b16 v[110:111], %[a1] offset:3072\nglobal_load_lds_dwordx4 %[w0], s[42:43]\nv_mfma_f32_32x32x16_bf16 v[64:79], v[104:107], v[116:119], v[64:79]\n"
        "global_load_lds_dwordx4 %[w1], s[42:43] offset:1024\nv_mfma_f32_32x32x16_bf16 v[80:95], v[108:111], v[116:119], v[80:95]\ns_add_u32 s40, s44, %[ro]\n"
        "s_addc_u32 s41, s45, 0\nglobal_load_dwordx4 v[112:115], %[h0], s[40:41] sc1\nglobal_load_dwordx4 v[116:119], %[h1], s[40:41] sc1\ns_add_u32 %[ro], %[ro], 1024\n"
        "s_cmp_eq_u32 %[ro], 14336\ns_cselect_b32 %[ro], 0, %[ro]\ns_waitcnt vmcnt(10) lgkmcnt(0)\nv_mfma_f32_32x32x16_bf16 v[32:47], v[96:99], v[120:123], v[32:47]\n"
        "ds_read_b64_tr_b16 v[96:97], %[a0] offset:4096\nds_read_b64_tr_b16 v[98:99], %[a0] offset:5120\ns_lshl_b32 s42, %[ro], 4\ns_add_u32 s42, s46, s42\ns_addc_u32 s43, s47, 0\n"
        "s_add_u32 m0, %[dw], 2048\nv_mfma_f32_32x32x16_bf16 v[48:63], v[100:103], v[120:123], v[48:63]\nds_read_b64_tr_b16 v[100:101], %[a1] offset:4096\n"
        "ds_read_b64_tr_b16 v[102:103], %[a1] offset:5120\nglobal_load_lds_dwordx4 %[w0], s[42:43]\nv_mfma_f32_32x32x16_bf16 v[64:79], v[96:99], v[124:127], v[64:79]\n"
        "global_load_lds_dwordx4 %[w1], s[42:43] offset:1024\nv_mfma_f32_32x32x16_bf16 v[80:95], v[100:103], v[124:127], v[80:95]\ns_add_u32 s40, s44, %[ro]\n"
        "s_addc_u32 s41, s45, 0\nglobal_load_dwordx4 v[120:123], %[h0], s[40:41] sc1\nglobal_load_dwordx4 v[124:127], %[h1], s[40:41] sc1\ns_add_u32 %[ro], %[ro], 1024\n"
        "s_cmp_eq_u32 %[ro], 14336\ns_cselect_b32 %[ro], 0, %[ro]\ns_waitcnt vmcnt(10) lgkmcnt(0)\nv_mfma_f32_32x32x16_bf16 v[32:47], v[104:107], v[16:19], v[32:47]\n"
        "ds_read_b64_tr_b16 v[104:105], %[a0] offset:6144\nds_read_b64_tr_b16 v[106:107], %[a0] offset:7168\nv_mfma_f32_32x32x16_bf16 v[48:63], v[108:111], v[16:19], v[48:63]\n"
        "ds_read_b64_tr_b16 v[108:109], %[a1] offset:6144\nds_read_b64_tr_b16 v[110:111], %[a1] offset:7168\nv_mfma_f32_32x32x16_bf16 v[64:79], v[104:107], v[20:23], v[64:79]\n"
        "v_mfma_f32_32x32x16_bf16 v[80:95], v[108:111], v[20:23], v[80:95]\ns_waitcnt vmcnt(6) lgkmcnt(0)\nv_mfma_f32_32x32x16_bf16 v[32:47], v[96:99], v[24:27], v[32:47]\n"
        "ds_read_b64_tr_b16 v[96:97], %[a0] offset:0\nds_read_b64_tr_b16 v[98:99], %[a0] offset:1024\nv_mfma_f32_32x32x16_bf16 v[48:63], v[100:103], v[24:27], v[48:63]\n"
        "ds_read_b64_tr_b16 v[100:101], %[a1] offset:0\nds_read_b64_tr_b16 v[102:103], %[a1] offset:1024\nv_mfma_f32_32x32x16_bf16 v[64:79], v[96:99], v[28:31], v[64:79]\n"
        "v_mfma_f32_32x32x16_bf16 v[80:95], v[100:103], v[28:31], v[80:95]\ns_waitcnt vmcnt(2) lgkmcnt(0)\nv_mfma_f32_32x32x16_bf16 v[32:47], v[104:107], v[112:115], v[32:47]\n"
        "ds_read_b64_tr_b16 v[104:105], %[a0] offset:2048\nds_read_b64_tr_b16 v[106:107], %[a0] offset:3072\nv_mfma_f32_32x32x16_bf16 v[48:63], v[108:111], v[112:115], v[48:63]\n"
        "ds_read_b64_tr_b16 v[108:109], %[a1] offset:2048\nds_read_b64_tr_b16 v[110:111], %[a1] offset:3072\nv_mfma_f32_32x32x16_bf16 v[64:79], v[104:107], v[116:119], v[64:79]\n"
        "v_mfma_f32_32x32x16_bf16 v[80:95], v[108:111], v[116:119], v[80:95]\ns_waitcnt vmcnt(0) lgkmcnt(0)\nv_mfma_f32_32x32x16_bf16 v[32:47], v[104:107], v[120:123], v[32:47]\n"
        "v_mfma_f32_32x32x16_bf16 v[48:63], v[108:111], v[120:123], v[48:63]\nv_mfma_f32_32x32x16_bf16 v[64:79], v[104:107], v[124:127], v[64:79]\n"
        "v_mfma_f32_32x32x16_bf16 v[80:95], v[108:111], v[124:127], v[80:95]\ns_mov_b32 m0, %[sv]\ns_nop 7\ns_nop 7\n"
          : "=&{v[32:47]}"(d00), "=&{v[48:63]}"(d01), "=&{v[64:79]}"(d10), "=&{v[80:95]}"(d11), [ro] "+s"(ro), [sv] "=&s"(sv)
          : [a0] "v"(dw + at), [a1] "v"(dw + (at ^ 64)), [h0] "v"(l16), [h1] "v"(l16 + 114688), [w0] "v"(vo), [w1] "v"(vo + 7168), [dw] "s"(dw), [sh] "s"(sh), [sw] "s"(sw)
          : "v96", "v97", "v98", "v99", "v100", "v101", "v102", "v103", "v104", "v105", "v106", "v107", "v108", "v109", "v110", "v111", "v112", "v113", "v114", "v115", "v116", "v117", "v118", "v119", "v120", "v121", "v122", "v123", "v124", "v125", "v126", "v127", "v16", "v17", "v18", "v19", "v20", "v21", "v22", "v23", "v24", "v25", "v26", "v27", "v28", "v29", "v30", "v31", "s40", "s41", "s42", "s43", "s44", "s45", "s46", "s47", "scc", "memory");
        PRIO(P_EPI);
        TS(256 + b, 4);
        asm volatile("global_load_dwordx2 %0, %2, %3\n\tglobal_load_dwordx2 %1, %2, %3 offset:16" : "=&v"(x0), "=&v"(x1) : "v"(ob + 8 * h), "s"(x) : "memory");
        const lptr pw = L + w * 2048 + l16, pr = L + kq * 16384 + l16; f32x4 a, c;
#define F2_RED(dA, dB, tt) do { BARL(); _Pragma("unroll") for (int u = 0; u < 4; ++u) { lw128f(pw + (u >> 1) * 16384 + (u & 1) * 1024, dA[4 * u], dA[4 * u + 1], dA[4 * u + 2], dA[4 * u + 3]); \
            lw128f(pw + 32768 + (u >> 1) * 16384 + (u & 1) * 1024, dB[4 * u], dB[4 * u + 1], dB[4 * u + 2], dB[4 * u + 3]); } BARL(); \
            if (t == tt) { a = lr128f(pr); c = lr128f(pr + 1024); _Pragma("unroll") for (int u = 1; u < 8; ++u) { a += lr128f(pr + u * 2048); c += lr128f(pr + u * 2048 + 1024); } } } while (0)
        F2_RED(d00, d01, 0); F2_RED(d10, d11, 1);
#pragma unroll
        for (int i = 0; i < 4; ++i) { gg[i] = a[i] * __builtin_amdgcn_rcpf(1.f + __builtin_amdgcn_exp2f(a[i] * -1.4426950408889634f));
                                      gg[4 + i] = c[i] * __builtin_amdgcn_rcpf(1.f + __builtin_amdgcn_exp2f(c[i] * -1.4426950408889634f)); } }
    BARL(); TS(256 + b, 15);
#pragma unroll
    for (int n = 0; n < 8; ++n) glds16(vo, wo + cg * 128 + (w * 8 + n) * 8192, L0 + (w * 8 + n) * 1024);
    BAR(0); asm volatile("" : "+v"(x0), "+v"(x1)); TS(256 + b, 5);
    wait_flags(ws, FL_O + tg * 8 + 2 * kq + (l & 1), tag);
    TS(256 + b, 6);
    bf16x8 of[8];
    { const char* so = uni(ws + WS_O + ((2 * tg + t) * 32 + 8 * kq) * 1024);
#pragma unroll
      for (int s = 0; s < 8; ++s) LD_SC1(of[s], l16, so + s * 1024); }
    f32x16 c0 = splat16(0.f), c1 = c0;
    const lptr a0 = L + kq * 16384 + at, a1 = L + kq * 16384 + (at ^ 64);
#pragma unroll
    for (int s = 0; s < 8; ++s) { const bf16x8 f0 = ltr(a0, s * 2048, s * 2048 + 1024), f1 = ltr(a1, s * 2048, s * 2048 + 1024);
        asm volatile("s_waitcnt vmcnt(%1)" : "+v"(of[s]) : "n"(7 - s) : "memory");
        c0 = MFMA(f0, of[s], c0); c1 = MFMA(f1, of[s], c1); }
    BARL();
    const lptr pw = L + (t * 16 + kq) * 2048 + l16;
#pragma unroll
    for (int u = 0; u < 4; ++u) { lw128f(pw + (u >> 1) * 8192 + (u & 1) * 1024, c0[4 * u], c0[4 * u + 1], c0[4 * u + 2], c0[4 * u + 3]);
                                  lw128f(pw + 16384 + (u >> 1) * 8192 + (u & 1) * 1024, c1[4 * u], c1[4 * u + 1], c1[4 * u + 2], c1[4 * u + 3]); }
    BARL();
    const lptr pr = L + (t * 16 + kq * 4) * 2048 + l16; f32x4 a = lr128f(pr), c = lr128f(pr + 1024);
#pragma unroll
    for (int u = 1; u < 4; ++u) { a += lr128f(pr + u * 2048); c += lr128f(pr + u * 2048 + 1024); }
    const unsigned A0 = pk(blo(x0[0]) + a[0] * gg[0], bhi(x0[0]) + a[1] * gg[1]), A1 = pk(blo(x0[1]) + a[2] * gg[2], bhi(x0[1]) + a[3] * gg[3]);
    const unsigned B0 = pk(blo(x1[0]) + c[0] * gg[4], bhi(x1[0]) + c[1] * gg[5]), B1 = pk(blo(x1[1]) + c[2] * gg[6], bhi(x1[1]) + c[3] * gg[7]);
    auto s0 = __builtin_amdgcn_permlane32_swap(A0, B0, false, false), s1 = __builtin_amdgcn_permlane32_swap(A1, B1, false, false);
    const u32x4 ov = {s0[0], s1[0], s0[1], s1[1]};
    if (WT & 2) asm volatile("global_store_dwordx4 %0, %1, %2 sc0 sc1" ::"v"(ob + 16 * h), "v"(ov), "s"(out) : "memory"); else *(u32x4*)(out + ob + 16 * h) = ov;
    TS(256 + b, 7);
}

__global__ __launch_bounds__(512, 4) void k2_kernel(const char* x, const char* wq, const char* wk, const char* wv, const char* wo, const char* wg, const char* wu, const char* wd, char* out, char* ws) {
    extern __shared__ char smem[];
    const lptr L = (lptr)smem; const unsigned L0 = (unsigned)(size_t)L;
    const int b = blockIdx.x;
    PRIO(3); TS(b, 0);
    if (b < 256) attn_role(b, L, L0, wq, ws); else ffn_role(b - 256, L, L0, x, wk, wv, wo, wg, wu, wd, out, ws);
}

extern "C" void kernel_launch(void* const* d_in, const int* in_sizes, int n_in, void* d_out, int out_size, void* d_ws, size_t ws_size, hipStream_t stream) {
    (void)hipFuncSetAttribute((const void*)k2_kernel, hipFuncAttributeMaxDynamicSharedMemorySize, LDS_K2);
    k2_kernel<<<512, 512, LDS_K2, stream>>>((const char*)d_in[0], (const char*)d_in[1], (const char*)d_in[2], (const char*)d_in[3], (const char*)d_in[4], (const char*)d_in[6], (const char*)d_in[7], (const char*)d_in[8], (char*)d_out, (char*)d_ws);
}
```

```cpp
#define FDL 80
#include <hip/hip_runtime.h>
#include <hip/hip_bf16.h>
#include <stdint.h>

typedef short bf16x8 __attribute__((ext_vector_type(8)));
typedef short s16x4 __attribute__((ext_vector_type(4)));
typedef float f32x16 __attribute__((ext_vector_type(16)));
typedef float f32x4 __attribute__((ext_vector_type(4)));
typedef float f32x2 __attribute__((ext_vector_type(2)));
typedef __bf16 b16x2 __attribute__((ext_vector_type(2)));
typedef unsigned u32x4 __attribute__((ext_vector_type(4)));
typedef unsigned u32x2 __attribute__((ext_vector_type(2)));
#define LAS __attribute__((address_space(3)))
#define GAS __attribute__((address_space(1)))
#define DI __device__ __forceinline__
#define MFMA(a, b, c) __builtin_amdgcn_mfma_f32_32x32x16_bf16((a), (b), (c), 0, 0, 0)
#define RLX_AGENT __ATOMIC_RELAXED, __HIP_MEMORY_SCOPE_AGENT
typedef LAS char* lptr;

constexpr unsigned WS_XN = 0, WS_KV = 2u << 20, WS_H = 4u << 20, WS_O = 12u << 20, WS_FL = 16u << 20;
constexpr int FL_O = 0, FL_H = 256, FL_X = 512, FL_K = 640, FL_A = 768, FL_E = 1280, FL_R = 1288;
constexpr int LDS_K2 = 81920;
constexpr unsigned SPIN_CAP = 1u << 20;
#define NOFFN 0
#define P_LOOP 0
#define P_LOOP2 0
#define P_ATT 1
#define TOG 0
#define BAL 1
#define P_EPI 3
#define TK 1
#define TX 0
#define WT 0
#define DLY 48
#define PSL 10
#define FS 128
#define TLBW 0
#define PRIO(n) __builtin_amdgcn_s_setprio(n)
#define TS(blk, i)

DI unsigned pk(float a, float b) { f32x2 v = {a, b}; return __builtin_bit_cast(unsigned, __builtin_convertvector(v, b16x2)); }
DI bf16x8 pack8(const f32x16& x, int o) { u32x4 r = {pk(x[o], x[o + 1]), pk(x[o + 2], x[o + 3]), pk(x[o + 4], x[o + 5]), pk(x[o + 6], x[o + 7])}; return __builtin_bit_cast(bf16x8, r); }
DI float blo(unsigned u) { return __uint_as_float(u << 16); }
DI float bhi(unsigned u) { return __uint_as_float(u & 0xffff0000u); }
DI bf16x8 lr128(lptr p) { return *(const LAS bf16x8*)p; }
DI f32x4 lr128f(lptr p) { return *(const LAS f32x4*)p; }
DI void lw128f(lptr p, float a, float b, float c, float d) { f32x4 v = {a, b, c, d}; *(LAS f32x4*)p = v; }
DI bf16x8 ltr(lptr p, int o0, int o1) {
    s16x4 a = __builtin_amdgcn_ds_read_tr16_b64_v4i16((LAS s16x4*)(p + o0)), b = __builtin_amdgcn_ds_read_tr16_b64_v4i16((LAS s16x4*)(p + o1));
    return __builtin_shufflevector(a, b, 0, 1, 2, 3, 4, 5, 6, 7); }
DI void glds16(unsigned voff, const void* sbase, unsigned lds) {
    unsigned sv; asm volatile("s_mov_b32 %0, m0\n\ts_mov_b32 m0, %3\n\ts_nop 0\n\tglobal_load_lds_dwordx4 %1, %2\n\ts_mov_b32 m0, %0" : "=&s"(sv) : "v"(voff), "s"(sbase), "s"(lds) : "memory"); }
DI void glds16c(unsigned voff, const void* sbase, unsigned lds) {
    unsigned sv; asm volatile("s_mov_b32 %0, m0\n\ts_mov_b32 m0, %3\n\ts_nop 0\n\tglobal_load_lds_dwordx4 %1, %2 sc1\n\ts_mov_b32 m0, %0" : "=&s"(sv) : "v"(voff), "s"(sbase), "s"(lds) : "memory"); }
DI const char* uni(const char* p) { const unsigned long long a = (unsigned long long)p; return (const char*)(((unsigned long long)(unsigned)__builtin_amdgcn_readfirstlane((unsigned)(a >> 32)) << 32) | (unsigned)__builtin_amdgcn_readfirstlane((unsigned)a)); }
#define BAR(N) asm volatile("s_waitcnt vmcnt(" #N ") lgkmcnt(0)\n\ts_barrier" ::: "memory")
#define BARL() asm volatile("s_waitcnt lgkmcnt(0)\n\ts_barrier" ::: "memory")
DI void st_sc1(unsigned voff, void* sbase, bf16x8 v) { asm volatile("global_store_dwordx4 %0, %1, %2 sc1\n\ts_nop 1" ::"v"(voff), "v"(v), "s"(sbase) : "memory"); }
#define LD_SC1(dst, voff, sbase) asm volatile("global_load_dwordx4 %0, %1, %2 sc1" : "=v"(dst) : "v"(voff), "s"(sbase) : "memory")
DI bf16x8 ldc(const char* p) { bf16x8 v; asm volatile("global_load_dwordx4 %0, %1, off sc1\n\ts_waitcnt vmcnt(0)" : "=v"(v) : "v"(p) : "memory"); return v; }
#define touch(voff, sbase) asm volatile("global_load_dword %0, %1, %2" : "+v"(jk) : "v"(voff), "s"(sbase) : "memory")
#define TOUCH_END() asm volatile("s_waitcnt vmcnt(0)" : "+v"(jk)::"memory")
template <int C> DI float dpp(float x) { return __builtin_bit_cast(float, __builtin_amdgcn_mov_dpp(__builtin_bit_cast(int, x), C, 0xf, 0xf, true)); }
DI float sum32(float x) {
    x += dpp<0xB1>(x); x += dpp<0x4E>(x); x += dpp<0x141>(x); x += dpp<0x140>(x);
    auto s = __builtin_amdgcn_permlane16_swap(__float_as_uint(x), __float_as_uint(x), false, false);
    return __uint_as_float(s[0]) + __uint_as_float(s[1]); }
DI float xhalf_max(float m) { auto r = __builtin_amdgcn_permlane32_swap(__float_as_uint(m), __float_as_uint(m), false, false); return fmaxf(__uint_as_float(r[0]), __uint_as_float(r[1])); }
DI float max16(const f32x16& s, float m) {
#pragma unroll
    for (int i = 0; i < 16; i += 2) m = fmaxf(fmaxf(m, s[i]), s[i + 1]);
    return m; }
DI f32x16 splat16(float v) { f32x16 r;
#pragma unroll
    for (int i = 0; i < 16; ++i) r[i] = v;
    return r; }

typedef unsigned long long u64;
DI u64 mk_tag(u64 e) { e = (u64)uni((const char*)e); return e * 0x9E3779B97F4A7C15ull + 0x632BE59BD9B4E019ull; }
DI void set_flag(char* ws, int f, u64 tag) { __hip_atomic_store((GAS u64*)(ws + WS_FL + f * FS), tag, RLX_AGENT); }
DI void wait_flags(char* ws, int f, u64 tag) {
    GAS u64* p = (GAS u64*)(ws + WS_FL + f * FS);
    for (unsigned sp = 0; sp < SPIN_CAP; ++sp) { if (__builtin_amdgcn_ballot_w64(__hip_atomic_load(p, RLX_AGENT) != tag) == 0ull) break; __builtin_amdgcn_s_sleep(PSL); } }

DI u64 ln_kv(int tok0, const lptr L, unsigned L0, const char* x, const char* wk, const char* wv, char* ws) {
    const int tid = threadIdx.x, l = tid & 63, w = __builtin_amdgcn_readfirstlane(tid >> 6), r = l & 31, h = l >> 5;
    constexpr int WK = 8192, WV = 32768, XL = 57344, RED = 8192;
    const int tl = 2 * w + h;
    u32x4 xa, xb; u64 ep;
    asm volatile("global_load_dwordx4 %0, %2, %3\n\tglobal_load_dwordx4 %1, %2, %3 offset:512" : "=&v"(xa), "=&v"(xb) : "v"((tok0 + tl) * 1024 + r * 16), "s"(x) : "memory");
    glds16(l * 16, wk + w * 2048, L0 + WK + w * 2048); glds16(l * 16, wk + w * 2048 + 1024, L0 + WK + w * 2048 + 1024);
    glds16(l * 16, wv + w * 2048, L0 + WV + w * 2048); glds16(l * 16, wv + w * 2048 + 1024, L0 + WV + w * 2048 + 1024);
    asm volatile("global_load_dwordx2 %0, %3, %4 sc1\n\ts_waitcnt vmcnt(5)" : "=&v"(ep), "+v"(xa), "+v"(xb) : "v"(0), "s"(ws + WS_FL + FL_E * FS) : "memory");
    float v[16];
#pragma unroll
    for (int i = 0; i < 4; ++i) { v[2 * i] = blo(xa[i]); v[2 * i + 1] = bhi(xa[i]); v[8 + 2 * i] = blo(xb[i]); v[9 + 2 * i] = bhi(xb[i]); }
    float s = 0.f;
#pragma unroll
    for (int i = 0; i < 16; ++i) s += v[i];
    const float mean = sum32(s) * (1.f / 512.f); float q2 = 0.f;
#pragma unroll
    for (int i = 0; i < 16; ++i) { v[i] -= mean; q2 += v[i] * v[i]; }
    const float rstd = __builtin_amdgcn_rsqf(sum32(q2) * (1.f / 512.f) + 1e-5f);
    u32x4 ya, yb;
#pragma unroll
    for (int i = 0; i < 4; ++i) { ya[i] = pk(v[2 * i] * rstd, v[2 * i + 1] * rstd); yb[i] = pk(v[8 + 2 * i] * rstd, v[9 + 2 * i] * rstd); }
    { const lptr p = L + XL + r * 256 + ((tl ^ (r & 15)) << 4); *(LAS u32x4*)p = ya; *(LAS u32x4*)(p + 8192) = yb; }
    BAR(0); asm volatile("" : "+v"(ep)); const u64 tag = mk_tag(ep); TS(256 + blockIdx.x, 1);
    { const int c = tid >> 4, tk = tid & 15; const lptr p = L + XL + c * 256 + ((tk ^ (c & 15)) << 4);
      char* g = ws + WS_XN + (tok0 >> 5) * 32768; const unsigned o = c * 512 + ((tok0 & 16) + tk) * 16;
      st_sc1(o, g, lr128(p)); st_sc1(o, g + 16384, lr128(p + 8192)); }
    f32x16 aK = splat16(0.f), aV = splat16(0.f);
    const lptr wp = L + ((l & 16) ? WV : WK) + (64 * w + 8 * h + ((l >> 2) & 3)) * 32 + (l & 3) * 8;
#pragma unroll
    for (int s4 = 0; s4 < 4; ++s4) { const int c = 8 * w + 2 * s4 + h;
        const bf16x8 xf = lr128(L + XL + c * 256 + (((l ^ c) & 15) << 4)), wf = ltr(wp, s4 * 512, s4 * 512 + 128);
        aK = MFMA(wf, xf, aK); aV = MFMA(xf, wf, aV); }
    { const bool vv = l & 16; const lptr p = L + RED + w * 2048 + l * 16;
      lw128f(p, vv ? aV[0] : aK[0], vv ? aV[1] : aK[1], vv ? aV[2] : aK[2], vv ? aV[3] : aK[3]);
      lw128f(p + 1024, vv ? aV[4] : aK[4], vv ? aV[5] : aK[5], vv ? aV[6] : aK[6], vv ? aV[7] : aK[7]); }
    BARL();
    if (w == 0) { f32x4 a = lr128f(L + RED + l * 16), c = lr128f(L + RED + 1024 + l * 16);
#pragma unroll
        for (int i = 1; i < 8; ++i) { a += lr128f(L + RED + i * 2048 + l * 16); c += lr128f(L + RED + i * 2048 + 1024 + l * 16); }
        const u32x4 o = {pk(a[0], a[1]), pk(a[2], a[3]), pk(c[0], c[1]), pk(c[2], c[3])}; const int sh = (tok0 >> 4) & 1;
        const unsigned off = (l & 16) ? 1024 + sh * 512 + h * 256 + (l & 15) * 16 : (16 * sh + (l & 15) + 32 * h) * 16;
        st_sc1(off, ws + WS_KV + (tok0 >> 5) * 2048, __builtin_bit_cast(bf16x8, o)); }
    TS(256 + blockIdx.x, 2);
    if (w == 0) { BAR(1); asm volatile("s_waitcnt vmcnt(0)" ::: "memory"); if (l == 0) set_flag(ws, FL_K + (tok0 >> 4), tag); }
    else { BAR(0); if (tid == 64) set_flag(ws, FL_X + (tok0 >> 4), tag); }
    return tag;
}

DI void attn_role(int b, const lptr L, unsigned L0, const char* wq, char* ws) {
    int tid = threadIdx.x; asm volatile("" : "+v"(tid));
    const int l = tid & 63, w = __builtin_amdgcn_readfirstlane(tid >> 6), h = l >> 5, t = w & 1, kq = w >> 1;
    const int i5 = b >> 3, tg = (b & 7) * 4 + (i5 & 1) + (i5 >> 4) * 2, hg = (i5 >> 1) & 7, TT = 2 * tg + t;
    constexpr int SL = 8320, TS_ = 2080;
    const unsigned l16 = l * 16;
    if (i5 < 16) __builtin_amdgcn_s_sleep(DLY);
    const u64 ep = __hip_atomic_load((GAS u64*)(ws + WS_FL + FL_E * FS), RLX_AGENT);

    { const unsigned vo = (l >> 3) * 1024 + (((l & 7) ^ (((l >> 4) & 1) << 2)) << 4);
#pragma unroll
      for (int n = 0; n < 8; ++n) glds16(vo, wq + hg * 128 + (w * 8 + n) * 8192, L0 + (w * 8 + n) * 1024); }
    const u64 tag = mk_tag(ep);
    wait_flags(ws, FL_X + 2 * TT + (l & 1), tag); TS(b, 5);
    bf16x8 xf[8];
    { const char* xp = uni(ws + WS_XN + (TT * 32 + 8 * kq) * 1024);
#pragma unroll
      for (int s = 0; s < 8; ++s) LD_SC1(xf[s], l16, xp + s * 1024); }
    const char* kvs = uni(ws + WS_KV + w * 1024); const unsigned kvd = L0 + (w >> 1) * TS_ + (w & 1) * 1024;
    const int fk = FL_R + (b & 7) * 4 + (i5 & 3);
    const bool early = __builtin_amdgcn_ballot_w64(__hip_atomic_load((GAS u64*)(ws + WS_FL + fk * FS), RLX_AGENT) != tag) == 0ull;
    asm volatile("s_waitcnt vmcnt(0)\n\ts_barrier" : "+v"(xf[0]), "+v"(xf[1]), "+v"(xf[2]), "+v"(xf[3]), "+v"(xf[4]), "+v"(xf[5]), "+v"(xf[6]), "+v"(xf[7])::"memory"); TS(b, 1);
    if (early) glds16c(l16, kvs, kvd + 8 * SL);
    if (tid == 0) set_flag(ws, FL_A + b, tag);
    bf16x8 qf;
    { const int q = (l >> 2) & 3, p = l & 3, ch = ((l >> 4) & 1) * 2 + (p >> 1);
      const int a0 = kq * 16384 + (8 * h + q) * 128 + ((ch ^ ((q >> 1) << 2)) << 4) + 8 * (p & 1);
      f32x16 c0 = splat16(0.f), c1 = splat16(0.f);
#pragma unroll
      for (int s = 0; s < 8; ++s) { c0 = MFMA(ltr(L + a0, s * 2048, s * 2048 + 512), xf[s], c0); c1 = MFMA(ltr(L + (a0 ^ 64), s * 2048, s * 2048 + 512), xf[s], c1); }
      if (!early) { wait_flags(ws, fk, tag); glds16c(l16, kvs, kvd + 8 * SL); }
      BARL(); TS(b, 6);
      glds16c(l16, kvs + 8192, kvd); glds16c(l16, kvs + 2 * 8192, kvd + SL);
      if (tid < 24) { const unsigned v = (tid & 1) ? 0u : 0x3F803F80u; const u32x4 one = {v, v, v, v}; *(LAS u32x4*)(L + ((tid >> 1) + (tid < 16 ? 0 : 24)) * TS_ + 2048 + (tid & 1) * 16) = one; }
      f32x4 a, c;
#pragma unroll
      for (int j = 0; j < 4; ++j) { const f32x16& s = j < 2 ? c0 : c1; const int o = 8 * (j & 1);
          if (j == kq) { a = {s[o], s[o + 1], s[o + 2], s[o + 3]}; c = {s[o + 4], s[o + 5], s[o + 6], s[o + 7]}; }
          else { const lptr pw = L + 2 * SL + ((t * 4 + j) * 3 + ((kq - j - 1) & 3)) * 2048 + l16; lw128f(pw, s[o], s[o + 1], s[o + 2], s[o + 3]); lw128f(pw + 1024, s[o + 4], s[o + 5], s[o + 6], s[o + 7]); } }
      BAR(2);
      const lptr pr = L + 2 * SL + (t * 4 + kq) * 6144 + l16;
#pragma unroll
      for (int u = 0; u < 3; ++u) { a += lr128f(pr + u * 2048); c += lr128f(pr + u * 2048 + 1024); }
      const float sc = 0.25f * 1.4426950408889634f; a *= sc; c *= sc;
      const u32x4 qq = {pk(a[0], a[1]), pk(a[2], a[3]), pk(c[0], c[1]), pk(c[2], c[3])}; qf = __builtin_bit_cast(bf16x8, qq); }
    f32x16 ns; const bf16x8 kf = lr128(L + 8 * SL + l16); bf16x8 k0 = lr128(L + 8 * SL + TS_ + l16), k1 = lr128(L + 8 * SL + 2 * TS_ + l16);
    { const f32x16 z = splat16(0.f); f32x16 s0 = MFMA(kf, qf, z), s1 = MFMA(k0, qf, z), s2 = MFMA(k1, qf, z), s3 = MFMA(lr128(L + 8 * SL + 3 * TS_ + l16), qf, z);
      asm volatile("" : "+v"(s0), "+v"(s1), "+v"(s2), "+v"(s3));
      const float m = fmaxf(max16(s1, max16(s0, -3.0e38f)), max16(s3, max16(s2, -3.0e38f)));
      ns = splat16(-(xhalf_max(m) + 90.f)); }
    f32x16 o = splat16(0.f);
    { const unsigned bk = L0 + l16, b0 = L0 + ((l & 16) ? 1024 + h * 256 + (l & 15) * 16 : (l & 11) ? 2064 : 2048), b1 = (l & 16) ? b0 + 512 : b0;
      f32x16 sA = MFMA(kf, qf, ns);
      bf16x8 v0 = lr128(L + 8 * SL + (b0 - L0)), v1 = lr128(L + 8 * SL + (b1 - L0)), vb = {0, 0, 0, 0, 0, 0, 0, 0}, pb = vb;
      unsigned aK = bk + 8 * SL, a0 = b0 + 8 * SL, a1 = b1 + 8 * SL, vo = l16 + 3 * 8192, so = 8 * SL, sd = 2 * SL, sn, c = 0, sv;
      BAR(1); TS(b, 2);
      if (tid < 48) { const unsigned v = (tid & 1) ? 0u : 0x3F803F80u; const u32x4 one = {v, v, v, v}; *(LAS u32x4*)(L + ((tid >> 1) + 8) * TS_ + 2048 + (tid & 1) * 16) = one; }
      unsigned hi = w >> 2, pc = 0; PRIO(P_ATT);
      asm volatile(
        "s_mov_b32 %[sv], m0\ns_nop 7\ns_nop 7\n9:\ns_add_u32 %[sn], %[so], 8320\ns_cmp_eq_u32 %[so], 66560\ns_cselect_b32 %[sn], 0, %[sn]\n"
        "v_mfma_f32_32x32x16_bf16 v[48:63], v[100:103], v[96:99], v[80:95]\nds_read_b128 v[100:103], v124 offset:6240\nv_exp_f32 v32, v32\nv_exp_f32 v33, v33\nv_exp_f32 v34, v34\n"
        "v_exp_f32 v35, v35\nv_exp_f32 v36, v36\nv_exp_f32 v37, v37\nv_mfma_f32_32x32x16_bf16 v[64:79], v[24:27], v[120:123], v[64:79]\nds_read_b128 v[24:27], v126 offset:2080\n"
        "v_exp_f32 v38, v38\nv_exp_f32 v39, v39\nv_cvt_pk_bf16_f32 v116, v32, v33\nv_cvt_pk_bf16_f32 v117, v34, v35\nv_cvt_pk_bf16_f32 v118, v36, v37\n"
        "v_cvt_pk_bf16_f32 v119, v38, v39\nv_exp_f32 v40, v40\nv_exp_f32 v41, v41\nv_exp_f32 v42, v42\ns_waitcnt lgkmcnt(2)\n"
        "v_mfma_f32_32x32x16_bf16 v[64:79], v[108:111], v[116:119], v[64:79]\nds_read_b128 v[108:111], v125 offset:2080\nv_add_u32 v127, %[sn], %[bk]\nv_add_u32 v31, %[sn], %[b0]\n"
        "v_add_u32 v30, %[sn], %[b1]\nv_exp_f32 v43, v43\nv_exp_f32 v44, v44\nv_exp_f32 v45, v45\nv_exp_f32 v46, v46\nv_exp_f32 v47, v47\nv_cvt_pk_bf16_f32 v120, v40, v41\n"
        "v_cvt_pk_bf16_f32 v121, v42, v43\nv_cvt_pk_bf16_f32 v122, v44, v45\nv_cvt_pk_bf16_f32 v123, v46, v47\nv_mfma_f32_32x32x16_bf16 v[32:47], v[104:107], v[96:99], v[80:95]\n"
        "ds_read_b128 v[104:107], v127 offset:0\nv_exp_f32 v48, v48\nv_exp_f32 v49, v49\nv_exp_f32 v50, v50\nv_exp_f32 v51, v51\nv_exp_f32 v52, v52\nv_exp_f32 v53, v53\n"
        "v_mfma_f32_32x32x16_bf16 v[64:79], v[112:115], v[120:123], v[64:79]\nds_read_b128 v[112:115], v126 offset:4160\nv_exp_f32 v54, v54\nv_exp_f32 v55, v55\n"
        "v_cvt_pk_bf16_f32 v116, v48, v49\nv_cvt_pk_bf16_f32 v117, v50, v51\nv_cvt_pk_bf16_f32 v118, v52, v53\nv_cvt_pk_bf16_f32 v119, v54, v55\nv_exp_f32 v56, v56\n"
        "v_exp_f32 v57, v57\nv_exp_f32 v58, v58\ns_waitcnt lgkmcnt(2)\nv_mfma_f32_32x32x16_bf16 v[64:79], v[108:111], v[116:119], v[64:79]\n"
        "ds_read_b128 v[108:111], v125 offset:4160\nv_exp_f32 v59, v59\nv_exp_f32 v60, v60\nv_exp_f32 v61, v61\nv_exp_f32 v62, v62\nv_exp_f32 v63, v63\n"
        "v_cvt_pk_bf16_f32 v120, v56, v57\nv_cvt_pk_bf16_f32 v121, v58, v59\nv_cvt_pk_bf16_f32 v122, v60, v61\nv_cvt_pk_bf16_f32 v123, v62, v63\n"
        "v_mfma_f32_32x32x16_bf16 v[48:63], v[100:103], v[96:99], v[80:95]\nds_read_b128 v[100:103], v127 offset:2080\nv_exp_f32 v32, v32\nv_exp_f32 v33, v33\nv_exp_f32 v34, v34\n"
        "v_exp_f32 v35, v35\nv_exp_f32 v36, v36\nv_exp_f32 v37, v37\nv_mfma_f32_32x32x16_bf16 v[64:79], v[24:27], v[120:123], v[64:79]\nds_read_b128 v[24:27], v126 offset:6240\n"
        "v_exp_f32 v38, v38\nv_exp_f32 v39, v39\nv_cvt_pk_bf16_f32 v116, v32, v33\nv_cvt_pk_bf16_f32 v117, v34, v35\nv_cvt_pk_bf16_f32 v118, v36, v37\n"
        "v_cvt_pk_bf16_f32 v119, v38, v39\nv_exp_f32 v40, v40\nv_exp_f32 v41, v41\nv_exp_f32 v42, v42\ns_waitcnt lgkmcnt(2)\n"
        "v_mfma_f32_32x32x16_bf16 v[64:79], v[108:111], v[116:119], v[64:79]\nds_read_b128 v[108:111], v125 offset:6240\ns_cmp_eq_u32 %[hi], 0\ns_cbranch_scc0 15f\ns_setprio 0\n"
        "15:\nv_exp_f32 v43, v43\nv_exp_f32 v44, v44\nv_exp_f32 v45, v45\nv_exp_f32 v46, v46\nv_exp_f32 v47, v47\nv_cvt_pk_bf16_f32 v120, v40, v41\n"
        "v_cvt_pk_bf16_f32 v121, v42, v43\nv_cvt_pk_bf16_f32 v122, v44, v45\nv_cvt_pk_bf16_f32 v123, v46, v47\nv_mfma_f32_32x32x16_bf16 v[32:47], v[104:107], v[96:99], v[80:95]\n"
        "ds_read_b128 v[104:107], v127 offset:4160\nv_exp_f32 v48, v48\nv_exp_f32 v49, v49\nv_exp_f32 v50, v50\nv_exp_f32 v51, v51\nv_exp_f32 v52, v52\nv_exp_f32 v53, v53\n"
        "v_mfma_f32_32x32x16_bf16 v[64:79], v[112:115], v[120:123], v[64:79]\nds_read_b128 v[112:115], v30 offset:0\nv_exp_f32 v54, v54\nv_exp_f32 v55, v55\n"
        "v_cvt_pk_bf16_f32 v116, v48, v49\nv_cvt_pk_bf16_f32 v117, v50, v51\nv_cvt_pk_bf16_f32 v118, v52, v53\nv_cvt_pk_bf16_f32 v119, v54, v55\nv_exp_f32 v56, v56\n"
        "v_exp_f32 v57, v57\nv_exp_f32 v58, v58\ns_waitcnt lgkmcnt(2)\nv_mfma_f32_32x32x16_bf16 v[64:79], v[108:111], v[116:119], v[64:79]\nds_read_b128 v[108:111], v31 offset:0\n"
        "s_cmp_eq_u32 %[hi], 0\ns_cbranch_scc0 14f\ns_setprio 2\n14:\nv_exp_f32 v59, v59\nv_exp_f32 v60, v60\nv_exp_f32 v61, v61\nv_exp_f32 v62, v62\nv_exp_f32 v63, v63\n"
        "v_cvt_pk_bf16_f32 v120, v56, v57\nv_cvt_pk_bf16_f32 v121, v58, v59\nv_cvt_pk_bf16_f32 v122, v60, v61\nv_cvt_pk_bf16_f32 v123, v62, v63\ns_cmp_gt_u32 %[c], 7\n"
        "s_cbranch_scc1 12f\ns_waitcnt vmcnt(0) lgkmcnt(0)\ns_barrier\ns_cmp_eq_u32 %[c], 7\ns_cbranch_scc1 12f\ns_add_u32 m0, %[sd], %[kd]\ns_add_u32 %[sd], %[sd], 8320\n"
        "global_load_lds_dwordx4 %[vo], %[src] sc1\ns_cmp_eq_u32 %[sd], 74880\ns_cselect_b32 %[sd], 0, %[sd]\nv_add_u32 %[vo], 0x2000, %[vo]\ns_cmp_eq_u32 %[c], 6\n"
        "s_cbranch_scc1 12f\ns_add_u32 m0, %[sd], %[kd]\ns_add_u32 %[sd], %[sd], 8320\nglobal_load_lds_dwordx4 %[vo], %[src] sc1\ns_cmp_eq_u32 %[sd], 74880\n"
        "s_cselect_b32 %[sd], 0, %[sd]\nv_add_u32 %[vo], 0x2000, %[vo]\n12:\ns_add_u32 %[c], %[c], 1\ns_mov_b32 %[so], %[sn]\ns_add_u32 %[sn], %[so], 8320\n"
        "s_cmp_eq_u32 %[so], 66560\ns_cselect_b32 %[sn], 0, %[sn]\nv_mfma_f32_32x32x16_bf16 v[48:63], v[100:103], v[96:99], v[80:95]\nds_read_b128 v[100:103], v127 offset:6240\n"
        "v_exp_f32 v32, v32\nv_exp_f32 v33, v33\nv_exp_f32 v34, v34\nv_exp_f32 v35, v35\nv_exp_f32 v36, v36\nv_exp_f32 v37, v37\n"
        "v_mfma_f32_32x32x16_bf16 v[64:79], v[24:27], v[120:123], v[64:79]\nds_read_b128 v[24:27], v30 offset:2080\nv_exp_f32 v38, v38\nv_exp_f32 v39, v39\n"
        "v_cvt_pk_bf16_f32 v116, v32, v33\nv_cvt_pk_bf16_f32 v117, v34, v35\nv_cvt_pk_bf16_f32 v118, v36, v37\nv_cvt_pk_bf16_f32 v119, v38, v39\nv_exp_f32 v40, v40\n"
        "v_exp_f32 v41, v41\nv_exp_f32 v42, v42\ns_waitcnt lgkmcnt(2)\nv_mfma_f32_32x32x16_bf16 v[64:79], v[108:111], v[116:119], v[64:79]\n"
        "ds_read_b128 v[108:111], v31 offset:2080\nv_add_u32 v124, %[sn], %[bk]\nv_add_u32 v125, %[sn], %[b0]\nv_add_u32 v126, %[sn], %[b1]\nv_exp_f32 v43, v43\nv_exp_f32 v44, v44\n"
        "v_exp_f32 v45, v45\nv_exp_f32 v46, v46\nv_exp_f32 v47, v47\nv_cvt_pk_bf16_f32 v120, v40, v41\nv_cvt_pk_bf16_f32 v121, v42, v43\nv_cvt_pk_bf16_f32 v122, v44, v45\n"
        "v_cvt_pk_bf16_f32 v123, v46, v47\nv_mfma_f32_32x32x16_bf16 v[32:47], v[104:107], v[96:99], v[80:95]\nds_read_b128 v[104:107], v124 offset:0\nv_exp_f32 v48, v48\n"
        "v_exp_f32 v49, v49\nv_exp_f32 v50, v50\nv_exp_f32 v51, v51\nv_exp_f32 v52, v52\nv_exp_f32 v53, v53\nv_mfma_f32_32x32x16_bf16 v[64:79], v[112:115], v[120:123], v[64:79]\n"
        "ds_read_b128 v[112:115], v30 offset:4160\nv_exp_f32 v54, v54\nv_exp_f32 v55, v55\nv_cvt_pk_bf16_f32 v116, v48, v49\nv_cvt_pk_bf16_f32 v117, v50, v51\n"
        "v_cvt_pk_bf16_f32 v118, v52, v53\nv_cvt_pk_bf16_f32 v119, v54, v55\nv_exp_f32 v56, v56\nv_exp_f32 v57, v57\nv_exp_f32 v58, v58\ns_waitcnt lgkmcnt(2)\n"
        "v_mfma_f32_32x32x16_bf16 v[64:79], v[108:111], v[116:119], v[64:79]\nds_read_b128 v[108:111], v31 offset:4160\nv_exp_f32 v59, v59\nv_exp_f32 v60, v60\nv_exp_f32 v61, v61\n"
        "v_exp_f32 v62, v62\nv_exp_f32 v63, v63\nv_cvt_pk_bf16_f32 v120, v56, v57\nv_cvt_pk_bf16_f32 v121, v58, v59\nv_cvt_pk_bf16_f32 v122, v60, v61\n"
        "v_cvt_pk_bf16_f32 v123, v62, v63\nv_mfma_f32_32x32x16_bf16 v[48:63], v[100:103], v[96:99], v[80:95]\nds_read_b128 v[100:103], v124 offset:2080\nv_exp_f32 v32, v32\n"
        "v_exp_f32 v33, v33\nv_exp_f32 v34, v34\nv_exp_f32 v35, v35\nv_exp_f32 v36, v36\nv_exp_f32 v37, v37\nv_mfma_f32_32x32x16_bf16 v[64:79], v[24:27], v[120:123], v[64:79]\n"
        "ds_read_b128 v[24:27], v30 offset:6240\nv_exp_f32 v38, v38\nv_exp_f32 v39, v39\nv_cvt_pk_bf16_f32 v116, v32, v33\nv_cvt_pk_bf16_f32 v117, v34, v35\n"
        "v_cvt_pk_bf16_f32 v118, v36, v37\nv_cvt_pk_bf16_f32 v119, v38, v39\nv_exp_f32 v40, v40\nv_exp_f32 v41, v41\nv_exp_f32 v42, v42\ns_waitcnt lgkmcnt(2)\n"
        "v_mfma_f32_32x32x16_bf16 v[64:79], v[108:111], v[116:119], v[64:79]\nds_read_b128 v[108:111], v31 offset:6240\ns_cmp_eq_u32 %[hi], 0\ns_cbranch_scc0 25f\ns_setprio 0\n25:\n"
        "v_exp_f32 v43, v43\nv_exp_f32 v44, v44\nv_exp_f32 v45, v45\nv_exp_f32 v46, v46\nv_exp_f32 v47, v47\nv_cvt_pk_bf16_f32 v120, v40, v41\nv_cvt_pk_bf16_f32 v121, v42, v43\n"
        "v_cvt_pk_bf16_f32 v122, v44, v45\nv_cvt_pk_bf16_f32 v123, v46, v47\nv_mfma_f32_32x32x16_bf16 v[32:47], v[104:107], v[96:99], v[80:95]\n"
        "ds_read_b128 v[104:107], v124 offset:4160\nv_exp_f32 v48, v48\nv_exp_f32 v49, v49\nv_exp_f32 v50, v50\nv_exp_f32 v51, v51\nv_exp_f32 v52, v52\nv_exp_f32 v53, v53\n"
        "v_mfma_f32_32x32x16_bf16 v[64:79], v[112:115], v[120:123], v[64:79]\nds_read_b128 v[112:115], v126 offset:0\nv_exp_f32 v54, v54\nv_exp_f32 v55, v55\n"
        "v_cvt_pk_bf16_f32 v116, v48, v49\nv_cvt_pk_bf16_f32 v117, v50, v51\nv_cvt_pk_bf16_f32 v118, v52, v53\nv_cvt_pk_bf16_f32 v119, v54, v55\nv_exp_f32 v56, v56\n"
        "v_exp_f32 v57, v57\nv_exp_f32 v58, v58\ns_waitcnt lgkmcnt(2)\nv_mfma_f32_32x32x16_bf16 v[64:79], v[108:111], v[116:119], v[64:79]\nds_read_b128 v[108:111], v125 offset:0\n"
        "s_cmp_eq_u32 %[hi], 0\ns_cbranch_scc0 24f\ns_setprio 2\n24:\nv_exp_f32 v59, v59\nv_exp_f32 v60, v60\nv_exp_f32 v61, v61\nv_exp_f32 v62, v62\nv_exp_f32 v63, v63\n"
        "v_cvt_pk_bf16_f32 v120, v56, v57\nv_cvt_pk_bf16_f32 v121, v58, v59\nv_cvt_pk_bf16_f32 v122, v60, v61\nv_cvt_pk_bf16_f32 v123, v62, v63\ns_cmp_gt_u32 %[c], 7\n"
        "s_cbranch_scc1 22f\ns_waitcnt vmcnt(0) lgkmcnt(0)\ns_barrier\ns_cmp_eq_u32 %[c], 7\ns_cbranch_scc1 22f\ns_add_u32 m0, %[sd], %[kd]\ns_add_u32 %[sd], %[sd], 8320\n"
        "global_load_lds_dwordx4 %[vo], %[src] sc1\ns_cmp_eq_u32 %[sd], 74880\ns_cselect_b32 %[sd], 0, %[sd]\nv_add_u32 %[vo], 0x2000, %[vo]\ns_cmp_eq_u32 %[c], 6\n"
        "s_cbranch_scc1 22f\ns_add_u32 m0, %[sd], %[kd]\ns_add_u32 %[sd], %[sd], 8320\nglobal_load_lds_dwordx4 %[vo], %[src] sc1\ns_cmp_eq_u32 %[sd], 74880\n"
        "s_cselect_b32 %[sd], 0, %[sd]\nv_add_u32 %[vo], 0x2000, %[vo]\n22:\ns_add_u32 %[c], %[c], 1\ns_mov_b32 %[so], %[sn]\ns_cmp_lt_u32 %[c], 16\ns_cbranch_scc1 9b\n"
        "s_waitcnt lgkmcnt(0)\nv_mfma_f32_32x32x16_bf16 v[64:79], v[24:27], v[120:123], v[64:79]\ns_mov_b32 m0, %[sv]\ns_nop 7\ns_nop 7\n"
        : "+{v[32:47]}"(sA), "+{v[64:79]}"(o), "+{v[100:103]}"(k0), "+{v[104:107]}"(k1), "+{v[108:111]}"(v0), "+{v[112:115]}"(v1), "+{v[24:27]}"(vb), "+{v[120:123]}"(pb),
          "+{v124}"(aK), "+{v125}"(a0), "+{v126}"(a1), [so] "+s"(so), [sd] "+s"(sd), [sn] "=&s"(sn), [c] "+s"(c), [sv] "=&s"(sv), [vo] "+v"(vo), [hi] "+s"(hi)
        : "{v[80:95]}"(ns), "{v[96:99]}"(qf), [bk] "v"(bk), [b0] "v"(b0), [b1] "v"(b1), [kd] "s"(kvd), [src] "s"(kvs)
        : "v48", "v49", "v50", "v51", "v52", "v53", "v54", "v55", "v56", "v57", "v58", "v59", "v60", "v61", "v62", "v63", "v116", "v117", "v118", "v119", "v127", "v30", "v31", "scc", "memory"
 );

      PRIO(3); }
    TS(b, 3);
    int m_; asm volatile("v_mbcnt_lo_u32_b32 %0, -1, 0\n\tv_mbcnt_hi_u32_b32 %0, -1, %0" : "=v"(m_));
    const unsigned m16 = m_ * 16;

    float bad = o[0];
#pragma unroll
    for (int i = 8; i < 16; ++i) bad = fmaxf(bad, fabsf(o[i]));
    if (__builtin_amdgcn_ballot_w64(!(bad < 1.0e33f)) != 0ull || __builtin_amdgcn_ballot_w64(!(o[0] > 0.f)) != 0ull) {
        const char* kv = ws + WS_KV; float m = -3.0e38f; const f32x16 z = splat16(0.f);
#pragma unroll 1
        for (int u = 0; u < 64; ++u) m = max16(MFMA(ldc(kv + u * 2048 + m16), qf, z), m);
        ns = splat16(-xhalf_max(m)); o = z;
        const u32x4 one = {0x3F803F80u, 0x3F803F80u, 0x3F803F80u, 0x3F803F80u}; const int vo = 1024 + (m_ >> 5) * 256 + (m_ & 15) * 16;
#pragma unroll 1
        for (int u = 0; u < 64; ++u) { f32x16 s = MFMA(ldc(kv + u * 2048 + m16), qf, ns);
            u32x4 v0 = __builtin_bit_cast(u32x4, ldc(kv + u * 2048 + vo)), v1 = __builtin_bit_cast(u32x4, ldc(kv + u * 2048 + vo + 512));
            if (!(m_ & 16)) { v0 = one; v1 = one; }
#pragma unroll
            for (int i = 0; i < 16; ++i) s[i] = __builtin_amdgcn_exp2f(s[i]);
            o = MFMA(__builtin_bit_cast(bf16x8, v0), pack8(s, 0), o); o = MFMA(__builtin_bit_cast(bf16x8, v1), pack8(s, 8), o); } }
    const float inv = 1.f / o[0];
#pragma unroll
    for (int i = 8; i < 16; ++i) o[i] *= inv;
    st_sc1(m16, ws + WS_O + (TT * 32 + 4 * hg + kq) * 1024, pack8(o, 8));
    BAR(0);
    if (w == 0 && m_ == 0) set_flag(ws, FL_O + tg * 8 + hg, tag);
    TS(b, 4);
}

DI void ffn_role(int b, const lptr L, unsigned L0, const char* x, const char* wk, const char* wv, const char* wo, const char* wg, const char* wu, const char* wd, char* out, char* ws) {
    const int tid = threadIdx.x, l = tid & 63, w = __builtin_amdgcn_readfirstlane(tid >> 6), h = l >> 5;
    const int xcd = b & 7, i5 = b >> 3, q = (l >> 2) & 3, p = l & 3; const unsigned l16 = l * 16;
    u64 tag; unsigned jk = 0;
    const int TB = (xcd >> 2) * 4 + (i5 & 3), fb = (xcd & 3) * 7 + (i5 >> 2), ft = w & 1, tk = w >> 1;
    const unsigned vw = (l >> 3) * 3584 + (((l & 7) ^ (((l >> 4) & 1) << 2)) << 4), dw = L0 + w * 1024, dx = L0 + 8192 + w * 2048;
    const char* sw = ((w & 4) ? wu : wg) + (w & 3) * 8 * 3584 + fb * 128;
    if (i5 < 16) {
        tag = ln_kv(xcd * 256 + i5 * 16, L, L0, x, wk, wv, ws);
    } else {
        const u64 ep = __hip_atomic_load((GAS u64*)(ws + WS_FL + FL_E * FS), RLX_AGENT);
        const int n = tid & 255, row = n / 7;
        if (n < 224) touch(((i5 - 16) * 32 + row) * 3584 + (xcd & 3) * 896 + (n - row * 7) * 128, w < 4 ? wg : wu);
        TOUCH_END(); BARL(); tag = mk_tag(ep); }
    if (tid == 128) set_flag(ws, FL_A + 256 + b, tag);
    TS(256 + b, 14);
    if (i5 < 28) {
        glds16(vw, sw, dw); glds16(vw, sw + 114688, dw + 24576); glds16(vw, sw + 2 * 114688, dw + 49152);
        wait_flags(ws, FL_X + TB * 16 + 2 * w + (l & 1), tag);
        if (FDL) __builtin_amdgcn_s_sleep(FDL);
        const char* sx = uni(ws + WS_XN + (TB * 8 + w) * 32768);
#pragma unroll
        for (int st = 0; st < 3; ++st) { glds16c(l16, sx + st * 2048, dx + st * 24576); glds16c(l16, sx + st * 2048 + 1024, dx + st * 24576 + 1024); }
        unsigned sv; const unsigned tv = (i5 * 512 + tid) * 128;
        const unsigned pa = L0 + (((8 * h + q) * 128 + (((((l >> 4) & 1) * 2 + (p >> 1)) ^ ((q >> 1) << 2)) << 4) + 8 * (p & 1)) ^ (ft * 64)), pb = L0 + 8192 + tk * 4096 + l16;
        f32x16 g0, g1, u0, u1;
        PRIO(P_LOOP);
        asm volatile(
        "s_mov_b32 %[sv], m0\ns_mov_b64 s[40:41], %[sw]\ns_mov_b64 s[42:43], %[sx]\ns_waitcnt vmcnt(4)\ns_barrier\nds_read_b64_tr_b16 v[96:97], %[pa] offset:0\n"
        "ds_read_b64_tr_b16 v[98:99], %[pa] offset:512\nds_read_b128 v[104:107], %[pb] offset:0\nds_read_b64_tr_b16 v[100:101], %[pa] offset:4096\n"
        "ds_read_b64_tr_b16 v[102:103], %[pa] offset:4608\nds_read_b128 v[108:111], %[pb] offset:2048\ns_waitcnt lgkmcnt(0)\n"
        "v_mfma_f32_32x32x16_bf16 v[32:47], v[96:99], v[104:107], 0\nds_read_b64_tr_b16 v[112:113], %[pa] offset:2048\nds_read_b64_tr_b16 v[114:115], %[pa] offset:2560\n"
        "v_mfma_f32_32x32x16_bf16 v[48:63], v[96:99], v[108:111], 0\nds_read_b128 v[120:123], %[pb] offset:1024\nds_read_b64_tr_b16 v[116:117], %[pa] offset:6144\n"
        "v_mfma_f32_32x32x16_bf16 v[64:79], v[100:103], v[104:107], 0\nds_read_b64_tr_b16 v[118:119], %[pa] offset:6656\nds_read_b128 v[124:127], %[pb] offset:3072\n"
        "v_mfma_f32_32x32x16_bf16 v[80:95], v[100:103], v[108:111], 0\ns_waitcnt vmcnt(2) lgkmcnt(0)\ns_barrier\n"
        "v_mfma_f32_32x32x16_bf16 v[32:47], v[112:115], v[120:123], v[32:47]\ns_add_u32 m0, %[dw], 0\ns_add_u32 s40, s40, 0x1c000\ns_addc_u32 s41, s41, 0\n"
        "global_load_lds_dwordx4 %[vw], s[40:41]\nds_read_b64_tr_b16 v[96:97], %[pa] offset:24576\nds_read_b64_tr_b16 v[98:99], %[pa] offset:25088\n"
        "v_mfma_f32_32x32x16_bf16 v[48:63], v[112:115], v[124:127], v[48:63]\ns_add_u32 m0, %[dx], 0\ns_add_u32 s42, s42, 0x800\ns_addc_u32 s43, s43, 0\n"
        "global_load_lds_dwordx4 %[vx], s[42:43] sc1\nds_read_b128 v[104:107], %[pb] offset:24576\nds_read_b64_tr_b16 v[100:101], %[pa] offset:28672\n"
        "v_mfma_f32_32x32x16_bf16 v[64:79], v[116:119], v[120:123], v[64:79]\nglobal_load_lds_dwordx4 %[vx], s[42:43] offset:1024 sc1\n"
        "ds_read_b64_tr_b16 v[102:103], %[pa] offset:29184\nds_read_b128 v[108:111], %[pb] offset:26624\nv_mfma_f32_32x32x16_bf16 v[80:95], v[116:119], v[124:127], v[80:95]\n"
        "s_waitcnt lgkmcnt(0)\nv_mfma_f32_32x32x16_bf16 v[32:47], v[96:99], v[104:107], v[32:47]\nds_read_b64_tr_b16 v[112:113], %[pa] offset:26624\n"
        "ds_read_b64_tr_b16 v[114:115], %[pa] offset:27136\nv_mfma_f32_32x32x16_bf16 v[48:63], v[96:99], v[108:111], v[48:63]\nds_read_b128 v[120:123], %[pb] offset:25600\n"
        "ds_read_b64_tr_b16 v[116:117], %[pa] offset:30720\nv_mfma_f32_32x32x16_bf16 v[64:79], v[100:103], v[104:107], v[64:79]\nds_read_b64_tr_b16 v[118:119], %[pa] offset:31232\n"
        "ds_read_b128 v[124:127], %[pb] offset:27648\nv_mfma_f32_32x32x16_bf16 v[80:95], v[100:103], v[108:111], v[80:95]\ns_waitcnt vmcnt(3) lgkmcnt(0)\ns_barrier\n"
        "v_mfma_f32_32x32x16_bf16 v[32:47], v[112:115], v[120:123], v[32:47]\ns_add_u32 m0, %[dw], 24576\ns_add_u32 s40, s40, 0x1c000\ns_addc_u32 s41, s41, 0\n"
        "global_load_lds_dwordx4 %[vw], s[40:41]\nds_read_b64_tr_b16 v[96:97], %[pa] offset:49152\nds_read_b64_tr_b16 v[98:99], %[pa] offset:49664\n"
        "v_mfma_f32_32x32x16_bf16 v[48:63], v[112:115], v[124:127], v[48:63]\ns_add_u32 m0, %[dx], 24576\ns_add_u32 s42, s42, 0x800\ns_addc_u32 s43, s43, 0\n"
        "global_load_lds_dwordx4 %[vx], s[42:43] sc1\nds_read_b128 v[104:107], %[pb] offset:49152\nds_read_b64_tr_b16 v[100:101], %[pa] offset:53248\n"
        "v_mfma_f32_32x32x16_bf16 v[64:79], v[116:119], v[120:123], v[64:79]\nglobal_load_lds_dwordx4 %[vx], s[42:43] offset:1024 sc1\n"
        "ds_read_b64_tr_b16 v[102:103], %[pa] offset:53760\nds_read_b128 v[108:111], %[pb] offset:51200\nv_mfma_f32_32x32x16_bf16 v[80:95], v[116:119], v[124:127], v[80:95]\n"
        "s_waitcnt lgkmcnt(0)\nv_mfma_f32_32x32x16_bf16 v[32:47], v[96:99], v[104:107], v[32:47]\nds_read_b64_tr_b16 v[112:113], %[pa] offset:51200\n"
        "ds_read_b64_tr_b16 v[114:115], %[pa] offset:51712\nv_mfma_f32_32x32x16_bf16 v[48:63], v[96:99], v[108:111], v[48:63]\nds_read_b128 v[120:123], %[pb] offset:50176\n"
        "ds_read_b64_tr_b16 v[116:117], %[pa] offset:55296\nv_mfma_f32_32x32x16_bf16 v[64:79], v[100:103], v[104:107], v[64:79]\nds_read_b64_tr_b16 v[118:119], %[pa] offset:55808\n"
        "ds_read_b128 v[124:127], %[pb] offset:52224\nv_mfma_f32_32x32x16_bf16 v[80:95], v[100:103], v[108:111], v[80:95]\ns_waitcnt vmcnt(3) lgkmcnt(0)\ns_barrier\n"
        "v_mfma_f32_32x32x16_bf16 v[32:47], v[112:115], v[120:123], v[32:47]\ns_add_u32 m0, %[dw], 49152\ns_add_u32 s40, s40, 0x1c000\ns_addc_u32 s41, s41, 0\n"
        "global_load_lds_dwordx4 %[vw], s[40:41]\nds_read_b64_tr_b16 v[96:97], %[pa] offset:0\nds_read_b64_tr_b16 v[98:99], %[pa] offset:512\n"
        "v_mfma_f32_32x32x16_bf16 v[48:63], v[112:115], v[124:127], v[48:63]\ns_add_u32 m0, %[dx], 49152\ns_add_u32 s42, s42, 0x800\ns_addc_u32 s43, s43, 0\n"
        "global_load_lds_dwordx4 %[vx], s[42:43] sc1\nds_read_b128 v[104:107], %[pb] offset:0\nds_read_b64_tr_b16 v[100:101], %[pa] offset:4096\n"
        "v_mfma_f32_32x32x16_bf16 v[64:79], v[116:119], v[120:123], v[64:79]\nglobal_load_lds_dwordx4 %[vx], s[42:43] offset:1024 sc1\nglobal_load_dword %[jk], %[tv], %[wd]\n"
        "ds_read_b64_tr_b16 v[102:103], %[pa] offset:4608\nds_read_b128 v[108:111], %[pb] offset:2048\nv_mfma_f32_32x32x16_bf16 v[80:95], v[116:119], v[124:127], v[80:95]\n"
        "s_waitcnt lgkmcnt(0)\nv_mfma_f32_32x32x16_bf16 v[32:47], v[96:99], v[104:107], v[32:47]\nds_read_b64_tr_b16 v[112:113], %[pa] offset:2048\n"
        "ds_read_b64_tr_b16 v[114:115], %[pa] offset:2560\nv_mfma_f32_32x32x16_bf16 v[48:63], v[96:99], v[108:111], v[48:63]\nds_read_b128 v[120:123], %[pb] offset:1024\n"
        "ds_read_b64_tr_b16 v[116:117], %[pa] offset:6144\nv_mfma_f32_32x32x16_bf16 v[64:79], v[100:103], v[104:107], v[64:79]\nds_read_b64_tr_b16 v[118:119], %[pa] offset:6656\n"
        "ds_read_b128 v[124:127], %[pb] offset:3072\nv_mfma_f32_32x32x16_bf16 v[80:95], v[100:103], v[108:111], v[80:95]\ns_waitcnt vmcnt(4) lgkmcnt(0)\ns_barrier\n"
        "v_mfma_f32_32x32x16_bf16 v[32:47], v[112:115], v[120:123], v[32:47]\ns_add_u32 m0, %[dw], 0\ns_add_u32 s40, s40, 0x1c000\ns_addc_u32 s41, s41, 0\n"
        "global_load_lds_dwordx4 %[vw], s[40:41]\nds_read_b64_tr_b16 v[96:97], %[pa] offset:24576\nds_read_b64_tr_b16 v[98:99], %[pa] offset:25088\n"
        "v_mfma_f32_32x32x16_bf16 v[48:63], v[112:115], v[124:127], v[48:63]\ns_add_u32 m0, %[dx], 0\ns_add_u32 s42, s42, 0x800\ns_addc_u32 s43, s43, 0\n"
        "global_load_lds_dwordx4 %[vx], s[42:43] sc1\nds_read_b128 v[104:107], %[pb] offset:24576\nds_read_b64_tr_b16 v[100:101], %[pa] offset:28672\n"
        "v_mfma_f32_32x32x16_bf16 v[64:79], v[116:119], v[120:123], v[64:79]\nglobal_load_lds_dwordx4 %[vx], s[42:43] offset:1024 sc1\n"
        "ds_read_b64_tr_b16 v[102:103], %[pa] offset:29184\nds_read_b128 v[108:111], %[pb] offset:26624\nv_mfma_f32_32x32x16_bf16 v[80:95], v[116:119], v[124:127], v[80:95]\n"
        "s_waitcnt lgkmcnt(0)\nv_mfma_f32_32x32x16_bf16 v[32:47], v[96:99], v[104:107], v[32:47]\nds_read_b64_tr_b16 v[112:113], %[pa] offset:26624\n"
        "ds_read_b64_tr_b16 v[114:115], %[pa] offset:27136\nv_mfma_f32_32x32x16_bf16 v[48:63], v[96:99], v[108:111], v[48:63]\nds_read_b128 v[120:123], %[pb] offset:25600\n"
        "ds_read_b64_tr_b16 v[116:117], %[pa] offset:30720\nv_mfma_f32_32x32x16_bf16 v[64:79], v[100:103], v[104:107], v[64:79]\nds_read_b64_tr_b16 v[118:119], %[pa] offset:31232\n"
        "ds_read_b128 v[124:127], %[pb] offset:27648\nv_mfma_f32_32x32x16_bf16 v[80:95], v[100:103], v[108:111], v[80:95]\ns_waitcnt vmcnt(4) lgkmcnt(0)\ns_barrier\n"
        "v_mfma_f32_32x32x16_bf16 v[32:47], v[112:115], v[120:123], v[32:47]\ns_add_u32 m0, %[dw], 24576\ns_add_u32 s40, s40, 0x1c000\ns_addc_u32 s41, s41, 0\n"
        "global_load_lds_dwordx4 %[vw], s[40:41]\nds_read_b64_tr_b16 v[96:97], %[pa] offset:49152\nds_read_b64_tr_b16 v[98:99], %[pa] offset:49664\n"
        "v_mfma_f32_32x32x16_bf16 v[48:63], v[112:115], v[124:127], v[48:63]\ns_add_u32 m0, %[dx], 24576\ns_add_u32 s42, s42, 0x800\ns_addc_u32 s43, s43, 0\n"
        "global_load_lds_dwordx4 %[vx], s[42:43] sc1\nds_read_b128 v[104:107], %[pb] offset:49152\nds_read_b64_tr_b16 v[100:101], %[pa] offset:53248\n"
        "v_mfma_f32_32x32x16_bf16 v[64:79], v[116:119], v[120:123], v[64:79]\nglobal_load_lds_dwordx4 %[vx], s[42:43] offset:1024 sc1\n"
        "ds_read_b64_tr_b16 v[102:103], %[pa] offset:53760\nds_read_b128 v[108:111], %[pb] offset:51200\nv_mfma_f32_32x32x16_bf16 v[80:95], v[116:119], v[124:127], v[80:95]\n"
        "s_waitcnt lgkmcnt(0)\nv_mfma_f32_32x32x16_bf16 v[32:47], v[96:99], v[104:107], v[32:47]\nds_read_b64_tr_b16 v[112:113], %[pa] offset:51200\n"
        "ds_read_b64_tr_b16 v[114:115], %[pa] offset:51712\nv_mfma_f32_32x32x16_bf16 v[48:63], v[96:99], v[108:111], v[48:63]\nds_read_b128 v[120:123], %[pb] offset:50176\n"
        "ds_read_b64_tr_b16 v[116:117], %[pa] offset:55296\nv_mfma_f32_32x32x16_bf16 v[64:79], v[100:103], v[104:107], v[64:79]\nds_read_b64_tr_b16 v[118:119], %[pa] offset:55808\n"
        "ds_read_b128 v[124:127], %[pb] offset:52224\nv_mfma_f32_32x32x16_bf16 v[80:95], v[100:103], v[108:111], v[80:95]\ns_waitcnt vmcnt(3) lgkmcnt(0)\ns_barrier\n"
        "v_mfma_f32_32x32x16_bf16 v[32:47], v[112:115], v[120:123], v[32:47]\ns_add_u32 m0, %[dw], 49152\ns_add_u32 s40, s40, 0x1c000\ns_addc_u32 s41, s41, 0\n"
        "global_load_lds_dwordx4 %[vw], s[40:41]\nds_read_b64_tr_b16 v[96:97], %[pa] offset:0\nds_read_b64_tr_b16 v[98:99], %[pa] offset:512\n"
        "v_mfma_f32_32x32x16_bf16 v[48:63], v[112:115], v[124:127], v[48:63]\ns_add_u32 m0, %[dx], 49152\ns_add_u32 s42, s42, 0x800\ns_addc_u32 s43, s43, 0\n"
        "global_load_lds_dwordx4 %[vx], s[42:43] sc1\nds_read_b128 v[104:107], %[pb] offset:0\nds_read_b64_tr_b16 v[100:101], %[pa] offset:4096\n"
        "v_mfma_f32_32x32x16_bf16 v[64:79], v[116:119], v[120:123], v[64:79]\nglobal_load_lds_dwordx4 %[vx], s[42:43] offset:1024 sc1\n"
        "ds_read_b64_tr_b16 v[102:103], %[pa] offset:4608\nds_read_b128 v[108:111], %[pb] offset:2048\nv_mfma_f32_32x32x16_bf16 v[80:95], v[116:119], v[124:127], v[80:95]\n"
        "s_waitcnt lgkmcnt(0)\nv_mfma_f32_32x32x16_bf16 v[32:47], v[96:99], v[104:107], v[32:47]\nds_read_b64_tr_b16 v[112:113], %[pa] offset:2048\n"
        "ds_read_b64_tr_b16 v[114:115], %[pa] offset:2560\nv_mfma_f32_32x32x16_bf16 v[48:63], v[96:99], v[108:111], v[48:63]\nds_read_b128 v[120:123], %[pb] offset:1024\n"
        "ds_read_b64_tr_b16 v[116:117], %[pa] offset:6144\nv_mfma_f32_32x32x16_bf16 v[64:79], v[100:103], v[104:107], v[64:79]\nds_read_b64_tr_b16 v[118:119], %[pa] offset:6656\n"
        "ds_read_b128 v[124:127], %[pb] offset:3072\nv_mfma_f32_32x32x16_bf16 v[80:95], v[100:103], v[108:111], v[80:95]\ns_waitcnt vmcnt(3) lgkmcnt(0)\ns_barrier\n"
        "v_mfma_f32_32x32x16_bf16 v[32:47], v[112:115], v[120:123], v[32:47]\ns_add_u32 m0, %[dw], 0\ns_add_u32 s40, s40, 0x1c000\ns_addc_u32 s41, s41, 0\n"
        "global_load_lds_dwordx4 %[vw], s[40:41]\nds_read_b64_tr_b16 v[96:97], %[pa] offset:24576\nds_read_b64_tr_b16 v[98:99], %[pa] offset:25088\n"
        "v_mfma_f32_32x32x16_bf16 v[48:63], v[112:115], v[124:127], v[48:63]\ns_add_u32 m0, %[dx], 0\ns_add_u32 s42, s42, 0x800\ns_addc_u32 s43, s43, 0\n"
        "global_load_lds_dwordx4 %[vx], s[42:43] sc1\nds_read_b128 v[104:107], %[pb] offset:24576\nds_read_b64_tr_b16 v[100:101], %[pa] offset:28672\n"
        "v_mfma_f32_32x32x16_bf16 v[64:79], v[116:119], v[120:123], v[64:79]\nglobal_load_lds_dwordx4 %[vx], s[42:43] offset:1024 sc1\n"
        "ds_read_b64_tr_b16 v[102:103], %[pa] offset:29184\nds_read_b128 v[108:111], %[pb] offset:26624\nv_mfma_f32_32x32x16_bf16 v[80:95], v[116:119], v[124:127], v[80:95]\n"
        "s_waitcnt lgkmcnt(0)\nv_mfma_f32_32x32x16_bf16 v[32:47], v[96:99], v[104:107], v[32:47]\nds_read_b64_tr_b16 v[112:113], %[pa] offset:26624\n"
        "ds_read_b64_tr_b16 v[114:115], %[pa] offset:27136\nv_mfma_f32_32x32x16_bf16 v[48:63], v[96:99], v[108:111], v[48:63]\nds_read_b128 v[120:123], %[pb] offset:25600\n"
        "ds_read_b64_tr_b16 v[116:117], %[pa] offset:30720\nv_mfma_f32_32x32x16_bf16 v[64:79], v[100:103], v[104:107], v[64:79]\nds_read_b64_tr_b16 v[118:119], %[pa] offset:31232\n"
        "ds_read_b128 v[124:127], %[pb] offset:27648\nv_mfma_f32_32x32x16_bf16 v[80:95], v[100:103], v[108:111], v[80:95]\ns_waitcnt vmcnt(3) lgkmcnt(0)\ns_barrier\n"
        "v_mfma_f32_32x32x16_bf16 v[32:47], v[112:115], v[120:123], v[32:47]\ns_add_u32 m0, %[dw], 24576\ns_add_u32 s40, s40, 0x1c000\ns_addc_u32 s41, s41, 0\n"
        "global_load_lds_dwordx4 %[vw], s[40:41]\nds_read_b64_tr_b16 v[96:97], %[pa] offset:49152\nds_read_b64_tr_b16 v[98:99], %[pa] offset:49664\n"
        "v_mfma_f32_32x32x16_bf16 v[48:63], v[112:115], v[124:127], v[48:63]\ns_add_u32 m0, %[dx], 24576\ns_add_u32 s42, s42, 0x800\ns_addc_u32 s43, s43, 0\n"
        "global_load_lds_dwordx4 %[vx], s[42:43] sc1\nds_read_b128 v[104:107], %[pb] offset:49152\nds_read_b64_tr_b16 v[100:101], %[pa] offset:53248\n"
        "v_mfma_f32_32x32x16_bf16 v[64:79], v[116:119], v[120:123], v[64:79]\nglobal_load_lds_dwordx4 %[vx], s[42:43] offset:1024 sc1\n"
        "ds_read_b64_tr_b16 v[102:103], %[pa] offset:53760\nds_read_b128 v[108:111], %[pb] offset:51200\nv_mfma_f32_32x32x16_bf16 v[80:95], v[116:119], v[124:127], v[80:95]\n"
        "s_waitcnt lgkmcnt(0)\nv_mfma_f32_32x32x16_bf16 v[32:47], v[96:99], v[104:107], v[32:47]\nds_read_b64_tr_b16 v[112:113], %[pa] offset:51200\n"
        "ds_read_b64_tr_b16 v[114:115], %[pa] offset:51712\nv_mfma_f32_32x32x16_bf16 v[48:63], v[96:99], v[108:111], v[48:63]\nds_read_b128 v[120:123], %[pb] offset:50176\n"
        "ds_read_b64_tr_b16 v[116:117], %[pa] offset:55296\nv_mfma_f32_32x32x16_bf16 v[64:79], v[100:103], v[104:107], v[64:79]\nds_read_b64_tr_b16 v[118:119], %[pa] offset:55808\n"
        "ds_read_b128 v[124:127], %[pb] offset:52224\nv_mfma_f32_32x32x16_bf16 v[80:95], v[100:103], v[108:111], v[80:95]\ns_waitcnt vmcnt(3) lgkmcnt(0)\ns_barrier\n"
        "v_mfma_f32_32x32x16_bf16 v[32:47], v[112:115], v[120:123], v[32:47]\ns_add_u32 m0, %[dw], 49152\ns_add_u32 s40, s40, 0x1c000\ns_addc_u32 s41, s41, 0\n"
        "global_load_lds_dwordx4 %[vw], s[40:41]\nds_read_b64_tr_b16 v[96:97], %[pa] offset:0\nds_read_b64_tr_b16 v[98:99], %[pa] offset:512\n"
        "v_mfma_f32_32x32x16_bf16 v[48:63], v[112:115], v[124:127], v[48:63]\ns_add_u32 m0, %[dx], 49152\ns_add_u32 s42, s42, 0x800\ns_addc_u32 s43, s43, 0\n"
        "global_load_lds_dwordx4 %[vx], s[42:43] sc1\nds_read_b128 v[104:107], %[pb] offset:0\nds_read_b64_tr_b16 v[100:101], %[pa] offset:4096\n"
        "v_mfma_f32_32x32x16_bf16 v[64:79], v[116:119], v[120:123], v[64:79]\nglobal_load_lds_dwordx4 %[vx], s[42:43] offset:1024 sc1\n"
        "ds_read_b64_tr_b16 v[102:103], %[pa] offset:4608\nds_read_b128 v[108:111], %[pb] offset:2048\nv_mfma_f32_32x32x16_bf16 v[80:95], v[116:119], v[124:127], v[80:95]\n"
        "s_waitcnt lgkmcnt(0)\nv_mfma_f32_32x32x16_bf16 v[32:47], v[96:99], v[104:107], v[32:47]\nds_read_b64_tr_b16 v[112:113], %[pa] offset:2048\n"
        "ds_read_b64_tr_b16 v[114:115], %[pa] offset:2560\nv_mfma_f32_32x32x16_bf16 v[48:63], v[96:99], v[108:111], v[48:63]\nds_read_b128 v[120:123], %[pb] offset:1024\n"
        "ds_read_b64_tr_b16 v[116:117], %[pa] offset:6144\nv_mfma_f32_32x32x16_bf16 v[64:79], v[100:103], v[104:107], v[64:79]\nds_read_b64_tr_b16 v[118:119], %[pa] offset:6656\n"
        "ds_read_b128 v[124:127], %[pb] offset:3072\nv_mfma_f32_32x32x16_bf16 v[80:95], v[100:103], v[108:111], v[80:95]\ns_waitcnt vmcnt(3) lgkmcnt(0)\ns_barrier\n"
        "v_mfma_f32_32x32x16_bf16 v[32:47], v[112:115], v[120:123], v[32:47]\ns_add_u32 m0, %[dw], 0\ns_add_u32 s40, s40, 0x1c000\ns_addc_u32 s41, s41, 0\n"
        "global_load_lds_dwordx4 %[vw], s[40:41]\nds_read_b64_tr_b16 v[96:97], %[pa] offset:24576\nds_read_b64_tr_b16 v[98:99], %[pa] offset:25088\n"
        "v_mfma_f32_32x32x16_bf16 v[48:63], v[112:115], v[124:127], v[48:63]\ns_add_u32 m0, %[dx], 0\ns_add_u32 s42, s42, 0x800\ns_addc_u32 s43, s43, 0\n"
        "global_load_lds_dwordx4 %[vx], s[42:43] sc1\nds_read_b128 v[104:107], %[pb] offset:24576\nds_read_b64_tr_b16 v[100:101], %[pa] offset:28672\n"
        "v_mfma_f32_32x32x16_bf16 v[64:79], v[116:119], v[120:123], v[64:79]\nglobal_load_lds_dwordx4 %[vx], s[42:43] offset:1024 sc1\n"
        "ds_read_b64_tr_b16 v[102:103], %[pa] offset:29184\nds_read_b128 v[108:111], %[pb] offset:26624\nv_mfma_f32_32x32x16_bf16 v[80:95], v[116:119], v[124:127], v[80:95]\n"
        "s_waitcnt lgkmcnt(0)\nv_mfma_f32_32x32x16_bf16 v[32:47], v[96:99], v[104:107], v[32:47]\nds_read_b64_tr_b16 v[112:113], %[pa] offset:26624\n"
        "ds_read_b64_tr_b16 v[114:115], %[pa] offset:27136\nv_mfma_f32_32x32x16_bf16 v[48:63], v[96:99], v[108:111], v[48:63]\nds_read_b128 v[120:123], %[pb] offset:25600\n"
        "ds_read_b64_tr_b16 v[116:117], %[pa] offset:30720\nv_mfma_f32_32x32x16_bf16 v[64:79], v[100:103], v[104:107], v[64:79]\nds_read_b64_tr_b16 v[118:119], %[pa] offset:31232\n"
        "ds_read_b128 v[124:127], %[pb] offset:27648\nv_mfma_f32_32x32x16_bf16 v[80:95], v[100:103], v[108:111], v[80:95]\ns_waitcnt vmcnt(3) lgkmcnt(0)\ns_barrier\n"
        "v_mfma_f32_32x32x16_bf16 v[32:47], v[112:115], v[120:123], v[32:47]\ns_add_u32 m0, %[dw], 24576\ns_add_u32 s40, s40, 0x1c000\ns_addc_u32 s41, s41, 0\n"
        "global_load_lds_dwordx4 %[vw], s[40:41]\nds_read_b64_tr_b16 v[96:97], %[pa] offset:49152\nds_read_b64_tr_b16 v[98:99], %[pa] offset:49664\n"
        "v_mfma_f32_32x32x16_bf16 v[48:63], v[112:115], v[124:127], v[48:63]\ns_add_u32 m0, %[dx], 24576\ns_add_u32 s42, s42, 0x800\ns_addc_u32 s43, s43, 0\n"
        "global_load_lds_dwordx4 %[vx], s[42:43] sc1\nds_read_b128 v[104:107], %[pb] offset:49152\nds_read_b64_tr_b16 v[100:101], %[pa] offset:53248\n"
        "v_mfma_f32_32x32x16_bf16 v[64:79], v[116:119], v[120:123], v[64:79]\nglobal_load_lds_dwordx4 %[vx], s[42:43] offset:1024 sc1\n"
        "ds_read_b64_tr_b16 v[102:103], %[pa] offset:53760\nds_read_b128 v[108:111], %[pb] offset:51200\nv_mfma_f32_32x32x16_bf16 v[80:95], v[116:119], v[124:127], v[80:95]\n"
        "s_waitcnt lgkmcnt(0)\nv_mfma_f32_32x32x16_bf16 v[32:47], v[96:99], v[104:107], v[32:47]\nds_read_b64_tr_b16 v[112:113], %[pa] offset:51200\n"
        "ds_read_b64_tr_b16 v[114:115], %[pa] offset:51712\nv_mfma_f32_32x32x16_bf16 v[48:63], v[96:99], v[108:111], v[48:63]\nds_read_b128 v[120:123], %[pb] offset:50176\n"
        "ds_read_b64_tr_b16 v[116:117], %[pa] offset:55296\nv_mfma_f32_32x32x16_bf16 v[64:79], v[100:103], v[104:107], v[64:79]\nds_read_b64_tr_b16 v[118:119], %[pa] offset:55808\n"
        "ds_read_b128 v[124:127], %[pb] offset:52224\nv_mfma_f32_32x32x16_bf16 v[80:95], v[100:103], v[108:111], v[80:95]\ns_waitcnt vmcnt(3) lgkmcnt(0)\ns_barrier\n"
        "v_mfma_f32_32x32x16_bf16 v[32:47], v[112:115], v[120:123], v[32:47]\ns_add_u32 m0, %[dw], 49152\ns_add_u32 s40, s40, 0x1c000\ns_addc_u32 s41, s41, 0\n"
        "global_load_lds_dwordx4 %[vw], s[40:41]\nds_read_b64_tr_b16 v[96:97], %[pa] offset:0\nds_read_b64_tr_b16 v[98:99], %[pa] offset:512\n"
        "v_mfma_f32_32x32x16_bf16 v[48:63], v[112:115], v[124:127], v[48:63]\ns_add_u32 m0, %[dx], 49152\ns_add_u32 s42, s42, 0x800\ns_addc_u32 s43, s43, 0\n"
        "global_load_lds_dwordx4 %[vx], s[42:43] sc1\nds_read_b128 v[104:107], %[pb] offset:0\nds_read_b64_tr_b16 v[100:101], %[pa] offset:4096\n"
        "v_mfma_f32_32x32x16_bf16 v[64:79], v[116:119], v[120:123], v[64:79]\nglobal_load_lds_dwordx4 %[vx], s[42:43] offset:1024 sc1\n"
        "ds_read_b64_tr_b16 v[102:103], %[pa] offset:4608\nds_read_b128 v[108:111], %[pb] offset:2048\nv_mfma_f32_32x32x16_bf16 v[80:95], v[116:119], v[124:127], v[80:95]\n"
        "s_waitcnt lgkmcnt(0)\nv_mfma_f32_32x32x16_bf16 v[32:47], v[96:99], v[104:107], v[32:47]\nds_read_b64_tr_b16 v[112:113], %[pa] offset:2048\n"
        "ds_read_b64_tr_b16 v[114:115], %[pa] offset:2560\nv_mfma_f32_32x32x16_bf16 v[48:63], v[96:99], v[108:111], v[48:63]\nds_read_b128 v[120:123], %[pb] offset:1024\n"
        "ds_read_b64_tr_b16 v[116:117], %[pa] offset:6144\nv_mfma_f32_32x32x16_bf16 v[64:79], v[100:103], v[104:107], v[64:79]\nds_read_b64_tr_b16 v[118:119], %[pa] offset:6656\n"
        "ds_read_b128 v[124:127], %[pb] offset:3072\nv_mfma_f32_32x32x16_bf16 v[80:95], v[100:103], v[108:111], v[80:95]\ns_waitcnt vmcnt(3) lgkmcnt(0)\ns_barrier\n"
        "v_mfma_f32_32x32x16_bf16 v[32:47], v[112:115], v[120:123], v[32:47]\ns_add_u32 m0, %[dw], 0\ns_add_u32 s40, s40, 0x1c000\ns_addc_u32 s41, s41, 0\n"
        "global_load_lds_dwordx4 %[vw], s[40:41]\nds_read_b64_tr_b16 v[96:97], %[pa] offset:24576\nds_read_b64_tr_b16 v[98:99], %[pa] offset:25088\n"
        "v_mfma_f32_32x32x16_bf16 v[48:63], v[112:115], v[124:127], v[48:63]\ns_add_u32 m0, %[dx], 0\ns_add_u32 s42, s42, 0x800\ns_addc_u32 s43, s43, 0\n"
        "global_load_lds_dwordx4 %[vx], s[42:43] sc1\nds_read_b128 v[104:107], %[pb] offset:24576\nds_read_b64_tr_b16 v[100:101], %[pa] offset:28672\n"
        "v_mfma_f32_32x32x16_bf16 v[64:79], v[116:119], v[120:123], v[64:79]\nglobal_load_lds_dwordx4 %[vx], s[42:43] offset:1024 sc1\n"
        "ds_read_b64_tr_b16 v[102:103], %[pa] offset:29184\nds_read_b128 v[108:111], %[pb] offset:26624\nv_mfma_f32_32x32x16_bf16 v[80:95], v[116:119], v[124:127], v[80:95]\n"
        "s_waitcnt lgkmcnt(0)\nv_mfma_f32_32x32x16_bf16 v[32:47], v[96:99], v[104:107], v[32:47]\nds_read_b64_tr_b16 v[112:113], %[pa] offset:26624\n"
        "ds_read_b64_tr_b16 v[114:115], %[pa] offset:27136\nv_mfma_f32_32x32x16_bf16 v[48:63], v[96:99], v[108:111], v[48:63]\nds_read_b128 v[120:123], %[pb] offset:25600\n"
        "ds_read_b64_tr_b16 v[116:117], %[pa] offset:30720\nv_mfma_f32_32x32x16_bf16 v[64:79], v[100:103], v[104:107], v[64:79]\nds_read_b64_tr_b16 v[118:119], %[pa] offset:31232\n"
        "ds_read_b128 v[124:127], %[pb] offset:27648\nv_mfma_f32_32x32x16_bf16 v[80:95], v[100:103], v[108:111], v[80:95]\ns_waitcnt vmcnt(3) lgkmcnt(0)\ns_barrier\n"
        "v_mfma_f32_32x32x16_bf16 v[32:47], v[112:115], v[120:123], v[32:47]\nds_read_b64_tr_b16 v[96:97], %[pa] offset:49152\nds_read_b64_tr_b16 v[98:99], %[pa] offset:49664\n"
        "v_mfma_f32_32x32x16_bf16 v[48:63], v[112:115], v[124:127], v[48:63]\nds_read_b128 v[104:107], %[pb] offset:49152\nds_read_b64_tr_b16 v[100:101], %[pa] offset:53248\n"
        "v_mfma_f32_32x32x16_bf16 v[64:79], v[116:119], v[120:123], v[64:79]\nds_read_b64_tr_b16 v[102:103], %[pa] offset:53760\nds_read_b128 v[108:111], %[pb] offset:51200\n"
        "v_mfma_f32_32x32x16_bf16 v[80:95], v[116:119], v[124:127], v[80:95]\ns_waitcnt lgkmcnt(0)\nv_mfma_f32_32x32x16_bf16 v[32:47], v[96:99], v[104:107], v[32:47]\n"
        "ds_read_b64_tr_b16 v[112:113], %[pa] offset:51200\nds_read_b64_tr_b16 v[114:115], %[pa] offset:51712\nv_mfma_f32_32x32x16_bf16 v[48:63], v[96:99], v[108:111], v[48:63]\n"
        "ds_read_b128 v[120:123], %[pb] offset:50176\nds_read_b64_tr_b16 v[116:117], %[pa] offset:55296\nv_mfma_f32_32x32x16_bf16 v[64:79], v[100:103], v[104:107], v[64:79]\n"
        "ds_read_b64_tr_b16 v[118:119], %[pa] offset:55808\nds_read_b128 v[124:127], %[pb] offset:52224\nv_mfma_f32_32x32x16_bf16 v[80:95], v[100:103], v[108:111], v[80:95]\n"
        "s_waitcnt vmcnt(0) lgkmcnt(0)\ns_barrier\nv_mfma_f32_32x32x16_bf16 v[32:47], v[112:115], v[120:123], v[32:47]\nds_read_b64_tr_b16 v[96:97], %[pa] offset:0\n"
        "ds_read_b64_tr_b16 v[98:99], %[pa] offset:512\nv_mfma_f32_32x32x16_bf16 v[48:63], v[112:115], v[124:127], v[48:63]\nds_read_b128 v[104:107], %[pb] offset:0\n"
        "ds_read_b64_tr_b16 v[100:101], %[pa] offset:4096\nv_mfma_f32_32x32x16_bf16 v[64:79], v[116:119], v[120:123], v[64:79]\nds_read_b64_tr_b16 v[102:103], %[pa] offset:4608\n"
        "ds_read_b128 v[108:111], %[pb] offset:2048\nv_mfma_f32_32x32x16_bf16 v[80:95], v[116:119], v[124:127], v[80:95]\ns_waitcnt lgkmcnt(0)\n"
        "v_mfma_f32_32x32x16_bf16 v[32:47], v[96:99], v[104:107], v[32:47]\nds_read_b64_tr_b16 v[112:113], %[pa] offset:2048\nds_read_b64_tr_b16 v[114:115], %[pa] offset:2560\n"
        "v_mfma_f32_32x32x16_bf16 v[48:63], v[96:99], v[108:111], v[48:63]\nds_read_b128 v[120:123], %[pb] offset:1024\nds_read_b64_tr_b16 v[116:117], %[pa] offset:6144\n"
        "v_mfma_f32_32x32x16_bf16 v[64:79], v[100:103], v[104:107], v[64:79]\nds_read_b64_tr_b16 v[118:119], %[pa] offset:6656\nds_read_b128 v[124:127], %[pb] offset:3072\n"
        "v_mfma_f32_32x32x16_bf16 v[80:95], v[100:103], v[108:111], v[80:95]\ns_waitcnt lgkmcnt(0)\nv_mfma_f32_32x32x16_bf16 v[32:47], v[112:115], v[120:123], v[32:47]\n"
        "v_mfma_f32_32x32x16_bf16 v[48:63], v[112:115], v[124:127], v[48:63]\nv_mfma_f32_32x32x16_bf16 v[64:79], v[116:119], v[120:123], v[64:79]\n"
        "v_mfma_f32_32x32x16_bf16 v[80:95], v[116:119], v[124:127], v[80:95]\ns_mov_b32 m0, %[sv]\ns_nop 7\ns_nop 7\n"
          : "=&{v[32:47]}"(g0), "=&{v[48:63]}"(g1), "=&{v[64:79]}"(u0), "=&{v[80:95]}"(u1), [jk] "+v"(jk), [sv] "=&s"(sv)
          : [pa] "v"(pa), [pb] "v"(pb), [vw] "v"(vw), [vx] "v"(l16), [tv] "v"(tv), [dw] "s"(dw), [dx] "s"(dx), [sw] "s"(sw + 2 * 114688), [sx] "s"(sx + 2 * 2048), [wd] "s"(wd)
          : "v96", "v97", "v98", "v99", "v100", "v101", "v102", "v103", "v104", "v105", "v106", "v107", "v108", "v109", "v110", "v111", "v112", "v113", "v114", "v115", "v116", "v117", "v118", "v119", "v120", "v121", "v122", "v123", "v124", "v125", "v126", "v127", "s40", "s41", "s42", "s43", "scc", "memory");
        PRIO(P_EPI);
#pragma unroll
        for (int i = 0; i < 16; ++i) { g0[i] = fmaxf(g0[i], 0.f) * u0[i]; g1[i] = fmaxf(g1[i], 0.f) * u1[i]; }
        TS(256 + b, 1);
        char* hp = ws + WS_H + ((TB * 8 + 2 * tk) * 112 + fb * 4 + ft * 2) * 1024;
        st_sc1(l16, hp, pack8(g0, 0)); st_sc1(l16, hp + 1024, pack8(g0, 8)); st_sc1(l16, hp + 114688, pack8(g1, 0)); st_sc1(l16, hp + 115712, pack8(g1, 8));
        BAR(0);
        if (tid == 0) set_flag(ws, FL_H + TB * 32 + fb, tag);
    } else {
        const int j = i5 - 28;
        if (w < 2) wait_flags(ws, FL_K + tid, tag);
        BARL(); if (tid == 0) set_flag(ws, FL_R + xcd * 4 + j, tag);
        __builtin_amdgcn_s_sleep(127);
        touch((j * 1024 + tid) * 128, wo); TOUCH_END(); touch((j * 1024 + 512 + tid) * 128, wo); TOUCH_END();
        if (b == 255) {
            wait_flags(ws, FL_A + tid, tag); BARL();
            if (tid == 0) set_flag(ws, FL_E, tag); }
    }
    TS(256 + b, 2);
    const int tg = xcd * 4 + (i5 & 3), cg = i5 >> 2, t = w & 1, kq = w >> 1;
    const unsigned vo = (l >> 3) * 1024 + (((l & 7) ^ (((l >> 4) & 1) << 2)) << 4);
    const int at = (4 * h + q) * 128 + (((((l >> 4) & 1) * 2 + (p >> 1)) ^ ((q >> 1) << 2)) << 4) + 8 * (p & 1);
    float gg[8]; u32x2 x0, x1;
    const unsigned ob = (tg * 64 + t * 32 + (l & 31)) * 1024 + cg * 128 + kq * 32;
    {   const char* sw = uni(wd + cg * 128 + w * 14 * 16384); const unsigned dw = L0 + w * 8192;
        const int r0 = 2 * cg - 14;
#define ROT(n) ((n) + r0 < 0 ? (n) + r0 + 14 : (n) + r0)
#define F2_W(n) do { const char* s_ = sw + ROT(n) * 16384; glds16(vo, s_, dw + ((n) & 3) * 2048); glds16(vo, s_ + 8192, dw + ((n) & 3) * 2048 + 1024); } while (0)
        F2_W(0); F2_W(1); F2_W(2); F2_W(3);
        wait_flags(ws, FL_H + xcd * 32 + (w * 7 >> 1) + (l & 3), tag);
        TS(256 + b, 3);
        const char* sh = uni(ws + WS_H + (2 * tg * 112 + w * 14) * 1024);
        f32x16 d00, d01, d10, d11; unsigned ro = ROT(0) * 1024, sv;
        PRIO(P_LOOP2);
        asm volatile(
        "s_mov_b32 %[sv], m0\ns_mov_b64 s[44:45], %[sh]\ns_mov_b64 s[46:47], %[sw]\ns_add_u32 s40, s44, %[ro]\ns_addc_u32 s41, s45, 0\n"
        "global_load_dwordx4 v[112:115], %[h0], s[40:41] sc1\nglobal_load_dwordx4 v[116:119], %[h1], s[40:41] sc1\ns_add_u32 %[ro], %[ro], 1024\ns_cmp_eq_u32 %[ro], 14336\n"
        "s_cselect_b32 %[ro], 0, %[ro]\ns_add_u32 s40, s44, %[ro]\ns_addc_u32 s41, s45, 0\nglobal_load_dwordx4 v[120:123], %[h0], s[40:41] sc1\n"
        "global_load_dwordx4 v[124:127], %[h1], s[40:41] sc1\ns_add_u32 %[ro], %[ro], 1024\ns_cmp_eq_u32 %[ro], 14336\ns_cselect_b32 %[ro], 0, %[ro]\ns_add_u32 s40, s44, %[ro]\n"
        "s_addc_u32 s41, s45, 0\nglobal_load_dwordx4 v[16:19], %[h0], s[40:41] sc1\nglobal_load_dwordx4 v[20:23], %[h1], s[40:41] sc1\ns_add_u32 %[ro], %[ro], 1024\n"
        "s_cmp_eq_u32 %[ro], 14336\ns_cselect_b32 %[ro], 0, %[ro]\ns_add_u32 s40, s44, %[ro]\ns_addc_u32 s41, s45, 0\nglobal_load_dwordx4 v[24:27], %[h0], s[40:41] sc1\n"
        "global_load_dwordx4 v[28:31], %[h1], s[40:41] sc1\ns_add_u32 %[ro], %[ro], 1024\ns_cmp_eq_u32 %[ro], 14336\ns_cselect_b32 %[ro], 0, %[ro]\ns_waitcnt vmcnt(8)\n"
        "ds_read_b64_tr_b16 v[96:97], %[a0] offset:0\nds_read_b64_tr_b16 v[98:99], %[a0] offset:1024\nds_read_b64_tr_b16 v[100:101], %[a1] offset:0\n"
        "ds_read_b64_tr_b16 v[102:103], %[a1] offset:1024\ns_waitcnt vmcnt(6) lgkmcnt(0)\nv_mfma_f32_32x32x16_bf16 v[32:47], v[104:107], v[112:115], 0\n"
        "ds_read_b64_tr_b16 v[104:105], %[a0] offset:2048\nds_read_b64_tr_b16 v[106:107], %[a0] offset:3072\ns_lshl_b32 s42, %[ro], 4\ns_add_u32 s42, s46, s42\n"
        "s_addc_u32 s43, s47, 0\ns_add_u32 m0, %[dw], 0\nv_mfma_f32_32x32x16_bf16 v[48:63], v[108:111], v[112:115], 0\nds_read_b64_tr_b16 v[108:109], %[a1] offset:2048\n"
        "ds_read_b64_tr_b16 v[110:111], %[a1] offset:3072\nglobal_load_lds_dwordx4 %[w0], s[42:43]\nv_mfma_f32_32x32x16_bf16 v[64:79], v[104:107], v[116:119], 0\n"
        "global_load_lds_dwordx4 %[w1], s[42:43] offset:1024\nv_mfma_f32_32x32x16_bf16 v[80:95], v[108:111], v[116:119], 0\ns_add_u32 s40, s44, %[ro]\ns_addc_u32 s41, s45, 0\n"
        "global_load_dwordx4 v[112:115], %[h0], s[40:41] sc1\nglobal_load_dwordx4 v[116:119], %[h1], s[40:41] sc1\ns_add_u32 %[ro], %[ro], 1024\ns_cmp_eq_u32 %[ro], 14336\n"
        "s_cselect_b32 %[ro], 0, %[ro]\ns_waitcnt vmcnt(8) lgkmcnt(0)\nv_mfma_f32_32x32x16_bf16 v[32:47], v[96:99], v[120:123], v[32:47]\n"
        "ds_read_b64_tr_b16 v[96:97], %[a0] offset:4096\nds_read_b64_tr_b16 v[98:99], %[a0] offset:5120\ns_lshl_b32 s42, %[ro], 4\ns_add_u32 s42, s46, s42\ns_addc_u32 s43, s47, 0\n"
        "s_add_u32 m0, %[dw], 2048\nv_mfma_f32_32x32x16_bf16 v[48:63], v[100:103], v[120:123], v[48:63]\nds_read_b64_tr_b16 v[100:101], %[a1] offset:4096\n"
        "ds_read_b64_tr_b16 v[102:103], %[a1] offset:5120\nglobal_load_lds_dwordx4 %[w0], s[42:43]\nv_mfma_f32_32x32x16_bf16 v[64:79], v[96:99], v[124:127], v[64:79]\n"
        "global_load_lds_dwordx4 %[w1], s[42:43] offset:1024\nv_mfma_f32_32x32x16_bf16 v[80:95], v[100:103], v[124:127], v[80:95]\ns_add_u32 s40, s44, %[ro]\n"
        "s_addc_u32 s41, s45, 0\nglobal_load_dwordx4 v[120:123], %[h0], s[40:41] sc1\nglobal_load_dwordx4 v[124:127], %[h1], s[40:41] sc1\ns_add_u32 %[ro], %[ro], 1024\n"
        "s_cmp_eq_u32 %[ro], 14336\ns_cselect_b32 %[ro], 0, %[ro]\ns_waitcnt vmcnt(10) lgkmcnt(0)\nv_mfma_f32_32x32x16_bf16 v[32:47], v[104:107], v[16:19], v[32:47]\n"
        "ds_read_b64_tr_b16 v[104:105], %[a0] offset:6144\nds_read_b64_tr_b16 v[106:107], %[a0] offset:7168\ns_lshl_b32 s42, %[ro], 4\ns_add_u32 s42, s46, s42\n"
        "s_addc_u32 s43, s47, 0\ns_add_u32 m0, %[dw], 4096\nv_mfma_f32_32x32x16_bf16 v[48:63], v[108:111], v[16:19], v[48:63]\nds_read_b64_tr_b16 v[108:109], %[a1] offset:6144\n"
        "ds_read_b64_tr_b16 v[110:111], %[a1] offset:7168\nglobal_load_lds_dwordx4 %[w0], s[42:43]\nv_mfma_f32_32x32x16_bf16 v[64:79], v[104:107], v[20:23], v[64:79]\n"
        "global_load_lds_dwordx4 %[w1], s[42:43] offset:1024\nv_mfma_f32_32x32x16_bf16 v[80:95], v[108:111], v[20:23], v[80:95]\ns_add_u32 s40, s44, %[ro]\ns_addc_u32 s41, s45, 0\n"
        "global_load_dwordx4 v[16:19], %[h0], s[40:41] sc1\nglobal_load_dwordx4 v[20:23], %[h1], s[40:41] sc1\ns_add_u32 %[ro], %[ro], 1024\ns_cmp_eq_u32 %[ro], 14336\n"
        "s_cselect_b32 %[ro], 0, %[ro]\ns_waitcnt vmcnt(10) lgkmcnt(0)\nv_mfma_f32_32x32x16_bf16 v[32:47], v[96:99], v[24:27], v[32:47]\n"
        "ds_read_b64_tr_b16 v[96:97], %[a0] offset:0\nds_read_b64_tr_b16 v[98:99], %[a0] offset:1024\ns_lshl_b32 s42, %[ro], 4\ns_add_u32 s42, s46, s42\ns_addc_u32 s43, s47, 0\n"
        "s_add_u32 m0, %[dw], 6144\nv_mfma_f32_32x32x16_bf16 v[48:63], v[100:103], v[24:27], v[48:63]\nds_read_b64_tr_b16 v[100:101], %[a1] offset:0\n"
        "ds_read_b64_tr_b16 v[102:103], %[a1] offset:1024\nglobal_load_lds_dwordx4 %[w0], s[42:43]\nv_mfma_f32_32x32x16_bf16 v[64:79], v[96:99], v[28:31], v[64:79]\n"
        "global_load_lds_dwordx4 %[w1], s[42:43] offset:1024\nv_mfma_f32_32x32x16_bf16 v[80:95], v[100:103], v[28:31], v[80:95]\ns_add_u32 s40, s44, %[ro]\ns_addc_u32 s41, s45, 0\n"
        "global_load_dwordx4 v[24:27], %[h0], s[40:41] sc1\nglobal_load_dwordx4 v[28:31], %[h1], s[40:41] sc1\ns_add_u32 %[ro], %[ro], 1024\ns_cmp_eq_u32 %[ro], 14336\n"
        "s_cselect_b32 %[ro], 0, %[ro]\ns_waitcnt vmcnt(10) lgkmcnt(0)\nv_mfma_f32_32x32x16_bf16 v[32:47], v[104:107], v[112:115], v[32:47]\n"
        "ds_read_b64_tr_b16 v[104:105], %[a0] offset:2048\nds_read_b64_tr_b16 v[106:107], %[a0] offset:3072\ns_lshl_b32 s42, %[ro], 4\ns_add_u32 s42, s46, s42\n"
        "s_addc_u32 s43, s47, 0\ns_add_u32 m0, %[dw], 0\nv_mfma_f32_32x32x16_bf16 v[48:63], v[108:111], v[112:115], v[48:63]\nds_read_b64_tr_b16 v[108:109], %[a1] offset:2048\n"
        "ds_read_b64_tr_b16 v[110:111], %[a1] offset:3072\nglobal_load_lds_dwordx4 %[w0], s[42:43]\nv_mfma_f32_32x32x16_bf16 v[64:79], v[104:107], v[116:119], v[64:79]\n"
        "global_load_lds_dwordx4 %[w1], s[42:43] offset:1024\nv_mfma_f32_32x32x16_bf16 v[80:95], v[108:111], v[116:119], v[80:95]\ns_add_u32 s40, s44, %[ro]\n"
        "s_addc_u32 s41, s45, 0\nglobal_load_dwordx4 v[112:115], %[h0], s[40:41] sc1\nglobal_load_dwordx4 v[116:119], %[h1], s[40:41] sc1\ns_add_u32 %[ro], %[ro], 1024\n"
        "s_cmp_eq_u32 %[ro], 14336\ns_cselect_b32 %[ro], 0, %[ro]\ns_waitcnt vmcnt(10) lgkmcnt(0)\nv_mfma_f32_32x32x16_bf16 v[32:47], v[96:99], v[120:123], v[32:47]\n"
        "ds_read_b64_tr_b16 v[96:97], %[a0] offset:4096\nds_read_b64_tr_b16 v[98:99], %[a0] offset:5120\ns_lshl_b32 s42, %[ro], 4\ns_add_u32 s42, s46, s42\ns_addc_u32 s43, s47, 0\n"
        "s_add_u32 m0, %[dw], 2048\nv_mfma_f32_32x32x16_bf16 v[48:63], v[100:103], v[120:123], v[48:63]\nds_read_b64_tr_b16 v[100:101], %[a1] offset:4096\n"
        "ds_read_b64_tr_b16 v[102:103], %[a1] offset:5120\nglobal_load_lds_dwordx4 %[w0], s[42:43]\nv_mfma_f32_32x32x16_bf16 v[64:79], v[96:99], v[124:127], v[64:79]\n"
        "global_load_lds_dwordx4 %[w1], s[42:43] offset:1024\nv_mfma_f32_32x32x16_bf16 v[80:95], v[100:103], v[124:127], v[80:95]\ns_add_u32 s40, s44, %[ro]\n"
        "s_addc_u32 s41, s45, 0\nglobal_load_dwordx4 v[120:123], %[h0], s[40:41] sc1\nglobal_load_dwordx4 v[124:127], %[h1], s[40:41] sc1\ns_add_u32 %[ro], %[ro], 1024\n"
        "s_cmp_eq_u32 %[ro], 14336\ns_cselect_b32 %[ro], 0, %[ro]\ns_waitcnt vmcnt(10) lgkmcnt(0)\nv_mfma_f32_32x32x16_bf16 v[32:47], v[104:107], v[16:19], v[32:47]\n"
        "ds_read_b64_tr_b16 v[104:105], %[a0] offset:6144\nds_read_b64_tr_b16 v[106:107], %[a0] offset:7168\ns_lshl_b32 s42, %[ro], 4\ns_add_u32 s42, s46, s42\n"
        "s_addc_u32 s43, s47, 0\ns_add_u32 m0, %[dw], 4096\nv_mfma_f32_32x32x16_bf16 v[48:63], v[108:111], v[16:19], v[48:63]\nds_read_b64_tr_b16 v[108:109], %[a1] offset:6144\n"
        "ds_read_b64_tr_b16 v[110:111], %[a1] offset:7168\nglobal_load_lds_dwordx4 %[w0], s[42:43]\nv_mfma_f32_32x32x16_bf16 v[64:79], v[104:107], v[20:23], v[64:79]\n"
        "global_load_lds_dwordx4 %[w1], s[42:43] offset:1024\nv_mfma_f32_32x32x16_bf16 v[80:95], v[108:111], v[20:23], v[80:95]\ns_add_u32 s40, s44, %[ro]\ns_addc_u32 s41, s45, 0\n"
        "global_load_dwordx4 v[16:19], %[h0], s[40:41] sc1\nglobal_load_dwordx4 v[20:23], %[h1], s[40:41] sc1\ns_add_u32 %[ro], %[ro], 1024\ns_cmp_eq_u32 %[ro], 14336\n"
        "s_cselect_b32 %[ro], 0, %[ro]\ns_waitcnt vmcnt(10) lgkmcnt(0)\nv_mfma_f32_32x32x16_bf16 v[32:47], v[96:99], v[24:27], v[32:47]\n"
        "ds_read_b64_tr_b16 v[96:97], %[a0] offset:0\nds_read_b64_tr_b16 v[98:99], %[a0] offset:1024\ns_lshl_b32 s42, %[ro], 4\ns_add_u32 s42, s46, s42\ns_addc_u32 s43, s47, 0\n"
        "s_add_u32 m0, %[dw], 6144\nv_mfma_f32_32x32x16_bf16 v[48:63], v[100:103], v[24:27], v[48:63]\nds_read_b64_tr_b16 v[100:101], %[a1] offset:0\n"
        "ds_read_b64_tr_b16 v[102:103], %[a1] offset:1024\nglobal_load_lds_dwordx4 %[w0], s[42:43]\nv_mfma_f32_32x32x16_bf16 v[64:79], v[96:99], v[28:31], v[64:79]\n"
        "global_load_lds_dwordx4 %[w1], s[42:43] offset:1024\nv_mfma_f32_32x32x16_bf16 v[80:95], v[100:103], v[28:31], v[80:95]\ns_add_u32 s40, s44, %[ro]\ns_addc_u32 s41, s45, 0\n"
        "global_load_dwordx4 v[24:27], %[h0], s[40:41] sc1\nglobal_load_dwordx4 v[28:31], %[h1], s[40:41] sc1\ns_add_u32 %[ro], %[ro], 1024\ns_cmp_eq_u32 %[ro], 14336\n"
        "s_cselect_b32 %[ro], 0, %[ro]\ns_waitcnt vmcnt(10) lgkmcnt(0)\nv_mfma_f32_32x32x16_bf16 v[32:47], v[104:107], v[112:115], v[32:47]\n"
        "ds_read_b64_tr_b16 v[104:105], %[a0] offset:2048\nds_read_b64_tr_b16 v[106:107], %[a0] offset:3072\ns_lshl_b32 s42, %[ro], 4\ns_add_u32 s42, s46, s42\n"
        "s_addc_u32 s43, s47, 0\ns_add_u32 m0, %[dw], 0\nv_mfma_f32_32x32x16_bf16 v[48:63], v[108:111], v[112:115], v[48:63]\nds_read_b64_tr_b16 v[108:109], %[a1] offset:2048\n"
        "ds_read_b64_tr_b16 v[110:111], %[a1] offset:3072\nglobal_load_lds_dwordx4 %[w0], s[42:43]\nv_mfma_f32_32x32x16_bf16 v[64:79], v[104:107], v[116:119], v[64:79]\n"
        "global_load_lds_dwordx4 %[w1], s[42:43] offset:1024\nv_mfma_f32_32x32x16_bf16 v[80:95], v[108:111], v[116:119], v[80:95]\ns_add_u32 s40, s44, %[ro]\n"
        "s_addc_u32 s41, s45, 0\nglobal_load_dwordx4 v[112:115], %[h0], s[40:41] sc1\nglobal_load_dwordx4 v[116:119], %[h1], s[40:41] sc1\ns_add_u32 %[ro], %[ro], 1024\n"
        "s_cmp_eq_u32 %[ro], 14336\ns_cselect_b32 %[ro], 0, %[ro]\ns_waitcnt vmcnt(10) lgkmcnt(0)\nv_mfma_f32_32x32x16_bf16 v[32:47], v[96:99], v[120:123], v[32:47]\n"
        "ds_read_b64_tr_b16 v[96:97], %[a0] offset:4096\nds_read_b64_tr_b16 v[98:99], %[a0] offset:5120\ns_lshl_b32 s42, %[ro], 4\ns_add_u32 s42, s46, s42\ns_addc_u32 s43, s47, 0\n"
        "s_add_u32 m0, %[dw], 2048\nv_mfma_f32_32x32x16_bf16 v[48:63], v[100:103], v[120:123], v[48:63]\nds_read_b64_tr_b16 v[100:101], %[a1] offset:4096\n"
        "ds_read_b64_tr_b16 v[102:103], %[a1] offset:5120\nglobal_load_lds_dwordx4 %[w0], s[42:43]\nv_mfma_f32_32x32x16_bf16 v[64:79], v[96:99], v[124:127], v[64:79]\n"
        "global_load_lds_dwordx4 %[w1], s[42:43] offset:1024\nv_mfma_f32_32x32x16_bf16 v[80:95], v[100:103], v[124:127], v[80:95]\ns_add_u32 s40, s44, %[ro]\n"
        "s_addc_u32 s41, s45, 0\nglobal_load_dwordx4 v[120:123], %[h0], s[40:41] sc1\nglobal_load_dwordx4 v[124:127], %[h1], s[40:41] sc1\ns_add_u32 %[ro], %[ro], 1024\n"
        "s_cmp_eq_u32 %[ro], 14336\ns_cselect_b32 %[ro], 0, %[ro]\ns_waitcnt vmcnt(10) lgkmcnt(0)\nv_mfma_f32_32x32x16_bf16 v[32:47], v[104:107], v[16:19], v[32:47]\n"
        "ds_read_b64_tr_b16 v[104:105], %[a0] offset:6144\nds_read_b64_tr_b16 v[106:107], %[a0] offset:7168\nv_mfma_f32_32x32x16_bf16 v[48:63], v[108:111], v[16:19], v[48:63]\n"
        "ds_read_b64_tr_b16 v[108:109], %[a1] offset:6144\nds_read_b64_tr_b16 v[110:111], %[a1] offset:7168\nv_mfma_f32_32x32x16_bf16 v[64:79], v[104:107], v[20:23], v[64:79]\n"
        "v_mfma_f32_32x32x16_bf16 v[80:95], v[108:111], v[20:23], v[80:95]\ns_waitcnt vmcnt(6) lgkmcnt(0)\nv_mfma_f32_32x32x16_bf16 v[32:47], v[96:99], v[24:27], v[32:47]\n"
        "ds_read_b64_tr_b16 v[96:97], %[a0] offset:0\nds_read_b64_tr_b16 v[98:99], %[a0] offset:1024\nv_mfma_f32_32x32x16_bf16 v[48:63], v[100:103], v[24:27], v[48:63]\n"
        "ds_read_b64_tr_b16 v[100:101], %[a1] offset:0\nds_read_b64_tr_b16 v[102:103], %[a1] offset:1024\nv_mfma_f32_32x32x16_bf16 v[64:79], v[96:99], v[28:31], v[64:79]\n"
        "v_mfma_f32_32x32x16_bf16 v[80:95], v[100:103], v[28:31], v[80:95]\ns_waitcnt vmcnt(2) lgkmcnt(0)\nv_mfma_f32_32x32x16_bf16 v[32:47], v[104:107], v[112:115], v[32:47]\n"
        "ds_read_b64_tr_b16 v[104:105], %[a0] offset:2048\nds_read_b64_tr_b16 v[106:107], %[a0] offset:3072\nv_mfma_f32_32x32x16_bf16 v[48:63], v[108:111], v[112:115], v[48:63]\n"
        "ds_read_b64_tr_b16 v[108:109], %[a1] offset:2048\nds_read_b64_tr_b16 v[110:111], %[a1] offset:3072\nv_mfma_f32_32x32x16_bf16 v[64:79], v[104:107], v[116:119], v[64:79]\n"
        "v_mfma_f32_32x32x16_bf16 v[80:95], v[108:111], v[116:119], v[80:95]\ns_waitcnt vmcnt(0) lgkmcnt(0)\nv_mfma_f32_32x32x16_bf16 v[32:47], v[104:107], v[120:123], v[32:47]\n"
        "v_mfma_f32_32x32x16_bf16 v[48:63], v[108:111], v[120:123], v[48:63]\nv_mfma_f32_32x32x16_bf16 v[64:79], v[104:107], v[124:127], v[64:79]\n"
        "v_mfma_f32_32x32x16_bf16 v[80:95], v[108:111], v[124:127], v[80:95]\ns_mov_b32 m0, %[sv]\ns_nop 7\ns_nop 7\n"
          : "=&{v[32:47]}"(d00), "=&{v[48:63]}"(d01), "=&{v[64:79]}"(d10), "=&{v[80:95]}"(d11), [ro] "+s"(ro), [sv] "=&s"(sv)
          : [a0] "v"(dw + at), [a1] "v"(dw + (at ^ 64)), [h0] "v"(l16), [h1] "v"(l16 + 114688), [w0] "v"(vo), [w1] "v"(vo + 7168), [dw] "s"(dw), [sh] "s"(sh), [sw] "s"(sw)
          : "v96", "v97", "v98", "v99", "v100", "v101", "v102", "v103", "v104", "v105", "v106", "v107", "v108", "v109", "v110", "v111", "v112", "v113", "v114", "v115", "v116", "v117", "v118", "v119", "v120", "v121", "v122", "v123", "v124", "v125", "v126", "v127", "v16", "v17", "v18", "v19", "v20", "v21", "v22", "v23", "v24", "v25", "v26", "v27", "v28", "v29", "v30", "v31", "s40", "s41", "s42", "s43", "s44", "s45", "s46", "s47", "scc", "memory");
        PRIO(P_EPI);
        TS(256 + b, 4);
        asm volatile("global_load_dwordx2 %0, %2, %3\n\tglobal_load_dwordx2 %1, %2, %3 offset:16" : "=&v"(x0), "=&v"(x1) : "v"(ob + 8 * h), "s"(x) : "memory");
        const lptr pw = L + w * 2048 + l16, pr = L + kq * 16384 + l16; f32x4 a, c;
#define F2_RED(dA, dB, tt) do { BARL(); _Pragma("unroll") for (int u = 0; u < 4; ++u) { lw128f(pw + (u >> 1) * 16384 + (u & 1) * 1024, dA[4 * u], dA[4 * u + 1], dA[4 * u + 2], dA[4 * u + 3]); \
            lw128f(pw + 32768 + (u >> 1) * 16384 + (u & 1) * 1024, dB[4 * u], dB[4 * u + 1], dB[4 * u + 2], dB[4 * u + 3]); } BARL(); \
            if (t == tt) { a = lr128f(pr); c = lr128f(pr + 1024); _Pragma("unroll") for (int u = 1; u < 8; ++u) { a += lr128f(pr + u * 2048); c += lr128f(pr + u * 2048 + 1024); } } } while (0)
        F2_RED(d00, d01, 0); F2_RED(d10, d11, 1);
#pragma unroll
        for (int i = 0; i < 4; ++i) { gg[i] = a[i] * __builtin_amdgcn_rcpf(1.f + __builtin_amdgcn_exp2f(a[i] * -1.4426950408889634f));
                                      gg[4 + i] = c[i] * __builtin_amdgcn_rcpf(1.f + __builtin_amdgcn_exp2f(c[i] * -1.4426950408889634f)); } }
    BARL(); TS(256 + b, 15);
#pragma unroll
    for (int n = 0; n < 8; ++n) glds16(vo, wo + cg * 128 + (w * 8 + n) * 8192, L0 + (w * 8 + n) * 1024);
    BAR(0); asm volatile("" : "+v"(x0), "+v"(x1)); TS(256 + b, 5);
    wait_flags(ws, FL_O + tg * 8 + 2 * kq + (l & 1), tag);
    TS(256 + b, 6);
    bf16x8 of[8];
    { const char* so = uni(ws + WS_O + ((2 * tg + t) * 32 + 8 * kq) * 1024);
#pragma unroll
      for (int s = 0; s < 8; ++s) LD_SC1(of[s], l16, so + s * 1024); }
    f32x16 c0 = splat16(0.f), c1 = c0;
    const lptr a0 = L + kq * 16384 + at, a1 = L + kq * 16384 + (at ^ 64);
#pragma unroll
    for (int s = 0; s < 8; ++s) { const bf16x8 f0 = ltr(a0, s * 2048, s * 2048 + 1024), f1 = ltr(a1, s * 2048, s * 2048 + 1024);
        asm volatile("s_waitcnt vmcnt(%1)" : "+v"(of[s]) : "n"(7 - s) : "memory");
        c0 = MFMA(f0, of[s], c0); c1 = MFMA(f1, of[s], c1); }
    BARL();
    const lptr pw = L + (t * 16 + kq) * 2048 + l16;
#pragma unroll
    for (int u = 0; u < 4; ++u) { lw128f(pw + (u >> 1) * 8192 + (u & 1) * 1024, c0[4 * u], c0[4 * u + 1], c0[4 * u + 2], c0[4 * u + 3]);
                                  lw128f(pw + 16384 + (u >> 1) * 8192 + (u & 1) * 1024, c1[4 * u], c1[4 * u + 1], c1[4 * u + 2], c1[4 * u + 3]); }
    BARL();
    const lptr pr = L + (t * 16 + kq * 4) * 2048 + l16; f32x4 a = lr128f(pr), c = lr128f(pr + 1024);
#pragma unroll
    for (int u = 1; u < 4; ++u) { a += lr128f(pr + u * 2048); c += lr128f(pr + u * 2048 + 1024); }
    const unsigned A0 = pk(blo(x0[0]) + a[0] * gg[0], bhi(x0[0]) + a[1] * gg[1]), A1 = pk(blo(x0[1]) + a[2] * gg[2], bhi(x0[1]) + a[3] * gg[3]);
    const unsigned B0 = pk(blo(x1[0]) + c[0] * gg[4], bhi(x1[0]) + c[1] * gg[5]), B1 = pk(blo(x1[1]) + c[2] * gg[6], bhi(x1[1]) + c[3] * gg[7]);
    auto s0 = __builtin_amdgcn_permlane32_swap(A0, B0, false, false), s1 = __builtin_amdgcn_permlane32_swap(A1, B1, false, false);
    const u32x4 ov = {s0[0], s1[0], s0[1], s1[1]};
    if (WT & 2) asm volatile("global_store_dwordx4 %0, %1, %2 sc0 sc1" ::"v"(ob + 16 * h), "v"(ov), "s"(out) : "memory"); else *(u32x4*)(out + ob + 16 * h) = ov;
    TS(256 + b, 7);
}

__global__ __launch_bounds__(512, 4) void k2_kernel(const char* x, const char* wq, const char* wk, const char* wv, const char* wo, const char* wg, const char* wu, const char* wd, char* out, char* ws) {
    extern __shared__ char smem[];
    const lptr L = (lptr)smem; const unsigned L0 = (unsigned)(size_t)L;
    const int b = blockIdx.x;
    PRIO(3); TS(b, 0);
    if (b < 256) attn_role(b, L, L0, wq, ws); else ffn_role(b - 256, L, L0, x, wk, wv, wo, wg, wu, wd, out, ws);
}

extern "C" void kernel_launch(void* const* d_in, const int* in_sizes, int n_in, void* d_out, int out_size, void* d_ws, size_t ws_size, hipStream_t stream) {
    (void)hipFuncSetAttribute((const void*)k2_kernel, hipFuncAttributeMaxDynamicSharedMemorySize, LDS_K2);
    k2_kernel<<<512, 512, LDS_K2, stream>>>((const char*)d_in[0], (const char*)d_in[1], (const char*)d_in[2], (const char*)d_in[3], (const char*)d_in[4], (const char*)d_in[6], (const char*)d_in[7], (const char*)d_in[8], (char*)d_out, (char*)d_ws);
}
```

```cpp
#include <hip/hip_runtime.h>
#include <hip/hip_bf16.h>
#include <stdint.h>

typedef short bf16x8 __attribute__((ext_vector_type(8)));
typedef short s16x4 __attribute__((ext_vector_type(4)));
typedef float f32x16 __attribute__((ext_vector_type(16)));
typedef float f32x4 __attribute__((ext_vector_type(4)));
typedef float f32x2 __attribute__((ext_vector_type(2)));
typedef __bf16 b16x2 __attribute__((ext_vector_type(2)));
typedef unsigned u32x4 __attribute__((ext_vector_type(4)));
typedef unsigned u32x2 __attribute__((ext_vector_type(2)));
#define LAS __attribute__((address_space(3)))
#define GAS __attribute__((address_space(1)))
#define DI __device__ __forceinline__
#define MFMA(a, b, c) __builtin_amdgcn_mfma_f32_32x32x16_bf16((a), (b), (c), 0, 0, 0)
#define RLX_AGENT __ATOMIC_RELAXED, __HIP_MEMORY_SCOPE_AGENT
typedef LAS char* lptr;

constexpr unsigned WS_XN = 0, WS_KV = 2u << 20, WS_H = 4u << 20, WS_O = 12u << 20, WS_FL = 16u << 20;
constexpr int FL_O = 0, FL_H = 256, FL_X = 512, FL_K = 640, FL_A = 768, FL_E = 1280, FL_R = 1288;
constexpr int LDS_K2 = 81920;
constexpr unsigned SPIN_CAP = 1u << 20;
#define P_LOOP 0
#define P_LOOP2 0
#define P_ATT 1
#define P_EPI 3
#define DLY 48
#define PSL 10
#define FDL 80
#define FS 128
#define PRIO(n) __builtin_amdgcn_s_setprio(n)

DI unsigned pk(float a, float b) { f32x2 v = {a, b}; return __builtin_bit_cast(unsigned, __builtin_convertvector(v, b16x2)); }
DI bf16x8 pack8(const f32x16& x, int o) { u32x4 r = {pk(x[o], x[o + 1]), pk(x[o + 2], x[o + 3]), pk(x[o + 4], x[o + 5]), pk(x[o + 6], x[o + 7])}; return __builtin_bit_cast(bf16x8, r); }
DI float blo(unsigned u) { return __uint_as_float(u << 16); }
DI float bhi(unsigned u) { return __uint_as_float(u & 0xffff0000u); }
DI bf16x8 lr128(lptr p) { return *(const LAS bf16x8*)p; }
DI f32x4 lr128f(lptr p) { return *(const LAS f32x4*)p; }
DI void lw128f(lptr p, float a, float b, float c, float d) { f32x4 v = {a, b, c, d}; *(LAS f32x4*)p = v; }
DI bf16x8 ltr(lptr p, int o0, int o1) {
    s16x4 a = __builtin_amdgcn_ds_read_tr16_b64_v4i16((LAS s16x4*)(p + o0)), b = __builtin_amdgcn_ds_read_tr16_b64_v4i16((LAS s16x4*)(p + o1));
    return __builtin_shufflevector(a, b, 0, 1, 2, 3, 4, 5, 6, 7); }
DI void glds16(unsigned voff, const void* sbase, unsigned lds) {
    unsigned sv; asm volatile("s_mov_b32 %0, m0\n\ts_mov_b32 m0, %3\n\ts_nop 0\n\tglobal_load_lds_dwordx4 %1, %2\n\ts_mov_b32 m0, %0" : "=&s"(sv) : "v"(voff), "s"(sbase), "s"(lds) : "memory"); }
DI void glds16c(unsigned voff, const void* sbase, unsigned lds) {
    unsigned sv; asm volatile("s_mov_b32 %0, m0\n\ts_mov_b32 m0, %3\n\ts_nop 0\n\tglobal_load_lds_dwordx4 %1, %2 sc1\n\ts_mov_b32 m0, %0" : "=&s"(sv) : "v"(voff), "s"(sbase), "s"(lds) : "memory"); }
DI const char* uni(const char* p) { const unsigned long long a = (unsigned long long)p; return (const char*)(((unsigned long long)(unsigned)__builtin_amdgcn_readfirstlane((unsigned)(a >> 32)) << 32) | (unsigned)__builtin_amdgcn_readfirstlane((unsigned)a)); }
#define BAR(N) asm volatile("s_waitcnt vmcnt(" #N ") lgkmcnt(0)\n\ts_barrier" ::: "memory")
#define BARL() asm volatile("s_waitcnt lgkmcnt(0)\n\ts_barrier" ::: "memory")
DI void st_sc1(unsigned voff, void* sbase, bf16x8 v) { asm volatile("global_store_dwordx4 %0, %1, %2 sc1\n\ts_nop 1" ::"v"(voff), "v"(v), "s"(sbase) : "memory"); }
#define LD_SC1(dst, voff, sbase) asm volatile("global_load_dwordx4 %0, %1, %2 sc1" : "=v"(dst) : "v"(voff), "s"(sbase) : "memory")
DI bf16x8 ldc(const char* p) { bf16x8 v; asm volatile("global_load_dwordx4 %0, %1, off sc1\n\ts_waitcnt vmcnt(0)" : "=v"(v) : "v"(p) : "memory"); return v; }
#define touch(voff, sbase) asm volatile("global_load_dword %0, %1, %2" : "+v"(jk) : "v"(voff), "s"(sbase) : "memory")
#define TOUCH_END() asm volatile("s_waitcnt vmcnt(0)" : "+v"(jk)::"memory")
template <int C> DI float dpp(float x) { return __builtin_bit_cast(float, __builtin_amdgcn_mov_dpp(__builtin_bit_cast(int, x), C, 0xf, 0xf, true)); }
DI float sum32(float x) {
    x += dpp<0xB1>(x); x += dpp<0x4E>(x); x += dpp<0x141>(x); x += dpp<0x140>(x);
    auto s = __builtin_amdgcn_permlane16_swap(__float_as_uint(x), __float_as_uint(x), false, false);
    return __uint_as_float(s[0]) + __uint_as_float(s[1]); }
DI float xhalf_max(float m) { auto r = __builtin_amdgcn_permlane32_swap(__float_as_uint(m), __float_as_uint(m), false, false); return fmaxf(__uint_as_float(r[0]), __uint_as_float(r[1])); }
DI float max16(const f32x16& s, float m) {
#pragma unroll
    for (int i = 0; i < 16; i += 2) m = fmaxf(fmaxf(m, s[i]), s[i + 1]);
    return m; }
DI f32x16 splat16(float v) { f32x16 r;
#pragma unroll
    for (int i = 0; i < 16; ++i) r[i] = v;
    return r; }

typedef unsigned long long u64;
DI u64 mk_tag(u64 e) { e = (u64)uni((const char*)e); return e * 0x9E3779B97F4A7C15ull + 0x632BE59BD9B4E019ull; }
DI void set_flag(char* ws, int f, u64 tag) { __hip_atomic_store((GAS u64*)(ws + WS_FL + f * FS), tag, RLX_AGENT); }
DI void wait_flags(char* ws, int f, u64 tag) {
    GAS u64* p = (GAS u64*)(ws + WS_FL + f * FS);
    for (unsigned sp = 0; sp < SPIN_CAP; ++sp) { if (__builtin_amdgcn_ballot_w64(__hip_atomic_load(p, RLX_AGENT) != tag) == 0ull) break; __builtin_amdgcn_s_sleep(PSL); } }

DI u64 ln_kv(int tok0, const lptr L, unsigned L0, const char* x, const char* wk, const char* wv, char* ws) {
    const int tid = threadIdx.x, l = tid & 63, w = __builtin_amdgcn_readfirstlane(tid >> 6), r = l & 31, h = l >> 5;
    constexpr int WK = 8192, WV = 32768, XL = 57344, RED = 8192;
    const int tl = 2 * w + h;
    u32x4 xa, xb; u64 ep;
    asm volatile("global_load_dwordx4 %0, %2, %3\n\tglobal_load_dwordx4 %1, %2, %3 offset:512" : "=&v"(xa), "=&v"(xb) : "v"((tok0 + tl) * 1024 + r * 16), "s"(x) : "memory");
    glds16(l * 16, wk + w * 2048, L0 + WK + w * 2048); glds16(l * 16, wk + w * 2048 + 1024, L0 + WK + w * 2048 + 1024);
    glds16(l * 16, wv + w * 2048, L0 + WV + w * 2048); glds16(l * 16, wv + w * 2048 + 1024, L0 + WV + w * 2048 + 1024);
    asm volatile("global_load_dwordx2 %0, %3, %4 sc1\n\ts_waitcnt vmcnt(5)" : "=&v"(ep), "+v"(xa), "+v"(xb) : "v"(0), "s"(ws + WS_FL + FL_E * FS) : "memory");
    float v[16];
#pragma unroll
    for (int i = 0; i < 4; ++i) { v[2 * i] = blo(xa[i]); v[2 * i + 1] = bhi(xa[i]); v[8 + 2 * i] = blo(xb[i]); v[9 + 2 * i] = bhi(xb[i]); }
    float s = 0.f;
#pragma unroll
    for (int i = 0; i < 16; ++i) s += v[i];
    const float mean = sum32(s) * (1.f / 512.f); float q2 = 0.f;
#pragma unroll
    for (int i = 0; i < 16; ++i) { v[i] -= mean; q2 += v[i] * v[i]; }
    const float rstd = __builtin_amdgcn_rsqf(sum32(q2) * (1.f / 512.f) + 1e-5f);
    u32x4 ya, yb;
#pragma unroll
    for (int i = 0; i < 4; ++i) { ya[i] = pk(v[2 * i] * rstd, v[2 * i + 1] * rstd); yb[i] = pk(v[8 + 2 * i] * rstd, v[9 + 2 * i] * rstd); }
    { const lptr p = L + XL + r * 256 + ((tl ^ (r & 15)) << 4); *(LAS u32x4*)p = ya; *(LAS u32x4*)(p + 8192) = yb; }
    BAR(0); asm volatile("" : "+v"(ep)); const u64 tag = mk_tag(ep);
    { const int c = tid >> 4, tk = tid & 15; const lptr p = L + XL + c * 256 + ((tk ^ (c & 15)) << 4);
      char* g = ws + WS_XN + (tok0 >> 5) * 32768; const unsigned o = c * 512 + ((tok0 & 16) + tk) * 16;
      st_sc1(o, g, lr128(p)); st_sc1(o, g + 16384, lr128(p + 8192)); }
    f32x16 aK = splat16(0.f), aV = splat16(0.f);
    const lptr wp = L + ((l & 16) ? WV : WK) + (64 * w + 8 * h + ((l >> 2) & 3)) * 32 + (l & 3) * 8;
#pragma unroll
    for (int s4 = 0; s4 < 4; ++s4) { const int c = 8 * w + 2 * s4 + h;
        const bf16x8 xf = lr128(L + XL + c * 256 + (((l ^ c) & 15) << 4)), wf = ltr(wp, s4 * 512, s4 * 512 + 128);
        aK = MFMA(wf, xf, aK); aV = MFMA(xf, wf, aV); }
    { const bool vv = l & 16; const lptr p = L + RED + w * 2048 + l * 16;
      lw128f(p, vv ? aV[0] : aK[0], vv ? aV[1] : aK[1], vv ? aV[2] : aK[2], vv ? aV[3] : aK[3]);
      lw128f(p + 1024, vv ? aV[4] : aK[4], vv ? aV[5] : aK[5], vv ? aV[6] : aK[6], vv ? aV[7] : aK[7]); }
    BARL();
    if (w == 0) { f32x4 a = lr128f(L + RED + l * 16), c = lr128f(L + RED + 1024 + l * 16);
#pragma unroll
        for (int i = 1; i < 8; ++i) { a += lr128f(L + RED + i * 2048 + l * 16); c += lr128f(L + RED + i * 2048 + 1024 + l * 16); }
        const u32x4 o = {pk(a[0], a[1]), pk(a[2], a[3]), pk(c[0], c[1]), pk(c[2], c[3])}; const int sh = (tok0 >> 4) & 1;
        const unsigned off = (l & 16) ? 1024 + sh * 512 + h * 256 + (l & 15) * 16 : (16 * sh + (l & 15) + 32 * h) * 16;
        st_sc1(off, ws + WS_KV + (tok0 >> 5) * 2048, __builtin_bit_cast(bf16x8, o)); }

    if (w == 0) { BAR(1); asm volatile("s_waitcnt vmcnt(0)" ::: "memory"); if (l == 0) set_flag(ws, FL_K + (tok0 >> 4), tag); }
    else { BAR(0); if (tid == 64) set_flag(ws, FL_X + (tok0 >> 4), tag); }
    return tag;
}

DI void attn_role(int b, const lptr L, unsigned L0, const char* wq, char* ws) {
    int tid = threadIdx.x; asm volatile("" : "+v"(tid));
    const int l = tid & 63, w = __builtin_amdgcn_readfirstlane(tid >> 6), h = l >> 5, t = w & 1, kq = w >> 1;
    const int i5 = b >> 3, tg = (b & 7) * 4 + (i5 & 1) + (i5 >> 4) * 2, hg = (i5 >> 1) & 7, TT = 2 * tg + t;
    constexpr int SL = 8320, TS_ = 2080;
    const unsigned l16 = l * 16;
    if (i5 < 16) __builtin_amdgcn_s_sleep(DLY);
    const u64 ep = __hip_atomic_load((GAS u64*)(ws + WS_FL + FL_E * FS), RLX_AGENT);

    { const unsigned vo = (l >> 3) * 1024 + (((l & 7) ^ (((l >> 4) & 1) << 2)) << 4);
#pragma unroll
      for (int n = 0; n < 8; ++n) glds16(vo, wq + hg * 128 + (w * 8 + n) * 8192, L0 + (w * 8 + n) * 1024); }
    const u64 tag = mk_tag(ep);
    wait_flags(ws, FL_X + 2 * TT + (l & 1), tag);
    bf16x8 xf[8];
    { const char* xp = uni(ws + WS_XN + (TT * 32 + 8 * kq) * 1024);
#pragma unroll
      for (int s = 0; s < 8; ++s) LD_SC1(xf[s], l16, xp + s * 1024); }
    const char* kvs = uni(ws + WS_KV + w * 1024); const unsigned kvd = L0 + (w >> 1) * TS_ + (w & 1) * 1024;
    const int fk = FL_R + (b & 7) * 4 + (i5 & 3);
    const bool early = __builtin_amdgcn_ballot_w64(__hip_atomic_load((GAS u64*)(ws + WS_FL + fk * FS), RLX_AGENT) != tag) == 0ull;
    asm volatile("s_waitcnt vmcnt(0)\n\ts_barrier" : "+v"(xf[0]), "+v"(xf[1]), "+v"(xf[2]), "+v"(xf[3]), "+v"(xf[4]), "+v"(xf[5]), "+v"(xf[6]), "+v"(xf[7])::"memory");
    if (early) glds16c(l16, kvs, kvd + 8 * SL);
    if (tid == 0) set_flag(ws, FL_A + b, tag);
    bf16x8 qf;
    { const int q = (l >> 2) & 3, p = l & 3, ch = ((l >> 4) & 1) * 2 + (p >> 1);
      const int a0 = kq * 16384 + (8 * h + q) * 128 + ((ch ^ ((q >> 1) << 2)) << 4) + 8 * (p & 1);
      f32x16 c0 = splat16(0.f), c1 = splat16(0.f);
#pragma unroll
      for (int s = 0; s < 8; ++s) { c0 = MFMA(ltr(L + a0, s * 2048, s * 2048 + 512), xf[s], c0); c1 = MFMA(ltr(L + (a0 ^ 64), s * 2048, s * 2048 + 512), xf[s], c1); }
      if (!early) { wait_flags(ws, fk, tag); glds16c(l16, kvs, kvd + 8 * SL); }
      BARL();
      glds16c(l16, kvs + 8192, kvd); glds16c(l16, kvs + 2 * 8192, kvd + SL);
      if (tid < 24) { const unsigned v = (tid & 1) ? 0u : 0x3F803F80u; const u32x4 one = {v, v, v, v}; *(LAS u32x4*)(L + ((tid >> 1) + (tid < 16 ? 0 : 24)) * TS_ + 2048 + (tid & 1) * 16) = one; }
      f32x4 a, c;
#pragma unroll
      for (int j = 0; j < 4; ++j) { const f32x16& s = j < 2 ? c0 : c1; const int o = 8 * (j & 1);
          if (j == kq) { a = {s[o], s[o + 1], s[o + 2], s[o + 3]}; c = {s[o + 4], s[o + 5], s[o + 6], s[o + 7]}; }
          else { const lptr pw = L + 2 * SL + ((t * 4 + j) * 3 + ((kq - j - 1) & 3)) * 2048 + l16; lw128f(pw, s[o], s[o + 1], s[o + 2], s[o + 3]); lw128f(pw + 1024, s[o + 4], s[o + 5], s[o + 6], s[o + 7]); } }
      BAR(2);
      const lptr pr = L + 2 * SL + (t * 4 + kq) * 6144 + l16;
#pragma unroll
      for (int u = 0; u < 3; ++u) { a += lr128f(pr + u * 2048); c += lr128f(pr + u * 2048 + 1024); }
      const float sc = 0.25f * 1.4426950408889634f; a *= sc; c *= sc;
      const u32x4 qq = {pk(a[0], a[1]), pk(a[2], a[3]), pk(c[0], c[1]), pk(c[2], c[3])}; qf = __builtin_bit_cast(bf16x8, qq); }
    f32x16 ns; const bf16x8 kf = lr128(L + 8 * SL + l16); bf16x8 k0 = lr128(L + 8 * SL + TS_ + l16), k1 = lr128(L + 8 * SL + 2 * TS_ + l16);
    { const f32x16 z = splat16(0.f); f32x16 s0 = MFMA(kf, qf, z), s1 = MFMA(k0, qf, z), s2 = MFMA(k1, qf, z), s3 = MFMA(lr128(L + 8 * SL + 3 * TS_ + l16), qf, z);
      asm volatile("" : "+v"(s0), "+v"(s1), "+v"(s2), "+v"(s3));
      const float m = fmaxf(max16(s1, max16(s0, -3.0e38f)), max16(s3, max16(s2, -3.0e38f)));
      ns = splat16(-(xhalf_max(m) + 90.f)); }
    f32x16 o = splat16(0.f);
    { const unsigned bk = L0 + l16, b0 = L0 + ((l & 16) ? 1024 + h * 256 + (l & 15) * 16 : (l & 11) ? 2064 : 2048), b1 = (l & 16) ? b0 + 512 : b0;
      f32x16 sA = MFMA(kf, qf, ns);
      bf16x8 v0 = lr128(L + 8 * SL + (b0 - L0)), v1 = lr128(L + 8 * SL + (b1 - L0)), vb = {0, 0, 0, 0, 0, 0, 0, 0}, pb = vb;
      unsigned aK = bk + 8 * SL, a0 = b0 + 8 * SL, a1 = b1 + 8 * SL, vo = l16 + 3 * 8192, so = 8 * SL, sd = 2 * SL, sn, c = 0, sv;
      BAR(1);
      if (tid < 48) { const unsigned v = (tid & 1) ? 0u : 0x3F803F80u; const u32x4 one = {v, v, v, v}; *(LAS u32x4*)(L + ((tid >> 1) + 8) * TS_ + 2048 + (tid & 1) * 16) = one; }
      unsigned hi = w >> 2, pc = 0; PRIO(P_ATT);
      asm volatile(
        "s_mov_b32 %[sv], m0\ns_nop 7\ns_nop 7\n9:\ns_add_u32 %[sn], %[so], 8320\ns_cmp_eq_u32 %[so], 66560\ns_cselect_b32 %[sn], 0, %[sn]\n"
        "v_mfma_f32_32x32x16_bf16 v[48:63], v[100:103], v[96:99], v[80:95]\nds_read_b128 v[100:103], v124 offset:6240\nv_exp_f32 v32, v32\nv_exp_f32 v33, v33\nv_exp_f32 v34, v34\n"
        "v_exp_f32 v35, v35\nv_exp_f32 v36, v36\nv_exp_f32 v37, v37\nv_mfma_f32_32x32x16_bf16 v[64:79], v[24:27], v[120:123], v[64:79]\nds_read_b128 v[24:27], v126 offset:2080\n"
        "v_exp_f32 v38, v38\nv_exp_f32 v39, v39\nv_cvt_pk_bf16_f32 v116, v32, v33\nv_cvt_pk_bf16_f32 v117, v34, v35\nv_cvt_pk_bf16_f32 v118, v36, v37\n"
        "v_cvt_pk_bf16_f32 v119, v38, v39\nv_exp_f32 v40, v40\nv_exp_f32 v41, v41\nv_exp_f32 v42, v42\ns_waitcnt lgkmcnt(2)\n"
        "v_mfma_f32_32x32x16_bf16 v[64:79], v[108:111], v[116:119], v[64:79]\nds_read_b128 v[108:111], v125 offset:2080\nv_add_u32 v127, %[sn], %[bk]\nv_add_u32 v31, %[sn], %[b0]\n"
        "v_add_u32 v30, %[sn], %[b1]\nv_exp_f32 v43, v43\nv_exp_f32 v44, v44\nv_exp_f32 v45, v45\nv_exp_f32 v46, v46\nv_exp_f32 v47, v47\nv_cvt_pk_bf16_f32 v120, v40, v41\n"
        "v_cvt_pk_bf16_f32 v121, v42, v43\nv_cvt_pk_bf16_f32 v122, v44, v45\nv_cvt_pk_bf16_f32 v123, v46, v47\nv_mfma_f32_32x32x16_bf16 v[32:47], v[104:107], v[96:99], v[80:95]\n"
        "ds_read_b128 v[104:107], v127 offset:0\nv_exp_f32 v48, v48\nv_exp_f32 v49, v49\nv_exp_f32 v50, v50\nv_exp_f32 v51, v51\nv_exp_f32 v52, v52\nv_exp_f32 v53, v53\n"
        "v_mfma_f32_32x32x16_bf16 v[64:79], v[112:115], v[120:123], v[64:79]\nds_read_b128 v[112:115], v126 offset:4160\nv_exp_f32 v54, v54\nv_exp_f32 v55, v55\n"
        "v_cvt_pk_bf16_f32 v116, v48, v49\nv_cvt_pk_bf16_f32 v117, v50, v51\nv_cvt_pk_bf16_f32 v118, v52, v53\nv_cvt_pk_bf16_f32 v119, v54, v55\nv_exp_f32 v56, v56\n"
        "v_exp_f32 v57, v57\nv_exp_f32 v58, v58\ns_waitcnt lgkmcnt(2)\nv_mfma_f32_32x32x16_bf16 v[64:79], v[108:111], v[116:119], v[64:79]\n"
        "ds_read_b128 v[108:111], v125 offset:4160\nv_exp_f32 v59, v59\nv_exp_f32 v60, v60\nv_exp_f32 v61, v61\nv_exp_f32 v62, v62\nv_exp_f32 v63, v63\n"
        "v_cvt_pk_bf16_f32 v120, v56, v57\nv_cvt_pk_bf16_f32 v121, v58, v59\nv_cvt_pk_bf16_f32 v122, v60, v61\nv_cvt_pk_bf16_f32 v123, v62, v63\n"
        "v_mfma_f32_32x32x16_bf16 v[48:63], v[100:103], v[96:99], v[80:95]\nds_read_b128 v[100:103], v127 offset:2080\nv_exp_f32 v32, v32\nv_exp_f32 v33, v33\nv_exp_f32 v34, v34\n"
        "v_exp_f32 v35, v35\nv_exp_f32 v36, v36\nv_exp_f32 v37, v37\nv_mfma_f32_32x32x16_bf16 v[64:79], v[24:27], v[120:123], v[64:79]\nds_read_b128 v[24:27], v126 offset:6240\n"
        "v_exp_f32 v38, v38\nv_exp_f32 v39, v39\nv_cvt_pk_bf16_f32 v116, v32, v33\nv_cvt_pk_bf16_f32 v117, v34, v35\nv_cvt_pk_bf16_f32 v118, v36, v37\n"
        "v_cvt_pk_bf16_f32 v119, v38, v39\nv_exp_f32 v40, v40\nv_exp_f32 v41, v41\nv_exp_f32 v42, v42\ns_waitcnt lgkmcnt(2)\n"
        "v_mfma_f32_32x32x16_bf16 v[64:79], v[108:111], v[116:119], v[64:79]\nds_read_b128 v[108:111], v125 offset:6240\ns_cmp_eq_u32 %[hi], 0\ns_cbranch_scc0 15f\ns_setprio 0\n"
        "15:\nv_exp_f32 v43, v43\nv_exp_f32 v44, v44\nv_exp_f32 v45, v45\nv_exp_f32 v46, v46\nv_exp_f32 v47, v47\nv_cvt_pk_bf16_f32 v120, v40, v41\n"
        "v_cvt_pk_bf16_f32 v121, v42, v43\nv_cvt_pk_bf16_f32 v122, v44, v45\nv_cvt_pk_bf16_f32 v123, v46, v47\nv_mfma_f32_32x32x16_bf16 v[32:47], v[104:107], v[96:99], v[80:95]\n"
        "ds_read_b128 v[104:107], v127 offset:4160\nv_exp_f32 v48, v48\nv_exp_f32 v49, v49\nv_exp_f32 v50, v50\nv_exp_f32 v51, v51\nv_exp_f32 v52, v52\nv_exp_f32 v53, v53\n"
        "v_mfma_f32_32x32x16_bf16 v[64:79], v[112:115], v[120:123], v[64:79]\nds_read_b128 v[112:115], v30 offset:0\nv_exp_f32 v54, v54\nv_exp_f32 v55, v55\n"
        "v_cvt_pk_bf16_f32 v116, v48, v49\nv_cvt_pk_bf16_f32 v117, v50, v51\nv_cvt_pk_bf16_f32 v118, v52, v53\nv_cvt_pk_bf16_f32 v119, v54, v55\nv_exp_f32 v56, v56\n"
        "v_exp_f32 v57, v57\nv_exp_f32 v58, v58\ns_waitcnt lgkmcnt(2)\nv_mfma_f32_32x32x16_bf16 v[64:79], v[108:111], v[116:119], v[64:79]\nds_read_b128 v[108:111], v31 offset:0\n"
        "s_cmp_eq_u32 %[hi], 0\ns_cbranch_scc0 14f\ns_setprio 2\n14:\nv_exp_f32 v59, v59\nv_exp_f32 v60, v60\nv_exp_f32 v61, v61\nv_exp_f32 v62, v62\nv_exp_f32 v63, v63\n"
        "v_cvt_pk_bf16_f32 v120, v56, v57\nv_cvt_pk_bf16_f32 v121, v58, v59\nv_cvt_pk_bf16_f32 v122, v60, v61\nv_cvt_pk_bf16_f32 v123, v62, v63\ns_cmp_gt_u32 %[c], 7\n"
        "s_cbranch_scc1 12f\ns_waitcnt vmcnt(0) lgkmcnt(0)\ns_barrier\ns_cmp_eq_u32 %[c], 7\ns_cbranch_scc1 12f\ns_add_u32 m0, %[sd], %[kd]\ns_add_u32 %[sd], %[sd], 8320\n"
        "global_load_lds_dwordx4 %[vo], %[src] sc1\ns_cmp_eq_u32 %[sd], 74880\ns_cselect_b32 %[sd], 0, %[sd]\nv_add_u32 %[vo], 0x2000, %[vo]\ns_cmp_eq_u32 %[c], 6\n"
        "s_cbranch_scc1 12f\ns_add_u32 m0, %[sd], %[kd]\ns_add_u32 %[sd], %[sd], 8320\nglobal_load_lds_dwordx4 %[vo], %[src] sc1\ns_cmp_eq_u32 %[sd], 74880\n"
        "s_cselect_b32 %[sd], 0, %[sd]\nv_add_u32 %[vo], 0x2000, %[vo]\n12:\ns_add_u32 %[c], %[c], 1\ns_mov_b32 %[so], %[sn]\ns_add_u32 %[sn], %[so], 8320\n"
        "s_cmp_eq_u32 %[so], 66560\ns_cselect_b32 %[sn], 0, %[sn]\nv_mfma_f32_32x32x16_bf16 v[48:63], v[100:103], v[96:99], v[80:95]\nds_read_b128 v[100:103], v127 offset:6240\n"
        "v_exp_f32 v32, v32\nv_exp_f32 v33, v33\nv_exp_f32 v34, v34\nv_exp_f32 v35, v35\nv_exp_f32 v36, v36\nv_exp_f32 v37, v37\n"
        "v_mfma_f32_32x32x16_bf16 v[64:79], v[24:27], v[120:123], v[64:79]\nds_read_b128 v[24:27], v30 offset:2080\nv_exp_f32 v38, v38\nv_exp_f32 v39, v39\n"
        "v_cvt_pk_bf16_f32 v116, v32, v33\nv_cvt_pk_bf16_f32 v117, v34, v35\nv_cvt_pk_bf16_f32 v118, v36, v37\nv_cvt_pk_bf16_f32 v119, v38, v39\nv_exp_f32 v40, v40\n"
        "v_exp_f32 v41, v41\nv_exp_f32 v42, v42\ns_waitcnt lgkmcnt(2)\nv_mfma_f32_32x32x16_bf16 v[64:79], v[108:111], v[116:119], v[64:79]\n"
        "ds_read_b128 v[108:111], v31 offset:2080\nv_add_u32 v124, %[sn], %[bk]\nv_add_u32 v125, %[sn], %[b0]\nv_add_u32 v126, %[sn], %[b1]\nv_exp_f32 v43, v43\nv_exp_f32 v44, v44\n"
        "v_exp_f32 v45, v45\nv_exp_f32 v46, v46\nv_exp_f32 v47, v47\nv_cvt_pk_bf16_f32 v120, v40, v41\nv_cvt_pk_bf16_f32 v121, v42, v43\nv_cvt_pk_bf16_f32 v122, v44, v45\n"
        "v_cvt_pk_bf16_f32 v123, v46, v47\nv_mfma_f32_32x32x16_bf16 v[32:47], v[104:107], v[96:99], v[80:95]\nds_read_b128 v[104:107], v124 offset:0\nv_exp_f32 v48, v48\n"
        "v_exp_f32 v49, v49\nv_exp_f32 v50, v50\nv_exp_f32 v51, v51\nv_exp_f32 v52, v52\nv_exp_f32 v53, v53\nv_mfma_f32_32x32x16_bf16 v[64:79], v[112:115], v[120:123], v[64:79]\n"
        "ds_read_b128 v[112:115], v30 offset:4160\nv_exp_f32 v54, v54\nv_exp_f32 v55, v55\nv_cvt_pk_bf16_f32 v116, v48, v49\nv_cvt_pk_bf16_f32 v117, v50, v51\n"
        "v_cvt_pk_bf16_f32 v118, v52, v53\nv_cvt_pk_bf16_f32 v119, v54, v55\nv_exp_f32 v56, v56\nv_exp_f32 v57, v57\nv_exp_f32 v58, v58\ns_waitcnt lgkmcnt(2)\n"
        "v_mfma_f32_32x32x16_bf16 v[64:79], v[108:111], v[116:119], v[64:79]\nds_read_b128 v[108:111], v31 offset:4160\nv_exp_f32 v59, v59\nv_exp_f32 v60, v60\nv_exp_f32 v61, v61\n"
        "v_exp_f32 v62, v62\nv_exp_f32 v63, v63\nv_cvt_pk_bf16_f32 v120, v56, v57\nv_cvt_pk_bf16_f32 v121, v58, v59\nv_cvt_pk_bf16_f32 v122, v60, v61\n"
        "v_cvt_pk_bf16_f32 v123, v62, v63\nv_mfma_f32_32x32x16_bf16 v[48:63], v[100:103], v[96:99], v[80:95]\nds_read_b128 v[100:103], v124 offset:2080\nv_exp_f32 v32, v32\n"
        "v_exp_f32 v33, v33\nv_exp_f32 v34, v34\nv_exp_f32 v35, v35\nv_exp_f32 v36, v36\nv_exp_f32 v37, v37\nv_mfma_f32_32x32x16_bf16 v[64:79], v[24:27], v[120:123], v[64:79]\n"
        "ds_read_b128 v[24:27], v30 offset:6240\nv_exp_f32 v38, v38\nv_exp_f32 v39, v39\nv_cvt_pk_bf16_f32 v116, v32, v33\nv_cvt_pk_bf16_f32 v117, v34, v35\n"
        "v_cvt_pk_bf16_f32 v118, v36, v37\nv_cvt_pk_bf16_f32 v119, v38, v39\nv_exp_f32 v40, v40\nv_exp_f32 v41, v41\nv_exp_f32 v42, v42\ns_waitcnt lgkmcnt(2)\n"
        "v_mfma_f32_32x32x16_bf16 v[64:79], v[108:111], v[116:119], v[64:79]\nds_read_b128 v[108:111], v31 offset:6240\ns_cmp_eq_u32 %[hi], 0\ns_cbranch_scc0 25f\ns_setprio 0\n25:\n"
        "v_exp_f32 v43, v43\nv_exp_f32 v44, v44\nv_exp_f32 v45, v45\nv_exp_f32 v46, v46\nv_exp_f32 v47, v47\nv_cvt_pk_bf16_f32 v120, v40, v41\nv_cvt_pk_bf16_f32 v121, v42, v43\n"
        "v_cvt_pk_bf16_f32 v122, v44, v45\nv_cvt_pk_bf16_f32 v123, v46, v47\nv_mfma_f32_32x32x16_bf16 v[32:47], v[104:107], v[96:99], v[80:95]\n"
        "ds_read_b128 v[104:107], v124 offset:4160\nv_exp_f32 v48, v48\nv_exp_f32 v49, v49\nv_exp_f32 v50, v50\nv_exp_f32 v51, v51\nv_exp_f32 v52, v52\nv_exp_f32 v53, v53\n"
        "v_mfma_f32_32x32x16_bf16 v[64:79], v[112:115], v[120:123], v[64:79]\nds_read_b128 v[112:115], v126 offset:0\nv_exp_f32 v54, v54\nv_exp_f32 v55, v55\n"
        "v_cvt_pk_bf16_f32 v116, v48, v49\nv_cvt_pk_bf16_f32 v117, v50, v51\nv_cvt_pk_bf16_f32 v118, v52, v53\nv_cvt_pk_bf16_f32 v119, v54, v55\nv_exp_f32 v56, v56\n"
        "v_exp_f32 v57, v57\nv_exp_f32 v58, v58\ns_waitcnt lgkmcnt(2)\nv_mfma_f32_32x32x16_bf16 v[64:79], v[108:111], v[116:119], v[64:79]\nds_read_b128 v[108:111], v125 offset:0\n"
        "s_cmp_eq_u32 %[hi], 0\ns_cbranch_scc0 24f\ns_setprio 2\n24:\nv_exp_f32 v59, v59\nv_exp_f32 v60, v60\nv_exp_f32 v61, v61\nv_exp_f32 v62, v62\nv_exp_f32 v63, v63\n"
        "v_cvt_pk_bf16_f32 v120, v56, v57\nv_cvt_pk_bf16_f32 v121, v58, v59\nv_cvt_pk_bf16_f32 v122, v60, v61\nv_cvt_pk_bf16_f32 v123, v62, v63\ns_cmp_gt_u32 %[c], 7\n"
        "s_cbranch_scc1 22f\ns_waitcnt vmcnt(0) lgkmcnt(0)\ns_barrier\ns_cmp_eq_u32 %[c], 7\ns_cbranch_scc1 22f\ns_add_u32 m0, %[sd], %[kd]\ns_add_u32 %[sd], %[sd], 8320\n"
        "global_load_lds_dwordx4 %[vo], %[src] sc1\ns_cmp_eq_u32 %[sd], 74880\ns_cselect_b32 %[sd], 0, %[sd]\nv_add_u32 %[vo], 0x2000, %[vo]\ns_cmp_eq_u32 %[c], 6\n"
        "s_cbranch_scc1 22f\ns_add_u32 m0, %[sd], %[kd]\ns_add_u32 %[sd], %[sd], 8320\nglobal_load_lds_dwordx4 %[vo], %[src] sc1\ns_cmp_eq_u32 %[sd], 74880\n"
        "s_cselect_b32 %[sd], 0, %[sd]\nv_add_u32 %[vo], 0x2000, %[vo]\n22:\ns_add_u32 %[c], %[c], 1\ns_mov_b32 %[so], %[sn]\ns_cmp_lt_u32 %[c], 16\ns_cbranch_scc1 9b\n"
        "s_waitcnt lgkmcnt(0)\nv_mfma_f32_32x32x16_bf16 v[64:79], v[24:27], v[120:123], v[64:79]\ns_mov_b32 m0, %[sv]\ns_nop 7\ns_nop 7\n"
        : "+{v[32:47]}"(sA), "+{v[64:79]}"(o), "+{v[100:103]}"(k0), "+{v[104:107]}"(k1), "+{v[108:111]}"(v0), "+{v[112:115]}"(v1), "+{v[24:27]}"(vb), "+{v[120:123]}"(pb),
          "+{v124}"(aK), "+{v125}"(a0), "+{v126}"(a1), [so] "+s"(so), [sd] "+s"(sd), [sn] "=&s"(sn), [c] "+s"(c), [sv] "=&s"(sv), [vo] "+v"(vo), [hi] "+s"(hi)
        : "{v[80:95]}"(ns), "{v[96:99]}"(qf), [bk] "v"(bk), [b0] "v"(b0), [b1] "v"(b1), [kd] "s"(kvd), [src] "s"(kvs)
        : "v48", "v49", "v50", "v51", "v52", "v53", "v54", "v55", "v56", "v57", "v58", "v59", "v60", "v61", "v62", "v63", "v116", "v117", "v118", "v119", "v127", "v30", "v31", "scc", "memory"
 );

      PRIO(3); }

    int m_; asm volatile("v_mbcnt_lo_u32_b32 %0, -1, 0\n\tv_mbcnt_hi_u32_b32 %0, -1, %0" : "=v"(m_));
    const unsigned m16 = m_ * 16;

    float bad = o[0];
#pragma unroll
    for (int i = 8; i < 16; ++i) bad = fmaxf(bad, fabsf(o[i]));
    if (__builtin_amdgcn_ballot_w64(!(bad < 1.0e33f)) != 0ull || __builtin_amdgcn_ballot_w64(!(o[0] > 0.f)) != 0ull) {
        const char* kv = ws + WS_KV; float m = -3.0e38f; const f32x16 z = splat16(0.f);
#pragma unroll 1
        for (int u = 0; u < 64; ++u) m = max16(MFMA(ldc(kv + u * 2048 + m16), qf, z), m);
        ns = splat16(-xhalf_max(m)); o = z;
        const u32x4 one = {0x3F803F80u, 0x3F803F80u, 0x3F803F80u, 0x3F803F80u}; const int vo = 1024 + (m_ >> 5) * 256 + (m_ & 15) * 16;
#pragma unroll 1
        for (int u = 0; u < 64; ++u) { f32x16 s = MFMA(ldc(kv + u * 2048 + m16), qf, ns);
            u32x4 v0 = __builtin_bit_cast(u32x4, ldc(kv + u * 2048 + vo)), v1 = __builtin_bit_cast(u32x4, ldc(kv + u * 2048 + vo + 512));
            if (!(m_ & 16)) { v0 = one; v1 = one; }
#pragma unroll
            for (int i = 0; i < 16; ++i) s[i] = __builtin_amdgcn_exp2f(s[i]);
            o = MFMA(__builtin_bit_cast(bf16x8, v0), pack8(s, 0), o); o = MFMA(__builtin_bit_cast(bf16x8, v1), pack8(s, 8), o); } }
    const float inv = 1.f / o[0];
#pragma unroll
    for (int i = 8; i < 16; ++i) o[i] *= inv;
    st_sc1(m16, ws + WS_O + (TT * 32 + 4 * hg + kq) * 1024, pack8(o, 8));
    BAR(0);
    if (w == 0 && m_ == 0) set_flag(ws, FL_O + tg * 8 + hg, tag);

}

DI void ffn_role(int b, const lptr L, unsigned L0, const char* x, const char* wk, const char* wv, const char* wo, const char* wg, const char* wu, const char* wd, char* out, char* ws) {
    const int tid = threadIdx.x, l = tid & 63, w = __builtin_amdgcn_readfirstlane(tid >> 6), h = l >> 5;
    const int xcd = b & 7, i5 = b >> 3, q = (l >> 2) & 3, p = l & 3; const unsigned l16 = l * 16;
    u64 tag; unsigned jk = 0;
    const int TB = (xcd >> 2) * 4 + (i5 & 3), fb = (xcd & 3) * 7 + (i5 >> 2), ft = w & 1, tk = w >> 1;
    const unsigned vw = (l >> 3) * 3584 + (((l & 7) ^ (((l >> 4) & 1) << 2)) << 4), dw = L0 + w * 1024, dx = L0 + 8192 + w * 2048;
    const char* sw = ((w & 4) ? wu : wg) + (w & 3) * 8 * 3584 + fb * 128;
    if (i5 < 16) {
        tag = ln_kv(xcd * 256 + i5 * 16, L, L0, x, wk, wv, ws);
    } else {
        const u64 ep = __hip_atomic_load((GAS u64*)(ws + WS_FL + FL_E * FS), RLX_AGENT);
        const int n = tid & 255, row = n / 7;
        if (n < 224) touch(((i5 - 16) * 32 + row) * 3584 + (xcd & 3) * 896 + (n - row * 7) * 128, w < 4 ? wg : wu);
        TOUCH_END(); BARL(); tag = mk_tag(ep); }
    if (tid == 128) set_flag(ws, FL_A + 256 + b, tag);

    if (i5 < 28) {
        glds16(vw, sw, dw); glds16(vw, sw + 114688, dw + 24576); glds16(vw, sw + 2 * 114688, dw + 49152);
        wait_flags(ws, FL_X + TB * 16 + 2 * w + (l & 1), tag);
        __builtin_amdgcn_s_sleep(FDL);
        const char* sx = uni(ws + WS_XN + (TB * 8 + w) * 32768);
#pragma unroll
        for (int st = 0; st < 3; ++st) { glds16c(l16, sx + st * 2048, dx + st * 24576); glds16c(l16, sx + st * 2048 + 1024, dx + st * 24576 + 1024); }
        unsigned sv; const unsigned tv = (i5 * 512 + tid) * 128;
        const unsigned pa = L0 + (((8 * h + q) * 128 + (((((l >> 4) & 1) * 2 + (p >> 1)) ^ ((q >> 1) << 2)) << 4) + 8 * (p & 1)) ^ (ft * 64)), pb = L0 + 8192 + tk * 4096 + l16;
        f32x16 g0, g1, u0, u1;
        PRIO(P_LOOP);
        asm volatile(
        "s_mov_b32 %[sv], m0\ns_mov_b64 s[40:41], %[sw]\ns_mov_b64 s[42:43], %[sx]\ns_waitcnt vmcnt(4)\ns_barrier\nds_read_b64_tr_b16 v[96:97], %[pa] offset:0\n"
        "ds_read_b64_tr_b16 v[98:99], %[pa] offset:512\nds_read_b128 v[104:107], %[pb] offset:0\nds_read_b64_tr_b16 v[100:101], %[pa] offset:4096\n"
        "ds_read_b64_tr_b16 v[102:103], %[pa] offset:4608\nds_read_b128 v[108:111], %[pb] offset:2048\ns_waitcnt lgkmcnt(0)\n"
        "v_mfma_f32_32x32x16_bf16 v[32:47], v[96:99], v[104:107], 0\nds_read_b64_tr_b16 v[112:113], %[pa] offset:2048\nds_read_b64_tr_b16 v[114:115], %[pa] offset:2560\n"
        "v_mfma_f32_32x32x16_bf16 v[48:63], v[96:99], v[108:111], 0\nds_read_b128 v[120:123], %[pb] offset:1024\nds_read_b64_tr_b16 v[116:117], %[pa] offset:6144\n"
        "v_mfma_f32_32x32x16_bf16 v[64:79], v[100:103], v[104:107], 0\nds_read_b64_tr_b16 v[118:119], %[pa] offset:6656\nds_read_b128 v[124:127], %[pb] offset:3072\n"
        "v_mfma_f32_32x32x16_bf16 v[80:95], v[100:103], v[108:111], 0\ns_waitcnt vmcnt(2) lgkmcnt(0)\ns_barrier\n"
        "v_mfma_f32_32x32x16_bf16 v[32:47], v[112:115], v[120:123], v[32:47]\ns_add_u32 m0, %[dw], 0\ns_add_u32 s40, s40, 0x1c000\ns_addc_u32 s41, s41, 0\n"
        "global_load_lds_dwordx4 %[vw], s[40:41]\nds_read_b64_tr_b16 v[96:97], %[pa] offset:24576\nds_read_b64_tr_b16 v[98:99], %[pa] offset:25088\n"
        "v_mfma_f32_32x32x16_bf16 v[48:63], v[112:115], v[124:127], v[48:63]\ns_add_u32 m0, %[dx], 0\ns_add_u32 s42, s42, 0x800\ns_addc_u32 s43, s43, 0\n"
        "global_load_lds_dwordx4 %[vx], s[42:43] sc1\nds_read_b128 v[104:107], %[pb] offset:24576\nds_read_b64_tr_b16 v[100:101], %[pa] offset:28672\n"
        "v_mfma_f32_32x32x16_bf16 v[64:79], v[116:119], v[120:123], v[64:79]\nglobal_load_lds_dwordx4 %[vx], s[42:43] offset:1024 sc1\n"
        "ds_read_b64_tr_b16 v[102:103], %[pa] offset:29184\nds_read_b128 v[108:111], %[pb] offset:26624\nv_mfma_f32_32x32x16_bf16 v[80:95], v[116:119], v[124:127], v[80:95]\n"
        "s_waitcnt lgkmcnt(0)\nv_mfma_f32_32x32x16_bf16 v[32:47], v[96:99], v[104:107], v[32:47]\nds_read_b64_tr_b16 v[112:113], %[pa] offset:26624\n"
        "ds_read_b64_tr_b16 v[114:115], %[pa] offset:27136\nv_mfma_f32_32x32x16_bf16 v[48:63], v[96:99], v[108:111], v[48:63]\nds_read_b128 v[120:123], %[pb] offset:25600\n"
        "ds_read_b64_tr_b16 v[116:117], %[pa] offset:30720\nv_mfma_f32_32x32x16_bf16 v[64:79], v[100:103], v[104:107], v[64:79]\nds_read_b64_tr_b16 v[118:119], %[pa] offset:31232\n"
        "ds_read_b128 v[124:127], %[pb] offset:27648\nv_mfma_f32_32x32x16_bf16 v[80:95], v[100:103], v[108:111], v[80:95]\ns_waitcnt vmcnt(3) lgkmcnt(0)\ns_barrier\n"
        "v_mfma_f32_32x32x16_bf16 v[32:47], v[112:115], v[120:123], v[32:47]\ns_add_u32 m0, %[dw], 24576\ns_add_u32 s40, s40, 0x1c000\ns_addc_u32 s41, s41, 0\n"
        "global_load_lds_dwordx4 %[vw], s[40:41]\nds_read_b64_tr_b16 v[96:97], %[pa] offset:49152\nds_read_b64_tr_b16 v[98:99], %[pa] offset:49664\n"
        "v_mfma_f32_32x32x16_bf16 v[48:63], v[112:115], v[124:127], v[48:63]\ns_add_u32 m0, %[dx], 24576\ns_add_u32 s42, s42, 0x800\ns_addc_u32 s43, s43, 0\n"
        "global_load_lds_dwordx4 %[vx], s[42:43] sc1\nds_read_b128 v[104:107], %[pb] offset:49152\nds_read_b64_tr_b16 v[100:101], %[pa] offset:53248\n"
        "v_mfma_f32_32x32x16_bf16 v[64:79], v[116:119], v[120:123], v[64:79]\nglobal_load_lds_dwordx4 %[vx], s[42:43] offset:1024 sc1\n"
        "ds_read_b64_tr_b16 v[102:103], %[pa] offset:53760\nds_read_b128 v[108:111], %[pb] offset:51200\nv_mfma_f32_32x32x16_bf16 v[80:95], v[116:119], v[124:127], v[80:95]\n"
        "s_waitcnt lgkmcnt(0)\nv_mfma_f32_32x32x16_bf16 v[32:47], v[96:99], v[104:107], v[32:47]\nds_read_b64_tr_b16 v[112:113], %[pa] offset:51200\n"
        "ds_read_b64_tr_b16 v[114:115], %[pa] offset:51712\nv_mfma_f32_32x32x16_bf16 v[48:63], v[96:99], v[108:111], v[48:63]\nds_read_b128 v[120:123], %[pb] offset:50176\n"
        "ds_read_b64_tr_b16 v[116:117], %[pa] offset:55296\nv_mfma_f32_32x32x16_bf16 v[64:79], v[100:103], v[104:107], v[64:79]\nds_read_b64_tr_b16 v[118:119], %[pa] offset:55808\n"
        "ds_read_b128 v[124:127], %[pb] offset:52224\nv_mfma_f32_32x32x16_bf16 v[80:95], v[100:103], v[108:111], v[80:95]\ns_waitcnt vmcnt(3) lgkmcnt(0)\ns_barrier\n"
        "v_mfma_f32_32x32x16_bf16 v[32:47], v[112:115], v[120:123], v[32:47]\ns_add_u32 m0, %[dw], 49152\ns_add_u32 s40, s40, 0x1c000\ns_addc_u32 s41, s41, 0\n"
        "global_load_lds_dwordx4 %[vw], s[40:41]\nds_read_b64_tr_b16 v[96:97], %[pa] offset:0\nds_read_b64_tr_b16 v[98:99], %[pa] offset:512\n"
        "v_mfma_f32_32x32x16_bf16 v[48:63], v[112:115], v[124:127], v[48:63]\ns_add_u32 m0, %[dx], 49152\ns_add_u32 s42, s42, 0x800\ns_addc_u32 s43, s43, 0\n"
        "global_load_lds_dwordx4 %[vx], s[42:43] sc1\nds_read_b128 v[104:107], %[pb] offset:0\nds_read_b64_tr_b16 v[100:101], %[pa] offset:4096\n"
        "v_mfma_f32_32x32x16_bf16 v[64:79], v[116:119], v[120:123], v[64:79]\nglobal_load_lds_dwordx4 %[vx], s[42:43] offset:1024 sc1\nglobal_load_dword %[jk], %[tv], %[wd]\n"
        "ds_read_b64_tr_b16 v[102:103], %[pa] offset:4608\nds_read_b128 v[108:111], %[pb] offset:2048\nv_mfma_f32_32x32x16_bf16 v[80:95], v[116:119], v[124:127], v[80:95]\n"
        "s_waitcnt lgkmcnt(0)\nv_mfma_f32_32x32x16_bf16 v[32:47], v[96:99], v[104:107], v[32:47]\nds_read_b64_tr_b16 v[112:113], %[pa] offset:2048\n"
        "ds_read_b64_tr_b16 v[114:115], %[pa] offset:2560\nv_mfma_f32_32x32x16_bf16 v[48:63], v[96:99], v[108:111], v[48:63]\nds_read_b128 v[120:123], %[pb] offset:1024\n"
        "ds_read_b64_tr_b16 v[116:117], %[pa] offset:6144\nv_mfma_f32_32x32x16_bf16 v[64:79], v[100:103], v[104:107], v[64:79]\nds_read_b64_tr_b16 v[118:119], %[pa] offset:6656\n"
        "ds_read_b128 v[124:127], %[pb] offset:3072\nv_mfma_f32_32x32x16_bf16 v[80:95], v[100:103], v[108:111], v[80:95]\ns_waitcnt vmcnt(4) lgkmcnt(0)\ns_barrier\n"
        "v_mfma_f32_32x32x16_bf16 v[32:47], v[112:115], v[120:123], v[32:47]\ns_add_u32 m0, %[dw], 0\ns_add_u32 s40, s40, 0x1c000\ns_addc_u32 s41, s41, 0\n"
        "global_load_lds_dwordx4 %[vw], s[40:41]\nds_read_b64_tr_b16 v[96:97], %[pa] offset:24576\nds_read_b64_tr_b16 v[98:99], %[pa] offset:25088\n"
        "v_mfma_f32_32x32x16_bf16 v[48:63], v[112:115], v[124:127], v[48:63]\ns_add_u32 m0, %[dx], 0\ns_add_u32 s42, s42, 0x800\ns_addc_u32 s43, s43, 0\n"
        "global_load_lds_dwordx4 %[vx], s[42:43] sc1\nds_read_b128 v[104:107], %[pb] offset:24576\nds_read_b64_tr_b16 v[100:101], %[pa] offset:28672\n"
        "v_mfma_f32_32x32x16_bf16 v[64:79], v[116:119], v[120:123], v[64:79]\nglobal_load_lds_dwordx4 %[vx], s[42:43] offset:1024 sc1\n"
        "ds_read_b64_tr_b16 v[102:103], %[pa] offset:29184\nds_read_b128 v[108:111], %[pb] offset:26624\nv_mfma_f32_32x32x16_bf16 v[80:95], v[116:119], v[124:127], v[80:95]\n"
        "s_waitcnt lgkmcnt(0)\nv_mfma_f32_32x32x16_bf16 v[32:47], v[96:99], v[104:107], v[32:47]\nds_read_b64_tr_b16 v[112:113], %[pa] offset:26624\n"
        "ds_read_b64_tr_b16 v[114:115], %[pa] offset:27136\nv_mfma_f32_32x32x16_bf16 v[48:63], v[96:99], v[108:111], v[48:63]\nds_read_b128 v[120:123], %[pb] offset:25600\n"
        "ds_read_b64_tr_b16 v[116:117], %[pa] offset:30720\nv_mfma_f32_32x32x16_bf16 v[64:79], v[100:103], v[104:107], v[64:79]\nds_read_b64_tr_b16 v[118:119], %[pa] offset:31232\n"
        "ds_read_b128 v[124:127], %[pb] offset:27648\nv_mfma_f32_32x32x16_bf16 v[80:95], v[100:103], v[108:111], v[80:95]\ns_waitcnt vmcnt(4) lgkmcnt(0)\ns_barrier\n"
        "v_mfma_f32_32x32x16_bf16 v[32:47], v[112:115], v[120:123], v[32:47]\ns_add_u32 m0, %[dw], 24576\ns_add_u32 s40, s40, 0x1c000\ns_addc_u32 s41, s41, 0\n"
        "global_load_lds_dwordx4 %[vw], s[40:41]\nds_read_b64_tr_b16 v[96:97], %[pa] offset:49152\nds_read_b64_tr_b16 v[98:99], %[pa] offset:49664\n"
        "v_mfma_f32_32x32x16_bf16 v[48:63], v[112:115], v[124:127], v[48:63]\ns_add_u32 m0, %[dx], 24576\ns_add_u32 s42, s42, 0x800\ns_addc_u32 s43, s43, 0\n"
        "global_load_lds_dwordx4 %[vx], s[42:43] sc1\nds_read_b128 v[104:107], %[pb] offset:49152\nds_read_b64_tr_b16 v[100:101], %[pa] offset:53248\n"
        "v_mfma_f32_32x32x16_bf16 v[64:79], v[116:119], v[120:123], v[64:79]\nglobal_load_lds_dwordx4 %[vx], s[42:43] offset:1024 sc1\n"
        "ds_read_b64_tr_b16 v[102:103], %[pa] offset:53760\nds_read_b128 v[108:111], %[pb] offset:51200\nv_mfma_f32_32x32x16_bf16 v[80:95], v[116:119], v[124:127], v[80:95]\n"
        "s_waitcnt lgkmcnt(0)\nv_mfma_f32_32x32x16_bf16 v[32:47], v[96:99], v[104:107], v[32:47]\nds_read_b64_tr_b16 v[112:113], %[pa] offset:51200\n"
        "ds_read_b64_tr_b16 v[114:115], %[pa] offset:51712\nv_mfma_f32_32x32x16_bf16 v[48:63], v[96:99], v[108:111], v[48:63]\nds_read_b128 v[120:123], %[pb] offset:50176\n"
        "ds_read_b64_tr_b16 v[116:117], %[pa] offset:55296\nv_mfma_f32_32x32x16_bf16 v[64:79], v[100:103], v[104:107], v[64:79]\nds_read_b64_tr_b16 v[118:119], %[pa] offset:55808\n"
        "ds_read_b128 v[124:127], %[pb] offset:52224\nv_mfma_f32_32x32x16_bf16 v[80:95], v[100:103], v[108:111], v[80:95]\ns_waitcnt vmcnt(3) lgkmcnt(0)\ns_barrier\n"
        "v_mfma_f32_32x32x16_bf16 v[32:47], v[112:115], v[120:123], v[32:47]\ns_add_u32 m0, %[dw], 49152\ns_add_u32 s40, s40, 0x1c000\ns_addc_u32 s41, s41, 0\n"
        "global_load_lds_dwordx4 %[vw], s[40:41]\nds_read_b64_tr_b16 v[96:97], %[pa] offset:0\nds_read_b64_tr_b16 v[98:99], %[pa] offset:512\n"
        "v_mfma_f32_32x32x16_bf16 v[48:63], v[112:115], v[124:127], v[48:63]\ns_add_u32 m0, %[dx], 49152\ns_add_u32 s42, s42, 0x800\ns_addc_u32 s43, s43, 0\n"
        "global_load_lds_dwordx4 %[vx], s[42:43] sc1\nds_read_b128 v[104:107], %[pb] offset:0\nds_read_b64_tr_b16 v[100:101], %[pa] offset:4096\n"
        "v_mfma_f32_32x32x16_bf16 v[64:79], v[116:119], v[120:123], v[64:79]\nglobal_load_lds_dwordx4 %[vx], s[42:43] offset:1024 sc1\n"
        "ds_read_b64_tr_b16 v[102:103], %[pa] offset:4608\nds_read_b128 v[108:111], %[pb] offset:2048\nv_mfma_f32_32x32x16_bf16 v[80:95], v[116:119], v[124:127], v[80:95]\n"
        "s_waitcnt lgkmcnt(0)\nv_mfma_f32_32x32x16_bf16 v[32:47], v[96:99], v[104:107], v[32:47]\nds_read_b64_tr_b16 v[112:113], %[pa] offset:2048\n"
        "ds_read_b64_tr_b16 v[114:115], %[pa] offset:2560\nv_mfma_f32_32x32x16_bf16 v[48:63], v[96:99], v[108:111], v[48:63]\nds_read_b128 v[120:123], %[pb] offset:1024\n"
        "ds_read_b64_tr_b16 v[116:117], %[pa] offset:6144\nv_mfma_f32_32x32x16_bf16 v[64:79], v[100:103], v[104:107], v[64:79]\nds_read_b64_tr_b16 v[118:119], %[pa] offset:6656\n"
        "ds_read_b128 v[124:127], %[pb] offset:3072\nv_mfma_f32_32x32x16_bf16 v[80:95], v[100:103], v[108:111], v[80:95]\ns_waitcnt vmcnt(3) lgkmcnt(0)\ns_barrier\n"
        "v_mfma_f32_32x32x16_bf16 v[32:47], v[112:115], v[120:123], v[32:47]\ns_add_u32 m0, %[dw], 0\ns_add_u32 s40, s40, 0x1c000\ns_addc_u32 s41, s41, 0\n"
        "global_load_lds_dwordx4 %[vw], s[40:41]\nds_read_b64_tr_b16 v[96:97], %[pa] offset:24576\nds_read_b64_tr_b16 v[98:99], %[pa] offset:25088\n"
        "v_mfma_f32_32x32x16_bf16 v[48:63], v[112:115], v[124:127], v[48:63]\ns_add_u32 m0, %[dx], 0\ns_add_u32 s42, s42, 0x800\ns_addc_u32 s43, s43, 0\n"
        "global_load_lds_dwordx4 %[vx], s[42:43] sc1\nds_read_b128 v[104:107], %[pb] offset:24576\nds_read_b64_tr_b16 v[100:101], %[pa] offset:28672\n"
        "v_mfma_f32_32x32x16_bf16 v[64:79], v[116:119], v[120:123], v[64:79]\nglobal_load_lds_dwordx4 %[vx], s[42:43] offset:1024 sc1\n"
        "ds_read_b64_tr_b16 v[102:103], %[pa] offset:29184\nds_read_b128 v[108:111], %[pb] offset:26624\nv_mfma_f32_32x32x16_bf16 v[80:95], v[116:119], v[124:127], v[80:95]\n"
        "s_waitcnt lgkmcnt(0)\nv_mfma_f32_32x32x16_bf16 v[32:47], v[96:99], v[104:107], v[32:47]\nds_read_b64_tr_b16 v[112:113], %[pa] offset:26624\n"
        "ds_read_b64_tr_b16 v[114:115], %[pa] offset:27136\nv_mfma_f32_32x32x16_bf16 v[48:63], v[96:99], v[108:111], v[48:63]\nds_read_b128 v[120:123], %[pb] offset:25600\n"
        "ds_read_b64_tr_b16 v[116:117], %[pa] offset:30720\nv_mfma_f32_32x32x16_bf16 v[64:79], v[100:103], v[104:107], v[64:79]\nds_read_b64_tr_b16 v[118:119], %[pa] offset:31232\n"
        "ds_read_b128 v[124:127], %[pb] offset:27648\nv_mfma_f32_32x32x16_bf16 v[80:95], v[100:103], v[108:111], v[80:95]\ns_waitcnt vmcnt(3) lgkmcnt(0)\ns_barrier\n"
        "v_mfma_f32_32x32x16_bf16 v[32:47], v[112:115], v[120:123], v[32:47]\ns_add_u32 m0, %[dw], 24576\ns_add_u32 s40, s40, 0x1c000\ns_addc_u32 s41, s41, 0\n"
        "global_load_lds_dwordx4 %[vw], s[40:41]\nds_read_b64_tr_b16 v[96:97], %[pa] offset:49152\nds_read_b64_tr_b16 v[98:99], %[pa] offset:49664\n"
        "v_mfma_f32_32x32x16_bf16 v[48:63], v[112:115], v[124:127], v[48:63]\ns_add_u32 m0, %[dx], 24576\ns_add_u32 s42, s42, 0x800\ns_addc_u32 s43, s43, 0\n"
        "global_load_lds_dwordx4 %[vx], s[42:43] sc1\nds_read_b128 v[104:107], %[pb] offset:49152\nds_read_b64_tr_b16 v[100:101], %[pa] offset:53248\n"
        "v_mfma_f32_32x32x16_bf16 v[64:79], v[116:119], v[120:123], v[64:79]\nglobal_load_lds_dwordx4 %[vx], s[42:43] offset:1024 sc1\n"
        "ds_read_b64_tr_b16 v[102:103], %[pa] offset:53760\nds_read_b128 v[108:111], %[pb] offset:51200\nv_mfma_f32_32x32x16_bf16 v[80:95], v[116:119], v[124:127], v[80:95]\n"
        "s_waitcnt lgkmcnt(0)\nv_mfma_f32_32x32x16_bf16 v[32:47], v[96:99], v[104:107], v[32:47]\nds_read_b64_tr_b16 v[112:113], %[pa] offset:51200\n"
        "ds_read_b64_tr_b16 v[114:115], %[pa] offset:51712\nv_mfma_f32_32x32x16_bf16 v[48:63], v[96:99], v[108:111], v[48:63]\nds_read_b128 v[120:123], %[pb] offset:50176\n"
        "ds_read_b64_tr_b16 v[116:117], %[pa] offset:55296\nv_mfma_f32_32x32x16_bf16 v[64:79], v[100:103], v[104:107], v[64:79]\nds_read_b64_tr_b16 v[118:119], %[pa] offset:55808\n"
        "ds_read_b128 v[124:127], %[pb] offset:52224\nv_mfma_f32_32x32x16_bf16 v[80:95], v[100:103], v[108:111], v[80:95]\ns_waitcnt vmcnt(3) lgkmcnt(0)\ns_barrier\n"
        "v_mfma_f32_32x32x16_bf16 v[32:47], v[112:115], v[120:123], v[32:47]\ns_add_u32 m0, %[dw], 49152\ns_add_u32 s40, s40, 0x1c000\ns_addc_u32 s41, s41, 0\n"
        "global_load_lds_dwordx4 %[vw], s[40:41]\nds_read_b64_tr_b16 v[96:97], %[pa] offset:0\nds_read_b64_tr_b16 v[98:99], %[pa] offset:512\n"
        "v_mfma_f32_32x32x16_bf16 v[48:63], v[112:115], v[124:127], v[48:63]\ns_add_u32 m0, %[dx], 49152\ns_add_u32 s42, s42, 0x800\ns_addc_u32 s43, s43, 0\n"
        "global_load_lds_dwordx4 %[vx], s[42:43] sc1\nds_read_b128 v[104:107], %[pb] offset:0\nds_read_b64_tr_b16 v[100:101], %[pa] offset:4096\n"
        "v_mfma_f32_32x32x16_bf16 v[64:79], v[116:119], v[120:123], v[64:79]\nglobal_load_lds_dwordx4 %[vx], s[42:43] offset:1024 sc1\n"
        "ds_read_b64_tr_b16 v[102:103], %[pa] offset:4608\nds_read_b128 v[108:111], %[pb] offset:2048\nv_mfma_f32_32x32x16_bf16 v[80:95], v[116:119], v[124:127], v[80:95]\n"
        "s_waitcnt lgkmcnt(0)\nv_mfma_f32_32x32x16_bf16 v[32:47], v[96:99], v[104:107], v[32:47]\nds_read_b64_tr_b16 v[112:113], %[pa] offset:2048\n"
        "ds_read_b64_tr_b16 v[114:115], %[pa] offset:2560\nv_mfma_f32_32x32x16_bf16 v[48:63], v[96:99], v[108:111], v[48:63]\nds_read_b128 v[120:123], %[pb] offset:1024\n"
        "ds_read_b64_tr_b16 v[116:117], %[pa] offset:6144\nv_mfma_f32_32x32x16_bf16 v[64:79], v[100:103], v[104:107], v[64:79]\nds_read_b64_tr_b16 v[118:119], %[pa] offset:6656\n"
        "ds_read_b128 v[124:127], %[pb] offset:3072\nv_mfma_f32_32x32x16_bf16 v[80:95], v[100:103], v[108:111], v[80:95]\ns_waitcnt vmcnt(3) lgkmcnt(0)\ns_barrier\n"
        "v_mfma_f32_32x32x16_bf16 v[32:47], v[112:115], v[120:123], v[32:47]\ns_add_u32 m0, %[dw], 0\ns_add_u32 s40, s40, 0x1c000\ns_addc_u32 s41, s41, 0\n"
        "global_load_lds_dwordx4 %[vw], s[40:41]\nds_read_b64_tr_b16 v[96:97], %[pa] offset:24576\nds_read_b64_tr_b16 v[98:99], %[pa] offset:25088\n"
        "v_mfma_f32_32x32x16_bf16 v[48:63], v[112:115], v[124:127], v[48:63]\ns_add_u32 m0, %[dx], 0\ns_add_u32 s42, s42, 0x800\ns_addc_u32 s43, s43, 0\n"
        "global_load_lds_dwordx4 %[vx], s[42:43] sc1\nds_read_b128 v[104:107], %[pb] offset:24576\nds_read_b64_tr_b16 v[100:101], %[pa] offset:28672\n"
        "v_mfma_f32_32x32x16_bf16 v[64:79], v[116:119], v[120:123], v[64:79]\nglobal_load_lds_dwordx4 %[vx], s[42:43] offset:1024 sc1\n"
        "ds_read_b64_tr_b16 v[102:103], %[pa] offset:29184\nds_read_b128 v[108:111], %[pb] offset:26624\nv_mfma_f32_32x32x16_bf16 v[80:95], v[116:119], v[124:127], v[80:95]\n"
        "s_waitcnt lgkmcnt(0)\nv_mfma_f32_32x32x16_bf16 v[32:47], v[96:99], v[104:107], v[32:47]\nds_read_b64_tr_b16 v[112:113], %[pa] offset:26624\n"
        "ds_read_b64_tr_b16 v[114:115], %[pa] offset:27136\nv_mfma_f32_32x32x16_bf16 v[48:63], v[96:99], v[108:111], v[48:63]\nds_read_b128 v[120:123], %[pb] offset:25600\n"
        "ds_read_b64_tr_b16 v[116:117], %[pa] offset:30720\nv_mfma_f32_32x32x16_bf16 v[64:79], v[100:103], v[104:107], v[64:79]\nds_read_b64_tr_b16 v[118:119], %[pa] offset:31232\n"
        "ds_read_b128 v[124:127], %[pb] offset:27648\nv_mfma_f32_32x32x16_bf16 v[80:95], v[100:103], v[108:111], v[80:95]\ns_waitcnt vmcnt(3) lgkmcnt(0)\ns_barrier\n"
        "v_mfma_f32_32x32x16_bf16 v[32:47], v[112:115], v[120:123], v[32:47]\ns_add_u32 m0, %[dw], 24576\ns_add_u32 s40, s40, 0x1c000\ns_addc_u32 s41, s41, 0\n"
        "global_load_lds_dwordx4 %[vw], s[40:41]\nds_read_b64_tr_b16 v[96:97], %[pa] offset:49152\nds_read_b64_tr_b16 v[98:99], %[pa] offset:49664\n"
        "v_mfma_f32_32x32x16_bf16 v[48:63], v[112:115], v[124:127], v[48:63]\ns_add_u32 m0, %[dx], 24576\ns_add_u32 s42, s42, 0x800\ns_addc_u32 s43, s43, 0\n"
        "global_load_lds_dwordx4 %[vx], s[42:43] sc1\nds_read_b128 v[104:107], %[pb] offset:49152\nds_read_b64_tr_b16 v[100:101], %[pa] offset:53248\n"
        "v_mfma_f32_32x32x16_bf16 v[64:79], v[116:119], v[120:123], v[64:79]\nglobal_load_lds_dwordx4 %[vx], s[42:43] offset:1024 sc1\n"
        "ds_read_b64_tr_b16 v[102:103], %[pa] offset:53760\nds_read_b128 v[108:111], %[pb] offset:51200\nv_mfma_f32_32x32x16_bf16 v[80:95], v[116:119], v[124:127], v[80:95]\n"
        "s_waitcnt lgkmcnt(0)\nv_mfma_f32_32x32x16_bf16 v[32:47], v[96:99], v[104:107], v[32:47]\nds_read_b64_tr_b16 v[112:113], %[pa] offset:51200\n"
        "ds_read_b64_tr_b16 v[114:115], %[pa] offset:51712\nv_mfma_f32_32x32x16_bf16 v[48:63], v[96:99], v[108:111], v[48:63]\nds_read_b128 v[120:123], %[pb] offset:50176\n"
        "ds_read_b64_tr_b16 v[116:117], %[pa] offset:55296\nv_mfma_f32_32x32x16_bf16 v[64:79], v[100:103], v[104:107], v[64:79]\nds_read_b64_tr_b16 v[118:119], %[pa] offset:55808\n"
        "ds_read_b128 v[124:127], %[pb] offset:52224\nv_mfma_f32_32x32x16_bf16 v[80:95], v[100:103], v[108:111], v[80:95]\ns_waitcnt vmcnt(3) lgkmcnt(0)\ns_barrier\n"
        "v_mfma_f32_32x32x16_bf16 v[32:47], v[112:115], v[120:123], v[32:47]\ns_add_u32 m0, %[dw], 49152\ns_add_u32 s40, s40, 0x1c000\ns_addc_u32 s41, s41, 0\n"
        "global_load_lds_dwordx4 %[vw], s[40:41]\nds_read_b64_tr_b16 v[96:97], %[pa] offset:0\nds_read_b64_tr_b16 v[98:99], %[pa] offset:512\n"
        "v_mfma_f32_32x32x16_bf16 v[48:63], v[112:115], v[124:127], v[48:63]\ns_add_u32 m0, %[dx], 49152\ns_add_u32 s42, s42, 0x800\ns_addc_u32 s43, s43, 0\n"
        "global_load_lds_dwordx4 %[vx], s[42:43] sc1\nds_read_b128 v[104:107], %[pb] offset:0\nds_read_b64_tr_b16 v[100:101], %[pa] offset:4096\n"
        "v_mfma_f32_32x32x16_bf16 v[64:79], v[116:119], v[120:123], v[64:79]\nglobal_load_lds_dwordx4 %[vx], s[42:43] offset:1024 sc1\n"
        "ds_read_b64_tr_b16 v[102:103], %[pa] offset:4608\nds_read_b128 v[108:111], %[pb] offset:2048\nv_mfma_f32_32x32x16_bf16 v[80:95], v[116:119], v[124:127], v[80:95]\n"
        "s_waitcnt lgkmcnt(0)\nv_mfma_f32_32x32x16_bf16 v[32:47], v[96:99], v[104:107], v[32:47]\nds_read_b64_tr_b16 v[112:113], %[pa] offset:2048\n"
        "ds_read_b64_tr_b16 v[114:115], %[pa] offset:2560\nv_mfma_f32_32x32x16_bf16 v[48:63], v[96:99], v[108:111], v[48:63]\nds_read_b128 v[120:123], %[pb] offset:1024\n"
        "ds_read_b64_tr_b16 v[116:117], %[pa] offset:6144\nv_mfma_f32_32x32x16_bf16 v[64:79], v[100:103], v[104:107], v[64:79]\nds_read_b64_tr_b16 v[118:119], %[pa] offset:6656\n"
        "ds_read_b128 v[124:127], %[pb] offset:3072\nv_mfma_f32_32x32x16_bf16 v[80:95], v[100:103], v[108:111], v[80:95]\ns_waitcnt vmcnt(3) lgkmcnt(0)\ns_barrier\n"
        "v_mfma_f32_32x32x16_bf16 v[32:47], v[112:115], v[120:123], v[32:47]\ns_add_u32 m0, %[dw], 0\ns_add_u32 s40, s40, 0x1c000\ns_addc_u32 s41, s41, 0\n"
        "global_load_lds_dwordx4 %[vw], s[40:41]\nds_read_b64_tr_b16 v[96:97], %[pa] offset:24576\nds_read_b64_tr_b16 v[98:99], %[pa] offset:25088\n"
        "v_mfma_f32_32x32x16_bf16 v[48:63], v[112:115], v[124:127], v[48:63]\ns_add_u32 m0, %[dx], 0\ns_add_u32 s42, s42, 0x800\ns_addc_u32 s43, s43, 0\n"
        "global_load_lds_dwordx4 %[vx], s[42:43] sc1\nds_read_b128 v[104:107], %[pb] offset:24576\nds_read_b64_tr_b16 v[100:101], %[pa] offset:28672\n"
        "v_mfma_f32_32x32x16_bf16 v[64:79], v[116:119], v[120:123], v[64:79]\nglobal_load_lds_dwordx4 %[vx], s[42:43] offset:1024 sc1\n"
        "ds_read_b64_tr_b16 v[102:103], %[pa] offset:29184\nds_read_b128 v[108:111], %[pb] offset:26624\nv_mfma_f32_32x32x16_bf16 v[80:95], v[116:119], v[124:127], v[80:95]\n"
        "s_waitcnt lgkmcnt(0)\nv_mfma_f32_32x32x16_bf16 v[32:47], v[96:99], v[104:107], v[32:47]\nds_read_b64_tr_b16 v[112:113], %[pa] offset:26624\n"
        "ds_read_b64_tr_b16 v[114:115], %[pa] offset:27136\nv_mfma_f32_32x32x16_bf16 v[48:63], v[96:99], v[108:111], v[48:63]\nds_read_b128 v[120:123], %[pb] offset:25600\n"
        "ds_read_b64_tr_b16 v[116:117], %[pa] offset:30720\nv_mfma_f32_32x32x16_bf16 v[64:79], v[100:103], v[104:107], v[64:79]\nds_read_b64_tr_b16 v[118:119], %[pa] offset:31232\n"
        "ds_read_b128 v[124:127], %[pb] offset:27648\nv_mfma_f32_32x32x16_bf16 v[80:95], v[100:103], v[108:111], v[80:95]\ns_waitcnt vmcnt(3) lgkmcnt(0)\ns_barrier\n"
        "v_mfma_f32_32x32x16_bf16 v[32:47], v[112:115], v[120:123], v[32:47]\nds_read_b64_tr_b16 v[96:97], %[pa] offset:49152\nds_read_b64_tr_b16 v[98:99], %[pa] offset:49664\n"
        "v_mfma_f32_32x32x16_bf16 v[48:63], v[112:115], v[124:127], v[48:63]\nds_read_b128 v[104:107], %[pb] offset:49152\nds_read_b64_tr_b16 v[100:101], %[pa] offset:53248\n"
        "v_mfma_f32_32x32x16_bf16 v[64:79], v[116:119], v[120:123], v[64:79]\nds_read_b64_tr_b16 v[102:103], %[pa] offset:53760\nds_read_b128 v[108:111], %[pb] offset:51200\n"
        "v_mfma_f32_32x32x16_bf16 v[80:95], v[116:119], v[124:127], v[80:95]\ns_waitcnt lgkmcnt(0)\nv_mfma_f32_32x32x16_bf16 v[32:47], v[96:99], v[104:107], v[32:47]\n"
        "ds_read_b64_tr_b16 v[112:113], %[pa] offset:51200\nds_read_b64_tr_b16 v[114:115], %[pa] offset:51712\nv_mfma_f32_32x32x16_bf16 v[48:63], v[96:99], v[108:111], v[48:63]\n"
        "ds_read_b128 v[120:123], %[pb] offset:50176\nds_read_b64_tr_b16 v[116:117], %[pa] offset:55296\nv_mfma_f32_32x32x16_bf16 v[64:79], v[100:103], v[104:107], v[64:79]\n"
        "ds_read_b64_tr_b16 v[118:119], %[pa] offset:55808\nds_read_b128 v[124:127], %[pb] offset:52224\nv_mfma_f32_32x32x16_bf16 v[80:95], v[100:103], v[108:111], v[80:95]\n"
        "s_waitcnt vmcnt(0) lgkmcnt(0)\ns_barrier\nv_mfma_f32_32x32x16_bf16 v[32:47], v[112:115], v[120:123], v[32:47]\nds_read_b64_tr_b16 v[96:97], %[pa] offset:0\n"
        "ds_read_b64_tr_b16 v[98:99], %[pa] offset:512\nv_mfma_f32_32x32x16_bf16 v[48:63], v[112:115], v[124:127], v[48:63]\nds_read_b128 v[104:107], %[pb] offset:0\n"
        "ds_read_b64_tr_b16 v[100:101], %[pa] offset:4096\nv_mfma_f32_32x32x16_bf16 v[64:79], v[116:119], v[120:123], v[64:79]\nds_read_b64_tr_b16 v[102:103], %[pa] offset:4608\n"
        "ds_read_b128 v[108:111], %[pb] offset:2048\nv_mfma_f32_32x32x16_bf16 v[80:95], v[116:119], v[124:127], v[80:95]\ns_waitcnt lgkmcnt(0)\n"
        "v_mfma_f32_32x32x16_bf16 v[32:47], v[96:99], v[104:107], v[32:47]\nds_read_b64_tr_b16 v[112:113], %[pa] offset:2048\nds_read_b64_tr_b16 v[114:115], %[pa] offset:2560\n"
        "v_mfma_f32_32x32x16_bf16 v[48:63], v[96:99], v[108:111], v[48:63]\nds_read_b128 v[120:123], %[pb] offset:1024\nds_read_b64_tr_b16 v[116:117], %[pa] offset:6144\n"
        "v_mfma_f32_32x32x16_bf16 v[64:79], v[100:103], v[104:107], v[64:79]\nds_read_b64_tr_b16 v[118:119], %[pa] offset:6656\nds_read_b128 v[124:127], %[pb] offset:3072\n"
        "v_mfma_f32_32x32x16_bf16 v[80:95], v[100:103], v[108:111], v[80:95]\ns_waitcnt lgkmcnt(0)\nv_mfma_f32_32x32x16_bf16 v[32:47], v[112:115], v[120:123], v[32:47]\n"
        "v_mfma_f32_32x32x16_bf16 v[48:63], v[112:115], v[124:127], v[48:63]\nv_mfma_f32_32x32x16_bf16 v[64:79], v[116:119], v[120:123], v[64:79]\n"
        "v_mfma_f32_32x32x16_bf16 v[80:95], v[116:119], v[124:127], v[80:95]\ns_mov_b32 m0, %[sv]\ns_nop 7\ns_nop 7\n"
          : "=&{v[32:47]}"(g0), "=&{v[48:63]}"(g1), "=&{v[64:79]}"(u0), "=&{v[80:95]}"(u1), [jk] "+v"(jk), [sv] "=&s"(sv)
          : [pa] "v"(pa), [pb] "v"(pb), [vw] "v"(vw), [vx] "v"(l16), [tv] "v"(tv), [dw] "s"(dw), [dx] "s"(dx), [sw] "s"(sw + 2 * 114688), [sx] "s"(sx + 2 * 2048), [wd] "s"(wd)
          : "v96", "v97", "v98", "v99", "v100", "v101", "v102", "v103", "v104", "v105", "v106", "v107", "v108", "v109", "v110", "v111", "v112", "v113", "v114", "v115", "v116", "v117", "v118", "v119", "v120", "v121", "v122", "v123", "v124", "v125", "v126", "v127", "s40", "s41", "s42", "s43", "scc", "memory");
        PRIO(P_EPI);
#pragma unroll
        for (int i = 0; i < 16; ++i) { g0[i] = fmaxf(g0[i], 0.f) * u0[i]; g1[i] = fmaxf(g1[i], 0.f) * u1[i]; }

        char* hp = ws + WS_H + ((TB * 8 + 2 * tk) * 112 + fb * 4 + ft * 2) * 1024;
        st_sc1(l16, hp, pack8(g0, 0)); st_sc1(l16, hp + 1024, pack8(g0, 8)); st_sc1(l16, hp + 114688, pack8(g1, 0)); st_sc1(l16, hp + 115712, pack8(g1, 8));
        BAR(0);
        if (tid == 0) set_flag(ws, FL_H + TB * 32 + fb, tag);
    } else {
        const int j = i5 - 28;
        if (w < 2) wait_flags(ws, FL_K + tid, tag);
        BARL(); if (tid == 0) set_flag(ws, FL_R + xcd * 4 + j, tag);
        __builtin_amdgcn_s_sleep(127);
        touch((j * 1024 + tid) * 128, wo); TOUCH_END(); touch((j * 1024 + 512 + tid) * 128, wo); TOUCH_END();
        if (b == 255) {
            wait_flags(ws, FL_A + tid, tag); BARL();
            if (tid == 0) set_flag(ws, FL_E, tag); }
    }

    const int tg = xcd * 4 + (i5 & 3), cg = i5 >> 2, t = w & 1, kq = w >> 1;
    const unsigned vo = (l >> 3) * 1024 + (((l & 7) ^ (((l >> 4) & 1) << 2)) << 4);
    const int at = (4 * h + q) * 128 + (((((l >> 4) & 1) * 2 + (p >> 1)) ^ ((q >> 1) << 2)) << 4) + 8 * (p & 1);
    float gg[8]; u32x2 x0, x1;
    const unsigned ob = (tg * 64 + t * 32 + (l & 31)) * 1024 + cg * 128 + kq * 32;
    {   const char* sw = uni(wd + cg * 128 + w * 14 * 16384); const unsigned dw = L0 + w * 8192;
        const int r0 = 2 * cg - 14;
#define ROT(n) ((n) + r0 < 0 ? (n) + r0 + 14 : (n) + r0)
#define F2_W(n) do { const char* s_ = sw + ROT(n) * 16384; glds16(vo, s_, dw + ((n) & 3) * 2048); glds16(vo, s_ + 8192, dw + ((n) & 3) * 2048 + 1024); } while (0)
        F2_W(0); F2_W(1); F2_W(2); F2_W(3);
        wait_flags(ws, FL_H + xcd * 32 + (w * 7 >> 1) + (l & 3), tag);

        const char* sh = uni(ws + WS_H + (2 * tg * 112 + w * 14) * 1024);
        f32x16 d00, d01, d10, d11; unsigned ro = ROT(0) * 1024, sv;
        PRIO(P_LOOP2);
        asm volatile(
        "s_mov_b32 %[sv], m0\ns_mov_b64 s[44:45], %[sh]\ns_mov_b64 s[46:47], %[sw]\ns_add_u32 s40, s44, %[ro]\ns_addc_u32 s41, s45, 0\n"
        "global_load_dwordx4 v[112:115], %[h0], s[40:41] sc1\nglobal_load_dwordx4 v[116:119], %[h1], s[40:41] sc1\ns_add_u32 %[ro], %[ro], 1024\ns_cmp_eq_u32 %[ro], 14336\n"
        "s_cselect_b32 %[ro], 0, %[ro]\ns_add_u32 s40, s44, %[ro]\ns_addc_u32 s41, s45, 0\nglobal_load_dwordx4 v[120:123], %[h0], s[40:41] sc1\n"
        "global_load_dwordx4 v[124:127], %[h1], s[40:41] sc1\ns_add_u32 %[ro], %[ro], 1024\ns_cmp_eq_u32 %[ro], 14336\ns_cselect_b32 %[ro], 0, %[ro]\ns_add_u32 s40, s44, %[ro]\n"
        "s_addc_u32 s41, s45, 0\nglobal_load_dwordx4 v[16:19], %[h0], s[40:41] sc1\nglobal_load_dwordx4 v[20:23], %[h1], s[40:41] sc1\ns_add_u32 %[ro], %[ro], 1024\n"
        "s_cmp_eq_u32 %[ro], 14336\ns_cselect_b32 %[ro], 0, %[ro]\ns_add_u32 s40, s44, %[ro]\ns_addc_u32 s41, s45, 0\nglobal_load_dwordx4 v[24:27], %[h0], s[40:41] sc1\n"
        "global_load_dwordx4 v[28:31], %[h1], s[40:41] sc1\ns_add_u32 %[ro], %[ro], 1024\ns_cmp_eq_u32 %[ro], 14336\ns_cselect_b32 %[ro], 0, %[ro]\ns_waitcnt vmcnt(8)\n"
        "ds_read_b64_tr_b16 v[96:97], %[a0] offset:0\nds_read_b64_tr_b16 v[98:99], %[a0] offset:1024\nds_read_b64_tr_b16 v[100:101], %[a1] offset:0\n"
        "ds_read_b64_tr_b16 v[102:103], %[a1] offset:1024\ns_waitcnt vmcnt(6) lgkmcnt(0)\nv_mfma_f32_32x32x16_bf16 v[32:47], v[96:99], v[112:115], 0\n"
        "ds_read_b64_tr_b16 v[104:105], %[a0] offset:2048\nds_read_b64_tr_b16 v[106:107], %[a0] offset:3072\ns_lshl_b32 s42, %[ro], 4\ns_add_u32 s42, s46, s42\n"
        "s_addc_u32 s43, s47, 0\ns_add_u32 m0, %[dw], 0\nv_mfma_f32_32x32x16_bf16 v[48:63], v[100:103], v[112:115], 0\nds_read_b64_tr_b16 v[108:109], %[a1] offset:2048\n"
        "ds_read_b64_tr_b16 v[110:111], %[a1] offset:3072\nglobal_load_lds_dwordx4 %[w0], s[42:43]\nv_mfma_f32_32x32x16_bf16 v[64:79], v[96:99], v[116:119], 0\n"
        "global_load_lds_dwordx4 %[w1], s[42:43] offset:1024\nv_mfma_f32_32x32x16_bf16 v[80:95], v[100:103], v[116:119], 0\ns_add_u32 s40, s44, %[ro]\ns_addc_u32 s41, s45, 0\n"
        "global_load_dwordx4 v[112:115], %[h0], s[40:41] sc1\nglobal_load_dwordx4 v[116:119], %[h1], s[40:41] sc1\ns_add_u32 %[ro], %[ro], 1024\ns_cmp_eq_u32 %[ro], 14336\n"
        "s_cselect_b32 %[ro], 0, %[ro]\ns_waitcnt vmcnt(8) lgkmcnt(0)\nv_mfma_f32_32x32x16_bf16 v[32:47], v[104:107], v[120:123], v[32:47]\n"
        "ds_read_b64_tr_b16 v[96:97], %[a0] offset:4096\nds_read_b64_tr_b16 v[98:99], %[a0] offset:5120\ns_lshl_b32 s42, %[ro], 4\ns_add_u32 s42, s46, s42\ns_addc_u32 s43, s47, 0\n"
        "s_add_u32 m0, %[dw], 2048\nv_mfma_f32_32x32x16_bf16 v[48:63], v[108:111], v[120:123], v[48:63]\nds_read_b64_tr_b16 v[100:101], %[a1] offset:4096\n"
        "ds_read_b64_tr_b16 v[102:103], %[a1] offset:5120\nglobal_load_lds_dwordx4 %[w0], s[42:43]\nv_mfma_f32_32x32x16_bf16 v[64:79], v[104:107], v[124:127], v[64:79]\n"
        "global_load_lds_dwordx4 %[w1], s[42:43] offset:1024\nv_mfma_f32_32x32x16_bf16 v[80:95], v[108:111], v[124:127], v[80:95]\ns_add_u32 s40, s44, %[ro]\n"
        "s_addc_u32 s41, s45, 0\nglobal_load_dwordx4 v[120:123], %[h0], s[40:41] sc1\nglobal_load_dwordx4 v[124:127], %[h1], s[40:41] sc1\ns_add_u32 %[ro], %[ro], 1024\n"
        "s_cmp_eq_u32 %[ro], 14336\ns_cselect_b32 %[ro], 0, %[ro]\ns_waitcnt vmcnt(10) lgkmcnt(0)\nv_mfma_f32_32x32x16_bf16 v[32:47], v[96:99], v[16:19], v[32:47]\n"
        "ds_read_b64_tr_b16 v[104:105], %[a0] offset:6144\nds_read_b64_tr_b16 v[106:107], %[a0] offset:7168\ns_lshl_b32 s42, %[ro], 4\ns_add_u32 s42, s46, s42\n"
        "s_addc_u32 s43, s47, 0\ns_add_u32 m0, %[dw], 4096\nv_mfma_f32_32x32x16_bf16 v[48:63], v[100:103], v[16:19], v[48:63]\nds_read_b64_tr_b16 v[108:109], %[a1] offset:6144\n"
        "ds_read_b64_tr_b16 v[110:111], %[a1] offset:7168\nglobal_load_lds_dwordx4 %[w0], s[42:43]\nv_mfma_f32_32x32x16_bf16 v[64:79], v[96:99], v[20:23], v[64:79]\n"
        "global_load_lds_dwordx4 %[w1], s[42:43] offset:1024\nv_mfma_f32_32x32x16_bf16 v[80:95], v[100:103], v[20:23], v[80:95]\ns_add_u32 s40, s44, %[ro]\ns_addc_u32 s41, s45, 0\n"
        "global_load_dwordx4 v[16:19], %[h0], s[40:41] sc1\nglobal_load_dwordx4 v[20:23], %[h1], s[40:41] sc1\ns_add_u32 %[ro], %[ro], 1024\ns_cmp_eq_u32 %[ro], 14336\n"
        "s_cselect_b32 %[ro], 0, %[ro]\ns_waitcnt vmcnt(10) lgkmcnt(0)\nv_mfma_f32_32x32x16_bf16 v[32:47], v[104:107], v[24:27], v[32:47]\n"
        "ds_read_b64_tr_b16 v[96:97], %[a0] offset:0\nds_read_b64_tr_b16 v[98:99], %[a0] offset:1024\ns_lshl_b32 s42, %[ro], 4\ns_add_u32 s42, s46, s42\ns_addc_u32 s43, s47, 0\n"
        "s_add_u32 m0, %[dw], 6144\nv_mfma_f32_32x32x16_bf16 v[48:63], v[108:111], v[24:27], v[48:63]\nds_read_b64_tr_b16 v[100:101], %[a1] offset:0\n"
        "ds_read_b64_tr_b16 v[102:103], %[a1] offset:1024\nglobal_load_lds_dwordx4 %[w0], s[42:43]\nv_mfma_f32_32x32x16_bf16 v[64:79], v[104:107], v[28:31], v[64:79]\n"
        "global_load_lds_dwordx4 %[w1], s[42:43] offset:1024\nv_mfma_f32_32x32x16_bf16 v[80:95], v[108:111], v[28:31], v[80:95]\ns_add_u32 s40, s44, %[ro]\ns_addc_u32 s41, s45, 0\n"
        "global_load_dwordx4 v[24:27], %[h0], s[40:41] sc1\nglobal_load_dwordx4 v[28:31], %[h1], s[40:41] sc1\ns_add_u32 %[ro], %[ro], 1024\ns_cmp_eq_u32 %[ro], 14336\n"
        "s_cselect_b32 %[ro], 0, %[ro]\ns_waitcnt vmcnt(10) lgkmcnt(0)\nv_mfma_f32_32x32x16_bf16 v[32:47], v[96:99], v[112:115], v[32:47]\n"
        "ds_read_b64_tr_b16 v[104:105], %[a0] offset:2048\nds_read_b64_tr_b16 v[106:107], %[a0] offset:3072\ns_lshl_b32 s42, %[ro], 4\ns_add_u32 s42, s46, s42\n"
        "s_addc_u32 s43, s47, 0\ns_add_u32 m0, %[dw], 0\nv_mfma_f32_32x32x16_bf16 v[48:63], v[100:103], v[112:115], v[48:63]\nds_read_b64_tr_b16 v[108:109], %[a1] offset:2048\n"
        "ds_read_b64_tr_b16 v[110:111], %[a1] offset:3072\nglobal_load_lds_dwordx4 %[w0], s[42:43]\nv_mfma_f32_32x32x16_bf16 v[64:79], v[96:99], v[116:119], v[64:79]\n"
        "global_load_lds_dwordx4 %[w1], s[42:43] offset:1024\nv_mfma_f32_32x32x16_bf16 v[80:95], v[100:103], v[116:119], v[80:95]\ns_add_u32 s40, s44, %[ro]\n"
        "s_addc_u32 s41, s45, 0\nglobal_load_dwordx4 v[112:115], %[h0], s[40:41] sc1\nglobal_load_dwordx4 v[116:119], %[h1], s[40:41] sc1\ns_add_u32 %[ro], %[ro], 1024\n"
        "s_cmp_eq_u32 %[ro], 14336\ns_cselect_b32 %[ro], 0, %[ro]\ns_waitcnt vmcnt(10) lgkmcnt(0)\nv_mfma_f32_32x32x16_bf16 v[32:47], v[104:107], v[120:123], v[32:47]\n"
        "ds_read_b64_tr_b16 v[96:97], %[a0] offset:4096\nds_read_b64_tr_b16 v[98:99], %[a0] offset:5120\ns_lshl_b32 s42, %[ro], 4\ns_add_u32 s42, s46, s42\ns_addc_u32 s43, s47, 0\n"
        "s_add_u32 m0, %[dw], 2048\nv_mfma_f32_32x32x16_bf16 v[48:63], v[108:111], v[120:123], v[48:63]\nds_read_b64_tr_b16 v[100:101], %[a1] offset:4096\n"
        "ds_read_b64_tr_b16 v[102:103], %[a1] offset:5120\nglobal_load_lds_dwordx4 %[w0], s[42:43]\nv_mfma_f32_32x32x16_bf16 v[64:79], v[104:107], v[124:127], v[64:79]\n"
        "global_load_lds_dwordx4 %[w1], s[42:43] offset:1024\nv_mfma_f32_32x32x16_bf16 v[80:95], v[108:111], v[124:127], v[80:95]\ns_add_u32 s40, s44, %[ro]\n"
        "s_addc_u32 s41, s45, 0\nglobal_load_dwordx4 v[120:123], %[h0], s[40:41] sc1\nglobal_load_dwordx4 v[124:127], %[h1], s[40:41] sc1\ns_add_u32 %[ro], %[ro], 1024\n"
        "s_cmp_eq_u32 %[ro], 14336\ns_cselect_b32 %[ro], 0, %[ro]\ns_waitcnt vmcnt(10) lgkmcnt(0)\nv_mfma_f32_32x32x16_bf16 v[32:47], v[96:99], v[16:19], v[32:47]\n"
        "ds_read_b64_tr_b16 v[104:105], %[a0] offset:6144\nds_read_b64_tr_b16 v[106:107], %[a0] offset:7168\ns_lshl_b32 s42, %[ro], 4\ns_add_u32 s42, s46, s42\n"
        "s_addc_u32 s43, s47, 0\ns_add_u32 m0, %[dw], 4096\nv_mfma_f32_32x32x16_bf16 v[48:63], v[100:103], v[16:19], v[48:63]\nds_read_b64_tr_b16 v[108:109], %[a1] offset:6144\n"
        "ds_read_b64_tr_b16 v[110:111], %[a1] offset:7168\nglobal_load_lds_dwordx4 %[w0], s[42:43]\nv_mfma_f32_32x32x16_bf16 v[64:79], v[96:99], v[20:23], v[64:79]\n"
        "global_load_lds_dwordx4 %[w1], s[42:43] offset:1024\nv_mfma_f32_32x32x16_bf16 v[80:95], v[100:103], v[20:23], v[80:95]\ns_add_u32 s40, s44, %[ro]\ns_addc_u32 s41, s45, 0\n"
        "global_load_dwordx4 v[16:19], %[h0], s[40:41] sc1\nglobal_load_dwordx4 v[20:23], %[h1], s[40:41] sc1\ns_add_u32 %[ro], %[ro], 1024\ns_cmp_eq_u32 %[ro], 14336\n"
        "s_cselect_b32 %[ro], 0, %[ro]\ns_waitcnt vmcnt(10) lgkmcnt(0)\nv_mfma_f32_32x32x16_bf16 v[32:47], v[104:107], v[24:27], v[32:47]\n"
        "ds_read_b64_tr_b16 v[96:97], %[a0] offset:0\nds_read_b64_tr_b16 v[98:99], %[a0] offset:1024\ns_lshl_b32 s42, %[ro], 4\ns_add_u32 s42, s46, s42\ns_addc_u32 s43, s47, 0\n"
        "s_add_u32 m0, %[dw], 6144\nv_mfma_f32_32x32x16_bf16 v[48:63], v[108:111], v[24:27], v[48:63]\nds_read_b64_tr_b16 v[100:101], %[a1] offset:0\n"
        "ds_read_b64_tr_b16 v[102:103], %[a1] offset:1024\nglobal_load_lds_dwordx4 %[w0], s[42:43]\nv_mfma_f32_32x32x16_bf16 v[64:79], v[104:107], v[28:31], v[64:79]\n"
        "global_load_lds_dwordx4 %[w1], s[42:43] offset:1024\nv_mfma_f32_32x32x16_bf16 v[80:95], v[108:111], v[28:31], v[80:95]\ns_add_u32 s40, s44, %[ro]\ns_addc_u32 s41, s45, 0\n"
        "global_load_dwordx4 v[24:27], %[h0], s[40:41] sc1\nglobal_load_dwordx4 v[28:31], %[h1], s[40:41] sc1\ns_add_u32 %[ro], %[ro], 1024\ns_cmp_eq_u32 %[ro], 14336\n"
        "s_cselect_b32 %[ro], 0, %[ro]\ns_waitcnt vmcnt(10) lgkmcnt(0)\nv_mfma_f32_32x32x16_bf16 v[32:47], v[96:99], v[112:115], v[32:47]\n"
        "ds_read_b64_tr_b16 v[104:105], %[a0] offset:2048\nds_read_b64_tr_b16 v[106:107], %[a0] offset:3072\ns_lshl_b32 s42, %[ro], 4\ns_add_u32 s42, s46, s42\n"
        "s_addc_u32 s43, s47, 0\ns_add_u32 m0, %[dw], 0\nv_mfma_f32_32x32x16_bf16 v[48:63], v[100:103], v[112:115], v[48:63]\nds_read_b64_tr_b16 v[108:109], %[a1] offset:2048\n"
        "ds_read_b64_tr_b16 v[110:111], %[a1] offset:3072\nglobal_load_lds_dwordx4 %[w0], s[42:43]\nv_mfma_f32_32x32x16_bf16 v[64:79], v[96:99], v[116:119], v[64:79]\n"
        "global_load_lds_dwordx4 %[w1], s[42:43] offset:1024\nv_mfma_f32_32x32x16_bf16 v[80:95], v[100:103], v[116:119], v[80:95]\ns_add_u32 s40, s44, %[ro]\n"
        "s_addc_u32 s41, s45, 0\nglobal_load_dwordx4 v[112:115], %[h0], s[40:41] sc1\nglobal_load_dwordx4 v[116:119], %[h1], s[40:41] sc1\ns_add_u32 %[ro], %[ro], 1024\n"
        "s_cmp_eq_u32 %[ro], 14336\ns_cselect_b32 %[ro], 0, %[ro]\ns_waitcnt vmcnt(10) lgkmcnt(0)\nv_mfma_f32_32x32x16_bf16 v[32:47], v[104:107], v[120:123], v[32:47]\n"
        "ds_read_b64_tr_b16 v[96:97], %[a0] offset:4096\nds_read_b64_tr_b16 v[98:99], %[a0] offset:5120\ns_lshl_b32 s42, %[ro], 4\ns_add_u32 s42, s46, s42\ns_addc_u32 s43, s47, 0\n"
        "s_add_u32 m0, %[dw], 2048\nv_mfma_f32_32x32x16_bf16 v[48:63], v[108:111], v[120:123], v[48:63]\nds_read_b64_tr_b16 v[100:101], %[a1] offset:4096\n"
        "ds_read_b64_tr_b16 v[102:103], %[a1] offset:5120\nglobal_load_lds_dwordx4 %[w0], s[42:43]\nv_mfma_f32_32x32x16_bf16 v[64:79], v[104:107], v[124:127], v[64:79]\n"
        "global_load_lds_dwordx4 %[w1], s[42:43] offset:1024\nv_mfma_f32_32x32x16_bf16 v[80:95], v[108:111], v[124:127], v[80:95]\ns_add_u32 s40, s44, %[ro]\n"
        "s_addc_u32 s41, s45, 0\nglobal_load_dwordx4 v[120:123], %[h0], s[40:41] sc1\nglobal_load_dwordx4 v[124:127], %[h1], s[40:41] sc1\ns_add_u32 %[ro], %[ro], 1024\n"
        "s_cmp_eq_u32 %[ro], 14336\ns_cselect_b32 %[ro], 0, %[ro]\ns_waitcnt vmcnt(10) lgkmcnt(0)\nv_mfma_f32_32x32x16_bf16 v[32:47], v[96:99], v[16:19], v[32:47]\n"
        "ds_read_b64_tr_b16 v[104:105], %[a0] offset:6144\nds_read_b64_tr_b16 v[106:107], %[a0] offset:7168\nv_mfma_f32_32x32x16_bf16 v[48:63], v[100:103], v[16:19], v[48:63]\n"
        "ds_read_b64_tr_b16 v[108:109], %[a1] offset:6144\nds_read_b64_tr_b16 v[110:111], %[a1] offset:7168\nv_mfma_f32_32x32x16_bf16 v[64:79], v[96:99], v[20:23], v[64:79]\n"
        "v_mfma_f32_32x32x16_bf16 v[80:95], v[100:103], v[20:23], v[80:95]\ns_waitcnt vmcnt(6) lgkmcnt(0)\nv_mfma_f32_32x32x16_bf16 v[32:47], v[104:107], v[24:27], v[32:47]\n"
        "ds_read_b64_tr_b16 v[96:97], %[a0] offset:0\nds_read_b64_tr_b16 v[98:99], %[a0] offset:1024\nv_mfma_f32_32x32x16_bf16 v[48:63], v[108:111], v[24:27], v[48:63]\n"
        "ds_read_b64_tr_b16 v[100:101], %[a1] offset:0\nds_read_b64_tr_b16 v[102:103], %[a1] offset:1024\nv_mfma_f32_32x32x16_bf16 v[64:79], v[104:107], v[28:31], v[64:79]\n"
        "v_mfma_f32_32x32x16_bf16 v[80:95], v[108:111], v[28:31], v[80:95]\ns_waitcnt vmcnt(2) lgkmcnt(0)\nv_mfma_f32_32x32x16_bf16 v[32:47], v[96:99], v[112:115], v[32:47]\n"
        "ds_read_b64_tr_b16 v[104:105], %[a0] offset:2048\nds_read_b64_tr_b16 v[106:107], %[a0] offset:3072\nv_mfma_f32_32x32x16_bf16 v[48:63], v[100:103], v[112:115], v[48:63]\n"
        "ds_read_b64_tr_b16 v[108:109], %[a1] offset:2048\nds_read_b64_tr_b16 v[110:111], %[a1] offset:3072\nv_mfma_f32_32x32x16_bf16 v[64:79], v[96:99], v[116:119], v[64:79]\n"
        "v_mfma_f32_32x32x16_bf16 v[80:95], v[100:103], v[116:119], v[80:95]\ns_waitcnt vmcnt(0) lgkmcnt(0)\nv_mfma_f32_32x32x16_bf16 v[32:47], v[104:107], v[120:123], v[32:47]\n"
        "v_mfma_f32_32x32x16_bf16 v[48:63], v[108:111], v[120:123], v[48:63]\nv_mfma_f32_32x32x16_bf16 v[64:79], v[104:107], v[124:127], v[64:79]\n"
        "v_mfma_f32_32x32x16_bf16 v[80:95], v[108:111], v[124:127], v[80:95]\ns_mov_b32 m0, %[sv]\ns_nop 7\ns_nop 7\n"
          : "=&{v[32:47]}"(d00), "=&{v[48:63]}"(d01), "=&{v[64:79]}"(d10), "=&{v[80:95]}"(d11), [ro] "+s"(ro), [sv] "=&s"(sv)
          : [a0] "v"(dw + at), [a1] "v"(dw + (at ^ 64)), [h0] "v"(l16), [h1] "v"(l16 + 114688), [w0] "v"(vo), [w1] "v"(vo + 7168), [dw] "s"(dw), [sh] "s"(sh), [sw] "s"(sw)
          : "v96", "v97", "v98", "v99", "v100", "v101", "v102", "v103", "v104", "v105", "v106", "v107", "v108", "v109", "v110", "v111", "v112", "v113", "v114", "v115", "v116", "v117", "v118", "v119", "v120", "v121", "v122", "v123", "v124", "v125", "v126", "v127", "v16", "v17", "v18", "v19", "v20", "v21", "v22", "v23", "v24", "v25", "v26", "v27", "v28", "v29", "v30", "v31", "s40", "s41", "s42", "s43", "s44", "s45", "s46", "s47", "scc", "memory");
        PRIO(P_EPI);

        asm volatile("global_load_dwordx2 %0, %2, %3\n\tglobal_load_dwordx2 %1, %2, %3 offset:16" : "=&v"(x0), "=&v"(x1) : "v"(ob + 8 * h), "s"(x) : "memory");
        const lptr pw = L + w * 2048 + l16, pr = L + kq * 16384 + l16; f32x4 a, c;
#define F2_RED(dA, dB, tt) do { BARL(); _Pragma("unroll") for (int u = 0; u < 4; ++u) { lw128f(pw + (u >> 1) * 16384 + (u & 1) * 1024, dA[4 * u], dA[4 * u + 1], dA[4 * u + 2], dA[4 * u + 3]); \
            lw128f(pw + 32768 + (u >> 1) * 16384 + (u & 1) * 1024, dB[4 * u], dB[4 * u + 1], dB[4 * u + 2], dB[4 * u + 3]); } BARL(); \
            if (t == tt) { a = lr128f(pr); c = lr128f(pr + 1024); _Pragma("unroll") for (int u = 1; u < 8; ++u) { a += lr128f(pr + u * 2048); c += lr128f(pr + u * 2048 + 1024); } } } while (0)
        F2_RED(d00, d01, 0); F2_RED(d10, d11, 1);
#pragma unroll
        for (int i = 0; i < 4; ++i) { gg[i] = a[i] * __builtin_amdgcn_rcpf(1.f + __builtin_amdgcn_exp2f(a[i] * -1.4426950408889634f));
                                      gg[4 + i] = c[i] * __builtin_amdgcn_rcpf(1.f + __builtin_amdgcn_exp2f(c[i] * -1.4426950408889634f)); } }
    BARL();
#pragma unroll
    for (int n = 0; n < 8; ++n) glds16(vo, wo + cg * 128 + (w * 8 + n) * 8192, L0 + (w * 8 + n) * 1024);
    BAR(0); asm volatile("" : "+v"(x0), "+v"(x1));
    wait_flags(ws, FL_O + tg * 8 + 2 * kq + (l & 1), tag);

    bf16x8 of[8];
    { const char* so = uni(ws + WS_O + ((2 * tg + t) * 32 + 8 * kq) * 1024);
#pragma unroll
      for (int s = 0; s < 8; ++s) LD_SC1(of[s], l16, so + s * 1024); }
    f32x16 c0 = splat16(0.f), c1 = c0;
    const lptr a0 = L + kq * 16384 + at, a1 = L + kq * 16384 + (at ^ 64);
#pragma unroll
    for (int s = 0; s < 8; ++s) { const bf16x8 f0 = ltr(a0, s * 2048, s * 2048 + 1024), f1 = ltr(a1, s * 2048, s * 2048 + 1024);
        asm volatile("s_waitcnt vmcnt(%1)" : "+v"(of[s]) : "n"(7 - s) : "memory");
        c0 = MFMA(f0, of[s], c0); c1 = MFMA(f1, of[s], c1); }
    BARL();
    const lptr pw = L + (t * 16 + kq) * 2048 + l16;
#pragma unroll
    for (int u = 0; u < 4; ++u) { lw128f(pw + (u >> 1) * 8192 + (u & 1) * 1024, c0[4 * u], c0[4 * u + 1], c0[4 * u + 2], c0[4 * u + 3]);
                                  lw128f(pw + 16384 + (u >> 1) * 8192 + (u & 1) * 1024, c1[4 * u], c1[4 * u + 1], c1[4 * u + 2], c1[4 * u + 3]); }
    BARL();
    const lptr pr = L + (t * 16 + kq * 4) * 2048 + l16; f32x4 a = lr128f(pr), c = lr128f(pr + 1024);
#pragma unroll
    for (int u = 1; u < 4; ++u) { a += lr128f(pr + u * 2048); c += lr128f(pr + u * 2048 + 1024); }
    const unsigned A0 = pk(blo(x0[0]) + a[0] * gg[0], bhi(x0[0]) + a[1] * gg[1]), A1 = pk(blo(x0[1]) + a[2] * gg[2], bhi(x0[1]) + a[3] * gg[3]);
    const unsigned B0 = pk(blo(x1[0]) + c[0] * gg[4], bhi(x1[0]) + c[1] * gg[5]), B1 = pk(blo(x1[1]) + c[2] * gg[6], bhi(x1[1]) + c[3] * gg[7]);
    auto s0 = __builtin_amdgcn_permlane32_swap(A0, B0, false, false), s1 = __builtin_amdgcn_permlane32_swap(A1, B1, false, false);
    const u32x4 ov = {s0[0], s1[0], s0[1], s1[1]};
    *(u32x4*)(out + ob + 16 * h) = ov;

}

__global__ __launch_bounds__(512, 4) void k2_kernel(const char* x, const char* wq, const char* wk, const char* wv, const char* wo, const char* wg, const char* wu, const char* wd, char* out, char* ws) {
    extern __shared__ char smem[];
    const lptr L = (lptr)smem; const unsigned L0 = (unsigned)(size_t)L;
    const int b = blockIdx.x;
    PRIO(3);
    if (b < 256) attn_role(b, L, L0, wq, ws); else ffn_role(b - 256, L, L0, x, wk, wv, wo, wg, wu, wd, out, ws);
}

extern "C" void kernel_launch(void* const* d_in, const int* in_sizes, int n_in, void* d_out, int out_size, void* d_ws, size_t ws_size, hipStream_t stream) {
    (void)hipFuncSetAttribute((const void*)k2_kernel, hipFuncAttributeMaxDynamicSharedMemorySize, LDS_K2);
    k2_kernel<<<512, 512, LDS_K2, stream>>>((const char*)d_in[0], (const char*)d_in[1], (const char*)d_in[2], (const char*)d_in[3], (const char*)d_in[4], (const char*)d_in[6], (const char*)d_in[7], (const char*)d_in[8], (char*)d_out, (char*)d_ws);
}
```
